```python
import torch

def _softmax(x, axis=-1):
    return torch.softmax(x, dim=axis)
def _gelu(x):
    return torch.nn.functional.gelu(x, approximate="tanh")
def _silu(x):
    return torch.nn.functional.silu(x)
def _sigmoid(x):
    return torch.sigmoid(x)
def _layernorm(x, axis=-1, eps=1e-5):
    m = x.mean(dim=axis, keepdim=True)
    v = x.var(dim=axis, keepdim=True, unbiased=False)
    return (x - m) / torch.sqrt(v + eps)
def _scatter_add(tgt, idx, src, axis=0):
    return tgt.clone().index_add_(axis, idx, src)
def _segment_sum(src, seg, n):
    out = torch.zeros((n,) + src.shape[1:], dtype=src.dtype)
    return out.index_add_(0, seg, src)
def _pad(x, pw):
    flat = tuple(v for pair in reversed(pw) for v in pair)
    return torch.nn.functional.pad(x, flat)

kernel_name = 'interleave_layers__gqa_block_cached__dense_activation_silu__decode'

def setup_inputs():

    _w = (lambda a: (lambda t: t.to(torch.bfloat16) if t.is_floating_point() else t)(torch.as_tensor(a)))
    def _A_setup():
        B, Sq, D = 8, 1, 512
        Hq, Hkv, Sk, Dh = 32, 8, 8192, 128
        assert Hq % Hkv == 0, (Hq, Hkv)
        return {
            "x": _w((lambda *s: torch.randn(*s, dtype=torch.bfloat16) * 0.1)(B, Sq, D)),
            "k_cache": _w((lambda *s: torch.randn(*s, dtype=torch.bfloat16) * 0.8)(B, Hkv, Sk, Dh)),
            "v_cache": _w((lambda *s: torch.randn(*s, dtype=torch.bfloat16) * 3.2)(B, Hkv, Sk, Dh)),
            "wq": _w((lambda *s: torch.randn(*s, dtype=torch.bfloat16) * 0.8)(D, Hq * Dh) * (D ** -0.5)),
            "wo": _w((lambda *s: torch.randn(*s, dtype=torch.bfloat16) * 3.2)(Hq * Dh, D) * ((Hq * Dh) ** -0.5)),
        }
    _A_in = _A_setup()

    def _B_setup():
        B, S, D, F = 8, 1, 512, 1792
        return {
            "x": _w((lambda *s: torch.randn(*s, dtype=torch.bfloat16) * 0.1)(B, S, D)),
            "w1": _w((lambda *s: torch.randn(*s, dtype=torch.bfloat16) * 1.6)(D, F) * (D ** -0.5)),
            "w2": _w((lambda *s: torch.randn(*s, dtype=torch.bfloat16) * 0.4)(F, D) * (F ** -0.5)),
        }
    _B_in = _B_setup()

    return {
        "A_x": _A_in["x"],
        "A_k_cache": _A_in["k_cache"],
        "A_v_cache": _A_in["v_cache"],
        "A_wq": _A_in["wq"],
        "A_wo": _A_in["wo"],
        "B_x": _B_in["x"],
        "B_w1": _B_in["w1"],
        "B_w2": _B_in["w2"],
    }

def reference(A_x, A_k_cache, A_v_cache, A_wq, A_wo, B_x, B_w1, B_w2):
    def _A_ref(x, k_cache, v_cache, wq, wo):
        B, Sq, D = x.shape
        Hq, Dh = 32, 128
        Hkv = k_cache.shape[1]
        rep = Hq // Hkv
        q = (lambda x, *d: x.permute(*d))((x @ wq).reshape(B, Sq, Hq, Dh), 0, 2, 1, 3)
        k = (lambda x, n, axis: torch.repeat_interleave(x, n, dim=axis))(k_cache, rep, axis=1)
        v = (lambda x, n, axis: torch.repeat_interleave(x, n, dim=axis))(v_cache, rep, axis=1)
        scores = torch.einsum("bhqd,bhkd->bhqk", q, k) * (Dh ** -0.5)
        p = _softmax(scores, axis=-1)
        o = torch.einsum("bhqk,bhkd->bhqd", p, v)
        return (lambda x, *d: x.permute(*d))(o, 0, 2, 1, 3).reshape(B, Sq, Hq * Dh) @ wo

    def _B_ref(x, w1, w2):
        h = torch.einsum("bsd,df->bsf", x, w1)
        h = _silu(h)
        out = torch.einsum("bsf,fd->bsd", h, w2)
        return out

    x = A_x
    x = x + _A_ref(_layernorm(x, axis=-1), A_k_cache, A_v_cache, A_wq, A_wo)
    x = x + _B_ref(_layernorm(x, axis=-1), B_w1, B_w2)
    x = x + _A_ref(_layernorm(x, axis=-1), A_k_cache, A_v_cache, A_wq, A_wo)
    x = x + _B_ref(_layernorm(x, axis=-1), B_w1, B_w2)
    return x
```

```cpp
#include <hip/hip_runtime.h>
#include <hip/hip_bf16.h>
#include <stdint.h>

typedef unsigned short u16;
using u32x4  = __attribute__((ext_vector_type(4))) unsigned;
using f32x4  = __attribute__((ext_vector_type(4))) float;
using bf16x2 = __attribute__((ext_vector_type(2))) __bf16;

constexpr int B = 8, D = 512, HQ = 32, HKV = 8, SK = 8192, DH = 128, F = 1792;
constexpr int NSPLIT = 8;

__device__ __forceinline__ float bf2f(u16 v) { return __uint_as_float((unsigned)v << 16); }
__device__ __forceinline__ u16 f2bf(float f) { unsigned u = __float_as_uint(f); u += 0x7FFFu + ((u >> 16) & 1u); return (u16)(u >> 16); }
__device__ __forceinline__ float dot2(unsigned k, unsigned q, float acc) {
  return __builtin_amdgcn_fdot2_f32_bf16(__builtin_bit_cast(bf16x2, k), __builtin_bit_cast(bf16x2, q), acc, false);
}
template <int CTRL> __device__ __forceinline__ float dpp(float x) {
  return __builtin_bit_cast(float, __builtin_amdgcn_mov_dpp(__builtin_bit_cast(int, x), CTRL, 0xf, 0xf, true));
}
constexpr int XOR1 = 0xB1, XOR2 = 0x4E, XOR7 = 0x141, XOR8 = 0x128, ROR4 = 0x124;
__device__ __forceinline__ float xrow16_max(float x) {
  auto s = __builtin_amdgcn_permlane16_swap(__float_as_uint(x), __float_as_uint(x), false, false);
  x = fmaxf(__uint_as_float(s[0]), __uint_as_float(s[1]));
  auto t = __builtin_amdgcn_permlane32_swap(__float_as_uint(x), __float_as_uint(x), false, false);
  return fmaxf(__uint_as_float(t[0]), __uint_as_float(t[1]));
}
__device__ __forceinline__ float xrow16_sum(float x) {
  auto s = __builtin_amdgcn_permlane16_swap(__float_as_uint(x), __float_as_uint(x), false, false);
  x = __uint_as_float(s[0]) + __uint_as_float(s[1]);
  auto t = __builtin_amdgcn_permlane32_swap(__float_as_uint(x), __float_as_uint(x), false, false);
  return __uint_as_float(t[0]) + __uint_as_float(t[1]);
}

template <int MODE>
__global__ void __launch_bounds__(64) k_ln_gemv(const float* __restrict__ xin, const u16* __restrict__ W, int N, float* __restrict__ out) {
  __shared__ float ln[B][D];
  const int tid = threadIdx.x;
  for (int b = 0; b < B; ++b) {
    float v[8]; float s = 0.f;
    for (int i = 0; i < 8; ++i) { v[i] = xin[b * D + tid + 64 * i]; s += v[i]; }
    for (int o = 32; o; o >>= 1) s += __shfl_xor(s, o);
    const float m = s * (1.f / D);
    float q = 0.f;
    for (int i = 0; i < 8; ++i) { float d = v[i] - m; q += d * d; }
    for (int o = 32; o; o >>= 1) q += __shfl_xor(q, o);
    const float r = 1.f / sqrtf(q * (1.f / D) + 1e-5f);
    for (int i = 0; i < 8; ++i) ln[b][tid + 64 * i] = (v[i] - m) * r;
  }
  __syncthreads();
  const int n = blockIdx.x * 64 + tid;
  if (n >= N) return;
  float acc[B];
  for (int b = 0; b < B; ++b) acc[b] = 0.f;
  for (int k = 0; k < D; ++k) {
    const float w = bf2f(W[(size_t)k * N + n]);
    for (int b = 0; b < B; ++b) acc[b] = fmaf(ln[b][k], w, acc[b]);
  }
  for (int b = 0; b < B; ++b) {
    float r = acc[b];
    if (MODE == 1) r = r / (1.f + __expf(-r));
    out[b * N + n] = r;
  }
}

__global__ void __launch_bounds__(256) k_gemv_res(const float* __restrict__ in, int K, const u16* __restrict__ W, const float* __restrict__ xres, float* __restrict__ outf, u16* __restrict__ outb) {
  __shared__ float red[16][B][16];
  const int c = threadIdx.x & 15, kp = threadIdx.x >> 4;
  const int n = blockIdx.x * 16 + c;
  float acc[B];
  for (int b = 0; b < B; ++b) acc[b] = 0.f;
  for (int k = kp; k < K; k += 16) {
    const float w = bf2f(W[(size_t)k * D + n]);
    for (int b = 0; b < B; ++b) acc[b] = fmaf(in[b * K + k], w, acc[b]);
  }
  for (int b = 0; b < B; ++b) red[kp][b][c] = acc[b];
  __syncthreads();
  if (threadIdx.x < 128) {
    const int b = threadIdx.x >> 4;
    float s = 0.f;
    for (int j = 0; j < 16; ++j) s += red[j][b][c];
    s += xres[b * D + n];
    if (outf) outf[b * D + n] = s;
    if (outb) outb[b * D + n] = f2bf(s);
  }
}

__global__ void k_cvt(const u16* __restrict__ x, float* __restrict__ o) { int i = blockIdx.x * 256 + threadIdx.x; o[i] = bf2f(x[i]); }

__global__ void __launch_bounds__(256) k_attn(const float* __restrict__ q, const u16* __restrict__ kc, const u16* __restrict__ vc, float* __restrict__ pml, float* __restrict__ po) {
  constexpr int G = 4, WAVES = 4;
  __shared__ __attribute__((aligned(16))) float lds[4 * 64 + 4 * 4 * 132];
  const int s = blockIdx.x, kvh = blockIdx.y, b = blockIdx.z;
  const int tid = threadIdx.x, lane = tid & 63, wave = __builtin_amdgcn_readfirstlane(tid >> 6), r = lane >> 4, c = lane & 15;
  constexpr float C2 = 0.088388347648318440f * 1.4426950408889634f;
  constexpr int PPS = SK / 16 / NSPLIT, PPW = PPS / WAVES;
  u32x4 qpk[G];
  for (int g = 0; g < G; ++g) {
    const float* qp = q + b * 4096 + (kvh * G + g) * DH + 8 * c;
    for (int k = 0; k < 4; ++k) qpk[g][k] = (unsigned)f2bf(qp[2 * k]) | ((unsigned)f2bf(qp[2 * k + 1]) << 16);
  }
  const size_t base = ((size_t)(b * HKV + kvh) * SK + (size_t)s * PPS * 16) * DH + (size_t)r * DH + 8 * c;
  u32x4 Kn[4], Vn[4];
  float m = -INFINITY, l = 0.f, acc[G * 8];
  for (int k = 0; k < G * 8; ++k) acc[k] = 0.f;
#define ISSUE(i_) do { const u16* kb_ = kc + base + (size_t)(wave + 4 * (i_)) * 16 * DH; const u16* vb_ = vc + base + (size_t)(wave + 4 * (i_)) * 16 * DH; \
    _Pragma("unroll") for (int t_ = 0; t_ < 4; ++t_) { Kn[t_] = __builtin_nontemporal_load(reinterpret_cast<const u32x4*>(kb_ + t_ * 4 * DH)); Vn[t_] = __builtin_nontemporal_load(reinterpret_cast<const u32x4*>(vb_ + t_ * 4 * DH)); } } while (0)
  ISSUE(0);
  for (int i = 0; i < PPW; ++i) {
    u32x4 Kc[4], Vc[4];
#pragma unroll
    for (int t = 0; t < 4; ++t) { Kc[t] = Kn[t]; Vc[t] = Vn[t]; }
    if (i + 1 < PPW) ISSUE(i + 1);
    float x[16];
#pragma unroll
    for (int t = 0; t < 4; ++t)
#pragma unroll
      for (int g = 0; g < G; ++g) { float d = 0.f;
#pragma unroll
        for (int k = 0; k < 4; ++k) d = dot2(Kc[t][k], qpk[g][k], d);
        x[t * 4 + g] = d; }
#define TR_STEP(HALF, CTRL, BIT) _Pragma("unroll") for (int h = 0; h < (HALF); ++h) { \
      const float lo_ = x[h], hi_ = x[h + (HALF)]; const float t1_ = lo_ + dpp<CTRL>(lo_), t2_ = hi_ + dpp<CTRL>(hi_); \
      x[h] = (c & (BIT)) ? t2_ : t1_; }
    TR_STEP(8, XOR8, 8) TR_STEP(4, XOR7, 4) TR_STEP(2, XOR2, 2) TR_STEP(1, XOR1, 1)
#undef TR_STEP
    const float sc = x[0] * C2;
    float pm = sc; pm = fmaxf(pm, dpp<ROR4>(pm)); pm = fmaxf(pm, dpp<XOR8>(pm)); pm = xrow16_max(pm);
    const float mn = fmaxf(m, pm);
    const float alpha = __builtin_amdgcn_exp2f(m - mn), p = __builtin_amdgcn_exp2f(sc - mn);
    l = fmaf(l, alpha, p); m = mn;
#pragma unroll
    for (int q4 = 0; q4 < 4; ++q4) {
      const float al = q4 == 0 ? dpp<0x00>(alpha) : q4 == 1 ? dpp<0x55>(alpha) : q4 == 2 ? dpp<0xAA>(alpha) : dpp<0xFF>(alpha);
#pragma unroll
      for (int k = 0; k < 8; ++k) acc[q4 * 8 + k] *= al; }
    float* prow = lds + (wave * 4 + r) * 16;
    prow[c] = p;
    asm volatile("s_waitcnt lgkmcnt(0)" ::: "memory");
    f32x4 pr[4];
#pragma unroll
    for (int u = 0; u < 4; ++u) pr[u] = *reinterpret_cast<const f32x4*>(prow + 4 * u);
#pragma unroll
    for (int t = 0; t < 4; ++t) {
      float vf[8];
#pragma unroll
      for (int k = 0; k < 4; ++k) { vf[2 * k] = __builtin_bit_cast(float, Vc[t][k] << 16); vf[2 * k + 1] = __builtin_bit_cast(float, Vc[t][k] & 0xffff0000u); }
#pragma unroll
      for (int g = 0; g < G; ++g) { const float pg_ = pr[t][g];
#pragma unroll
        for (int k = 0; k < 8; ++k) acc[g * 8 + k] = fmaf(pg_, vf[k], acc[g * 8 + k]); } }
  }
#pragma unroll
  for (int k = 0; k < G * 8; ++k) acc[k] = xrow16_sum(acc[k]);
  { float t = l; t += dpp<ROR4>(t); t += dpp<XOR8>(t); l = xrow16_sum(t); }
  float* mrg = lds + 256;
  if (r == 0) {
#pragma unroll
    for (int g = 0; g < G; ++g) {
      float* rec = mrg + (wave * 4 + g) * 132;
#pragma unroll
      for (int k = 0; k < 8; k += 4) *reinterpret_cast<f32x4*>(rec + 4 + 8 * c + k) = f32x4{acc[g * 8 + k], acc[g * 8 + k + 1], acc[g * 8 + k + 2], acc[g * 8 + k + 3]};
      if (c == g) { rec[0] = m; rec[1] = l; } } }
  __syncthreads();
  {
    const int g = wave, d0 = 2 * lane;
    const size_t row = (size_t)b * HQ + kvh * G + g;
    float M = -INFINITY;
    for (int w = 0; w < WAVES; ++w) M = fmaxf(M, mrg[(w * 4 + g) * 132]);
    float L = 0.f, o0 = 0.f, o1 = 0.f;
    for (int w = 0; w < WAVES; ++w) { const float* rec = mrg + (w * 4 + g) * 132; const float lw = rec[1];
      const float wt = __builtin_amdgcn_exp2f(rec[0] - M);
      L = fmaf(wt, lw, L); o0 = fmaf(wt, rec[4 + d0], o0); o1 = fmaf(wt, rec[4 + d0 + 1], o1); }
    const size_t slot = row * NSPLIT + s;
    *reinterpret_cast<float2*>(po + slot * DH + d0) = make_float2(o0, o1);
    if (lane == 0) *reinterpret_cast<float2*>(pml + 2 * slot) = make_float2(M, L);
  }
}
__global__ void __launch_bounds__(64) k_combine(const float* __restrict__ pml, const float* __restrict__ po, float* __restrict__ o) {
  const int row = blockIdx.x; const int d0 = 2 * threadIdx.x;
  const float* ml = pml + (size_t)row * NSPLIT * 2; const float* p = po + (size_t)row * NSPLIT * DH;
  float M = -INFINITY;
  for (int s = 0; s < NSPLIT; ++s) M = fmaxf(M, ml[2 * s]);
  float L = 0.f, o0 = 0.f, o1 = 0.f;
  for (int s = 0; s < NSPLIT; ++s) { const float w = __builtin_amdgcn_exp2f(ml[2 * s] - M); L = fmaf(w, ml[2 * s + 1], L); o0 = fmaf(w, p[s * DH + d0], o0); o1 = fmaf(w, p[s * DH + d0 + 1], o1); }
  o[row * DH + d0] = o0 / L; o[row * DH + d0 + 1] = o1 / L;
}

extern "C" void kernel_launch(void* const* d_in, const int* in_sizes, int n_in, void* d_out, int out_size, void* d_ws, size_t ws_size, hipStream_t stream) {
  const u16* x = (const u16*)d_in[0]; const u16* kc = (const u16*)d_in[1]; const u16* vc = (const u16*)d_in[2];
  const u16* wq = (const u16*)d_in[3]; const u16* wo = (const u16*)d_in[4]; const u16* w1 = (const u16*)d_in[6]; const u16* w2 = (const u16*)d_in[7];
  float* ws = (float*)d_ws;
  float* x0 = ws; float* x1 = ws + 4096; float* x2 = ws + 8192; float* q = ws + 16384; float* o = q + 32768; float* h = o + 32768; float* pml = h + 32768; float* po = pml + 8 * 32 * NSPLIT * 2;
  k_cvt<<<16, 256, 0, stream>>>(x, x0);
  for (int layer = 0; layer < 2; ++layer) {
    float* xin = layer == 0 ? x0 : x2;
    k_ln_gemv<0><<<4096 / 64, 64, 0, stream>>>(xin, wq, 4096, q);
    k_attn<<<dim3(NSPLIT, HKV, B), 256, 0, stream>>>(q, kc, vc, pml, po);
    k_combine<<<256, 64, 0, stream>>>(pml, po, o);
    k_gemv_res<<<32, 256, 0, stream>>>(o, 4096, wo, xin, x1, nullptr);
    k_ln_gemv<1><<<F / 64, 64, 0, stream>>>(x1, w1, F, h);
    k_gemv_res<<<32, 256, 0, stream>>>(h, F, w2, x1, layer == 0 ? x2 : nullptr, layer == 1 ? (u16*)d_out : nullptr);
  }
}
```

```cpp
#include <hip/hip_runtime.h>
#include <hip/hip_bf16.h>
#include <stdint.h>

constexpr int NS = 27;
constexpr int NSTREAM = 216;
constexpr int WS_EPOCH = 0;
constexpr int WS_TMO = 256;
constexpr int WS_FLAGS = 1024;
constexpr int WS_STAMP = 4096;
constexpr int WS_QB = 528384;
constexpr int QB_INST = 8192;
constexpr int WS_X1P = 659456;
constexpr int X1P_INST = 331776;
constexpr int X1R = 384;
constexpr int WS_X2P = 5967872;
constexpr int X2P_INST = 16384;
constexpr int WS_X4P = 6230016;
constexpr int X4P_INST = 14336;
constexpr int WS_END = 6459392;
constexpr int LDS_BYTES = 163840;
typedef unsigned short u16;

__global__ void __launch_bounds__(256, 1) fused_decode_kernel(const u16* x, const u16* K, const u16* V, const u16* wq, const u16* wo,
                                                              const u16* w1, const u16* w2, u16* out, unsigned char* ws) {
  const int bid = blockIdx.x;
  const int wave = __builtin_amdgcn_readfirstlane(threadIdx.x >> 6);
  if (bid < NSTREAM) {
    asm volatile(
      "s_mov_b64 s[40:41], %[K]\n"
      "s_mov_b64 s[42:43], %[V]\n"
      "s_mov_b64 s[58:59], %[ws]\n"
      "s_mov_b32 s68, %[bid]\n"
      "s_mov_b32 s60, %[wave]\n"
      "s_mov_b32 s54, 0\n"
      "s_load_dword s50, s[58:59], 0\n"
      "L_begin_%=:\n"
      "fdk_stream_begin:\n"
      "s_getpc_b64 s[70:71]\n"
      "v_mbcnt_lo_u32_b32 v49, -1, 0\n"
      "v_mbcnt_hi_u32_b32 v49, -1, v49\n"
      "v_lshlrev_b32 v0, 4, v49\n"
      "s_and_b32 s62, s68, 7\n"
      "s_lshr_b32 s63, s68, 3\n"
      "s_lshl_b32 s53, s63, 2\n"
      "s_add_u32 s53, s53, s60\n"
      "s_lshl_b32 s56, s62, 21\n"
      "s_lshl_b32 s57, s53, 10\n"
      "s_add_u32 s56, s56, s57\n"
      "s_add_u32 s40, s40, s56\n"
      "s_addc_u32 s41, s41, 0\n"
      "s_add_u32 s42, s42, s56\n"
      "s_addc_u32 s43, s43, 0\n"
      "s_cmp_lt_u32 s53, 104\n"
      "s_cselect_b32 s51, 110592, 0\n"
      "s_cselect_b32 s69, 0, 0xf149f2ca\n"
      "s_sub_u32 s52, 14897152, s51\n"
      "v_and_b32 v1, 12, v49\n"
      "v_lshlrev_b32 v1, 6, v1\n"
      "v_lshrrev_b32 v50, 4, v49\n"
      "v_lshlrev_b32 v50, 6, v50\n"
      "v_add_u32 v1, v1, v50\n"
      "v_and_b32 v50, 3, v49\n"
      "v_lshlrev_b32 v50, 4, v50\n"
      "v_add_u32 v1, v1, v50\n"
      "s_mul_i32 s61, s60, 38912\n"
      "s_add_u32 s61, s61, 0\n"
      "s_mov_b64 s[72:73], %[wq]\n"
      "s_mov_b64 s[74:75], %[wo]\n"
      "s_mov_b64 s[76:77], %[w1]\n"
      "s_mov_b64 s[78:79], %[w2]\n"
      "s_sub_u32 s68, 107, s53\n"
      "s_add_u32 s81, s53, 0\n"
      "s_cmp_lt_u32 s81, 512\n"
      "s_cselect_b32 s81, s81, s53\n"
      "s_lshl_b32 s80, s81, 13\n"
      "s_lshl_b32 s81, s62, 10\n"
      "s_add_u32 s80, s80, s81\n"
      "s_add_u32 s56, s72, s80\n"
      "s_addc_u32 s57, s73, 0\n"
      "global_load_dwordx4 v[252:255], v0, s[56:57] \n"
      "s_add_u32 s81, s53, 108\n"
      "s_cmp_lt_u32 s81, 512\n"
      "s_cselect_b32 s81, s81, s53\n"
      "s_lshl_b32 s80, s81, 13\n"
      "s_lshl_b32 s81, s62, 10\n"
      "s_add_u32 s80, s80, s81\n"
      "s_add_u32 s56, s72, s80\n"
      "s_addc_u32 s57, s73, 0\n"
      "global_load_dwordx4 v[252:255], v0, s[56:57] \n"
      "s_add_u32 s81, s53, 216\n"
      "s_cmp_lt_u32 s81, 512\n"
      "s_cselect_b32 s81, s81, s53\n"
      "s_lshl_b32 s80, s81, 13\n"
      "s_lshl_b32 s81, s62, 10\n"
      "s_add_u32 s80, s80, s81\n"
      "s_add_u32 s56, s72, s80\n"
      "s_addc_u32 s57, s73, 0\n"
      "global_load_dwordx4 v[252:255], v0, s[56:57] \n"
      "s_add_u32 s81, s53, 324\n"
      "s_cmp_lt_u32 s81, 512\n"
      "s_cselect_b32 s81, s81, s53\n"
      "s_lshl_b32 s80, s81, 13\n"
      "s_lshl_b32 s81, s62, 10\n"
      "s_add_u32 s80, s80, s81\n"
      "s_add_u32 s56, s72, s80\n"
      "s_addc_u32 s57, s73, 0\n"
      "global_load_dwordx4 v[252:255], v0, s[56:57] \n"
      "s_add_u32 s81, s53, 432\n"
      "s_cmp_lt_u32 s81, 512\n"
      "s_cselect_b32 s81, s81, s53\n"
      "s_lshl_b32 s80, s81, 13\n"
      "s_lshl_b32 s81, s62, 10\n"
      "s_add_u32 s80, s80, s81\n"
      "s_add_u32 s56, s72, s80\n"
      "s_addc_u32 s57, s73, 0\n"
      "global_load_dwordx4 v[252:255], v0, s[56:57] \n"
      "s_add_u32 m0, s61, 0\n"
      "s_nop 0\n"
      "global_load_lds_dwordx4 v1, s[40:41] nt\n"
      "s_add_u32 s40, s40, 110592\n"
      "s_addc_u32 s41, s41, 0\n"
      "s_add_u32 m0, s61, 1024\n"
      "s_nop 0\n"
      "global_load_lds_dwordx4 v0, s[42:43] nt\n"
      "s_add_u32 s42, s42, 110592\n"
      "s_addc_u32 s43, s43, 0\n"
      "s_lshl_b32 s80, s68, 10\n"
      "s_cmp_lt_u32 s80, L_end_%=-L_begin_%=-1024\n"
      "s_cselect_b32 s56, s80, 0\n"
      "s_add_u32 s56, s70, s56\n"
      "s_addc_u32 s57, s71, 0\n"
      "global_load_dwordx4 v[252:255], v0, s[56:57]\n"
      "s_lshl_b32 s80, s53, 10\n"
      "s_cmp_lt_u32 s80, fdk_chain_end-fdk_chain_begin-1024\n"
      "s_cselect_b32 s80, s80, 0\n"
      "s_getpc_b64 s[56:57]\n"
      "L_pc2_%=:\n"
      "s_add_u32 s80, s80, fdk_chain_begin-L_pc2_%=\n"
      "s_ashr_i32 s81, s80, 31\n"
      "s_add_u32 s56, s56, s80\n"
      "s_addc_u32 s57, s57, s81\n"
      "global_load_dwordx4 v[252:255], v0, s[56:57]\n"
      "v_lshrrev_b32 v41, 1, v49\n"
      "v_and_b32 v41, 7, v41\n"
      "v_lshlrev_b32 v41, 24, v41\n"
      "v_and_b32 v40, 1, v49\n"
      "v_mul_u32_u24 v40, 2097088, v40\n"
      "v_add_u32 v41, v41, v40\n"
      "s_lshl_b32 s80, s53, 10\n"
      "s_add_u32 s80, s80, 110592\n"
      "s_cmp_lt_u32 s53, 4\n"
      "s_cselect_b32 s81, 0xffff, 1\n"
      "s_mov_b32 exec_lo, s81\n"
      "s_mov_b32 exec_hi, 0\n"
      "s_sub_u32 s56, s40, s80\n"
      "s_subb_u32 s57, s41, 0\n"
      "global_load_dword v40, v41, s[56:57] nt\n"
      "s_sub_u32 s56, s42, s80\n"
      "s_subb_u32 s57, s43, 0\n"
      "global_load_dword v40, v41, s[56:57] nt\n"
      "s_mov_b64 exec, -1\n"
      "s_mul_i32 s80, s53, 6554\n"
      "s_lshr_b32 s80, s80, 16\n"
      "s_mul_i32 s80, s80, 10\n"
      "s_sub_u32 s80, s53, s80\n"
      "s_lshl_b32 s87, s80, 10\n"
      "s_sub_u32 s84, s70, 4\n"
      "s_subb_u32 s85, s71, 0\n"
      "s_lshl_b32 s86, s60, 8\n"
      "s_add_u32 s86, s86, 161920\n"
      "v_lshrrev_b32 v42, 4, v49\n"
      "v_mul_u32_u24 v42, 3584, v42\n"
      "v_and_b32 v48, 0xf0, v0\n"
      "v_add_u32 v42, v42, v48\n"
      "s_add_u32 s81, s68, 0\n"
      "s_cmp_lt_u32 s81, 512\n"
      "s_cselect_b32 s81, s81, s68\n"
      "s_lshl_b32 s80, s81, 10\n"
      "s_lshl_b32 s81, s62, 19\n"
      "s_add_u32 s80, s80, s81\n"
      "s_add_u32 s56, s74, s80\n"
      "s_addc_u32 s57, s75, 0\n"
      "global_load_dwordx4 v[252:255], v0, s[56:57] \n"
      "s_add_u32 s81, s68, 108\n"
      "s_cmp_lt_u32 s81, 512\n"
      "s_cselect_b32 s81, s81, s68\n"
      "s_lshl_b32 s80, s81, 10\n"
      "s_lshl_b32 s81, s62, 19\n"
      "s_add_u32 s80, s80, s81\n"
      "s_add_u32 s56, s74, s80\n"
      "s_addc_u32 s57, s75, 0\n"
      "global_load_dwordx4 v[252:255], v0, s[56:57] \n"
      "s_add_u32 s81, s68, 216\n"
      "s_cmp_lt_u32 s81, 512\n"
      "s_cselect_b32 s81, s81, s68\n"
      "s_lshl_b32 s80, s81, 10\n"
      "s_lshl_b32 s81, s62, 19\n"
      "s_add_u32 s80, s80, s81\n"
      "s_add_u32 s56, s74, s80\n"
      "s_addc_u32 s57, s75, 0\n"
      "global_load_dwordx4 v[252:255], v0, s[56:57] \n"
      "s_add_u32 s81, s68, 324\n"
      "s_cmp_lt_u32 s81, 512\n"
      "s_cselect_b32 s81, s81, s68\n"
      "s_lshl_b32 s80, s81, 10\n"
      "s_lshl_b32 s81, s62, 19\n"
      "s_add_u32 s80, s80, s81\n"
      "s_add_u32 s56, s74, s80\n"
      "s_addc_u32 s57, s75, 0\n"
      "global_load_dwordx4 v[252:255], v0, s[56:57] \n"
      "s_add_u32 s81, s68, 432\n"
      "s_cmp_lt_u32 s81, 512\n"
      "s_cselect_b32 s81, s81, s68\n"
      "s_lshl_b32 s80, s81, 10\n"
      "s_lshl_b32 s81, s62, 19\n"
      "s_add_u32 s80, s80, s81\n"
      "s_add_u32 s56, s74, s80\n"
      "s_addc_u32 s57, s75, 0\n"
      "global_load_dwordx4 v[252:255], v0, s[56:57] \n"
      "s_mov_b32 s82, s62\n"
      "s_add_u32 s81, s53, 0\n"
      "s_cmp_lt_u32 s81, 128\n"
      "s_cselect_b32 s81, s81, s53\n"
      "s_mul_i32 s80, s81, 14336\n"
      "s_lshl_b32 s83, s82, 8\n"
      "s_add_u32 s80, s80, s83\n"
      "s_add_u32 s56, s76, s80\n"
      "s_addc_u32 s57, s77, 0\n"
      "global_load_dwordx4 v[252:255], v42, s[56:57] \n"
      "s_mov_b32 s82, s62\n"
      "s_add_u32 s81, s53, 108\n"
      "s_cmp_lt_u32 s81, 128\n"
      "s_cselect_b32 s81, s81, s53\n"
      "s_mul_i32 s80, s81, 14336\n"
      "s_lshl_b32 s83, s82, 8\n"
      "s_add_u32 s80, s80, s83\n"
      "s_add_u32 s56, s76, s80\n"
      "s_addc_u32 s57, s77, 0\n"
      "global_load_dwordx4 v[252:255], v42, s[56:57] \n"
      "s_add_u32 s82, s62, 8\n"
      "s_cmp_lt_u32 s82, 14\n"
      "s_cselect_b32 s82, s82, s62\n"
      "s_add_u32 s81, s68, 0\n"
      "s_cmp_lt_u32 s81, 128\n"
      "s_cselect_b32 s81, s81, s68\n"
      "s_mul_i32 s80, s81, 14336\n"
      "s_lshl_b32 s83, s82, 8\n"
      "s_add_u32 s80, s80, s83\n"
      "s_add_u32 s56, s76, s80\n"
      "s_addc_u32 s57, s77, 0\n"
      "global_load_dwordx4 v[252:255], v42, s[56:57] \n"
      "s_add_u32 s82, s62, 8\n"
      "s_cmp_lt_u32 s82, 14\n"
      "s_cselect_b32 s82, s82, s62\n"
      "s_add_u32 s81, s68, 108\n"
      "s_cmp_lt_u32 s81, 128\n"
      "s_cselect_b32 s81, s81, s68\n"
      "s_mul_i32 s80, s81, 14336\n"
      "s_lshl_b32 s83, s82, 8\n"
      "s_add_u32 s80, s80, s83\n"
      "s_add_u32 s56, s76, s80\n"
      "s_addc_u32 s57, s77, 0\n"
      "global_load_dwordx4 v[252:255], v42, s[56:57] \n"
      "s_mov_b32 s82, s62\n"
      "s_add_u32 s81, s53, 0\n"
      "s_cmp_lt_u32 s81, 128\n"
      "s_cselect_b32 s81, s81, s53\n"
      "s_lshl_b32 s80, s81, 10\n"
      "s_lshl_b32 s83, s82, 17\n"
      "s_add_u32 s80, s80, s83\n"
      "s_add_u32 s56, s78, s80\n"
      "s_addc_u32 s57, s79, 0\n"
      "global_load_dwordx4 v[252:255], v0, s[56:57] \n"
      "s_mov_b32 s82, s62\n"
      "s_add_u32 s81, s53, 108\n"
      "s_cmp_lt_u32 s81, 128\n"
      "s_cselect_b32 s81, s81, s53\n"
      "s_lshl_b32 s80, s81, 10\n"
      "s_lshl_b32 s83, s82, 17\n"
      "s_add_u32 s80, s80, s83\n"
      "s_add_u32 s56, s78, s80\n"
      "s_addc_u32 s57, s79, 0\n"
      "global_load_dwordx4 v[252:255], v0, s[56:57] \n"
      "s_add_u32 s82, s62, 8\n"
      "s_cmp_lt_u32 s82, 14\n"
      "s_cselect_b32 s82, s82, s62\n"
      "s_add_u32 s81, s68, 0\n"
      "s_cmp_lt_u32 s81, 128\n"
      "s_cselect_b32 s81, s81, s68\n"
      "s_lshl_b32 s80, s81, 10\n"
      "s_lshl_b32 s83, s82, 17\n"
      "s_add_u32 s80, s80, s83\n"
      "s_add_u32 s56, s78, s80\n"
      "s_addc_u32 s57, s79, 0\n"
      "global_load_dwordx4 v[252:255], v0, s[56:57] \n"
      "s_add_u32 s82, s62, 8\n"
      "s_cmp_lt_u32 s82, 14\n"
      "s_cselect_b32 s82, s82, s62\n"
      "s_add_u32 s81, s68, 108\n"
      "s_cmp_lt_u32 s81, 128\n"
      "s_cselect_b32 s81, s81, s68\n"
      "s_lshl_b32 s80, s81, 10\n"
      "s_lshl_b32 s83, s82, 17\n"
      "s_add_u32 s80, s80, s83\n"
      "s_add_u32 s56, s78, s80\n"
      "s_addc_u32 s57, s79, 0\n"
      "global_load_dwordx4 v[252:255], v0, s[56:57] \n"
      "s_add_u32 m0, s61, 2048\n"
      "s_nop 0\n"
      "global_load_lds_dwordx4 v1, s[40:41] nt\n"
      "s_add_u32 s40, s40, 110592\n"
      "s_addc_u32 s41, s41, 0\n"
      "s_add_u32 m0, s61, 3072\n"
      "s_nop 0\n"
      "global_load_lds_dwordx4 v0, s[42:43] nt\n"
      "s_add_u32 s42, s42, 110592\n"
      "s_addc_u32 s43, s43, 0\n"
      "s_add_u32 m0, s61, 4096\n"
      "s_nop 0\n"
      "global_load_lds_dwordx4 v1, s[40:41] nt\n"
      "s_add_u32 s40, s40, 110592\n"
      "s_addc_u32 s41, s41, 0\n"
      "s_add_u32 m0, s61, 5120\n"
      "s_nop 0\n"
      "global_load_lds_dwordx4 v0, s[42:43] nt\n"
      "s_add_u32 s42, s42, 110592\n"
      "s_addc_u32 s43, s43, 0\n"
      "s_add_u32 m0, s61, 6144\n"
      "s_nop 0\n"
      "global_load_lds_dwordx4 v1, s[40:41] nt\n"
      "s_add_u32 s40, s40, 110592\n"
      "s_addc_u32 s41, s41, 0\n"
      "v_and_b32 v2, 12, v49\n"
      "v_lshlrev_b32 v2, 6, v2\n"
      "v_lshrrev_b32 v50, 4, v49\n"
      "v_lshlrev_b32 v50, 6, v50\n"
      "v_add_u32 v2, v2, v50\n"
      "v_and_b32 v50, 3, v49\n"
      "v_lshlrev_b32 v50, 4, v50\n"
      "v_add_u32 v2, v2, v50\n"
      "v_and_b32 v50, 1, v49\n"
      "v_lshlrev_b32 v50, 4, v50\n"
      "v_mov_b32 v51, 0xffff\n"
      "v_lshlrev_b32 v50, v50, v51\n"
      "v_and_b32 v51, 2, v49\n"
      "v_cmp_eq_u32 vcc, 0, v51\n"
      "s_nop 1\n"
      "v_cndmask_b32 v4, 0, v50, vcc\n"
      "v_cndmask_b32 v5, v50, 0, vcc\n"
      "s_lshl_b32 s56, s62, 10\n"
      "s_lshl_b32 s57, s60, 8\n"
      "s_add_u32 s56, s56, s57\n"
      "s_add_u32 s56, s56, 528384\n"
      "s_add_u32 s48, s58, s56\n"
      "s_addc_u32 s49, s59, 0\n"
      "v_add_u32 v2, 159872, v2\n"
      "s_lshl_b32 s56, s62, 2\n"
      "s_add_u32 s56, s56, s60\n"
      "s_mul_i32 s56, s56, 27\n"
      "s_add_u32 s56, s56, s63\n"
      "s_mul_i32 s56, s56, 384\n"
      "s_add_u32 s56, s56, 659456\n"
      "s_add_u32 s44, s58, s56\n"
      "s_addc_u32 s45, s59, 0\n"
      "s_lshl_b32 s56, s62, 8\n"
      "s_add_u32 s47, s56, 1024\n"
      "s_lshl_b32 s56, s63, 2\n"
      "s_add_u32 s46, s47, s56\n"
      "v_mov_b32 v37, 331776\n"
      "v_cmp_lt_u32 vcc, 31, v49\n"
      "v_and_b32 v36, 31, v49\n"
      "v_lshlrev_b32 v36, 4, v36\n"
      "v_cndmask_b32 v37, 0, v37, vcc\n"
      "v_add_u32 v36, v36, v37\n"
      "v_mov_b32 v28, -1\n"
      "v_mov_b32 v29, -1\n"
      "v_mov_b32 v30, -1\n"
      "v_mov_b32 v31, -1\n"
      "s_mov_b64 s[56:57], s[44:45]\n"
      "s_mov_b32 exec_lo, 0x00ffffff\n"
      "s_mov_b32 exec_hi, 0x00ffffff\n"
      "global_store_dwordx4 v36, v[28:31], s[56:57] sc1\n"
      "s_add_u32 s56, s56, 663552\n"
      "s_addc_u32 s57, s57, 0\n"
      "global_store_dwordx4 v36, v[28:31], s[56:57] sc1\n"
      "s_add_u32 s56, s56, 663552\n"
      "s_addc_u32 s57, s57, 0\n"
      "global_store_dwordx4 v36, v[28:31], s[56:57] sc1\n"
      "s_add_u32 s56, s56, 663552\n"
      "s_addc_u32 s57, s57, 0\n"
      "global_store_dwordx4 v36, v[28:31], s[56:57] sc1\n"
      "s_add_u32 s56, s56, 663552\n"
      "s_addc_u32 s57, s57, 0\n"
      "global_store_dwordx4 v36, v[28:31], s[56:57] sc1\n"
      "s_add_u32 s56, s56, 663552\n"
      "s_addc_u32 s57, s57, 0\n"
      "global_store_dwordx4 v36, v[28:31], s[56:57] sc1\n"
      "s_add_u32 s56, s56, 663552\n"
      "s_addc_u32 s57, s57, 0\n"
      "global_store_dwordx4 v36, v[28:31], s[56:57] sc1\n"
      "s_add_u32 s56, s56, 663552\n"
      "s_addc_u32 s57, s57, 0\n"
      "global_store_dwordx4 v36, v[28:31], s[56:57] sc1\n"
      "s_mov_b64 exec, -1\n"
      "s_add_u32 m0, s61, 7168\n"
      "s_nop 0\n"
      "global_load_lds_dwordx4 v0, s[42:43] nt\n"
      "s_add_u32 s42, s42, 110592\n"
      "s_addc_u32 s43, s43, 0\n"
      "s_add_u32 m0, s61, 8192\n"
      "s_nop 0\n"
      "global_load_lds_dwordx4 v1, s[40:41] nt\n"
      "s_add_u32 s40, s40, 110592\n"
      "s_addc_u32 s41, s41, 0\n"
      "s_add_u32 m0, s61, 9216\n"
      "s_nop 0\n"
      "global_load_lds_dwordx4 v0, s[42:43] nt\n"
      "s_add_u32 s42, s42, 110592\n"
      "s_addc_u32 s43, s43, 0\n"
      "s_add_u32 m0, s61, 10240\n"
      "s_nop 0\n"
      "global_load_lds_dwordx4 v1, s[40:41] nt\n"
      "s_add_u32 s40, s40, 110592\n"
      "s_addc_u32 s41, s41, 0\n"
      "s_add_u32 m0, s61, 11264\n"
      "s_nop 0\n"
      "global_load_lds_dwordx4 v0, s[42:43] nt\n"
      "s_add_u32 s42, s42, 110592\n"
      "s_addc_u32 s43, s43, 0\n"
      "s_add_u32 m0, s61, 12288\n"
      "s_nop 0\n"
      "global_load_lds_dwordx4 v1, s[40:41] nt\n"
      "s_add_u32 s40, s40, 110592\n"
      "s_addc_u32 s41, s41, 0\n"
      "s_add_u32 m0, s61, 13312\n"
      "s_nop 0\n"
      "global_load_lds_dwordx4 v0, s[42:43] nt\n"
      "s_add_u32 s42, s42, 110592\n"
      "s_addc_u32 s43, s43, 0\n"
      "s_waitcnt vmcnt(6)\n"
      "s_waitcnt lgkmcnt(0)\n"
      "s_barrier\n"
      "s_add_u32 s50, s50, 1\n"
      "v_mov_b32 v49, s46\n"
      "v_mov_b32 v50, s50\n"
      "s_mov_b64 exec, 1\n"
      "global_store_dword v49, v50, s[58:59] sc1\n"
      "s_mov_b64 exec, -1\n"
      "v_add_u32 v3, s61, v0\n"
      "v_lshrrev_b32 v50, 4, v0\n"
      "v_min_u32 v50, 4, v50\n"
      "v_lshlrev_b32 v50, 2, v50\n"
      "s_add_u32 s56, s47, 128\n"
      "s_add_u32 s56, s58, s56\n"
      "s_addc_u32 s57, s59, 0\n"
      "global_load_dword v24, v50, s[56:57] sc1\n"
      "s_add_u32 m0, s61, 14336\n"
      "s_nop 0\n"
      "global_load_lds_dwordx4 v1, s[40:41] nt\n"
      "s_add_u32 s40, s40, 110592\n"
      "s_addc_u32 s41, s41, 0\n"
      "s_add_u32 m0, s61, 15360\n"
      "s_nop 0\n"
      "global_load_lds_dwordx4 v0, s[42:43] nt\n"
      "s_add_u32 s42, s42, 110592\n"
      "s_addc_u32 s43, s43, 0\n"
      "s_add_u32 m0, s61, 16384\n"
      "s_nop 0\n"
      "global_load_lds_dwordx4 v1, s[40:41] nt\n"
      "s_add_u32 s40, s40, 110592\n"
      "s_addc_u32 s41, s41, 0\n"
      "s_waitcnt vmcnt(9)\n"
      "s_add_u32 m0, s61, 17408\n"
      "s_nop 0\n"
      "global_load_lds_dwordx4 v0, s[42:43] nt\n"
      "s_add_u32 s42, s42, 110592\n"
      "s_addc_u32 s43, s43, 0\n"
      "s_waitcnt vmcnt(4)\n"
      "s_mov_b32 s53, 0\n"
      "L_fc1_%=:\n"
      "v_cmp_ne_u32 vcc, s50, v24\n"
      "s_nop 0\n"
      "s_cbranch_vccz L_fo2_%=\n"
      "v_lshrrev_b32 v50, 4, v0\n"
      "v_min_u32 v50, 4, v50\n"
      "v_lshlrev_b32 v50, 2, v50\n"
      "s_add_u32 s56, s47, 128\n"
      "s_add_u32 s56, s58, s56\n"
      "s_addc_u32 s57, s59, 0\n"
      "global_load_dword v24, v50, s[56:57] sc1\n"
      "s_add_u32 s53, s53, 1\n"
      "s_waitcnt vmcnt(0)\n"
      "s_cmp_lt_u32 s53, 30000\n"
      "s_cbranch_scc1 L_fc1_%=\n"
      "s_mov_b32 s54, 77\n"
      "v_mov_b32 v49, 77\n"
      "v_mov_b32 v50, 256\n"
      "global_store_dword v50, v49, s[58:59] sc1\n"
      "L_fo2_%=:\n"
      "L_per0_%=:\n"
      "s_add_u32 s56, s87, L_per1_%=-L_begin_%=\n"
      "s_min_u32 s56, s56, L_end_%=-L_begin_%=-1024\n"
      "s_add_u32 s56, s84, s56\n"
      "s_addc_u32 s57, s85, 0\n"
      "s_mov_b32 m0, s86\n"
      "s_nop 0\n"
      "global_load_lds_dword v0, s[56:57]\n"
      "s_add_u32 m0, s61, 18432\n"
      "s_nop 0\n"
      "global_load_lds_dwordx4 v1, s[40:41] nt\n"
      "s_add_u32 s40, s40, 110592\n"
      "s_addc_u32 s41, s41, 0\n"
      "s_add_u32 m0, s61, 19456\n"
      "s_nop 0\n"
      "global_load_lds_dwordx4 v0, s[42:43] nt\n"
      "s_add_u32 s42, s42, 110592\n"
      "s_addc_u32 s43, s43, 0\n"
      "s_add_u32 m0, s61, 20480\n"
      "s_nop 0\n"
      "global_load_lds_dwordx4 v1, s[40:41] nt\n"
      "s_add_u32 s40, s40, 110592\n"
      "s_addc_u32 s41, s41, 0\n"
      "s_add_u32 m0, s61, 21504\n"
      "s_nop 0\n"
      "global_load_lds_dwordx4 v0, s[42:43] nt\n"
      "s_add_u32 s42, s42, 110592\n"
      "s_addc_u32 s43, s43, 0\n"
      "s_add_u32 s56, s48, 0\n"
      "s_addc_u32 s57, s49, 0\n"
      "s_mov_b64 exec, 0xffff\n"
      "global_load_dwordx4 v[8:11], v0, s[56:57] sc1\n"
      "s_mov_b64 exec, -1\n"
      "s_add_u32 m0, s61, 22528\n"
      "s_nop 0\n"
      "global_load_lds_dwordx4 v1, s[40:41] nt\n"
      "s_add_u32 s40, s40, 110592\n"
      "s_addc_u32 s41, s41, 0\n"
      "s_waitcnt vmcnt(10)\n"
      "s_add_u32 m0, s61, 23552\n"
      "s_nop 0\n"
      "global_load_lds_dwordx4 v0, s[42:43] nt\n"
      "s_add_u32 s42, s42, 110592\n"
      "s_addc_u32 s43, s43, 0\n"
      "s_waitcnt vmcnt(10)\n"
      "s_add_u32 m0, s61, 24576\n"
      "s_nop 0\n"
      "global_load_lds_dwordx4 v1, s[40:41] nt\n"
      "s_add_u32 s40, s40, 110592\n"
      "s_addc_u32 s41, s41, 0\n"
      "s_waitcnt vmcnt(10)\n"
      "s_add_u32 m0, s61, 25600\n"
      "s_nop 0\n"
      "global_load_lds_dwordx4 v0, s[42:43] nt\n"
      "s_add_u32 s42, s42, 110592\n"
      "s_addc_u32 s43, s43, 0\n"
      "s_waitcnt vmcnt(10)\n"
      "s_add_u32 m0, s61, 26624\n"
      "s_nop 0\n"
      "global_load_lds_dwordx4 v1, s[40:41] nt\n"
      "s_add_u32 s40, s40, 110592\n"
      "s_addc_u32 s41, s41, 0\n"
      "s_waitcnt vmcnt(9)\n"
      "s_add_u32 m0, s61, 27648\n"
      "s_nop 0\n"
      "global_load_lds_dwordx4 v0, s[42:43] nt\n"
      "s_add_u32 s42, s42, 110592\n"
      "s_addc_u32 s43, s43, 0\n"
      "s_waitcnt vmcnt(9)\n"
      "s_add_u32 m0, s61, 28672\n"
      "s_nop 0\n"
      "global_load_lds_dwordx4 v1, s[40:41] nt\n"
      "s_add_u32 s40, s40, 110592\n"
      "s_addc_u32 s41, s41, 0\n"
      "s_waitcnt vmcnt(9)\n"
      "s_add_u32 m0, s61, 29696\n"
      "s_nop 0\n"
      "global_load_lds_dwordx4 v0, s[42:43] nt\n"
      "s_add_u32 s42, s42, 110592\n"
      "s_addc_u32 s43, s43, 0\n"
      "s_waitcnt vmcnt(9)\n"
      "s_add_u32 m0, s61, 30720\n"
      "s_nop 0\n"
      "global_load_lds_dwordx4 v1, s[40:41] nt\n"
      "s_add_u32 s40, s40, 110592\n"
      "s_addc_u32 s41, s41, 0\n"
      "s_waitcnt vmcnt(9)\n"
      "s_mov_b64 exec, 0xffff\n"
      "s_mov_b32 s53, 0\n"
      "L_qc3_%=:\n"
      "v_max3_u32 v49, v8, v9, v10\n"
      "v_max_u32 v49, v49, v11\n"
      "v_cmp_eq_u32 vcc, -1, v49\n"
      "s_nop 0\n"
      "s_cbranch_vccz L_qo4_%=\n"
      "s_cmp_lg_u32 s54, 0\n"
      "s_cbranch_scc1 L_qo4_%=\n"
      "s_add_u32 s56, s48, 0\n"
      "s_addc_u32 s57, s49, 0\n"
      "global_load_dwordx4 v[8:11], v0, s[56:57] sc1\n"
      "s_add_u32 s53, s53, 1\n"
      "s_waitcnt vmcnt(0)\n"
      "s_cmp_lt_u32 s53, 30000\n"
      "s_cbranch_scc1 L_qc3_%=\n"
      "s_mov_b32 s54, 100\n"
      "v_mov_b32 v49, 100\n"
      "v_mov_b32 v50, 256\n"
      "global_store_dword v50, v49, s[58:59] sc1\n"
      "L_qo4_%=:\n"
      "s_lshl_b32 s62, s60, 8\n"
      "s_add_u32 s62, s62, 159872\n"
      "v_add_u32 v49, s62, v0\n"
      "ds_write_b128 v49, v[8:11]\n"
      "s_mov_b64 exec, -1\n"
      "s_waitcnt lgkmcnt(0)\n"
      "s_barrier\n"
      "ds_read_b128 v[8:11], v2 offset:0\n"
      "s_waitcnt lgkmcnt(0)\n"
      "ds_read_b128 v[40:43], v3 offset:0\n"
      "ds_read_b128 v[44:47], v3 offset:2048\n"
      "ds_read_b128 v[48:51], v3 offset:1024\n"
      "ds_read_b128 v[52:55], v3 offset:3072\n"
      "s_waitcnt lgkmcnt(3)\n"
      "v_mfma_f32_16x16x32_bf16 v[24:27], v[40:43], v[8:11], 0\n"
      "ds_read_b128 v[40:43], v3 offset:4096\n"
      "s_waitcnt lgkmcnt(3)\n"
      "v_mfma_f32_16x16x32_bf16 v[28:31], v[44:47], v[8:11], 0\n"
      "ds_read_b128 v[44:47], v3 offset:6144\n"
      "s_nop 3\n"
      "v_mov_b32_dpp v34, v24 quad_perm:[0,0,0,0] row_mask:0xf bank_mask:0xf\n"
      "v_mov_b32_dpp v33, v26 quad_perm:[2,2,2,2] row_mask:0xf bank_mask:0xf\n"
      "s_nop 0\n"
      "v_add_f32_dpp v34, v25, v34 quad_perm:[1,1,1,1] row_mask:0xf bank_mask:0xf\n"
      "v_add_f32_dpp v33, v27, v33 quad_perm:[3,3,3,3] row_mask:0xf bank_mask:0xf\n"
      "v_add_f32 v34, v34, v33\n"
      "v_mov_b32_dpp v32, v28 quad_perm:[0,0,0,0] row_mask:0xf bank_mask:0xf\n"
      "v_mov_b32_dpp v33, v30 quad_perm:[2,2,2,2] row_mask:0xf bank_mask:0xf\n"
      "s_nop 0\n"
      "v_add_f32_dpp v32, v29, v32 quad_perm:[1,1,1,1] row_mask:0xf bank_mask:0xf\n"
      "v_add_f32_dpp v33, v31, v33 quad_perm:[3,3,3,3] row_mask:0xf bank_mask:0xf\n"
      "v_add_f32 v32, v32, v33\n"
      "v_max_f32 v35, v34, v32\n"
      "v_mov_b32 v33, v35\n"
      "s_nop 1\n"
      "v_permlane32_swap_b32 v33, v35\n"
      "v_max_f32 v35, v33, v35\n"
      "v_mov_b32 v33, v35\n"
      "s_nop 1\n"
      "v_permlane16_swap_b32 v33, v35\n"
      "v_max_f32 v6, v33, v35\n"
      "v_sub_f32 v34, v34, v6\n"
      "v_sub_f32 v32, v32, v6\n"
      "v_mov_b32 v35, 0\n"
      "v_exp_f32 v34, v34\n"
      "s_nop 0\n"
      "v_mov_b32 v7, v34\n"
      "v_cvt_pk_bf16_f32 v34, v34, v34\n"
      "v_and_b32 v36, v4, v34\n"
      "v_and_b32 v37, v5, v34\n"
      "s_waitcnt lgkmcnt(1)\n"
      "v_mfma_f32_16x16x32_bf16 v[24:27], v[40:43], v[8:11], 0\n"
      "ds_read_b128 v[40:43], v3 offset:8192\n"
      "v_mfma_f32_16x16x16_bf16 v[16:19], v[48:49], v[36:37], 0\n"
      "v_mfma_f32_16x16x16_bf16 v[20:23], v[50:51], v[36:37], 0\n"
      "ds_read_b128 v[48:51], v3 offset:5120\n"
      "v_exp_f32 v34, v32\n"
      "v_max_f32 v35, v35, v32\n"
      "v_add_f32 v7, v7, v34\n"
      "v_cvt_pk_bf16_f32 v34, v34, v34\n"
      "v_and_b32 v38, v4, v34\n"
      "v_and_b32 v39, v5, v34\n"
      "s_waitcnt lgkmcnt(2)\n"
      "v_mfma_f32_16x16x32_bf16 v[28:31], v[44:47], v[8:11], 0\n"
      "ds_read_b128 v[44:47], v3 offset:10240\n"
      "v_sub_f32_dpp v32, v24, v6 quad_perm:[0,0,0,0] row_mask:0xf bank_mask:0xf\n"
      "v_mov_b32_dpp v33, v26 quad_perm:[2,2,2,2] row_mask:0xf bank_mask:0xf\n"
      "v_mfma_f32_16x16x16_bf16 v[16:19], v[52:53], v[38:39], v[16:19]\n"
      "v_add_f32_dpp v32, v25, v32 quad_perm:[1,1,1,1] row_mask:0xf bank_mask:0xf\n"
      "v_add_f32_dpp v33, v27, v33 quad_perm:[3,3,3,3] row_mask:0xf bank_mask:0xf\n"
      "v_mfma_f32_16x16x16_bf16 v[20:23], v[54:55], v[38:39], v[20:23]\n"
      "ds_read_b128 v[52:55], v3 offset:7168\n"
      "v_add_f32 v32, v32, v33\n"
      "v_exp_f32 v34, v32\n"
      "v_max_f32 v35, v35, v32\n"
      "v_add_f32 v7, v7, v34\n"
      "v_cvt_pk_bf16_f32 v34, v34, v34\n"
      "v_and_b32 v36, v4, v34\n"
      "v_and_b32 v37, v5, v34\n"
      "s_waitcnt lgkmcnt(3)\n"
      "v_mfma_f32_16x16x32_bf16 v[24:27], v[40:43], v[8:11], 0\n"
      "ds_read_b128 v[40:43], v3 offset:12288\n"
      "v_sub_f32_dpp v32, v28, v6 quad_perm:[0,0,0,0] row_mask:0xf bank_mask:0xf\n"
      "v_mov_b32_dpp v33, v30 quad_perm:[2,2,2,2] row_mask:0xf bank_mask:0xf\n"
      "s_waitcnt lgkmcnt(3)\n"
      "v_mfma_f32_16x16x16_bf16 v[16:19], v[48:49], v[36:37], v[16:19]\n"
      "v_add_f32_dpp v32, v29, v32 quad_perm:[1,1,1,1] row_mask:0xf bank_mask:0xf\n"
      "v_add_f32_dpp v33, v31, v33 quad_perm:[3,3,3,3] row_mask:0xf bank_mask:0xf\n"
      "v_mfma_f32_16x16x16_bf16 v[20:23], v[50:51], v[36:37], v[20:23]\n"
      "ds_read_b128 v[48:51], v3 offset:9216\n"
      "v_add_f32 v32, v32, v33\n"
      "v_exp_f32 v34, v32\n"
      "v_max_f32 v35, v35, v32\n"
      "v_add_f32 v7, v7, v34\n"
      "v_cvt_pk_bf16_f32 v34, v34, v34\n"
      "v_and_b32 v38, v4, v34\n"
      "v_and_b32 v39, v5, v34\n"
      "s_waitcnt vmcnt(8)\n"
      "s_add_u32 m0, s61, 31744\n"
      "s_nop 0\n"
      "global_load_lds_dwordx4 v0, s[42:43] nt\n"
      "s_add_u32 s42, s42, 110592\n"
      "s_addc_u32 s43, s43, 0\n"
      "s_waitcnt lgkmcnt(3)\n"
      "v_mfma_f32_16x16x32_bf16 v[28:31], v[44:47], v[8:11], 0\n"
      "ds_read_b128 v[44:47], v3 offset:14336\n"
      "v_sub_f32_dpp v32, v24, v6 quad_perm:[0,0,0,0] row_mask:0xf bank_mask:0xf\n"
      "v_mov_b32_dpp v33, v26 quad_perm:[2,2,2,2] row_mask:0xf bank_mask:0xf\n"
      "s_waitcnt lgkmcnt(3)\n"
      "v_mfma_f32_16x16x16_bf16 v[16:19], v[52:53], v[38:39], v[16:19]\n"
      "v_add_f32_dpp v32, v25, v32 quad_perm:[1,1,1,1] row_mask:0xf bank_mask:0xf\n"
      "v_add_f32_dpp v33, v27, v33 quad_perm:[3,3,3,3] row_mask:0xf bank_mask:0xf\n"
      "v_mfma_f32_16x16x16_bf16 v[20:23], v[54:55], v[38:39], v[20:23]\n"
      "ds_read_b128 v[52:55], v3 offset:11264\n"
      "v_add_f32 v32, v32, v33\n"
      "v_exp_f32 v34, v32\n"
      "v_max_f32 v35, v35, v32\n"
      "v_add_f32 v7, v7, v34\n"
      "v_cvt_pk_bf16_f32 v34, v34, v34\n"
      "v_and_b32 v36, v4, v34\n"
      "v_and_b32 v37, v5, v34\n"
      "s_waitcnt lgkmcnt(3)\n"
      "v_mfma_f32_16x16x32_bf16 v[24:27], v[40:43], v[8:11], 0\n"
      "ds_read_b128 v[40:43], v3 offset:16384\n"
      "v_sub_f32_dpp v32, v28, v6 quad_perm:[0,0,0,0] row_mask:0xf bank_mask:0xf\n"
      "v_mov_b32_dpp v33, v30 quad_perm:[2,2,2,2] row_mask:0xf bank_mask:0xf\n"
      "s_waitcnt lgkmcnt(3)\n"
      "v_mfma_f32_16x16x16_bf16 v[16:19], v[48:49], v[36:37], v[16:19]\n"
      "v_add_f32_dpp v32, v29, v32 quad_perm:[1,1,1,1] row_mask:0xf bank_mask:0xf\n"
      "v_add_f32_dpp v33, v31, v33 quad_perm:[3,3,3,3] row_mask:0xf bank_mask:0xf\n"
      "v_mfma_f32_16x16x16_bf16 v[20:23], v[50:51], v[36:37], v[20:23]\n"
      "ds_read_b128 v[48:51], v3 offset:13312\n"
      "v_add_f32 v32, v32, v33\n"
      "v_exp_f32 v34, v32\n"
      "v_max_f32 v35, v35, v32\n"
      "v_add_f32 v7, v7, v34\n"
      "v_cvt_pk_bf16_f32 v34, v34, v34\n"
      "v_and_b32 v38, v4, v34\n"
      "v_and_b32 v39, v5, v34\n"
      "s_waitcnt vmcnt(8)\n"
      "s_add_u32 m0, s61, 32768\n"
      "s_nop 0\n"
      "global_load_lds_dwordx4 v1, s[40:41] nt\n"
      "s_add_u32 s40, s40, 110592\n"
      "s_addc_u32 s41, s41, 0\n"
      "s_waitcnt lgkmcnt(3)\n"
      "v_mfma_f32_16x16x32_bf16 v[28:31], v[44:47], v[8:11], 0\n"
      "ds_read_b128 v[44:47], v3 offset:18432\n"
      "v_sub_f32_dpp v32, v24, v6 quad_perm:[0,0,0,0] row_mask:0xf bank_mask:0xf\n"
      "v_mov_b32_dpp v33, v26 quad_perm:[2,2,2,2] row_mask:0xf bank_mask:0xf\n"
      "s_waitcnt lgkmcnt(3)\n"
      "v_mfma_f32_16x16x16_bf16 v[16:19], v[52:53], v[38:39], v[16:19]\n"
      "v_add_f32_dpp v32, v25, v32 quad_perm:[1,1,1,1] row_mask:0xf bank_mask:0xf\n"
      "v_add_f32_dpp v33, v27, v33 quad_perm:[3,3,3,3] row_mask:0xf bank_mask:0xf\n"
      "v_mfma_f32_16x16x16_bf16 v[20:23], v[54:55], v[38:39], v[20:23]\n"
      "ds_read_b128 v[52:55], v3 offset:15360\n"
      "v_add_f32 v32, v32, v33\n"
      "v_exp_f32 v34, v32\n"
      "v_max_f32 v35, v35, v32\n"
      "v_add_f32 v7, v7, v34\n"
      "v_cvt_pk_bf16_f32 v34, v34, v34\n"
      "v_and_b32 v36, v4, v34\n"
      "v_and_b32 v37, v5, v34\n"
      "s_waitcnt lgkmcnt(3)\n"
      "v_mfma_f32_16x16x32_bf16 v[24:27], v[40:43], v[8:11], 0\n"
      "ds_read_b128 v[40:43], v3 offset:20480\n"
      "v_sub_f32_dpp v32, v28, v6 quad_perm:[0,0,0,0] row_mask:0xf bank_mask:0xf\n"
      "v_mov_b32_dpp v33, v30 quad_perm:[2,2,2,2] row_mask:0xf bank_mask:0xf\n"
      "s_waitcnt lgkmcnt(3)\n"
      "v_mfma_f32_16x16x16_bf16 v[16:19], v[48:49], v[36:37], v[16:19]\n"
      "v_add_f32_dpp v32, v29, v32 quad_perm:[1,1,1,1] row_mask:0xf bank_mask:0xf\n"
      "v_add_f32_dpp v33, v31, v33 quad_perm:[3,3,3,3] row_mask:0xf bank_mask:0xf\n"
      "v_mfma_f32_16x16x16_bf16 v[20:23], v[50:51], v[36:37], v[20:23]\n"
      "ds_read_b128 v[48:51], v3 offset:17408\n"
      "v_add_f32 v32, v32, v33\n"
      "v_exp_f32 v34, v32\n"
      "v_max_f32 v35, v35, v32\n"
      "v_add_f32 v7, v7, v34\n"
      "v_cvt_pk_bf16_f32 v34, v34, v34\n"
      "v_and_b32 v38, v4, v34\n"
      "v_and_b32 v39, v5, v34\n"
      "s_waitcnt lgkmcnt(3)\n"
      "v_mfma_f32_16x16x32_bf16 v[28:31], v[44:47], v[8:11], 0\n"
      "ds_read_b128 v[44:47], v3 offset:22528\n"
      "v_sub_f32_dpp v32, v24, v6 quad_perm:[0,0,0,0] row_mask:0xf bank_mask:0xf\n"
      "v_mov_b32_dpp v33, v26 quad_perm:[2,2,2,2] row_mask:0xf bank_mask:0xf\n"
      "s_waitcnt lgkmcnt(3)\n"
      "v_mfma_f32_16x16x16_bf16 v[16:19], v[52:53], v[38:39], v[16:19]\n"
      "v_add_f32_dpp v32, v25, v32 quad_perm:[1,1,1,1] row_mask:0xf bank_mask:0xf\n"
      "v_add_f32_dpp v33, v27, v33 quad_perm:[3,3,3,3] row_mask:0xf bank_mask:0xf\n"
      "v_mfma_f32_16x16x16_bf16 v[20:23], v[54:55], v[38:39], v[20:23]\n"
      "ds_read_b128 v[52:55], v3 offset:19456\n"
      "v_add_f32 v32, v32, v33\n"
      "v_exp_f32 v34, v32\n"
      "v_max_f32 v35, v35, v32\n"
      "v_add_f32 v7, v7, v34\n"
      "v_cvt_pk_bf16_f32 v34, v34, v34\n"
      "v_and_b32 v36, v4, v34\n"
      "v_and_b32 v37, v5, v34\n"
      "s_waitcnt vmcnt(8)\n"
      "s_add_u32 m0, s61, 33792\n"
      "s_nop 0\n"
      "global_load_lds_dwordx4 v0, s[42:43] nt\n"
      "s_add_u32 s42, s42, 110592\n"
      "s_addc_u32 s43, s43, 0\n"
      "s_waitcnt lgkmcnt(3)\n"
      "v_mfma_f32_16x16x32_bf16 v[24:27], v[40:43], v[8:11], 0\n"
      "ds_read_b128 v[40:43], v3 offset:24576\n"
      "v_sub_f32_dpp v32, v28, v6 quad_perm:[0,0,0,0] row_mask:0xf bank_mask:0xf\n"
      "v_mov_b32_dpp v33, v30 quad_perm:[2,2,2,2] row_mask:0xf bank_mask:0xf\n"
      "s_waitcnt lgkmcnt(3)\n"
      "v_mfma_f32_16x16x16_bf16 v[16:19], v[48:49], v[36:37], v[16:19]\n"
      "v_add_f32_dpp v32, v29, v32 quad_perm:[1,1,1,1] row_mask:0xf bank_mask:0xf\n"
      "v_add_f32_dpp v33, v31, v33 quad_perm:[3,3,3,3] row_mask:0xf bank_mask:0xf\n"
      "v_mfma_f32_16x16x16_bf16 v[20:23], v[50:51], v[36:37], v[20:23]\n"
      "ds_read_b128 v[48:51], v3 offset:21504\n"
      "v_add_f32 v32, v32, v33\n"
      "v_exp_f32 v34, v32\n"
      "v_max_f32 v35, v35, v32\n"
      "v_add_f32 v7, v7, v34\n"
      "v_cvt_pk_bf16_f32 v34, v34, v34\n"
      "v_and_b32 v38, v4, v34\n"
      "v_and_b32 v39, v5, v34\n"
      "s_waitcnt lgkmcnt(3)\n"
      "v_mfma_f32_16x16x32_bf16 v[28:31], v[44:47], v[8:11], 0\n"
      "s_waitcnt vmcnt(8)\n"
      "s_add_u32 m0, s61, 34816\n"
      "s_nop 0\n"
      "global_load_lds_dwordx4 v1, s[40:41] nt\n"
      "s_add_u32 s40, s40, s51\n"
      "s_addc_u32 s41, s41, 0\n"
      "s_waitcnt vmcnt(8)\n"
      "ds_read_b128 v[44:47], v3 offset:26624\n"
      "v_sub_f32_dpp v32, v24, v6 quad_perm:[0,0,0,0] row_mask:0xf bank_mask:0xf\n"
      "v_mov_b32_dpp v33, v26 quad_perm:[2,2,2,2] row_mask:0xf bank_mask:0xf\n"
      "s_waitcnt lgkmcnt(3)\n"
      "v_mfma_f32_16x16x16_bf16 v[16:19], v[52:53], v[38:39], v[16:19]\n"
      "v_add_f32_dpp v32, v25, v32 quad_perm:[1,1,1,1] row_mask:0xf bank_mask:0xf\n"
      "v_add_f32_dpp v33, v27, v33 quad_perm:[3,3,3,3] row_mask:0xf bank_mask:0xf\n"
      "v_mfma_f32_16x16x16_bf16 v[20:23], v[54:55], v[38:39], v[20:23]\n"
      "ds_read_b128 v[52:55], v3 offset:23552\n"
      "v_add_f32 v32, v32, v33\n"
      "v_exp_f32 v34, v32\n"
      "v_max_f32 v35, v35, v32\n"
      "v_add_f32 v7, v7, v34\n"
      "v_cvt_pk_bf16_f32 v34, v34, v34\n"
      "v_and_b32 v36, v4, v34\n"
      "v_and_b32 v37, v5, v34\n"
      "s_add_u32 m0, s61, 35840\n"
      "s_nop 0\n"
      "global_load_lds_dwordx4 v0, s[42:43] nt\n"
      "s_add_u32 s42, s42, s51\n"
      "s_addc_u32 s43, s43, 0\n"
      "s_waitcnt lgkmcnt(3)\n"
      "v_mfma_f32_16x16x32_bf16 v[24:27], v[40:43], v[8:11], 0\n"
      "s_waitcnt vmcnt(8)\n"
      "s_add_u32 m0, s61, 36864\n"
      "s_nop 0\n"
      "global_load_lds_dwordx4 v1, s[40:41] nt\n"
      "s_add_u32 s40, s40, s52\n"
      "s_addc_u32 s41, s41, 0\n"
      "s_waitcnt vmcnt(8)\n"
      "ds_read_b128 v[40:43], v3 offset:28672\n"
      "v_sub_f32_dpp v32, v28, v6 quad_perm:[0,0,0,0] row_mask:0xf bank_mask:0xf\n"
      "v_mov_b32_dpp v33, v30 quad_perm:[2,2,2,2] row_mask:0xf bank_mask:0xf\n"
      "s_waitcnt lgkmcnt(3)\n"
      "v_mfma_f32_16x16x16_bf16 v[16:19], v[48:49], v[36:37], v[16:19]\n"
      "v_add_f32_dpp v32, v29, v32 quad_perm:[1,1,1,1] row_mask:0xf bank_mask:0xf\n"
      "v_add_f32_dpp v33, v31, v33 quad_perm:[3,3,3,3] row_mask:0xf bank_mask:0xf\n"
      "v_mfma_f32_16x16x16_bf16 v[20:23], v[50:51], v[36:37], v[20:23]\n"
      "ds_read_b128 v[48:51], v3 offset:25600\n"
      "v_add_f32 v32, v32, v33\n"
      "v_exp_f32 v34, v32\n"
      "v_max_f32 v35, v35, v32\n"
      "v_add_f32 v7, v7, v34\n"
      "v_cvt_pk_bf16_f32 v34, v34, v34\n"
      "v_and_b32 v38, v4, v34\n"
      "v_and_b32 v39, v5, v34\n"
      "s_waitcnt lgkmcnt(3)\n"
      "v_mfma_f32_16x16x32_bf16 v[28:31], v[44:47], v[8:11], 0\n"
      "s_add_u32 m0, s61, 37888\n"
      "s_nop 0\n"
      "global_load_lds_dwordx4 v0, s[42:43] nt\n"
      "s_add_u32 s42, s42, s52\n"
      "s_addc_u32 s43, s43, 0\n"
      "s_waitcnt vmcnt(8)\n"
      "global_load_dwordx4 a[0:3], v1, s[40:41] nt\n"
      "s_add_u32 s40, s40, 110592\n"
      "s_addc_u32 s41, s41, 0\n"
      "s_waitcnt vmcnt(8)\n"
      "ds_read_b128 v[44:47], v3 offset:30720\n"
      "v_sub_f32_dpp v32, v24, v6 quad_perm:[0,0,0,0] row_mask:0xf bank_mask:0xf\n"
      "v_mov_b32_dpp v33, v26 quad_perm:[2,2,2,2] row_mask:0xf bank_mask:0xf\n"
      "s_waitcnt lgkmcnt(3)\n"
      "v_mfma_f32_16x16x16_bf16 v[16:19], v[52:53], v[38:39], v[16:19]\n"
      "v_add_f32_dpp v32, v25, v32 quad_perm:[1,1,1,1] row_mask:0xf bank_mask:0xf\n"
      "v_add_f32_dpp v33, v27, v33 quad_perm:[3,3,3,3] row_mask:0xf bank_mask:0xf\n"
      "v_mfma_f32_16x16x16_bf16 v[20:23], v[54:55], v[38:39], v[20:23]\n"
      "ds_read_b128 v[52:55], v3 offset:27648\n"
      "v_add_f32 v32, v32, v33\n"
      "v_exp_f32 v34, v32\n"
      "v_max_f32 v35, v35, v32\n"
      "v_add_f32 v7, v7, v34\n"
      "v_cvt_pk_bf16_f32 v34, v34, v34\n"
      "v_and_b32 v36, v4, v34\n"
      "v_and_b32 v37, v5, v34\n"
      "global_load_dwordx4 a[4:7], v0, s[42:43] nt\n"
      "s_add_u32 s42, s42, 110592\n"
      "s_addc_u32 s43, s43, 0\n"
      "s_waitcnt lgkmcnt(3)\n"
      "v_mfma_f32_16x16x32_bf16 v[24:27], v[40:43], v[8:11], 0\n"
      "s_waitcnt vmcnt(8)\n"
      "global_load_dwordx4 a[8:11], v1, s[40:41] nt\n"
      "s_add_u32 s40, s40, 110592\n"
      "s_addc_u32 s41, s41, 0\n"
      "s_waitcnt vmcnt(8)\n"
      "ds_read_b128 v[40:43], v3 offset:32768\n"
      "v_sub_f32_dpp v32, v28, v6 quad_perm:[0,0,0,0] row_mask:0xf bank_mask:0xf\n"
      "v_mov_b32_dpp v33, v30 quad_perm:[2,2,2,2] row_mask:0xf bank_mask:0xf\n"
      "s_waitcnt lgkmcnt(3)\n"
      "v_mfma_f32_16x16x16_bf16 v[16:19], v[48:49], v[36:37], v[16:19]\n"
      "v_add_f32_dpp v32, v29, v32 quad_perm:[1,1,1,1] row_mask:0xf bank_mask:0xf\n"
      "v_add_f32_dpp v33, v31, v33 quad_perm:[3,3,3,3] row_mask:0xf bank_mask:0xf\n"
      "v_mfma_f32_16x16x16_bf16 v[20:23], v[50:51], v[36:37], v[20:23]\n"
      "ds_read_b128 v[48:51], v3 offset:29696\n"
      "v_add_f32 v32, v32, v33\n"
      "v_exp_f32 v34, v32\n"
      "v_max_f32 v35, v35, v32\n"
      "v_add_f32 v7, v7, v34\n"
      "v_cvt_pk_bf16_f32 v34, v34, v34\n"
      "v_and_b32 v38, v4, v34\n"
      "v_and_b32 v39, v5, v34\n"
      "s_waitcnt lgkmcnt(3)\n"
      "v_mfma_f32_16x16x32_bf16 v[28:31], v[44:47], v[8:11], 0\n"
      "global_load_dwordx4 a[12:15], v0, s[42:43] nt\n"
      "s_add_u32 s42, s42, 110592\n"
      "s_addc_u32 s43, s43, 0\n"
      "s_waitcnt vmcnt(8)\n"
      "global_load_dwordx4 a[16:19], v1, s[40:41] nt\n"
      "s_add_u32 s40, s40, 110592\n"
      "s_addc_u32 s41, s41, 0\n"
      "s_waitcnt vmcnt(8)\n"
      "ds_read_b128 v[44:47], v3 offset:34816\n"
      "v_sub_f32_dpp v32, v24, v6 quad_perm:[0,0,0,0] row_mask:0xf bank_mask:0xf\n"
      "v_mov_b32_dpp v33, v26 quad_perm:[2,2,2,2] row_mask:0xf bank_mask:0xf\n"
      "s_waitcnt lgkmcnt(3)\n"
      "v_mfma_f32_16x16x16_bf16 v[16:19], v[52:53], v[38:39], v[16:19]\n"
      "v_add_f32_dpp v32, v25, v32 quad_perm:[1,1,1,1] row_mask:0xf bank_mask:0xf\n"
      "v_add_f32_dpp v33, v27, v33 quad_perm:[3,3,3,3] row_mask:0xf bank_mask:0xf\n"
      "v_mfma_f32_16x16x16_bf16 v[20:23], v[54:55], v[38:39], v[20:23]\n"
      "ds_read_b128 v[52:55], v3 offset:31744\n"
      "v_add_f32 v32, v32, v33\n"
      "v_exp_f32 v34, v32\n"
      "v_max_f32 v35, v35, v32\n"
      "v_add_f32 v7, v7, v34\n"
      "v_cvt_pk_bf16_f32 v34, v34, v34\n"
      "v_and_b32 v36, v4, v34\n"
      "v_and_b32 v37, v5, v34\n"
      "s_waitcnt lgkmcnt(3)\n"
      "v_mfma_f32_16x16x32_bf16 v[24:27], v[40:43], v[8:11], 0\n"
      "global_load_dwordx4 a[20:23], v0, s[42:43] nt\n"
      "s_add_u32 s42, s42, 110592\n"
      "s_addc_u32 s43, s43, 0\n"
      "s_waitcnt vmcnt(8)\n"
      "global_load_dwordx4 a[24:27], v1, s[40:41] nt\n"
      "s_add_u32 s40, s40, 110592\n"
      "s_addc_u32 s41, s41, 0\n"
      "s_waitcnt vmcnt(8)\n"
      "ds_read_b128 v[40:43], v3 offset:36864\n"
      "v_sub_f32_dpp v32, v28, v6 quad_perm:[0,0,0,0] row_mask:0xf bank_mask:0xf\n"
      "v_mov_b32_dpp v33, v30 quad_perm:[2,2,2,2] row_mask:0xf bank_mask:0xf\n"
      "s_waitcnt lgkmcnt(3)\n"
      "v_mfma_f32_16x16x16_bf16 v[16:19], v[48:49], v[36:37], v[16:19]\n"
      "v_add_f32_dpp v32, v29, v32 quad_perm:[1,1,1,1] row_mask:0xf bank_mask:0xf\n"
      "v_add_f32_dpp v33, v31, v33 quad_perm:[3,3,3,3] row_mask:0xf bank_mask:0xf\n"
      "v_mfma_f32_16x16x16_bf16 v[20:23], v[50:51], v[36:37], v[20:23]\n"
      "ds_read_b128 v[48:51], v3 offset:33792\n"
      "v_add_f32 v32, v32, v33\n"
      "v_exp_f32 v34, v32\n"
      "v_max_f32 v35, v35, v32\n"
      "v_add_f32 v7, v7, v34\n"
      "v_cvt_pk_bf16_f32 v34, v34, v34\n"
      "v_and_b32 v38, v4, v34\n"
      "v_and_b32 v39, v5, v34\n"
      "global_load_dwordx4 a[28:31], v0, s[42:43] nt\n"
      "s_add_u32 s42, s42, 110592\n"
      "s_addc_u32 s43, s43, 0\n"
      "s_waitcnt lgkmcnt(3)\n"
      "v_mfma_f32_16x16x32_bf16 v[28:31], v[44:47], v[8:11], 0\n"
      "v_sub_f32_dpp v32, v24, v6 quad_perm:[0,0,0,0] row_mask:0xf bank_mask:0xf\n"
      "v_mov_b32_dpp v33, v26 quad_perm:[2,2,2,2] row_mask:0xf bank_mask:0xf\n"
      "s_waitcnt lgkmcnt(2)\n"
      "v_mfma_f32_16x16x16_bf16 v[16:19], v[52:53], v[38:39], v[16:19]\n"
      "v_add_f32_dpp v32, v25, v32 quad_perm:[1,1,1,1] row_mask:0xf bank_mask:0xf\n"
      "v_add_f32_dpp v33, v27, v33 quad_perm:[3,3,3,3] row_mask:0xf bank_mask:0xf\n"
      "v_mfma_f32_16x16x16_bf16 v[20:23], v[54:55], v[38:39], v[20:23]\n"
      "ds_read_b128 v[52:55], v3 offset:35840\n"
      "v_add_f32 v32, v32, v33\n"
      "v_exp_f32 v34, v32\n"
      "v_max_f32 v35, v35, v32\n"
      "v_add_f32 v7, v7, v34\n"
      "v_cvt_pk_bf16_f32 v34, v34, v34\n"
      "v_and_b32 v36, v4, v34\n"
      "v_and_b32 v37, v5, v34\n"
      "s_waitcnt lgkmcnt(2)\n"
      "v_mfma_f32_16x16x32_bf16 v[24:27], v[40:43], v[8:11], 0\n"
      "v_sub_f32_dpp v32, v28, v6 quad_perm:[0,0,0,0] row_mask:0xf bank_mask:0xf\n"
      "v_mov_b32_dpp v33, v30 quad_perm:[2,2,2,2] row_mask:0xf bank_mask:0xf\n"
      "s_waitcnt lgkmcnt(1)\n"
      "v_mfma_f32_16x16x16_bf16 v[16:19], v[48:49], v[36:37], v[16:19]\n"
      "v_add_f32_dpp v32, v29, v32 quad_perm:[1,1,1,1] row_mask:0xf bank_mask:0xf\n"
      "v_add_f32_dpp v33, v31, v33 quad_perm:[3,3,3,3] row_mask:0xf bank_mask:0xf\n"
      "v_mfma_f32_16x16x16_bf16 v[20:23], v[50:51], v[36:37], v[20:23]\n"
      "s_waitcnt vmcnt(8)\n"
      "ds_read_b128 v[48:51], v3 offset:37888\n"
      "v_add_f32 v32, v32, v33\n"
      "v_exp_f32 v34, v32\n"
      "v_max_f32 v35, v35, v32\n"
      "v_add_f32 v7, v7, v34\n"
      "v_cvt_pk_bf16_f32 v34, v34, v34\n"
      "v_and_b32 v38, v4, v34\n"
      "v_and_b32 v39, v5, v34\n"
      "global_load_dwordx4 a[32:35], v1, s[40:41] nt\n"
      "s_add_u32 s40, s40, 110592\n"
      "s_addc_u32 s41, s41, 0\n"
      "v_sub_f32_dpp v32, v24, v6 quad_perm:[0,0,0,0] row_mask:0xf bank_mask:0xf\n"
      "v_mov_b32_dpp v33, v26 quad_perm:[2,2,2,2] row_mask:0xf bank_mask:0xf\n"
      "s_waitcnt lgkmcnt(1)\n"
      "v_mfma_f32_16x16x16_bf16 v[16:19], v[52:53], v[38:39], v[16:19]\n"
      "v_add_f32_dpp v32, v25, v32 quad_perm:[1,1,1,1] row_mask:0xf bank_mask:0xf\n"
      "v_add_f32_dpp v33, v27, v33 quad_perm:[3,3,3,3] row_mask:0xf bank_mask:0xf\n"
      "v_mfma_f32_16x16x16_bf16 v[20:23], v[54:55], v[38:39], v[20:23]\n"
      "v_add_f32 v32, v32, v33\n"
      "v_add_f32 v32, s69, v32\n"
      "v_exp_f32 v34, v32\n"
      "v_max_f32 v35, v35, v32\n"
      "v_add_f32 v7, v7, v34\n"
      "v_cvt_pk_bf16_f32 v34, v34, v34\n"
      "v_and_b32 v36, v4, v34\n"
      "v_and_b32 v37, v5, v34\n"
      "s_waitcnt lgkmcnt(0)\n"
      "s_nop 0\n"
      "v_mfma_f32_16x16x16_bf16 v[16:19], v[48:49], v[36:37], v[16:19]\n"
      "v_mfma_f32_16x16x16_bf16 v[20:23], v[50:51], v[36:37], v[20:23]\n"
      "s_waitcnt vmcnt(8)\n"
      "global_load_dwordx4 a[36:39], v0, s[42:43] nt\n"
      "s_add_u32 s42, s42, 110592\n"
      "s_addc_u32 s43, s43, 0\n"
      "v_cmp_lt_f32 vcc, 0x42800000, v35\n"
      "s_nop 0\n"
      "s_cbranch_vccnz L_fb0_%=\n"
      "L_ret0_%=:\n"
      "v_mov_b32 v49, v7\n"
      "v_lshlrev_b32 v41, 2, v0\n"
      "v_and_b32 v41, 0x300, v41\n"
      "v_permlane32_swap_b32 v49, v7\n"
      "v_lshrrev_b32 v42, 4, v0\n"
      "v_add_f32 v7, v49, v7\n"
      "v_and_b32 v42, 0x30, v42\n"
      "v_mov_b32 v49, v7\n"
      "v_add_u32 v41, v41, v42\n"
      "v_lshrrev_b32 v40, 2, v0\n"
      "v_permlane16_swap_b32 v49, v7\n"
      "v_and_b32 v42, 12, v40\n"
      "v_add_f32 v7, v49, v7\n"
      "v_add_u32 v41, v41, v42\n"
      "s_lshl_b32 s62, s60, 10\n"
      "s_add_u32 s62, s62, 155648\n"
      "v_add_u32 v41, s62, v41\n"
      "v_lshrrev_b32 v43, 3, v0\n"
      "v_and_b32 v43, 24, v43\n"
      "s_lshl_b32 s62, s60, 5\n"
      "s_add_u32 s62, s62, 159744\n"
      "v_add_u32 v43, s62, v43\n"
      "v_cvt_pk_bf16_f32 v36, v16, v17\n"
      "v_cvt_pk_bf16_f32 v37, v18, v19\n"
      "v_cvt_pk_bf16_f32 v38, v20, v21\n"
      "v_cvt_pk_bf16_f32 v39, v22, v23\n"
      "s_mov_b64 exec, 0x1111\n"
      "ds_write_b64 v43, v[6:7]\n"
      "s_mov_b64 exec, -1\n"
      "ds_write_b32 v41, v36 offset:0\n"
      "ds_write_b32 v41, v37 offset:64\n"
      "ds_write_b32 v41, v38 offset:128\n"
      "ds_write_b32 v41, v39 offset:192\n"
      "s_lshl_b32 s62, s60, 8\n"
      "s_add_u32 s62, s62, 155648\n"
      "v_add_u32 v42, s62, v40\n"
      "s_lshl_b32 s62, s60, 3\n"
      "s_add_u32 s62, s62, 159744\n"
      "v_mov_b32 v50, s62\n"
      "s_waitcnt lgkmcnt(0)\n"
      "s_barrier\n"
      "ds_read_b64 v[28:29], v50 offset:0\n"
      "ds_read_b64 v[30:31], v50 offset:32\n"
      "ds_read_b64 v[32:33], v50 offset:64\n"
      "ds_read_b64 v[34:35], v50 offset:96\n"
      "ds_read_b32 v24, v42 offset:0\n"
      "ds_read_b32 v25, v42 offset:1024\n"
      "ds_read_b32 v26, v42 offset:2048\n"
      "ds_read_b32 v27, v42 offset:3072\n"
      "s_add_u32 s56, s44, 0\n"
      "s_addc_u32 s57, s45, 0\n"
      "s_waitcnt lgkmcnt(4)\n"
      "v_max3_f32 v43, v28, v30, v32\n"
      "v_max_f32 v43, v43, v34\n"
      "v_sub_f32 v28, v28, v43\n"
      "v_sub_f32 v30, v30, v43\n"
      "v_sub_f32 v32, v32, v43\n"
      "v_sub_f32 v34, v34, v43\n"
      "v_exp_f32 v28, v28\n"
      "v_exp_f32 v30, v30\n"
      "v_exp_f32 v32, v32\n"
      "v_exp_f32 v34, v34\n"
      "s_waitcnt lgkmcnt(0)\n"
      "v_lshlrev_b32 v44, 16, v24\n"
      "v_and_b32 v45, 0xffff0000, v24\n"
      "v_lshlrev_b32 v46, 16, v25\n"
      "v_and_b32 v47, 0xffff0000, v25\n"
      "v_lshlrev_b32 v48, 16, v26\n"
      "v_and_b32 v49, 0xffff0000, v26\n"
      "v_lshlrev_b32 v50, 16, v27\n"
      "v_and_b32 v51, 0xffff0000, v27\n"
      "v_mul_f32 v38, v28, v29\n"
      "v_pk_mul_f32 v[36:37], v[44:45], v[28:29] op_sel_hi:[1,0]\n"
      "v_fmac_f32 v38, v30, v31\n"
      "v_pk_fma_f32 v[36:37], v[46:47], v[30:31], v[36:37] op_sel_hi:[1,0,1]\n"
      "v_fmac_f32 v38, v32, v33\n"
      "v_pk_fma_f32 v[36:37], v[48:49], v[32:33], v[36:37] op_sel_hi:[1,0,1]\n"
      "v_fmac_f32 v38, v34, v35\n"
      "v_pk_fma_f32 v[36:37], v[50:51], v[34:35], v[36:37] op_sel_hi:[1,0,1]\n"
      "v_rcp_f32 v39, v38\n"
      "v_log_f32 v38, v38\n"
      "v_mul_f32 v36, v39, v36\n"
      "v_mul_f32 v37, v39, v37\n"
      "v_add_f32 v38, v38, v43\n"
      "v_cvt_pk_bf16_f32 v36, v36, v37\n"
      "v_min_u32 v38, -2, v38\n"
      "v_min_u32 v36, -2, v36\n"
      "global_store_dword v40, v36, s[56:57] sc1\n"
      "s_mov_b32 exec_hi, 0\n"
      "global_store_dword v40, v38, s[56:57] offset:256 sc1\n"
      "s_mov_b64 exec, -1\n"
      "s_waitcnt vmcnt(8)\n"
      "global_load_dwordx4 a[40:43], v1, s[40:41] nt\n"
      "s_add_u32 s40, s40, 110592\n"
      "s_addc_u32 s41, s41, 0\n"
      "s_waitcnt vmcnt(8)\n"
      "global_load_dwordx4 a[44:47], v0, s[42:43] nt\n"
      "s_add_u32 s42, s42, 110592\n"
      "s_addc_u32 s43, s43, 0\n"
      "s_waitcnt vmcnt(8)\n"
      "global_load_dwordx4 a[48:51], v1, s[40:41] nt\n"
      "s_add_u32 s40, s40, 110592\n"
      "s_addc_u32 s41, s41, 0\n"
      "s_waitcnt vmcnt(8)\n"
      "global_load_dwordx4 a[52:55], v0, s[42:43] nt\n"
      "s_add_u32 s42, s42, 110592\n"
      "s_addc_u32 s43, s43, 0\n"
      "s_barrier\n"
      "L_per1_%=:\n"
      "s_add_u32 s56, s87, L_per2_%=-L_begin_%=\n"
      "s_min_u32 s56, s56, L_end_%=-L_begin_%=-1024\n"
      "s_add_u32 s56, s84, s56\n"
      "s_addc_u32 s57, s85, 0\n"
      "s_mov_b32 m0, s86\n"
      "s_nop 0\n"
      "global_load_lds_dword v0, s[56:57]\n"
      "s_waitcnt vmcnt(9)\n"
      "global_load_dwordx4 a[56:59], v1, s[40:41] nt\n"
      "s_add_u32 s40, s40, 110592\n"
      "s_addc_u32 s41, s41, 0\n"
      "s_waitcnt vmcnt(9)\n"
      "global_load_dwordx4 a[60:63], v0, s[42:43] nt\n"
      "s_add_u32 s42, s42, 110592\n"
      "s_addc_u32 s43, s43, 0\n"
      "s_waitcnt vmcnt(9)\n"
      "global_load_dwordx4 a[64:67], v1, s[40:41] nt\n"
      "s_add_u32 s40, s40, 110592\n"
      "s_addc_u32 s41, s41, 0\n"
      "s_waitcnt vmcnt(9)\n"
      "global_load_dwordx4 a[68:71], v0, s[42:43] nt\n"
      "s_add_u32 s42, s42, 110592\n"
      "s_addc_u32 s43, s43, 0\n"
      "s_waitcnt vmcnt(9)\n"
      "global_load_dwordx4 a[72:75], v1, s[40:41] nt\n"
      "s_add_u32 s40, s40, 110592\n"
      "s_addc_u32 s41, s41, 0\n"
      "s_add_u32 s56, s48, 8192\n"
      "s_addc_u32 s57, s49, 0\n"
      "s_mov_b64 exec, 0xffff\n"
      "global_load_dwordx4 v[8:11], v0, s[56:57] sc1\n"
      "s_mov_b64 exec, -1\n"
      "s_waitcnt vmcnt(10)\n"
      "global_load_dwordx4 a[76:79], v0, s[42:43] nt\n"
      "s_add_u32 s42, s42, 110592\n"
      "s_addc_u32 s43, s43, 0\n"
      "s_waitcnt vmcnt(10)\n"
      "global_load_dwordx4 a[80:83], v1, s[40:41] nt\n"
      "s_add_u32 s40, s40, 110592\n"
      "s_addc_u32 s41, s41, 0\n"
      "s_waitcnt vmcnt(10)\n"
      "global_load_dwordx4 a[84:87], v0, s[42:43] nt\n"
      "s_add_u32 s42, s42, 110592\n"
      "s_addc_u32 s43, s43, 0\n"
      "s_waitcnt vmcnt(10)\n"
      "global_load_dwordx4 a[88:91], v1, s[40:41] nt\n"
      "s_add_u32 s40, s40, 110592\n"
      "s_addc_u32 s41, s41, 0\n"
      "s_waitcnt vmcnt(9)\n"
      "global_load_dwordx4 a[92:95], v0, s[42:43] nt\n"
      "s_add_u32 s42, s42, 110592\n"
      "s_addc_u32 s43, s43, 0\n"
      "s_waitcnt vmcnt(9)\n"
      "global_load_dwordx4 a[96:99], v1, s[40:41] nt\n"
      "s_add_u32 s40, s40, 110592\n"
      "s_addc_u32 s41, s41, 0\n"
      "s_waitcnt vmcnt(9)\n"
      "global_load_dwordx4 a[100:103], v0, s[42:43] nt\n"
      "s_add_u32 s42, s42, 110592\n"
      "s_addc_u32 s43, s43, 0\n"
      "s_waitcnt vmcnt(9)\n"
      "global_load_dwordx4 a[104:107], v1, s[40:41] nt\n"
      "s_add_u32 s40, s40, 110592\n"
      "s_addc_u32 s41, s41, 0\n"
      "s_waitcnt vmcnt(9)\n"
      "global_load_dwordx4 a[108:111], v0, s[42:43] nt\n"
      "s_add_u32 s42, s42, 110592\n"
      "s_addc_u32 s43, s43, 0\n"
      "s_waitcnt vmcnt(9)\n"
      "s_mov_b64 exec, 0xffff\n"
      "s_mov_b32 s53, 0\n"
      "L_qc5_%=:\n"
      "v_max3_u32 v49, v8, v9, v10\n"
      "v_max_u32 v49, v49, v11\n"
      "v_cmp_eq_u32 vcc, -1, v49\n"
      "s_nop 0\n"
      "s_cbranch_vccz L_qo6_%=\n"
      "s_cmp_lg_u32 s54, 0\n"
      "s_cbranch_scc1 L_qo6_%=\n"
      "s_add_u32 s56, s48, 8192\n"
      "s_addc_u32 s57, s49, 0\n"
      "global_load_dwordx4 v[8:11], v0, s[56:57] sc1\n"
      "s_add_u32 s53, s53, 1\n"
      "s_waitcnt vmcnt(0)\n"
      "s_cmp_lt_u32 s53, 30000\n"
      "s_cbranch_scc1 L_qc5_%=\n"
      "s_mov_b32 s54, 101\n"
      "v_mov_b32 v49, 101\n"
      "v_mov_b32 v50, 256\n"
      "global_store_dword v50, v49, s[58:59] sc1\n"
      "L_qo6_%=:\n"
      "s_lshl_b32 s62, s60, 8\n"
      "s_add_u32 s62, s62, 159872\n"
      "v_add_u32 v49, s62, v0\n"
      "ds_write_b128 v49, v[8:11]\n"
      "s_mov_b64 exec, -1\n"
      "s_waitcnt lgkmcnt(0)\n"
      "s_barrier\n"
      "ds_read_b128 v[8:11], v2 offset:0\n"
      "s_waitcnt lgkmcnt(0)\n"
      "v_mfma_f32_16x16x32_bf16 v[24:27], a[0:3], v[8:11], 0\n"
      "v_mfma_f32_16x16x32_bf16 v[28:31], a[8:11], v[8:11], 0\n"
      "s_nop 6\n"
      "v_mov_b32_dpp v34, v24 quad_perm:[0,0,0,0] row_mask:0xf bank_mask:0xf\n"
      "v_mov_b32_dpp v33, v26 quad_perm:[2,2,2,2] row_mask:0xf bank_mask:0xf\n"
      "s_nop 0\n"
      "v_add_f32_dpp v34, v25, v34 quad_perm:[1,1,1,1] row_mask:0xf bank_mask:0xf\n"
      "v_add_f32_dpp v33, v27, v33 quad_perm:[3,3,3,3] row_mask:0xf bank_mask:0xf\n"
      "v_add_f32 v34, v34, v33\n"
      "v_mov_b32_dpp v32, v28 quad_perm:[0,0,0,0] row_mask:0xf bank_mask:0xf\n"
      "v_mov_b32_dpp v33, v30 quad_perm:[2,2,2,2] row_mask:0xf bank_mask:0xf\n"
      "s_nop 0\n"
      "v_add_f32_dpp v32, v29, v32 quad_perm:[1,1,1,1] row_mask:0xf bank_mask:0xf\n"
      "v_add_f32_dpp v33, v31, v33 quad_perm:[3,3,3,3] row_mask:0xf bank_mask:0xf\n"
      "v_add_f32 v32, v32, v33\n"
      "v_max_f32 v35, v34, v32\n"
      "v_mov_b32 v33, v35\n"
      "s_nop 1\n"
      "v_permlane32_swap_b32 v33, v35\n"
      "v_max_f32 v35, v33, v35\n"
      "v_mov_b32 v33, v35\n"
      "s_nop 1\n"
      "v_permlane16_swap_b32 v33, v35\n"
      "v_max_f32 v6, v33, v35\n"
      "v_sub_f32 v34, v34, v6\n"
      "v_sub_f32 v32, v32, v6\n"
      "v_mov_b32 v35, 0\n"
      "v_exp_f32 v34, v34\n"
      "s_nop 0\n"
      "v_mov_b32 v7, v34\n"
      "v_cvt_pk_bf16_f32 v34, v34, v34\n"
      "v_and_b32 v36, v4, v34\n"
      "v_and_b32 v37, v5, v34\n"
      "v_mfma_f32_16x16x32_bf16 v[24:27], a[16:19], v[8:11], 0\n"
      "s_nop 0\n"
      "v_mfma_f32_16x16x16_bf16 v[16:19], a[4:5], v[36:37], 0\n"
      "v_mfma_f32_16x16x16_bf16 v[20:23], a[6:7], v[36:37], 0\n"
      "v_exp_f32 v34, v32\n"
      "v_max_f32 v35, v35, v32\n"
      "v_add_f32 v7, v7, v34\n"
      "v_cvt_pk_bf16_f32 v34, v34, v34\n"
      "v_and_b32 v38, v4, v34\n"
      "v_and_b32 v39, v5, v34\n"
      "v_mfma_f32_16x16x32_bf16 v[28:31], a[24:27], v[8:11], 0\n"
      "v_sub_f32_dpp v32, v24, v6 quad_perm:[0,0,0,0] row_mask:0xf bank_mask:0xf\n"
      "v_mov_b32_dpp v33, v26 quad_perm:[2,2,2,2] row_mask:0xf bank_mask:0xf\n"
      "v_mfma_f32_16x16x16_bf16 v[16:19], a[12:13], v[38:39], v[16:19]\n"
      "v_add_f32_dpp v32, v25, v32 quad_perm:[1,1,1,1] row_mask:0xf bank_mask:0xf\n"
      "v_add_f32_dpp v33, v27, v33 quad_perm:[3,3,3,3] row_mask:0xf bank_mask:0xf\n"
      "v_mfma_f32_16x16x16_bf16 v[20:23], a[14:15], v[38:39], v[20:23]\n"
      "v_add_f32 v32, v32, v33\n"
      "v_exp_f32 v34, v32\n"
      "v_max_f32 v35, v35, v32\n"
      "v_add_f32 v7, v7, v34\n"
      "v_cvt_pk_bf16_f32 v34, v34, v34\n"
      "v_and_b32 v36, v4, v34\n"
      "v_and_b32 v37, v5, v34\n"
      "v_mfma_f32_16x16x32_bf16 v[24:27], a[32:35], v[8:11], 0\n"
      "v_sub_f32_dpp v32, v28, v6 quad_perm:[0,0,0,0] row_mask:0xf bank_mask:0xf\n"
      "v_mov_b32_dpp v33, v30 quad_perm:[2,2,2,2] row_mask:0xf bank_mask:0xf\n"
      "v_mfma_f32_16x16x16_bf16 v[16:19], a[20:21], v[36:37], v[16:19]\n"
      "v_add_f32_dpp v32, v29, v32 quad_perm:[1,1,1,1] row_mask:0xf bank_mask:0xf\n"
      "v_add_f32_dpp v33, v31, v33 quad_perm:[3,3,3,3] row_mask:0xf bank_mask:0xf\n"
      "v_mfma_f32_16x16x16_bf16 v[20:23], a[22:23], v[36:37], v[20:23]\n"
      "v_add_f32 v32, v32, v33\n"
      "v_exp_f32 v34, v32\n"
      "v_max_f32 v35, v35, v32\n"
      "v_add_f32 v7, v7, v34\n"
      "v_cvt_pk_bf16_f32 v34, v34, v34\n"
      "v_and_b32 v38, v4, v34\n"
      "v_and_b32 v39, v5, v34\n"
      "s_waitcnt vmcnt(8)\n"
      "global_load_dwordx4 a[112:115], v1, s[40:41] nt\n"
      "s_add_u32 s40, s40, 110592\n"
      "s_addc_u32 s41, s41, 0\n"
      "v_mfma_f32_16x16x32_bf16 v[28:31], a[40:43], v[8:11], 0\n"
      "v_sub_f32_dpp v32, v24, v6 quad_perm:[0,0,0,0] row_mask:0xf bank_mask:0xf\n"
      "v_mov_b32_dpp v33, v26 quad_perm:[2,2,2,2] row_mask:0xf bank_mask:0xf\n"
      "v_mfma_f32_16x16x16_bf16 v[16:19], a[28:29], v[38:39], v[16:19]\n"
      "v_add_f32_dpp v32, v25, v32 quad_perm:[1,1,1,1] row_mask:0xf bank_mask:0xf\n"
      "v_add_f32_dpp v33, v27, v33 quad_perm:[3,3,3,3] row_mask:0xf bank_mask:0xf\n"
      "v_mfma_f32_16x16x16_bf16 v[20:23], a[30:31], v[38:39], v[20:23]\n"
      "v_add_f32 v32, v32, v33\n"
      "v_exp_f32 v34, v32\n"
      "v_max_f32 v35, v35, v32\n"
      "v_add_f32 v7, v7, v34\n"
      "v_cvt_pk_bf16_f32 v34, v34, v34\n"
      "v_and_b32 v36, v4, v34\n"
      "v_and_b32 v37, v5, v34\n"
      "v_mfma_f32_16x16x32_bf16 v[24:27], a[48:51], v[8:11], 0\n"
      "v_sub_f32_dpp v32, v28, v6 quad_perm:[0,0,0,0] row_mask:0xf bank_mask:0xf\n"
      "v_mov_b32_dpp v33, v30 quad_perm:[2,2,2,2] row_mask:0xf bank_mask:0xf\n"
      "v_mfma_f32_16x16x16_bf16 v[16:19], a[36:37], v[36:37], v[16:19]\n"
      "v_add_f32_dpp v32, v29, v32 quad_perm:[1,1,1,1] row_mask:0xf bank_mask:0xf\n"
      "v_add_f32_dpp v33, v31, v33 quad_perm:[3,3,3,3] row_mask:0xf bank_mask:0xf\n"
      "v_mfma_f32_16x16x16_bf16 v[20:23], a[38:39], v[36:37], v[20:23]\n"
      "v_add_f32 v32, v32, v33\n"
      "v_exp_f32 v34, v32\n"
      "v_max_f32 v35, v35, v32\n"
      "v_add_f32 v7, v7, v34\n"
      "v_cvt_pk_bf16_f32 v34, v34, v34\n"
      "v_and_b32 v38, v4, v34\n"
      "v_and_b32 v39, v5, v34\n"
      "s_waitcnt vmcnt(8)\n"
      "global_load_dwordx4 a[116:119], v0, s[42:43] nt\n"
      "s_add_u32 s42, s42, 110592\n"
      "s_addc_u32 s43, s43, 0\n"
      "v_mfma_f32_16x16x32_bf16 v[28:31], a[56:59], v[8:11], 0\n"
      "v_sub_f32_dpp v32, v24, v6 quad_perm:[0,0,0,0] row_mask:0xf bank_mask:0xf\n"
      "v_mov_b32_dpp v33, v26 quad_perm:[2,2,2,2] row_mask:0xf bank_mask:0xf\n"
      "v_mfma_f32_16x16x16_bf16 v[16:19], a[44:45], v[38:39], v[16:19]\n"
      "v_add_f32_dpp v32, v25, v32 quad_perm:[1,1,1,1] row_mask:0xf bank_mask:0xf\n"
      "v_add_f32_dpp v33, v27, v33 quad_perm:[3,3,3,3] row_mask:0xf bank_mask:0xf\n"
      "v_mfma_f32_16x16x16_bf16 v[20:23], a[46:47], v[38:39], v[20:23]\n"
      "v_add_f32 v32, v32, v33\n"
      "v_exp_f32 v34, v32\n"
      "v_max_f32 v35, v35, v32\n"
      "v_add_f32 v7, v7, v34\n"
      "v_cvt_pk_bf16_f32 v34, v34, v34\n"
      "v_and_b32 v36, v4, v34\n"
      "v_and_b32 v37, v5, v34\n"
      "v_mfma_f32_16x16x32_bf16 v[24:27], a[64:67], v[8:11], 0\n"
      "v_sub_f32_dpp v32, v28, v6 quad_perm:[0,0,0,0] row_mask:0xf bank_mask:0xf\n"
      "v_mov_b32_dpp v33, v30 quad_perm:[2,2,2,2] row_mask:0xf bank_mask:0xf\n"
      "v_mfma_f32_16x16x16_bf16 v[16:19], a[52:53], v[36:37], v[16:19]\n"
      "v_add_f32_dpp v32, v29, v32 quad_perm:[1,1,1,1] row_mask:0xf bank_mask:0xf\n"
      "v_add_f32_dpp v33, v31, v33 quad_perm:[3,3,3,3] row_mask:0xf bank_mask:0xf\n"
      "v_mfma_f32_16x16x16_bf16 v[20:23], a[54:55], v[36:37], v[20:23]\n"
      "v_add_f32 v32, v32, v33\n"
      "v_exp_f32 v34, v32\n"
      "v_max_f32 v35, v35, v32\n"
      "v_add_f32 v7, v7, v34\n"
      "v_cvt_pk_bf16_f32 v34, v34, v34\n"
      "v_and_b32 v38, v4, v34\n"
      "v_and_b32 v39, v5, v34\n"
      "v_mfma_f32_16x16x32_bf16 v[28:31], a[72:75], v[8:11], 0\n"
      "v_sub_f32_dpp v32, v24, v6 quad_perm:[0,0,0,0] row_mask:0xf bank_mask:0xf\n"
      "v_mov_b32_dpp v33, v26 quad_perm:[2,2,2,2] row_mask:0xf bank_mask:0xf\n"
      "v_mfma_f32_16x16x16_bf16 v[16:19], a[60:61], v[38:39], v[16:19]\n"
      "v_add_f32_dpp v32, v25, v32 quad_perm:[1,1,1,1] row_mask:0xf bank_mask:0xf\n"
      "v_add_f32_dpp v33, v27, v33 quad_perm:[3,3,3,3] row_mask:0xf bank_mask:0xf\n"
      "v_mfma_f32_16x16x16_bf16 v[20:23], a[62:63], v[38:39], v[20:23]\n"
      "v_add_f32 v32, v32, v33\n"
      "v_exp_f32 v34, v32\n"
      "v_max_f32 v35, v35, v32\n"
      "v_add_f32 v7, v7, v34\n"
      "v_cvt_pk_bf16_f32 v34, v34, v34\n"
      "v_and_b32 v36, v4, v34\n"
      "v_and_b32 v37, v5, v34\n"
      "s_waitcnt vmcnt(8)\n"
      "global_load_dwordx4 a[120:123], v1, s[40:41] nt\n"
      "s_add_u32 s40, s40, 110592\n"
      "s_addc_u32 s41, s41, 0\n"
      "v_mfma_f32_16x16x32_bf16 v[24:27], a[80:83], v[8:11], 0\n"
      "v_sub_f32_dpp v32, v28, v6 quad_perm:[0,0,0,0] row_mask:0xf bank_mask:0xf\n"
      "v_mov_b32_dpp v33, v30 quad_perm:[2,2,2,2] row_mask:0xf bank_mask:0xf\n"
      "v_mfma_f32_16x16x16_bf16 v[16:19], a[68:69], v[36:37], v[16:19]\n"
      "v_add_f32_dpp v32, v29, v32 quad_perm:[1,1,1,1] row_mask:0xf bank_mask:0xf\n"
      "v_add_f32_dpp v33, v31, v33 quad_perm:[3,3,3,3] row_mask:0xf bank_mask:0xf\n"
      "v_mfma_f32_16x16x16_bf16 v[20:23], a[70:71], v[36:37], v[20:23]\n"
      "v_add_f32 v32, v32, v33\n"
      "v_exp_f32 v34, v32\n"
      "v_max_f32 v35, v35, v32\n"
      "v_add_f32 v7, v7, v34\n"
      "v_cvt_pk_bf16_f32 v34, v34, v34\n"
      "v_and_b32 v38, v4, v34\n"
      "v_and_b32 v39, v5, v34\n"
      "s_waitcnt vmcnt(8)\n"
      "v_mfma_f32_16x16x32_bf16 v[28:31], a[88:91], v[8:11], 0\n"
      "v_sub_f32_dpp v32, v24, v6 quad_perm:[0,0,0,0] row_mask:0xf bank_mask:0xf\n"
      "v_mov_b32_dpp v33, v26 quad_perm:[2,2,2,2] row_mask:0xf bank_mask:0xf\n"
      "v_mfma_f32_16x16x16_bf16 v[16:19], a[76:77], v[38:39], v[16:19]\n"
      "v_add_f32_dpp v32, v25, v32 quad_perm:[1,1,1,1] row_mask:0xf bank_mask:0xf\n"
      "v_add_f32_dpp v33, v27, v33 quad_perm:[3,3,3,3] row_mask:0xf bank_mask:0xf\n"
      "v_mfma_f32_16x16x16_bf16 v[20:23], a[78:79], v[38:39], v[20:23]\n"
      "v_add_f32 v32, v32, v33\n"
      "v_exp_f32 v34, v32\n"
      "v_max_f32 v35, v35, v32\n"
      "v_add_f32 v7, v7, v34\n"
      "v_cvt_pk_bf16_f32 v34, v34, v34\n"
      "v_and_b32 v36, v4, v34\n"
      "v_and_b32 v37, v5, v34\n"
      "global_load_dwordx4 a[124:127], v0, s[42:43] nt\n"
      "s_add_u32 s42, s42, 110592\n"
      "s_addc_u32 s43, s43, 0\n"
      "s_waitcnt vmcnt(8)\n"
      "global_load_dwordx4 a[128:131], v1, s[40:41] nt\n"
      "s_add_u32 s40, s40, 110592\n"
      "s_addc_u32 s41, s41, 0\n"
      "s_waitcnt vmcnt(8)\n"
      "v_mfma_f32_16x16x32_bf16 v[24:27], a[96:99], v[8:11], 0\n"
      "v_sub_f32_dpp v32, v28, v6 quad_perm:[0,0,0,0] row_mask:0xf bank_mask:0xf\n"
      "v_mov_b32_dpp v33, v30 quad_perm:[2,2,2,2] row_mask:0xf bank_mask:0xf\n"
      "v_mfma_f32_16x16x16_bf16 v[16:19], a[84:85], v[36:37], v[16:19]\n"
      "v_add_f32_dpp v32, v29, v32 quad_perm:[1,1,1,1] row_mask:0xf bank_mask:0xf\n"
      "v_add_f32_dpp v33, v31, v33 quad_perm:[3,3,3,3] row_mask:0xf bank_mask:0xf\n"
      "v_mfma_f32_16x16x16_bf16 v[20:23], a[86:87], v[36:37], v[20:23]\n"
      "v_add_f32 v32, v32, v33\n"
      "v_exp_f32 v34, v32\n"
      "v_max_f32 v35, v35, v32\n"
      "v_add_f32 v7, v7, v34\n"
      "v_cvt_pk_bf16_f32 v34, v34, v34\n"
      "v_and_b32 v38, v4, v34\n"
      "v_and_b32 v39, v5, v34\n"
      "global_load_dwordx4 a[132:135], v0, s[42:43] nt\n"
      "s_add_u32 s42, s42, 110592\n"
      "s_addc_u32 s43, s43, 0\n"
      "s_waitcnt vmcnt(8)\n"
      "global_load_dwordx4 a[136:139], v1, s[40:41] nt\n"
      "s_add_u32 s40, s40, s51\n"
      "s_addc_u32 s41, s41, 0\n"
      "s_waitcnt vmcnt(8)\n"
      "v_mfma_f32_16x16x32_bf16 v[28:31], a[104:107], v[8:11], 0\n"
      "v_sub_f32_dpp v32, v24, v6 quad_perm:[0,0,0,0] row_mask:0xf bank_mask:0xf\n"
      "v_mov_b32_dpp v33, v26 quad_perm:[2,2,2,2] row_mask:0xf bank_mask:0xf\n"
      "v_mfma_f32_16x16x16_bf16 v[16:19], a[92:93], v[38:39], v[16:19]\n"
      "v_add_f32_dpp v32, v25, v32 quad_perm:[1,1,1,1] row_mask:0xf bank_mask:0xf\n"
      "v_add_f32_dpp v33, v27, v33 quad_perm:[3,3,3,3] row_mask:0xf bank_mask:0xf\n"
      "v_mfma_f32_16x16x16_bf16 v[20:23], a[94:95], v[38:39], v[20:23]\n"
      "v_add_f32 v32, v32, v33\n"
      "v_exp_f32 v34, v32\n"
      "v_max_f32 v35, v35, v32\n"
      "v_add_f32 v7, v7, v34\n"
      "v_cvt_pk_bf16_f32 v34, v34, v34\n"
      "v_and_b32 v36, v4, v34\n"
      "v_and_b32 v37, v5, v34\n"
      "global_load_dwordx4 a[140:143], v0, s[42:43] nt\n"
      "s_add_u32 s42, s42, s51\n"
      "s_addc_u32 s43, s43, 0\n"
      "s_waitcnt vmcnt(8)\n"
      "global_load_dwordx4 a[144:147], v1, s[40:41] nt\n"
      "s_add_u32 s40, s40, s52\n"
      "s_addc_u32 s41, s41, 0\n"
      "s_waitcnt vmcnt(8)\n"
      "v_mfma_f32_16x16x32_bf16 v[24:27], a[112:115], v[8:11], 0\n"
      "v_sub_f32_dpp v32, v28, v6 quad_perm:[0,0,0,0] row_mask:0xf bank_mask:0xf\n"
      "v_mov_b32_dpp v33, v30 quad_perm:[2,2,2,2] row_mask:0xf bank_mask:0xf\n"
      "v_mfma_f32_16x16x16_bf16 v[16:19], a[100:101], v[36:37], v[16:19]\n"
      "v_add_f32_dpp v32, v29, v32 quad_perm:[1,1,1,1] row_mask:0xf bank_mask:0xf\n"
      "v_add_f32_dpp v33, v31, v33 quad_perm:[3,3,3,3] row_mask:0xf bank_mask:0xf\n"
      "v_mfma_f32_16x16x16_bf16 v[20:23], a[102:103], v[36:37], v[20:23]\n"
      "v_add_f32 v32, v32, v33\n"
      "v_exp_f32 v34, v32\n"
      "v_max_f32 v35, v35, v32\n"
      "v_add_f32 v7, v7, v34\n"
      "v_cvt_pk_bf16_f32 v34, v34, v34\n"
      "v_and_b32 v38, v4, v34\n"
      "v_and_b32 v39, v5, v34\n"
      "global_load_dwordx4 a[148:151], v0, s[42:43] nt\n"
      "s_add_u32 s42, s42, s52\n"
      "s_addc_u32 s43, s43, 0\n"
      "s_waitcnt vmcnt(8)\n"
      "global_load_dwordx4 a[152:155], v1, s[40:41] nt\n"
      "s_add_u32 s40, s40, 110592\n"
      "s_addc_u32 s41, s41, 0\n"
      "s_waitcnt vmcnt(8)\n"
      "v_mfma_f32_16x16x32_bf16 v[28:31], a[120:123], v[8:11], 0\n"
      "v_sub_f32_dpp v32, v24, v6 quad_perm:[0,0,0,0] row_mask:0xf bank_mask:0xf\n"
      "v_mov_b32_dpp v33, v26 quad_perm:[2,2,2,2] row_mask:0xf bank_mask:0xf\n"
      "v_mfma_f32_16x16x16_bf16 v[16:19], a[108:109], v[38:39], v[16:19]\n"
      "v_add_f32_dpp v32, v25, v32 quad_perm:[1,1,1,1] row_mask:0xf bank_mask:0xf\n"
      "v_add_f32_dpp v33, v27, v33 quad_perm:[3,3,3,3] row_mask:0xf bank_mask:0xf\n"
      "v_mfma_f32_16x16x16_bf16 v[20:23], a[110:111], v[38:39], v[20:23]\n"
      "v_add_f32 v32, v32, v33\n"
      "v_exp_f32 v34, v32\n"
      "v_max_f32 v35, v35, v32\n"
      "v_add_f32 v7, v7, v34\n"
      "v_cvt_pk_bf16_f32 v34, v34, v34\n"
      "v_and_b32 v36, v4, v34\n"
      "v_and_b32 v37, v5, v34\n"
      "global_load_dwordx4 a[156:159], v0, s[42:43] nt\n"
      "s_add_u32 s42, s42, 110592\n"
      "s_addc_u32 s43, s43, 0\n"
      "s_waitcnt vmcnt(8)\n"
      "global_load_dwordx4 a[160:163], v1, s[40:41] nt\n"
      "s_add_u32 s40, s40, 110592\n"
      "s_addc_u32 s41, s41, 0\n"
      "s_waitcnt vmcnt(8)\n"
      "v_mfma_f32_16x16x32_bf16 v[24:27], a[128:131], v[8:11], 0\n"
      "v_sub_f32_dpp v32, v28, v6 quad_perm:[0,0,0,0] row_mask:0xf bank_mask:0xf\n"
      "v_mov_b32_dpp v33, v30 quad_perm:[2,2,2,2] row_mask:0xf bank_mask:0xf\n"
      "v_mfma_f32_16x16x16_bf16 v[16:19], a[116:117], v[36:37], v[16:19]\n"
      "v_add_f32_dpp v32, v29, v32 quad_perm:[1,1,1,1] row_mask:0xf bank_mask:0xf\n"
      "v_add_f32_dpp v33, v31, v33 quad_perm:[3,3,3,3] row_mask:0xf bank_mask:0xf\n"
      "v_mfma_f32_16x16x16_bf16 v[20:23], a[118:119], v[36:37], v[20:23]\n"
      "v_add_f32 v32, v32, v33\n"
      "v_exp_f32 v34, v32\n"
      "v_max_f32 v35, v35, v32\n"
      "v_add_f32 v7, v7, v34\n"
      "v_cvt_pk_bf16_f32 v34, v34, v34\n"
      "v_and_b32 v38, v4, v34\n"
      "v_and_b32 v39, v5, v34\n"
      "global_load_dwordx4 a[164:167], v0, s[42:43] nt\n"
      "s_add_u32 s42, s42, 110592\n"
      "s_addc_u32 s43, s43, 0\n"
      "s_waitcnt vmcnt(8)\n"
      "global_load_dwordx4 a[168:171], v1, s[40:41] nt\n"
      "s_add_u32 s40, s40, 110592\n"
      "s_addc_u32 s41, s41, 0\n"
      "s_waitcnt vmcnt(8)\n"
      "v_mfma_f32_16x16x32_bf16 v[28:31], a[136:139], v[8:11], 0\n"
      "v_sub_f32_dpp v32, v24, v6 quad_perm:[0,0,0,0] row_mask:0xf bank_mask:0xf\n"
      "v_mov_b32_dpp v33, v26 quad_perm:[2,2,2,2] row_mask:0xf bank_mask:0xf\n"
      "v_mfma_f32_16x16x16_bf16 v[16:19], a[124:125], v[38:39], v[16:19]\n"
      "v_add_f32_dpp v32, v25, v32 quad_perm:[1,1,1,1] row_mask:0xf bank_mask:0xf\n"
      "v_add_f32_dpp v33, v27, v33 quad_perm:[3,3,3,3] row_mask:0xf bank_mask:0xf\n"
      "v_mfma_f32_16x16x16_bf16 v[20:23], a[126:127], v[38:39], v[20:23]\n"
      "v_add_f32 v32, v32, v33\n"
      "v_exp_f32 v34, v32\n"
      "v_max_f32 v35, v35, v32\n"
      "v_add_f32 v7, v7, v34\n"
      "v_cvt_pk_bf16_f32 v34, v34, v34\n"
      "v_and_b32 v36, v4, v34\n"
      "v_and_b32 v37, v5, v34\n"
      "global_load_dwordx4 a[172:175], v0, s[42:43] nt\n"
      "s_add_u32 s42, s42, 110592\n"
      "s_addc_u32 s43, s43, 0\n"
      "s_waitcnt vmcnt(8)\n"
      "global_load_dwordx4 a[176:179], v1, s[40:41] nt\n"
      "s_add_u32 s40, s40, 110592\n"
      "s_addc_u32 s41, s41, 0\n"
      "s_waitcnt vmcnt(8)\n"
      "v_mfma_f32_16x16x32_bf16 v[24:27], a[144:147], v[8:11], 0\n"
      "v_sub_f32_dpp v32, v28, v6 quad_perm:[0,0,0,0] row_mask:0xf bank_mask:0xf\n"
      "v_mov_b32_dpp v33, v30 quad_perm:[2,2,2,2] row_mask:0xf bank_mask:0xf\n"
      "v_mfma_f32_16x16x16_bf16 v[16:19], a[132:133], v[36:37], v[16:19]\n"
      "v_add_f32_dpp v32, v29, v32 quad_perm:[1,1,1,1] row_mask:0xf bank_mask:0xf\n"
      "v_add_f32_dpp v33, v31, v33 quad_perm:[3,3,3,3] row_mask:0xf bank_mask:0xf\n"
      "v_mfma_f32_16x16x16_bf16 v[20:23], a[134:135], v[36:37], v[20:23]\n"
      "v_add_f32 v32, v32, v33\n"
      "v_exp_f32 v34, v32\n"
      "v_max_f32 v35, v35, v32\n"
      "v_add_f32 v7, v7, v34\n"
      "v_cvt_pk_bf16_f32 v34, v34, v34\n"
      "v_and_b32 v38, v4, v34\n"
      "v_and_b32 v39, v5, v34\n"
      "global_load_dwordx4 a[180:183], v0, s[42:43] nt\n"
      "s_add_u32 s42, s42, 110592\n"
      "s_addc_u32 s43, s43, 0\n"
      "v_sub_f32_dpp v32, v24, v6 quad_perm:[0,0,0,0] row_mask:0xf bank_mask:0xf\n"
      "v_mov_b32_dpp v33, v26 quad_perm:[2,2,2,2] row_mask:0xf bank_mask:0xf\n"
      "v_mfma_f32_16x16x16_bf16 v[16:19], a[140:141], v[38:39], v[16:19]\n"
      "v_add_f32_dpp v32, v25, v32 quad_perm:[1,1,1,1] row_mask:0xf bank_mask:0xf\n"
      "v_add_f32_dpp v33, v27, v33 quad_perm:[3,3,3,3] row_mask:0xf bank_mask:0xf\n"
      "v_mfma_f32_16x16x16_bf16 v[20:23], a[142:143], v[38:39], v[20:23]\n"
      "v_add_f32 v32, v32, v33\n"
      "v_add_f32 v32, s69, v32\n"
      "v_exp_f32 v34, v32\n"
      "v_max_f32 v35, v35, v32\n"
      "v_add_f32 v7, v7, v34\n"
      "v_cvt_pk_bf16_f32 v34, v34, v34\n"
      "v_and_b32 v36, v4, v34\n"
      "v_and_b32 v37, v5, v34\n"
      "s_waitcnt vmcnt(8)\n"
      "s_nop 0\n"
      "v_mfma_f32_16x16x16_bf16 v[16:19], a[148:149], v[36:37], v[16:19]\n"
      "v_mfma_f32_16x16x16_bf16 v[20:23], a[150:151], v[36:37], v[20:23]\n"
      "global_load_dwordx4 a[184:187], v1, s[40:41] nt\n"
      "s_add_u32 s40, s40, 110592\n"
      "s_addc_u32 s41, s41, 0\n"
      "v_cmp_lt_f32 vcc, 0x42800000, v35\n"
      "s_nop 0\n"
      "s_cbranch_vccnz L_fb1_%=\n"
      "L_ret1_%=:\n"
      "v_mov_b32 v49, v7\n"
      "v_lshlrev_b32 v41, 2, v0\n"
      "v_and_b32 v41, 0x300, v41\n"
      "v_permlane32_swap_b32 v49, v7\n"
      "v_lshrrev_b32 v42, 4, v0\n"
      "v_add_f32 v7, v49, v7\n"
      "v_and_b32 v42, 0x30, v42\n"
      "v_mov_b32 v49, v7\n"
      "v_add_u32 v41, v41, v42\n"
      "v_lshrrev_b32 v40, 2, v0\n"
      "v_permlane16_swap_b32 v49, v7\n"
      "v_and_b32 v42, 12, v40\n"
      "v_add_f32 v7, v49, v7\n"
      "v_add_u32 v41, v41, v42\n"
      "s_lshl_b32 s62, s60, 10\n"
      "s_add_u32 s62, s62, 155648\n"
      "v_add_u32 v41, s62, v41\n"
      "v_lshrrev_b32 v43, 3, v0\n"
      "v_and_b32 v43, 24, v43\n"
      "s_lshl_b32 s62, s60, 5\n"
      "s_add_u32 s62, s62, 159744\n"
      "v_add_u32 v43, s62, v43\n"
      "v_cvt_pk_bf16_f32 v36, v16, v17\n"
      "v_cvt_pk_bf16_f32 v37, v18, v19\n"
      "v_cvt_pk_bf16_f32 v38, v20, v21\n"
      "v_cvt_pk_bf16_f32 v39, v22, v23\n"
      "s_mov_b64 exec, 0x1111\n"
      "ds_write_b64 v43, v[6:7]\n"
      "s_mov_b64 exec, -1\n"
      "ds_write_b32 v41, v36 offset:0\n"
      "ds_write_b32 v41, v37 offset:64\n"
      "ds_write_b32 v41, v38 offset:128\n"
      "ds_write_b32 v41, v39 offset:192\n"
      "s_lshl_b32 s62, s60, 8\n"
      "s_add_u32 s62, s62, 155648\n"
      "v_add_u32 v42, s62, v40\n"
      "s_lshl_b32 s62, s60, 3\n"
      "s_add_u32 s62, s62, 159744\n"
      "v_mov_b32 v50, s62\n"
      "s_waitcnt lgkmcnt(0)\n"
      "s_barrier\n"
      "ds_read_b64 v[28:29], v50 offset:0\n"
      "ds_read_b64 v[30:31], v50 offset:32\n"
      "ds_read_b64 v[32:33], v50 offset:64\n"
      "ds_read_b64 v[34:35], v50 offset:96\n"
      "ds_read_b32 v24, v42 offset:0\n"
      "ds_read_b32 v25, v42 offset:1024\n"
      "ds_read_b32 v26, v42 offset:2048\n"
      "ds_read_b32 v27, v42 offset:3072\n"
      "s_add_u32 s56, s44, 331776\n"
      "s_addc_u32 s57, s45, 0\n"
      "s_waitcnt lgkmcnt(4)\n"
      "v_max3_f32 v43, v28, v30, v32\n"
      "v_max_f32 v43, v43, v34\n"
      "v_sub_f32 v28, v28, v43\n"
      "v_sub_f32 v30, v30, v43\n"
      "v_sub_f32 v32, v32, v43\n"
      "v_sub_f32 v34, v34, v43\n"
      "v_exp_f32 v28, v28\n"
      "v_exp_f32 v30, v30\n"
      "v_exp_f32 v32, v32\n"
      "v_exp_f32 v34, v34\n"
      "s_waitcnt lgkmcnt(0)\n"
      "v_lshlrev_b32 v44, 16, v24\n"
      "v_and_b32 v45, 0xffff0000, v24\n"
      "v_lshlrev_b32 v46, 16, v25\n"
      "v_and_b32 v47, 0xffff0000, v25\n"
      "v_lshlrev_b32 v48, 16, v26\n"
      "v_and_b32 v49, 0xffff0000, v26\n"
      "v_lshlrev_b32 v50, 16, v27\n"
      "v_and_b32 v51, 0xffff0000, v27\n"
      "v_mul_f32 v38, v28, v29\n"
      "v_pk_mul_f32 v[36:37], v[44:45], v[28:29] op_sel_hi:[1,0]\n"
      "v_fmac_f32 v38, v30, v31\n"
      "v_pk_fma_f32 v[36:37], v[46:47], v[30:31], v[36:37] op_sel_hi:[1,0,1]\n"
      "v_fmac_f32 v38, v32, v33\n"
      "v_pk_fma_f32 v[36:37], v[48:49], v[32:33], v[36:37] op_sel_hi:[1,0,1]\n"
      "v_fmac_f32 v38, v34, v35\n"
      "v_pk_fma_f32 v[36:37], v[50:51], v[34:35], v[36:37] op_sel_hi:[1,0,1]\n"
      "v_rcp_f32 v39, v38\n"
      "v_log_f32 v38, v38\n"
      "v_mul_f32 v36, v39, v36\n"
      "v_mul_f32 v37, v39, v37\n"
      "v_add_f32 v38, v38, v43\n"
      "v_cvt_pk_bf16_f32 v36, v36, v37\n"
      "v_min_u32 v38, -2, v38\n"
      "v_min_u32 v36, -2, v36\n"
      "global_store_dword v40, v36, s[56:57] sc1\n"
      "s_mov_b32 exec_hi, 0\n"
      "global_store_dword v40, v38, s[56:57] offset:256 sc1\n"
      "s_mov_b64 exec, -1\n"
      "s_waitcnt vmcnt(8)\n"
      "global_load_dwordx4 a[188:191], v0, s[42:43] nt\n"
      "s_add_u32 s42, s42, 110592\n"
      "s_addc_u32 s43, s43, 0\n"
      "s_waitcnt vmcnt(8)\n"
      "global_load_dwordx4 a[192:195], v1, s[40:41] nt\n"
      "s_add_u32 s40, s40, 110592\n"
      "s_addc_u32 s41, s41, 0\n"
      "s_waitcnt vmcnt(8)\n"
      "global_load_dwordx4 a[196:199], v0, s[42:43] nt\n"
      "s_add_u32 s42, s42, 110592\n"
      "s_addc_u32 s43, s43, 0\n"
      "s_waitcnt vmcnt(8)\n"
      "global_load_dwordx4 a[200:203], v1, s[40:41] nt\n"
      "s_add_u32 s40, s40, 110592\n"
      "s_addc_u32 s41, s41, 0\n"
      "s_barrier\n"
      "L_per2_%=:\n"
      "s_add_u32 s56, s87, L_per3_%=-L_begin_%=\n"
      "s_min_u32 s56, s56, L_end_%=-L_begin_%=-1024\n"
      "s_add_u32 s56, s84, s56\n"
      "s_addc_u32 s57, s85, 0\n"
      "s_mov_b32 m0, s86\n"
      "s_nop 0\n"
      "global_load_lds_dword v0, s[56:57]\n"
      "s_waitcnt vmcnt(9)\n"
      "global_load_dwordx4 a[204:207], v0, s[42:43] nt\n"
      "s_add_u32 s42, s42, 110592\n"
      "s_addc_u32 s43, s43, 0\n"
      "s_waitcnt vmcnt(9)\n"
      "global_load_dwordx4 a[208:211], v1, s[40:41] nt\n"
      "s_add_u32 s40, s40, 110592\n"
      "s_addc_u32 s41, s41, 0\n"
      "s_waitcnt vmcnt(9)\n"
      "global_load_dwordx4 a[212:215], v0, s[42:43] nt\n"
      "s_add_u32 s42, s42, 110592\n"
      "s_addc_u32 s43, s43, 0\n"
      "s_waitcnt vmcnt(9)\n"
      "global_load_dwordx4 a[216:219], v1, s[40:41] nt\n"
      "s_add_u32 s40, s40, 110592\n"
      "s_addc_u32 s41, s41, 0\n"
      "s_waitcnt vmcnt(9)\n"
      "global_load_dwordx4 a[220:223], v0, s[42:43] nt\n"
      "s_add_u32 s42, s42, 110592\n"
      "s_addc_u32 s43, s43, 0\n"
      "s_waitcnt vmcnt(9)\n"
      "global_load_dwordx4 a[224:227], v1, s[40:41] nt\n"
      "s_add_u32 s40, s40, 110592\n"
      "s_addc_u32 s41, s41, 0\n"
      "s_add_u32 s56, s48, 16384\n"
      "s_addc_u32 s57, s49, 0\n"
      "s_mov_b64 exec, 0xffff\n"
      "global_load_dwordx4 v[8:11], v0, s[56:57] sc1\n"
      "s_mov_b64 exec, -1\n"
      "s_waitcnt vmcnt(10)\n"
      "global_load_dwordx4 a[228:231], v0, s[42:43] nt\n"
      "s_add_u32 s42, s42, 110592\n"
      "s_addc_u32 s43, s43, 0\n"
      "s_waitcnt vmcnt(10)\n"
      "global_load_dwordx4 a[232:235], v1, s[40:41] nt\n"
      "s_add_u32 s40, s40, 110592\n"
      "s_addc_u32 s41, s41, 0\n"
      "s_waitcnt vmcnt(10)\n"
      "global_load_dwordx4 a[236:239], v0, s[42:43] nt\n"
      "s_add_u32 s42, s42, 110592\n"
      "s_addc_u32 s43, s43, 0\n"
      "s_waitcnt vmcnt(9)\n"
      "global_load_dwordx4 a[240:243], v1, s[40:41] nt\n"
      "s_add_u32 s40, s40, 110592\n"
      "s_addc_u32 s41, s41, 0\n"
      "s_waitcnt vmcnt(9)\n"
      "global_load_dwordx4 a[244:247], v0, s[42:43] nt\n"
      "s_add_u32 s42, s42, 110592\n"
      "s_addc_u32 s43, s43, 0\n"
      "s_waitcnt vmcnt(9)\n"
      "global_load_dwordx4 a[248:251], v1, s[40:41] nt\n"
      "s_add_u32 s40, s40, 110592\n"
      "s_addc_u32 s41, s41, 0\n"
      "s_waitcnt vmcnt(9)\n"
      "global_load_dwordx4 a[252:255], v0, s[42:43] nt\n"
      "s_add_u32 s42, s42, 110592\n"
      "s_addc_u32 s43, s43, 0\n"
      "s_waitcnt vmcnt(9)\n"
      "global_load_dwordx4 v[56:59], v1, s[40:41] nt\n"
      "s_add_u32 s40, s40, 110592\n"
      "s_addc_u32 s41, s41, 0\n"
      "s_waitcnt vmcnt(9)\n"
      "global_load_dwordx4 v[60:63], v0, s[42:43] nt\n"
      "s_add_u32 s42, s42, 110592\n"
      "s_addc_u32 s43, s43, 0\n"
      "s_waitcnt vmcnt(9)\n"
      "s_mov_b64 exec, 0xffff\n"
      "s_mov_b32 s53, 0\n"
      "L_qc7_%=:\n"
      "v_max3_u32 v49, v8, v9, v10\n"
      "v_max_u32 v49, v49, v11\n"
      "v_cmp_eq_u32 vcc, -1, v49\n"
      "s_nop 0\n"
      "s_cbranch_vccz L_qo8_%=\n"
      "s_cmp_lg_u32 s54, 0\n"
      "s_cbranch_scc1 L_qo8_%=\n"
      "s_add_u32 s56, s48, 16384\n"
      "s_addc_u32 s57, s49, 0\n"
      "global_load_dwordx4 v[8:11], v0, s[56:57] sc1\n"
      "s_add_u32 s53, s53, 1\n"
      "s_waitcnt vmcnt(0)\n"
      "s_cmp_lt_u32 s53, 30000\n"
      "s_cbranch_scc1 L_qc7_%=\n"
      "s_mov_b32 s54, 102\n"
      "v_mov_b32 v49, 102\n"
      "v_mov_b32 v50, 256\n"
      "global_store_dword v50, v49, s[58:59] sc1\n"
      "L_qo8_%=:\n"
      "s_lshl_b32 s62, s60, 8\n"
      "s_add_u32 s62, s62, 159872\n"
      "v_add_u32 v49, s62, v0\n"
      "ds_write_b128 v49, v[8:11]\n"
      "s_mov_b64 exec, -1\n"
      "s_waitcnt lgkmcnt(0)\n"
      "s_barrier\n"
      "ds_read_b128 v[8:11], v2 offset:0\n"
      "s_waitcnt lgkmcnt(0)\n"
      "v_mfma_f32_16x16x32_bf16 v[24:27], a[152:155], v[8:11], 0\n"
      "v_mfma_f32_16x16x32_bf16 v[28:31], a[160:163], v[8:11], 0\n"
      "s_nop 6\n"
      "v_mov_b32_dpp v34, v24 quad_perm:[0,0,0,0] row_mask:0xf bank_mask:0xf\n"
      "v_mov_b32_dpp v33, v26 quad_perm:[2,2,2,2] row_mask:0xf bank_mask:0xf\n"
      "s_nop 0\n"
      "v_add_f32_dpp v34, v25, v34 quad_perm:[1,1,1,1] row_mask:0xf bank_mask:0xf\n"
      "v_add_f32_dpp v33, v27, v33 quad_perm:[3,3,3,3] row_mask:0xf bank_mask:0xf\n"
      "v_add_f32 v34, v34, v33\n"
      "v_mov_b32_dpp v32, v28 quad_perm:[0,0,0,0] row_mask:0xf bank_mask:0xf\n"
      "v_mov_b32_dpp v33, v30 quad_perm:[2,2,2,2] row_mask:0xf bank_mask:0xf\n"
      "s_nop 0\n"
      "v_add_f32_dpp v32, v29, v32 quad_perm:[1,1,1,1] row_mask:0xf bank_mask:0xf\n"
      "v_add_f32_dpp v33, v31, v33 quad_perm:[3,3,3,3] row_mask:0xf bank_mask:0xf\n"
      "v_add_f32 v32, v32, v33\n"
      "v_max_f32 v35, v34, v32\n"
      "v_mov_b32 v33, v35\n"
      "s_nop 1\n"
      "v_permlane32_swap_b32 v33, v35\n"
      "v_max_f32 v35, v33, v35\n"
      "v_mov_b32 v33, v35\n"
      "s_nop 1\n"
      "v_permlane16_swap_b32 v33, v35\n"
      "v_max_f32 v6, v33, v35\n"
      "v_sub_f32 v34, v34, v6\n"
      "v_sub_f32 v32, v32, v6\n"
      "v_mov_b32 v35, 0\n"
      "v_exp_f32 v34, v34\n"
      "s_nop 0\n"
      "v_mov_b32 v7, v34\n"
      "v_cvt_pk_bf16_f32 v34, v34, v34\n"
      "v_and_b32 v36, v4, v34\n"
      "v_and_b32 v37, v5, v34\n"
      "v_mfma_f32_16x16x32_bf16 v[24:27], a[168:171], v[8:11], 0\n"
      "s_nop 0\n"
      "v_mfma_f32_16x16x16_bf16 v[16:19], a[156:157], v[36:37], 0\n"
      "v_mfma_f32_16x16x16_bf16 v[20:23], a[158:159], v[36:37], 0\n"
      "v_exp_f32 v34, v32\n"
      "v_max_f32 v35, v35, v32\n"
      "v_add_f32 v7, v7, v34\n"
      "v_cvt_pk_bf16_f32 v34, v34, v34\n"
      "v_and_b32 v38, v4, v34\n"
      "v_and_b32 v39, v5, v34\n"
      "v_mfma_f32_16x16x32_bf16 v[28:31], a[176:179], v[8:11], 0\n"
      "v_sub_f32_dpp v32, v24, v6 quad_perm:[0,0,0,0] row_mask:0xf bank_mask:0xf\n"
      "v_mov_b32_dpp v33, v26 quad_perm:[2,2,2,2] row_mask:0xf bank_mask:0xf\n"
      "v_mfma_f32_16x16x16_bf16 v[16:19], a[164:165], v[38:39], v[16:19]\n"
      "v_add_f32_dpp v32, v25, v32 quad_perm:[1,1,1,1] row_mask:0xf bank_mask:0xf\n"
      "v_add_f32_dpp v33, v27, v33 quad_perm:[3,3,3,3] row_mask:0xf bank_mask:0xf\n"
      "v_mfma_f32_16x16x16_bf16 v[20:23], a[166:167], v[38:39], v[20:23]\n"
      "v_add_f32 v32, v32, v33\n"
      "v_exp_f32 v34, v32\n"
      "v_max_f32 v35, v35, v32\n"
      "v_add_f32 v7, v7, v34\n"
      "v_cvt_pk_bf16_f32 v34, v34, v34\n"
      "v_and_b32 v36, v4, v34\n"
      "v_and_b32 v37, v5, v34\n"
      "v_mfma_f32_16x16x32_bf16 v[24:27], a[184:187], v[8:11], 0\n"
      "v_sub_f32_dpp v32, v28, v6 quad_perm:[0,0,0,0] row_mask:0xf bank_mask:0xf\n"
      "v_mov_b32_dpp v33, v30 quad_perm:[2,2,2,2] row_mask:0xf bank_mask:0xf\n"
      "v_mfma_f32_16x16x16_bf16 v[16:19], a[172:173], v[36:37], v[16:19]\n"
      "v_add_f32_dpp v32, v29, v32 quad_perm:[1,1,1,1] row_mask:0xf bank_mask:0xf\n"
      "v_add_f32_dpp v33, v31, v33 quad_perm:[3,3,3,3] row_mask:0xf bank_mask:0xf\n"
      "v_mfma_f32_16x16x16_bf16 v[20:23], a[174:175], v[36:37], v[20:23]\n"
      "v_add_f32 v32, v32, v33\n"
      "v_exp_f32 v34, v32\n"
      "v_max_f32 v35, v35, v32\n"
      "v_add_f32 v7, v7, v34\n"
      "v_cvt_pk_bf16_f32 v34, v34, v34\n"
      "v_and_b32 v38, v4, v34\n"
      "v_and_b32 v39, v5, v34\n"
      "s_waitcnt vmcnt(8)\n"
      "global_load_dwordx4 v[64:67], v1, s[40:41] nt\n"
      "s_add_u32 s40, s40, 110592\n"
      "s_addc_u32 s41, s41, 0\n"
      "v_mfma_f32_16x16x32_bf16 v[28:31], a[192:195], v[8:11], 0\n"
      "v_sub_f32_dpp v32, v24, v6 quad_perm:[0,0,0,0] row_mask:0xf bank_mask:0xf\n"
      "v_mov_b32_dpp v33, v26 quad_perm:[2,2,2,2] row_mask:0xf bank_mask:0xf\n"
      "v_mfma_f32_16x16x16_bf16 v[16:19], a[180:181], v[38:39], v[16:19]\n"
      "v_add_f32_dpp v32, v25, v32 quad_perm:[1,1,1,1] row_mask:0xf bank_mask:0xf\n"
      "v_add_f32_dpp v33, v27, v33 quad_perm:[3,3,3,3] row_mask:0xf bank_mask:0xf\n"
      "v_mfma_f32_16x16x16_bf16 v[20:23], a[182:183], v[38:39], v[20:23]\n"
      "v_add_f32 v32, v32, v33\n"
      "v_exp_f32 v34, v32\n"
      "v_max_f32 v35, v35, v32\n"
      "v_add_f32 v7, v7, v34\n"
      "v_cvt_pk_bf16_f32 v34, v34, v34\n"
      "v_and_b32 v36, v4, v34\n"
      "v_and_b32 v37, v5, v34\n"
      "v_mfma_f32_16x16x32_bf16 v[24:27], a[200:203], v[8:11], 0\n"
      "v_sub_f32_dpp v32, v28, v6 quad_perm:[0,0,0,0] row_mask:0xf bank_mask:0xf\n"
      "v_mov_b32_dpp v33, v30 quad_perm:[2,2,2,2] row_mask:0xf bank_mask:0xf\n"
      "v_mfma_f32_16x16x16_bf16 v[16:19], a[188:189], v[36:37], v[16:19]\n"
      "v_add_f32_dpp v32, v29, v32 quad_perm:[1,1,1,1] row_mask:0xf bank_mask:0xf\n"
      "v_add_f32_dpp v33, v31, v33 quad_perm:[3,3,3,3] row_mask:0xf bank_mask:0xf\n"
      "v_mfma_f32_16x16x16_bf16 v[20:23], a[190:191], v[36:37], v[20:23]\n"
      "v_add_f32 v32, v32, v33\n"
      "v_exp_f32 v34, v32\n"
      "v_max_f32 v35, v35, v32\n"
      "v_add_f32 v7, v7, v34\n"
      "v_cvt_pk_bf16_f32 v34, v34, v34\n"
      "v_and_b32 v38, v4, v34\n"
      "v_and_b32 v39, v5, v34\n"
      "s_waitcnt vmcnt(8)\n"
      "global_load_dwordx4 v[68:71], v0, s[42:43] nt\n"
      "s_add_u32 s42, s42, 110592\n"
      "s_addc_u32 s43, s43, 0\n"
      "v_mfma_f32_16x16x32_bf16 v[28:31], a[208:211], v[8:11], 0\n"
      "v_sub_f32_dpp v32, v24, v6 quad_perm:[0,0,0,0] row_mask:0xf bank_mask:0xf\n"
      "v_mov_b32_dpp v33, v26 quad_perm:[2,2,2,2] row_mask:0xf bank_mask:0xf\n"
      "v_mfma_f32_16x16x16_bf16 v[16:19], a[196:197], v[38:39], v[16:19]\n"
      "v_add_f32_dpp v32, v25, v32 quad_perm:[1,1,1,1] row_mask:0xf bank_mask:0xf\n"
      "v_add_f32_dpp v33, v27, v33 quad_perm:[3,3,3,3] row_mask:0xf bank_mask:0xf\n"
      "v_mfma_f32_16x16x16_bf16 v[20:23], a[198:199], v[38:39], v[20:23]\n"
      "v_add_f32 v32, v32, v33\n"
      "v_exp_f32 v34, v32\n"
      "v_max_f32 v35, v35, v32\n"
      "v_add_f32 v7, v7, v34\n"
      "v_cvt_pk_bf16_f32 v34, v34, v34\n"
      "v_and_b32 v36, v4, v34\n"
      "v_and_b32 v37, v5, v34\n"
      "v_mfma_f32_16x16x32_bf16 v[24:27], a[216:219], v[8:11], 0\n"
      "v_sub_f32_dpp v32, v28, v6 quad_perm:[0,0,0,0] row_mask:0xf bank_mask:0xf\n"
      "v_mov_b32_dpp v33, v30 quad_perm:[2,2,2,2] row_mask:0xf bank_mask:0xf\n"
      "v_mfma_f32_16x16x16_bf16 v[16:19], a[204:205], v[36:37], v[16:19]\n"
      "v_add_f32_dpp v32, v29, v32 quad_perm:[1,1,1,1] row_mask:0xf bank_mask:0xf\n"
      "v_add_f32_dpp v33, v31, v33 quad_perm:[3,3,3,3] row_mask:0xf bank_mask:0xf\n"
      "v_mfma_f32_16x16x16_bf16 v[20:23], a[206:207], v[36:37], v[20:23]\n"
      "v_add_f32 v32, v32, v33\n"
      "v_exp_f32 v34, v32\n"
      "v_max_f32 v35, v35, v32\n"
      "v_add_f32 v7, v7, v34\n"
      "v_cvt_pk_bf16_f32 v34, v34, v34\n"
      "v_and_b32 v38, v4, v34\n"
      "v_and_b32 v39, v5, v34\n"
      "v_mfma_f32_16x16x32_bf16 v[28:31], a[224:227], v[8:11], 0\n"
      "v_sub_f32_dpp v32, v24, v6 quad_perm:[0,0,0,0] row_mask:0xf bank_mask:0xf\n"
      "v_mov_b32_dpp v33, v26 quad_perm:[2,2,2,2] row_mask:0xf bank_mask:0xf\n"
      "v_mfma_f32_16x16x16_bf16 v[16:19], a[212:213], v[38:39], v[16:19]\n"
      "v_add_f32_dpp v32, v25, v32 quad_perm:[1,1,1,1] row_mask:0xf bank_mask:0xf\n"
      "v_add_f32_dpp v33, v27, v33 quad_perm:[3,3,3,3] row_mask:0xf bank_mask:0xf\n"
      "v_mfma_f32_16x16x16_bf16 v[20:23], a[214:215], v[38:39], v[20:23]\n"
      "v_add_f32 v32, v32, v33\n"
      "v_exp_f32 v34, v32\n"
      "v_max_f32 v35, v35, v32\n"
      "v_add_f32 v7, v7, v34\n"
      "v_cvt_pk_bf16_f32 v34, v34, v34\n"
      "v_and_b32 v36, v4, v34\n"
      "v_and_b32 v37, v5, v34\n"
      "s_waitcnt vmcnt(8)\n"
      "global_load_dwordx4 v[72:75], v1, s[40:41] nt\n"
      "s_add_u32 s40, s40, 110592\n"
      "s_addc_u32 s41, s41, 0\n"
      "v_mfma_f32_16x16x32_bf16 v[24:27], a[232:235], v[8:11], 0\n"
      "v_sub_f32_dpp v32, v28, v6 quad_perm:[0,0,0,0] row_mask:0xf bank_mask:0xf\n"
      "v_mov_b32_dpp v33, v30 quad_perm:[2,2,2,2] row_mask:0xf bank_mask:0xf\n"
      "v_mfma_f32_16x16x16_bf16 v[16:19], a[220:221], v[36:37], v[16:19]\n"
      "v_add_f32_dpp v32, v29, v32 quad_perm:[1,1,1,1] row_mask:0xf bank_mask:0xf\n"
      "v_add_f32_dpp v33, v31, v33 quad_perm:[3,3,3,3] row_mask:0xf bank_mask:0xf\n"
      "v_mfma_f32_16x16x16_bf16 v[20:23], a[222:223], v[36:37], v[20:23]\n"
      "v_add_f32 v32, v32, v33\n"
      "v_exp_f32 v34, v32\n"
      "v_max_f32 v35, v35, v32\n"
      "v_add_f32 v7, v7, v34\n"
      "v_cvt_pk_bf16_f32 v34, v34, v34\n"
      "v_and_b32 v38, v4, v34\n"
      "v_and_b32 v39, v5, v34\n"
      "s_waitcnt vmcnt(8)\n"
      "v_mfma_f32_16x16x32_bf16 v[28:31], a[240:243], v[8:11], 0\n"
      "v_sub_f32_dpp v32, v24, v6 quad_perm:[0,0,0,0] row_mask:0xf bank_mask:0xf\n"
      "v_mov_b32_dpp v33, v26 quad_perm:[2,2,2,2] row_mask:0xf bank_mask:0xf\n"
      "v_mfma_f32_16x16x16_bf16 v[16:19], a[228:229], v[38:39], v[16:19]\n"
      "v_add_f32_dpp v32, v25, v32 quad_perm:[1,1,1,1] row_mask:0xf bank_mask:0xf\n"
      "v_add_f32_dpp v33, v27, v33 quad_perm:[3,3,3,3] row_mask:0xf bank_mask:0xf\n"
      "v_mfma_f32_16x16x16_bf16 v[20:23], a[230:231], v[38:39], v[20:23]\n"
      "v_add_f32 v32, v32, v33\n"
      "v_exp_f32 v34, v32\n"
      "v_max_f32 v35, v35, v32\n"
      "v_add_f32 v7, v7, v34\n"
      "v_cvt_pk_bf16_f32 v34, v34, v34\n"
      "v_and_b32 v36, v4, v34\n"
      "v_and_b32 v37, v5, v34\n"
      "global_load_dwordx4 v[76:79], v0, s[42:43] nt\n"
      "s_add_u32 s42, s42, 110592\n"
      "s_addc_u32 s43, s43, 0\n"
      "s_waitcnt vmcnt(8)\n"
      "global_load_dwordx4 v[80:83], v1, s[40:41] nt\n"
      "s_add_u32 s40, s40, 110592\n"
      "s_addc_u32 s41, s41, 0\n"
      "s_waitcnt vmcnt(8)\n"
      "v_mfma_f32_16x16x32_bf16 v[24:27], a[248:251], v[8:11], 0\n"
      "v_sub_f32_dpp v32, v28, v6 quad_perm:[0,0,0,0] row_mask:0xf bank_mask:0xf\n"
      "v_mov_b32_dpp v33, v30 quad_perm:[2,2,2,2] row_mask:0xf bank_mask:0xf\n"
      "v_mfma_f32_16x16x16_bf16 v[16:19], a[236:237], v[36:37], v[16:19]\n"
      "v_add_f32_dpp v32, v29, v32 quad_perm:[1,1,1,1] row_mask:0xf bank_mask:0xf\n"
      "v_add_f32_dpp v33, v31, v33 quad_perm:[3,3,3,3] row_mask:0xf bank_mask:0xf\n"
      "v_mfma_f32_16x16x16_bf16 v[20:23], a[238:239], v[36:37], v[20:23]\n"
      "v_add_f32 v32, v32, v33\n"
      "v_exp_f32 v34, v32\n"
      "v_max_f32 v35, v35, v32\n"
      "v_add_f32 v7, v7, v34\n"
      "v_cvt_pk_bf16_f32 v34, v34, v34\n"
      "v_and_b32 v38, v4, v34\n"
      "v_and_b32 v39, v5, v34\n"
      "global_load_dwordx4 v[84:87], v0, s[42:43] nt\n"
      "s_add_u32 s42, s42, 110592\n"
      "s_addc_u32 s43, s43, 0\n"
      "s_waitcnt vmcnt(8)\n"
      "global_load_dwordx4 v[88:91], v1, s[40:41] nt\n"
      "s_add_u32 s40, s40, s51\n"
      "s_addc_u32 s41, s41, 0\n"
      "s_waitcnt vmcnt(8)\n"
      "v_mfma_f32_16x16x32_bf16 v[28:31], v[56:59], v[8:11], 0\n"
      "v_sub_f32_dpp v32, v24, v6 quad_perm:[0,0,0,0] row_mask:0xf bank_mask:0xf\n"
      "v_mov_b32_dpp v33, v26 quad_perm:[2,2,2,2] row_mask:0xf bank_mask:0xf\n"
      "v_mfma_f32_16x16x16_bf16 v[16:19], a[244:245], v[38:39], v[16:19]\n"
      "v_add_f32_dpp v32, v25, v32 quad_perm:[1,1,1,1] row_mask:0xf bank_mask:0xf\n"
      "v_add_f32_dpp v33, v27, v33 quad_perm:[3,3,3,3] row_mask:0xf bank_mask:0xf\n"
      "v_mfma_f32_16x16x16_bf16 v[20:23], a[246:247], v[38:39], v[20:23]\n"
      "v_add_f32 v32, v32, v33\n"
      "v_exp_f32 v34, v32\n"
      "v_max_f32 v35, v35, v32\n"
      "v_add_f32 v7, v7, v34\n"
      "v_cvt_pk_bf16_f32 v34, v34, v34\n"
      "v_and_b32 v36, v4, v34\n"
      "v_and_b32 v37, v5, v34\n"
      "global_load_dwordx4 v[92:95], v0, s[42:43] nt\n"
      "s_add_u32 s42, s42, s51\n"
      "s_addc_u32 s43, s43, 0\n"
      "s_waitcnt vmcnt(8)\n"
      "global_load_dwordx4 v[96:99], v1, s[40:41] nt\n"
      "s_add_u32 s40, s40, s52\n"
      "s_addc_u32 s41, s41, 0\n"
      "s_waitcnt vmcnt(8)\n"
      "v_mfma_f32_16x16x32_bf16 v[24:27], v[64:67], v[8:11], 0\n"
      "v_sub_f32_dpp v32, v28, v6 quad_perm:[0,0,0,0] row_mask:0xf bank_mask:0xf\n"
      "v_mov_b32_dpp v33, v30 quad_perm:[2,2,2,2] row_mask:0xf bank_mask:0xf\n"
      "v_mfma_f32_16x16x16_bf16 v[16:19], a[252:253], v[36:37], v[16:19]\n"
      "v_add_f32_dpp v32, v29, v32 quad_perm:[1,1,1,1] row_mask:0xf bank_mask:0xf\n"
      "v_add_f32_dpp v33, v31, v33 quad_perm:[3,3,3,3] row_mask:0xf bank_mask:0xf\n"
      "v_mfma_f32_16x16x16_bf16 v[20:23], a[254:255], v[36:37], v[20:23]\n"
      "v_add_f32 v32, v32, v33\n"
      "v_exp_f32 v34, v32\n"
      "v_max_f32 v35, v35, v32\n"
      "v_add_f32 v7, v7, v34\n"
      "v_cvt_pk_bf16_f32 v34, v34, v34\n"
      "v_and_b32 v38, v4, v34\n"
      "v_and_b32 v39, v5, v34\n"
      "global_load_dwordx4 v[100:103], v0, s[42:43] nt\n"
      "s_add_u32 s42, s42, s52\n"
      "s_addc_u32 s43, s43, 0\n"
      "s_waitcnt vmcnt(8)\n"
      "global_load_dwordx4 v[104:107], v1, s[40:41] nt\n"
      "s_add_u32 s40, s40, 110592\n"
      "s_addc_u32 s41, s41, 0\n"
      "s_waitcnt vmcnt(8)\n"
      "v_mfma_f32_16x16x32_bf16 v[28:31], v[72:75], v[8:11], 0\n"
      "v_sub_f32_dpp v32, v24, v6 quad_perm:[0,0,0,0] row_mask:0xf bank_mask:0xf\n"
      "v_mov_b32_dpp v33, v26 quad_perm:[2,2,2,2] row_mask:0xf bank_mask:0xf\n"
      "v_mfma_f32_16x16x16_bf16 v[16:19], v[60:61], v[38:39], v[16:19]\n"
      "v_add_f32_dpp v32, v25, v32 quad_perm:[1,1,1,1] row_mask:0xf bank_mask:0xf\n"
      "v_add_f32_dpp v33, v27, v33 quad_perm:[3,3,3,3] row_mask:0xf bank_mask:0xf\n"
      "v_mfma_f32_16x16x16_bf16 v[20:23], v[62:63], v[38:39], v[20:23]\n"
      "v_add_f32 v32, v32, v33\n"
      "v_exp_f32 v34, v32\n"
      "v_max_f32 v35, v35, v32\n"
      "v_add_f32 v7, v7, v34\n"
      "v_cvt_pk_bf16_f32 v34, v34, v34\n"
      "v_and_b32 v36, v4, v34\n"
      "v_and_b32 v37, v5, v34\n"
      "global_load_dwordx4 v[108:111], v0, s[42:43] nt\n"
      "s_add_u32 s42, s42, 110592\n"
      "s_addc_u32 s43, s43, 0\n"
      "s_waitcnt vmcnt(8)\n"
      "global_load_dwordx4 v[112:115], v1, s[40:41] nt\n"
      "s_add_u32 s40, s40, 110592\n"
      "s_addc_u32 s41, s41, 0\n"
      "s_waitcnt vmcnt(8)\n"
      "v_mfma_f32_16x16x32_bf16 v[24:27], v[80:83], v[8:11], 0\n"
      "v_sub_f32_dpp v32, v28, v6 quad_perm:[0,0,0,0] row_mask:0xf bank_mask:0xf\n"
      "v_mov_b32_dpp v33, v30 quad_perm:[2,2,2,2] row_mask:0xf bank_mask:0xf\n"
      "v_mfma_f32_16x16x16_bf16 v[16:19], v[68:69], v[36:37], v[16:19]\n"
      "v_add_f32_dpp v32, v29, v32 quad_perm:[1,1,1,1] row_mask:0xf bank_mask:0xf\n"
      "v_add_f32_dpp v33, v31, v33 quad_perm:[3,3,3,3] row_mask:0xf bank_mask:0xf\n"
      "v_mfma_f32_16x16x16_bf16 v[20:23], v[70:71], v[36:37], v[20:23]\n"
      "v_add_f32 v32, v32, v33\n"
      "v_exp_f32 v34, v32\n"
      "v_max_f32 v35, v35, v32\n"
      "v_add_f32 v7, v7, v34\n"
      "v_cvt_pk_bf16_f32 v34, v34, v34\n"
      "v_and_b32 v38, v4, v34\n"
      "v_and_b32 v39, v5, v34\n"
      "global_load_dwordx4 v[116:119], v0, s[42:43] nt\n"
      "s_add_u32 s42, s42, 110592\n"
      "s_addc_u32 s43, s43, 0\n"
      "s_waitcnt vmcnt(8)\n"
      "global_load_dwordx4 v[120:123], v1, s[40:41] nt\n"
      "s_add_u32 s40, s40, 110592\n"
      "s_addc_u32 s41, s41, 0\n"
      "s_waitcnt vmcnt(8)\n"
      "v_mfma_f32_16x16x32_bf16 v[28:31], v[88:91], v[8:11], 0\n"
      "v_sub_f32_dpp v32, v24, v6 quad_perm:[0,0,0,0] row_mask:0xf bank_mask:0xf\n"
      "v_mov_b32_dpp v33, v26 quad_perm:[2,2,2,2] row_mask:0xf bank_mask:0xf\n"
      "v_mfma_f32_16x16x16_bf16 v[16:19], v[76:77], v[38:39], v[16:19]\n"
      "v_add_f32_dpp v32, v25, v32 quad_perm:[1,1,1,1] row_mask:0xf bank_mask:0xf\n"
      "v_add_f32_dpp v33, v27, v33 quad_perm:[3,3,3,3] row_mask:0xf bank_mask:0xf\n"
      "v_mfma_f32_16x16x16_bf16 v[20:23], v[78:79], v[38:39], v[20:23]\n"
      "v_add_f32 v32, v32, v33\n"
      "v_exp_f32 v34, v32\n"
      "v_max_f32 v35, v35, v32\n"
      "v_add_f32 v7, v7, v34\n"
      "v_cvt_pk_bf16_f32 v34, v34, v34\n"
      "v_and_b32 v36, v4, v34\n"
      "v_and_b32 v37, v5, v34\n"
      "global_load_dwordx4 v[124:127], v0, s[42:43] nt\n"
      "s_add_u32 s42, s42, 110592\n"
      "s_addc_u32 s43, s43, 0\n"
      "s_waitcnt vmcnt(8)\n"
      "global_load_dwordx4 v[128:131], v1, s[40:41] nt\n"
      "s_add_u32 s40, s40, 110592\n"
      "s_addc_u32 s41, s41, 0\n"
      "s_waitcnt vmcnt(8)\n"
      "v_mfma_f32_16x16x32_bf16 v[24:27], v[96:99], v[8:11], 0\n"
      "v_sub_f32_dpp v32, v28, v6 quad_perm:[0,0,0,0] row_mask:0xf bank_mask:0xf\n"
      "v_mov_b32_dpp v33, v30 quad_perm:[2,2,2,2] row_mask:0xf bank_mask:0xf\n"
      "v_mfma_f32_16x16x16_bf16 v[16:19], v[84:85], v[36:37], v[16:19]\n"
      "v_add_f32_dpp v32, v29, v32 quad_perm:[1,1,1,1] row_mask:0xf bank_mask:0xf\n"
      "v_add_f32_dpp v33, v31, v33 quad_perm:[3,3,3,3] row_mask:0xf bank_mask:0xf\n"
      "v_mfma_f32_16x16x16_bf16 v[20:23], v[86:87], v[36:37], v[20:23]\n"
      "v_add_f32 v32, v32, v33\n"
      "v_exp_f32 v34, v32\n"
      "v_max_f32 v35, v35, v32\n"
      "v_add_f32 v7, v7, v34\n"
      "v_cvt_pk_bf16_f32 v34, v34, v34\n"
      "v_and_b32 v38, v4, v34\n"
      "v_and_b32 v39, v5, v34\n"
      "global_load_dwordx4 v[132:135], v0, s[42:43] nt\n"
      "s_add_u32 s42, s42, 110592\n"
      "s_addc_u32 s43, s43, 0\n"
      "v_sub_f32_dpp v32, v24, v6 quad_perm:[0,0,0,0] row_mask:0xf bank_mask:0xf\n"
      "v_mov_b32_dpp v33, v26 quad_perm:[2,2,2,2] row_mask:0xf bank_mask:0xf\n"
      "v_mfma_f32_16x16x16_bf16 v[16:19], v[92:93], v[38:39], v[16:19]\n"
      "v_add_f32_dpp v32, v25, v32 quad_perm:[1,1,1,1] row_mask:0xf bank_mask:0xf\n"
      "v_add_f32_dpp v33, v27, v33 quad_perm:[3,3,3,3] row_mask:0xf bank_mask:0xf\n"
      "v_mfma_f32_16x16x16_bf16 v[20:23], v[94:95], v[38:39], v[20:23]\n"
      "v_add_f32 v32, v32, v33\n"
      "v_add_f32 v32, s69, v32\n"
      "v_exp_f32 v34, v32\n"
      "v_max_f32 v35, v35, v32\n"
      "v_add_f32 v7, v7, v34\n"
      "v_cvt_pk_bf16_f32 v34, v34, v34\n"
      "v_and_b32 v36, v4, v34\n"
      "v_and_b32 v37, v5, v34\n"
      "s_waitcnt vmcnt(8)\n"
      "s_nop 0\n"
      "v_mfma_f32_16x16x16_bf16 v[16:19], v[100:101], v[36:37], v[16:19]\n"
      "v_mfma_f32_16x16x16_bf16 v[20:23], v[102:103], v[36:37], v[20:23]\n"
      "global_load_dwordx4 v[136:139], v1, s[40:41] nt\n"
      "s_add_u32 s40, s40, 110592\n"
      "s_addc_u32 s41, s41, 0\n"
      "v_cmp_lt_f32 vcc, 0x42800000, v35\n"
      "s_nop 0\n"
      "s_cbranch_vccnz L_fb2_%=\n"
      "L_ret2_%=:\n"
      "v_mov_b32 v49, v7\n"
      "v_lshlrev_b32 v41, 2, v0\n"
      "v_and_b32 v41, 0x300, v41\n"
      "v_permlane32_swap_b32 v49, v7\n"
      "v_lshrrev_b32 v42, 4, v0\n"
      "v_add_f32 v7, v49, v7\n"
      "v_and_b32 v42, 0x30, v42\n"
      "v_mov_b32 v49, v7\n"
      "v_add_u32 v41, v41, v42\n"
      "v_lshrrev_b32 v40, 2, v0\n"
      "v_permlane16_swap_b32 v49, v7\n"
      "v_and_b32 v42, 12, v40\n"
      "v_add_f32 v7, v49, v7\n"
      "v_add_u32 v41, v41, v42\n"
      "s_lshl_b32 s62, s60, 10\n"
      "s_add_u32 s62, s62, 155648\n"
      "v_add_u32 v41, s62, v41\n"
      "v_lshrrev_b32 v43, 3, v0\n"
      "v_and_b32 v43, 24, v43\n"
      "s_lshl_b32 s62, s60, 5\n"
      "s_add_u32 s62, s62, 159744\n"
      "v_add_u32 v43, s62, v43\n"
      "v_cvt_pk_bf16_f32 v36, v16, v17\n"
      "v_cvt_pk_bf16_f32 v37, v18, v19\n"
      "v_cvt_pk_bf16_f32 v38, v20, v21\n"
      "v_cvt_pk_bf16_f32 v39, v22, v23\n"
      "s_mov_b64 exec, 0x1111\n"
      "ds_write_b64 v43, v[6:7]\n"
      "s_mov_b64 exec, -1\n"
      "ds_write_b32 v41, v36 offset:0\n"
      "ds_write_b32 v41, v37 offset:64\n"
      "ds_write_b32 v41, v38 offset:128\n"
      "ds_write_b32 v41, v39 offset:192\n"
      "s_lshl_b32 s62, s60, 8\n"
      "s_add_u32 s62, s62, 155648\n"
      "v_add_u32 v42, s62, v40\n"
      "s_lshl_b32 s62, s60, 3\n"
      "s_add_u32 s62, s62, 159744\n"
      "v_mov_b32 v50, s62\n"
      "s_waitcnt lgkmcnt(0)\n"
      "s_barrier\n"
      "ds_read_b64 v[28:29], v50 offset:0\n"
      "ds_read_b64 v[30:31], v50 offset:32\n"
      "ds_read_b64 v[32:33], v50 offset:64\n"
      "ds_read_b64 v[34:35], v50 offset:96\n"
      "ds_read_b32 v24, v42 offset:0\n"
      "ds_read_b32 v25, v42 offset:1024\n"
      "ds_read_b32 v26, v42 offset:2048\n"
      "ds_read_b32 v27, v42 offset:3072\n"
      "s_add_u32 s56, s44, 663552\n"
      "s_addc_u32 s57, s45, 0\n"
      "s_waitcnt lgkmcnt(4)\n"
      "v_max3_f32 v43, v28, v30, v32\n"
      "v_max_f32 v43, v43, v34\n"
      "v_sub_f32 v28, v28, v43\n"
      "v_sub_f32 v30, v30, v43\n"
      "v_sub_f32 v32, v32, v43\n"
      "v_sub_f32 v34, v34, v43\n"
      "v_exp_f32 v28, v28\n"
      "v_exp_f32 v30, v30\n"
      "v_exp_f32 v32, v32\n"
      "v_exp_f32 v34, v34\n"
      "s_waitcnt lgkmcnt(0)\n"
      "v_lshlrev_b32 v44, 16, v24\n"
      "v_and_b32 v45, 0xffff0000, v24\n"
      "v_lshlrev_b32 v46, 16, v25\n"
      "v_and_b32 v47, 0xffff0000, v25\n"
      "v_lshlrev_b32 v48, 16, v26\n"
      "v_and_b32 v49, 0xffff0000, v26\n"
      "v_lshlrev_b32 v50, 16, v27\n"
      "v_and_b32 v51, 0xffff0000, v27\n"
      "v_mul_f32 v38, v28, v29\n"
      "v_pk_mul_f32 v[36:37], v[44:45], v[28:29] op_sel_hi:[1,0]\n"
      "v_fmac_f32 v38, v30, v31\n"
      "v_pk_fma_f32 v[36:37], v[46:47], v[30:31], v[36:37] op_sel_hi:[1,0,1]\n"
      "v_fmac_f32 v38, v32, v33\n"
      "v_pk_fma_f32 v[36:37], v[48:49], v[32:33], v[36:37] op_sel_hi:[1,0,1]\n"
      "v_fmac_f32 v38, v34, v35\n"
      "v_pk_fma_f32 v[36:37], v[50:51], v[34:35], v[36:37] op_sel_hi:[1,0,1]\n"
      "v_rcp_f32 v39, v38\n"
      "v_log_f32 v38, v38\n"
      "v_mul_f32 v36, v39, v36\n"
      "v_mul_f32 v37, v39, v37\n"
      "v_add_f32 v38, v38, v43\n"
      "v_cvt_pk_bf16_f32 v36, v36, v37\n"
      "v_min_u32 v38, -2, v38\n"
      "v_min_u32 v36, -2, v36\n"
      "global_store_dword v40, v36, s[56:57] sc1\n"
      "s_mov_b32 exec_hi, 0\n"
      "global_store_dword v40, v38, s[56:57] offset:256 sc1\n"
      "s_mov_b64 exec, -1\n"
      "s_waitcnt vmcnt(8)\n"
      "global_load_dwordx4 v[140:143], v0, s[42:43] nt\n"
      "s_add_u32 s42, s42, 110592\n"
      "s_addc_u32 s43, s43, 0\n"
      "s_waitcnt vmcnt(8)\n"
      "global_load_dwordx4 v[144:147], v1, s[40:41] nt\n"
      "s_add_u32 s40, s40, 110592\n"
      "s_addc_u32 s41, s41, 0\n"
      "s_waitcnt vmcnt(8)\n"
      "global_load_dwordx4 v[148:151], v0, s[42:43] nt\n"
      "s_add_u32 s42, s42, 110592\n"
      "s_addc_u32 s43, s43, 0\n"
      "s_waitcnt vmcnt(8)\n"
      "global_load_dwordx4 v[152:155], v1, s[40:41] nt\n"
      "s_add_u32 s40, s40, 110592\n"
      "s_addc_u32 s41, s41, 0\n"
      "s_barrier\n"
      "L_per3_%=:\n"
      "s_add_u32 s56, s87, L_per4_%=-L_begin_%=\n"
      "s_min_u32 s56, s56, L_end_%=-L_begin_%=-1024\n"
      "s_add_u32 s56, s84, s56\n"
      "s_addc_u32 s57, s85, 0\n"
      "s_mov_b32 m0, s86\n"
      "s_nop 0\n"
      "global_load_lds_dword v0, s[56:57]\n"
      "s_waitcnt vmcnt(9)\n"
      "global_load_dwordx4 v[156:159], v0, s[42:43] nt\n"
      "s_add_u32 s42, s42, 110592\n"
      "s_addc_u32 s43, s43, 0\n"
      "s_waitcnt vmcnt(9)\n"
      "global_load_dwordx4 v[160:163], v1, s[40:41] nt\n"
      "s_add_u32 s40, s40, 110592\n"
      "s_addc_u32 s41, s41, 0\n"
      "s_waitcnt vmcnt(9)\n"
      "global_load_dwordx4 v[164:167], v0, s[42:43] nt\n"
      "s_add_u32 s42, s42, 110592\n"
      "s_addc_u32 s43, s43, 0\n"
      "s_waitcnt vmcnt(9)\n"
      "global_load_dwordx4 v[168:171], v1, s[40:41] nt\n"
      "s_add_u32 s40, s40, 110592\n"
      "s_addc_u32 s41, s41, 0\n"
      "s_waitcnt vmcnt(9)\n"
      "global_load_dwordx4 v[172:175], v0, s[42:43] nt\n"
      "s_add_u32 s42, s42, 110592\n"
      "s_addc_u32 s43, s43, 0\n"
      "s_waitcnt vmcnt(9)\n"
      "global_load_dwordx4 v[176:179], v1, s[40:41] nt\n"
      "s_add_u32 s40, s40, 110592\n"
      "s_addc_u32 s41, s41, 0\n"
      "s_add_u32 s56, s48, 24576\n"
      "s_addc_u32 s57, s49, 0\n"
      "s_mov_b64 exec, 0xffff\n"
      "global_load_dwordx4 v[8:11], v0, s[56:57] sc1\n"
      "s_mov_b64 exec, -1\n"
      "s_add_u32 s56, s48, 65536\n"
      "s_addc_u32 s57, s49, 0\n"
      "s_mov_b64 exec, 0xffff\n"
      "global_load_dwordx4 v[12:15], v0, s[56:57] sc1\n"
      "s_mov_b64 exec, -1\n"
      "s_waitcnt vmcnt(11)\n"
      "global_load_dwordx4 v[180:183], v0, s[42:43] nt\n"
      "s_add_u32 s42, s42, 110592\n"
      "s_addc_u32 s43, s43, 0\n"
      "s_waitcnt vmcnt(11)\n"
      "global_load_dwordx4 v[184:187], v1, s[40:41] nt\n"
      "s_add_u32 s40, s40, 110592\n"
      "s_addc_u32 s41, s41, 0\n"
      "s_waitcnt vmcnt(11)\n"
      "global_load_dwordx4 v[188:191], v0, s[42:43] nt\n"
      "s_add_u32 s42, s42, 110592\n"
      "s_addc_u32 s43, s43, 0\n"
      "s_waitcnt vmcnt(10)\n"
      "global_load_dwordx4 v[192:195], v1, s[40:41] nt\n"
      "s_add_u32 s40, s40, 110592\n"
      "s_addc_u32 s41, s41, 0\n"
      "s_waitcnt vmcnt(10)\n"
      "global_load_dwordx4 v[196:199], v0, s[42:43] nt\n"
      "s_add_u32 s42, s42, 110592\n"
      "s_addc_u32 s43, s43, 0\n"
      "s_waitcnt vmcnt(10)\n"
      "global_load_dwordx4 v[200:203], v1, s[40:41] nt\n"
      "s_add_u32 s40, s40, 110592\n"
      "s_addc_u32 s41, s41, 0\n"
      "s_waitcnt vmcnt(10)\n"
      "global_load_dwordx4 v[204:207], v0, s[42:43] nt\n"
      "s_add_u32 s42, s42, 110592\n"
      "s_addc_u32 s43, s43, 0\n"
      "s_waitcnt vmcnt(10)\n"
      "global_load_dwordx4 v[208:211], v1, s[40:41] nt\n"
      "s_add_u32 s40, s40, 110592\n"
      "s_addc_u32 s41, s41, 0\n"
      "s_waitcnt vmcnt(10)\n"
      "global_load_dwordx4 v[212:215], v0, s[42:43] nt\n"
      "s_add_u32 s42, s42, 110592\n"
      "s_addc_u32 s43, s43, 0\n"
      "s_waitcnt vmcnt(9)\n"
      "s_mov_b64 exec, 0xffff\n"
      "s_mov_b32 s53, 0\n"
      "L_qc9_%=:\n"
      "v_max3_u32 v49, v12, v13, v14\n"
      "v_max_u32 v49, v49, v15\n"
      "v_cmp_eq_u32 vcc, -1, v49\n"
      "s_nop 0\n"
      "s_cbranch_vccz L_qo10_%=\n"
      "s_cmp_lg_u32 s54, 0\n"
      "s_cbranch_scc1 L_qo10_%=\n"
      "s_add_u32 s56, s48, 65536\n"
      "s_addc_u32 s57, s49, 0\n"
      "global_load_dwordx4 v[12:15], v0, s[56:57] sc1\n"
      "s_add_u32 s53, s53, 1\n"
      "s_waitcnt vmcnt(0)\n"
      "s_cmp_lt_u32 s53, 30000\n"
      "s_cbranch_scc1 L_qc9_%=\n"
      "s_mov_b32 s54, 200\n"
      "v_mov_b32 v49, 200\n"
      "v_mov_b32 v50, 256\n"
      "global_store_dword v50, v49, s[58:59] sc1\n"
      "L_qo10_%=:\n"
      "s_lshl_b32 s62, s60, 8\n"
      "s_add_u32 s62, s62, 160896\n"
      "v_add_u32 v49, s62, v0\n"
      "ds_write_b128 v49, v[12:15]\n"
      "s_mov_b64 exec, -1\n"
      "s_waitcnt lgkmcnt(0)\n"
      "s_barrier\n"
      "ds_read_b128 v[12:15], v2 offset:1024\n"
      "s_waitcnt lgkmcnt(0)\n"
      "ds_read_b128 v[40:43], v3 offset:0\n"
      "ds_read_b128 v[44:47], v3 offset:2048\n"
      "ds_read_b128 v[48:51], v3 offset:1024\n"
      "ds_read_b128 v[52:55], v3 offset:3072\n"
      "s_waitcnt lgkmcnt(3)\n"
      "v_mfma_f32_16x16x32_bf16 v[24:27], v[40:43], v[12:15], 0\n"
      "ds_read_b128 v[40:43], v3 offset:4096\n"
      "s_waitcnt lgkmcnt(3)\n"
      "v_mfma_f32_16x16x32_bf16 v[28:31], v[44:47], v[12:15], 0\n"
      "ds_read_b128 v[44:47], v3 offset:6144\n"
      "s_nop 3\n"
      "v_mov_b32_dpp v34, v24 quad_perm:[0,0,0,0] row_mask:0xf bank_mask:0xf\n"
      "v_mov_b32_dpp v33, v26 quad_perm:[2,2,2,2] row_mask:0xf bank_mask:0xf\n"
      "s_nop 0\n"
      "v_add_f32_dpp v34, v25, v34 quad_perm:[1,1,1,1] row_mask:0xf bank_mask:0xf\n"
      "v_add_f32_dpp v33, v27, v33 quad_perm:[3,3,3,3] row_mask:0xf bank_mask:0xf\n"
      "v_add_f32 v34, v34, v33\n"
      "v_mov_b32_dpp v32, v28 quad_perm:[0,0,0,0] row_mask:0xf bank_mask:0xf\n"
      "v_mov_b32_dpp v33, v30 quad_perm:[2,2,2,2] row_mask:0xf bank_mask:0xf\n"
      "s_nop 0\n"
      "v_add_f32_dpp v32, v29, v32 quad_perm:[1,1,1,1] row_mask:0xf bank_mask:0xf\n"
      "v_add_f32_dpp v33, v31, v33 quad_perm:[3,3,3,3] row_mask:0xf bank_mask:0xf\n"
      "v_add_f32 v32, v32, v33\n"
      "v_max_f32 v35, v34, v32\n"
      "v_mov_b32 v33, v35\n"
      "s_nop 1\n"
      "v_permlane32_swap_b32 v33, v35\n"
      "v_max_f32 v35, v33, v35\n"
      "v_mov_b32 v33, v35\n"
      "s_nop 1\n"
      "v_permlane16_swap_b32 v33, v35\n"
      "v_max_f32 v6, v33, v35\n"
      "v_sub_f32 v34, v34, v6\n"
      "v_sub_f32 v32, v32, v6\n"
      "v_mov_b32 v35, 0\n"
      "v_exp_f32 v34, v34\n"
      "s_nop 0\n"
      "v_mov_b32 v7, v34\n"
      "v_cvt_pk_bf16_f32 v34, v34, v34\n"
      "v_and_b32 v36, v4, v34\n"
      "v_and_b32 v37, v5, v34\n"
      "s_waitcnt lgkmcnt(1)\n"
      "v_mfma_f32_16x16x32_bf16 v[24:27], v[40:43], v[12:15], 0\n"
      "ds_read_b128 v[40:43], v3 offset:8192\n"
      "v_mfma_f32_16x16x16_bf16 v[16:19], v[48:49], v[36:37], 0\n"
      "v_mfma_f32_16x16x16_bf16 v[20:23], v[50:51], v[36:37], 0\n"
      "ds_read_b128 v[48:51], v3 offset:5120\n"
      "v_exp_f32 v34, v32\n"
      "v_max_f32 v35, v35, v32\n"
      "v_add_f32 v7, v7, v34\n"
      "v_cvt_pk_bf16_f32 v34, v34, v34\n"
      "v_and_b32 v38, v4, v34\n"
      "v_and_b32 v39, v5, v34\n"
      "s_waitcnt lgkmcnt(2)\n"
      "v_mfma_f32_16x16x32_bf16 v[28:31], v[44:47], v[12:15], 0\n"
      "ds_read_b128 v[44:47], v3 offset:10240\n"
      "v_sub_f32_dpp v32, v24, v6 quad_perm:[0,0,0,0] row_mask:0xf bank_mask:0xf\n"
      "v_mov_b32_dpp v33, v26 quad_perm:[2,2,2,2] row_mask:0xf bank_mask:0xf\n"
      "v_mfma_f32_16x16x16_bf16 v[16:19], v[52:53], v[38:39], v[16:19]\n"
      "v_add_f32_dpp v32, v25, v32 quad_perm:[1,1,1,1] row_mask:0xf bank_mask:0xf\n"
      "v_add_f32_dpp v33, v27, v33 quad_perm:[3,3,3,3] row_mask:0xf bank_mask:0xf\n"
      "v_mfma_f32_16x16x16_bf16 v[20:23], v[54:55], v[38:39], v[20:23]\n"
      "ds_read_b128 v[52:55], v3 offset:7168\n"
      "v_add_f32 v32, v32, v33\n"
      "v_exp_f32 v34, v32\n"
      "v_max_f32 v35, v35, v32\n"
      "v_add_f32 v7, v7, v34\n"
      "v_cvt_pk_bf16_f32 v34, v34, v34\n"
      "v_and_b32 v36, v4, v34\n"
      "v_and_b32 v37, v5, v34\n"
      "s_waitcnt lgkmcnt(3)\n"
      "v_mfma_f32_16x16x32_bf16 v[24:27], v[40:43], v[12:15], 0\n"
      "ds_read_b128 v[40:43], v3 offset:12288\n"
      "v_sub_f32_dpp v32, v28, v6 quad_perm:[0,0,0,0] row_mask:0xf bank_mask:0xf\n"
      "v_mov_b32_dpp v33, v30 quad_perm:[2,2,2,2] row_mask:0xf bank_mask:0xf\n"
      "s_waitcnt lgkmcnt(3)\n"
      "v_mfma_f32_16x16x16_bf16 v[16:19], v[48:49], v[36:37], v[16:19]\n"
      "v_add_f32_dpp v32, v29, v32 quad_perm:[1,1,1,1] row_mask:0xf bank_mask:0xf\n"
      "v_add_f32_dpp v33, v31, v33 quad_perm:[3,3,3,3] row_mask:0xf bank_mask:0xf\n"
      "v_mfma_f32_16x16x16_bf16 v[20:23], v[50:51], v[36:37], v[20:23]\n"
      "ds_read_b128 v[48:51], v3 offset:9216\n"
      "v_add_f32 v32, v32, v33\n"
      "v_exp_f32 v34, v32\n"
      "v_max_f32 v35, v35, v32\n"
      "v_add_f32 v7, v7, v34\n"
      "v_cvt_pk_bf16_f32 v34, v34, v34\n"
      "v_and_b32 v38, v4, v34\n"
      "v_and_b32 v39, v5, v34\n"
      "s_waitcnt vmcnt(8)\n"
      "global_load_dwordx4 v[216:219], v1, s[40:41] nt\n"
      "s_add_u32 s40, s40, 110592\n"
      "s_addc_u32 s41, s41, 0\n"
      "s_waitcnt lgkmcnt(3)\n"
      "v_mfma_f32_16x16x32_bf16 v[28:31], v[44:47], v[12:15], 0\n"
      "ds_read_b128 v[44:47], v3 offset:14336\n"
      "v_sub_f32_dpp v32, v24, v6 quad_perm:[0,0,0,0] row_mask:0xf bank_mask:0xf\n"
      "v_mov_b32_dpp v33, v26 quad_perm:[2,2,2,2] row_mask:0xf bank_mask:0xf\n"
      "s_waitcnt lgkmcnt(3)\n"
      "v_mfma_f32_16x16x16_bf16 v[16:19], v[52:53], v[38:39], v[16:19]\n"
      "v_add_f32_dpp v32, v25, v32 quad_perm:[1,1,1,1] row_mask:0xf bank_mask:0xf\n"
      "v_add_f32_dpp v33, v27, v33 quad_perm:[3,3,3,3] row_mask:0xf bank_mask:0xf\n"
      "v_mfma_f32_16x16x16_bf16 v[20:23], v[54:55], v[38:39], v[20:23]\n"
      "ds_read_b128 v[52:55], v3 offset:11264\n"
      "v_add_f32 v32, v32, v33\n"
      "v_exp_f32 v34, v32\n"
      "v_max_f32 v35, v35, v32\n"
      "v_add_f32 v7, v7, v34\n"
      "v_cvt_pk_bf16_f32 v34, v34, v34\n"
      "v_and_b32 v36, v4, v34\n"
      "v_and_b32 v37, v5, v34\n"
      "s_waitcnt lgkmcnt(3)\n"
      "v_mfma_f32_16x16x32_bf16 v[24:27], v[40:43], v[12:15], 0\n"
      "ds_read_b128 v[40:43], v3 offset:16384\n"
      "v_sub_f32_dpp v32, v28, v6 quad_perm:[0,0,0,0] row_mask:0xf bank_mask:0xf\n"
      "v_mov_b32_dpp v33, v30 quad_perm:[2,2,2,2] row_mask:0xf bank_mask:0xf\n"
      "s_waitcnt lgkmcnt(3)\n"
      "v_mfma_f32_16x16x16_bf16 v[16:19], v[48:49], v[36:37], v[16:19]\n"
      "v_add_f32_dpp v32, v29, v32 quad_perm:[1,1,1,1] row_mask:0xf bank_mask:0xf\n"
      "v_add_f32_dpp v33, v31, v33 quad_perm:[3,3,3,3] row_mask:0xf bank_mask:0xf\n"
      "v_mfma_f32_16x16x16_bf16 v[20:23], v[50:51], v[36:37], v[20:23]\n"
      "ds_read_b128 v[48:51], v3 offset:13312\n"
      "v_add_f32 v32, v32, v33\n"
      "v_exp_f32 v34, v32\n"
      "v_max_f32 v35, v35, v32\n"
      "v_add_f32 v7, v7, v34\n"
      "v_cvt_pk_bf16_f32 v34, v34, v34\n"
      "v_and_b32 v38, v4, v34\n"
      "v_and_b32 v39, v5, v34\n"
      "s_waitcnt vmcnt(8)\n"
      "global_load_dwordx4 v[220:223], v0, s[42:43] nt\n"
      "s_add_u32 s42, s42, 110592\n"
      "s_addc_u32 s43, s43, 0\n"
      "s_waitcnt lgkmcnt(3)\n"
      "v_mfma_f32_16x16x32_bf16 v[28:31], v[44:47], v[12:15], 0\n"
      "ds_read_b128 v[44:47], v3 offset:18432\n"
      "v_sub_f32_dpp v32, v24, v6 quad_perm:[0,0,0,0] row_mask:0xf bank_mask:0xf\n"
      "v_mov_b32_dpp v33, v26 quad_perm:[2,2,2,2] row_mask:0xf bank_mask:0xf\n"
      "s_waitcnt lgkmcnt(3)\n"
      "v_mfma_f32_16x16x16_bf16 v[16:19], v[52:53], v[38:39], v[16:19]\n"
      "v_add_f32_dpp v32, v25, v32 quad_perm:[1,1,1,1] row_mask:0xf bank_mask:0xf\n"
      "v_add_f32_dpp v33, v27, v33 quad_perm:[3,3,3,3] row_mask:0xf bank_mask:0xf\n"
      "v_mfma_f32_16x16x16_bf16 v[20:23], v[54:55], v[38:39], v[20:23]\n"
      "ds_read_b128 v[52:55], v3 offset:15360\n"
      "v_add_f32 v32, v32, v33\n"
      "v_exp_f32 v34, v32\n"
      "v_max_f32 v35, v35, v32\n"
      "v_add_f32 v7, v7, v34\n"
      "v_cvt_pk_bf16_f32 v34, v34, v34\n"
      "v_and_b32 v36, v4, v34\n"
      "v_and_b32 v37, v5, v34\n"
      "s_waitcnt lgkmcnt(3)\n"
      "v_mfma_f32_16x16x32_bf16 v[24:27], v[40:43], v[12:15], 0\n"
      "ds_read_b128 v[40:43], v3 offset:20480\n"
      "v_sub_f32_dpp v32, v28, v6 quad_perm:[0,0,0,0] row_mask:0xf bank_mask:0xf\n"
      "v_mov_b32_dpp v33, v30 quad_perm:[2,2,2,2] row_mask:0xf bank_mask:0xf\n"
      "s_waitcnt lgkmcnt(3)\n"
      "v_mfma_f32_16x16x16_bf16 v[16:19], v[48:49], v[36:37], v[16:19]\n"
      "v_add_f32_dpp v32, v29, v32 quad_perm:[1,1,1,1] row_mask:0xf bank_mask:0xf\n"
      "v_add_f32_dpp v33, v31, v33 quad_perm:[3,3,3,3] row_mask:0xf bank_mask:0xf\n"
      "v_mfma_f32_16x16x16_bf16 v[20:23], v[50:51], v[36:37], v[20:23]\n"
      "ds_read_b128 v[48:51], v3 offset:17408\n"
      "v_add_f32 v32, v32, v33\n"
      "v_exp_f32 v34, v32\n"
      "v_max_f32 v35, v35, v32\n"
      "v_add_f32 v7, v7, v34\n"
      "v_cvt_pk_bf16_f32 v34, v34, v34\n"
      "v_and_b32 v38, v4, v34\n"
      "v_and_b32 v39, v5, v34\n"
      "s_waitcnt lgkmcnt(3)\n"
      "v_mfma_f32_16x16x32_bf16 v[28:31], v[44:47], v[12:15], 0\n"
      "ds_read_b128 v[44:47], v3 offset:22528\n"
      "v_sub_f32_dpp v32, v24, v6 quad_perm:[0,0,0,0] row_mask:0xf bank_mask:0xf\n"
      "v_mov_b32_dpp v33, v26 quad_perm:[2,2,2,2] row_mask:0xf bank_mask:0xf\n"
      "s_waitcnt lgkmcnt(3)\n"
      "v_mfma_f32_16x16x16_bf16 v[16:19], v[52:53], v[38:39], v[16:19]\n"
      "v_add_f32_dpp v32, v25, v32 quad_perm:[1,1,1,1] row_mask:0xf bank_mask:0xf\n"
      "v_add_f32_dpp v33, v27, v33 quad_perm:[3,3,3,3] row_mask:0xf bank_mask:0xf\n"
      "v_mfma_f32_16x16x16_bf16 v[20:23], v[54:55], v[38:39], v[20:23]\n"
      "ds_read_b128 v[52:55], v3 offset:19456\n"
      "v_add_f32 v32, v32, v33\n"
      "v_exp_f32 v34, v32\n"
      "v_max_f32 v35, v35, v32\n"
      "v_add_f32 v7, v7, v34\n"
      "v_cvt_pk_bf16_f32 v34, v34, v34\n"
      "v_and_b32 v36, v4, v34\n"
      "v_and_b32 v37, v5, v34\n"
      "s_waitcnt vmcnt(8)\n"
      "global_load_dwordx4 v[224:227], v1, s[40:41] nt\n"
      "s_add_u32 s40, s40, 110592\n"
      "s_addc_u32 s41, s41, 0\n"
      "s_waitcnt lgkmcnt(3)\n"
      "v_mfma_f32_16x16x32_bf16 v[24:27], v[40:43], v[12:15], 0\n"
      "ds_read_b128 v[40:43], v3 offset:24576\n"
      "v_sub_f32_dpp v32, v28, v6 quad_perm:[0,0,0,0] row_mask:0xf bank_mask:0xf\n"
      "v_mov_b32_dpp v33, v30 quad_perm:[2,2,2,2] row_mask:0xf bank_mask:0xf\n"
      "s_waitcnt lgkmcnt(3)\n"
      "v_mfma_f32_16x16x16_bf16 v[16:19], v[48:49], v[36:37], v[16:19]\n"
      "v_add_f32_dpp v32, v29, v32 quad_perm:[1,1,1,1] row_mask:0xf bank_mask:0xf\n"
      "v_add_f32_dpp v33, v31, v33 quad_perm:[3,3,3,3] row_mask:0xf bank_mask:0xf\n"
      "v_mfma_f32_16x16x16_bf16 v[20:23], v[50:51], v[36:37], v[20:23]\n"
      "ds_read_b128 v[48:51], v3 offset:21504\n"
      "v_add_f32 v32, v32, v33\n"
      "v_exp_f32 v34, v32\n"
      "v_max_f32 v35, v35, v32\n"
      "v_add_f32 v7, v7, v34\n"
      "v_cvt_pk_bf16_f32 v34, v34, v34\n"
      "v_and_b32 v38, v4, v34\n"
      "v_and_b32 v39, v5, v34\n"
      "s_waitcnt lgkmcnt(3)\n"
      "v_mfma_f32_16x16x32_bf16 v[28:31], v[44:47], v[12:15], 0\n"
      "ds_read_b128 v[44:47], v3 offset:26624\n"
      "v_sub_f32_dpp v32, v24, v6 quad_perm:[0,0,0,0] row_mask:0xf bank_mask:0xf\n"
      "v_mov_b32_dpp v33, v26 quad_perm:[2,2,2,2] row_mask:0xf bank_mask:0xf\n"
      "s_waitcnt lgkmcnt(3)\n"
      "v_mfma_f32_16x16x16_bf16 v[16:19], v[52:53], v[38:39], v[16:19]\n"
      "v_add_f32_dpp v32, v25, v32 quad_perm:[1,1,1,1] row_mask:0xf bank_mask:0xf\n"
      "v_add_f32_dpp v33, v27, v33 quad_perm:[3,3,3,3] row_mask:0xf bank_mask:0xf\n"
      "v_mfma_f32_16x16x16_bf16 v[20:23], v[54:55], v[38:39], v[20:23]\n"
      "ds_read_b128 v[52:55], v3 offset:23552\n"
      "v_add_f32 v32, v32, v33\n"
      "v_exp_f32 v34, v32\n"
      "v_max_f32 v35, v35, v32\n"
      "v_add_f32 v7, v7, v34\n"
      "v_cvt_pk_bf16_f32 v34, v34, v34\n"
      "v_and_b32 v36, v4, v34\n"
      "v_and_b32 v37, v5, v34\n"
      "s_waitcnt vmcnt(8)\n"
      "global_load_dwordx4 v[228:231], v0, s[42:43] nt\n"
      "s_add_u32 s42, s42, 110592\n"
      "s_addc_u32 s43, s43, 0\n"
      "s_waitcnt lgkmcnt(3)\n"
      "v_mfma_f32_16x16x32_bf16 v[24:27], v[40:43], v[12:15], 0\n"
      "ds_read_b128 v[40:43], v3 offset:28672\n"
      "v_sub_f32_dpp v32, v28, v6 quad_perm:[0,0,0,0] row_mask:0xf bank_mask:0xf\n"
      "v_mov_b32_dpp v33, v30 quad_perm:[2,2,2,2] row_mask:0xf bank_mask:0xf\n"
      "s_waitcnt lgkmcnt(3)\n"
      "v_mfma_f32_16x16x16_bf16 v[16:19], v[48:49], v[36:37], v[16:19]\n"
      "v_add_f32_dpp v32, v29, v32 quad_perm:[1,1,1,1] row_mask:0xf bank_mask:0xf\n"
      "v_add_f32_dpp v33, v31, v33 quad_perm:[3,3,3,3] row_mask:0xf bank_mask:0xf\n"
      "v_mfma_f32_16x16x16_bf16 v[20:23], v[50:51], v[36:37], v[20:23]\n"
      "ds_read_b128 v[48:51], v3 offset:25600\n"
      "v_add_f32 v32, v32, v33\n"
      "v_exp_f32 v34, v32\n"
      "v_max_f32 v35, v35, v32\n"
      "v_add_f32 v7, v7, v34\n"
      "v_cvt_pk_bf16_f32 v34, v34, v34\n"
      "v_and_b32 v38, v4, v34\n"
      "v_and_b32 v39, v5, v34\n"
      "s_waitcnt lgkmcnt(3)\n"
      "v_mfma_f32_16x16x32_bf16 v[28:31], v[44:47], v[12:15], 0\n"
      "ds_read_b128 v[44:47], v3 offset:30720\n"
      "v_sub_f32_dpp v32, v24, v6 quad_perm:[0,0,0,0] row_mask:0xf bank_mask:0xf\n"
      "v_mov_b32_dpp v33, v26 quad_perm:[2,2,2,2] row_mask:0xf bank_mask:0xf\n"
      "s_waitcnt lgkmcnt(3)\n"
      "v_mfma_f32_16x16x16_bf16 v[16:19], v[52:53], v[38:39], v[16:19]\n"
      "v_add_f32_dpp v32, v25, v32 quad_perm:[1,1,1,1] row_mask:0xf bank_mask:0xf\n"
      "v_add_f32_dpp v33, v27, v33 quad_perm:[3,3,3,3] row_mask:0xf bank_mask:0xf\n"
      "v_mfma_f32_16x16x16_bf16 v[20:23], v[54:55], v[38:39], v[20:23]\n"
      "ds_read_b128 v[52:55], v3 offset:27648\n"
      "v_add_f32 v32, v32, v33\n"
      "v_exp_f32 v34, v32\n"
      "v_max_f32 v35, v35, v32\n"
      "v_add_f32 v7, v7, v34\n"
      "v_cvt_pk_bf16_f32 v34, v34, v34\n"
      "v_and_b32 v36, v4, v34\n"
      "v_and_b32 v37, v5, v34\n"
      "s_waitcnt vmcnt(8)\n"
      "global_load_dwordx4 v[232:235], v1, s[40:41] nt\n"
      "s_add_u32 s40, s40, 110592\n"
      "s_addc_u32 s41, s41, 0\n"
      "s_waitcnt lgkmcnt(3)\n"
      "v_mfma_f32_16x16x32_bf16 v[24:27], v[40:43], v[12:15], 0\n"
      "ds_read_b128 v[40:43], v3 offset:32768\n"
      "v_sub_f32_dpp v32, v28, v6 quad_perm:[0,0,0,0] row_mask:0xf bank_mask:0xf\n"
      "v_mov_b32_dpp v33, v30 quad_perm:[2,2,2,2] row_mask:0xf bank_mask:0xf\n"
      "s_waitcnt lgkmcnt(3)\n"
      "v_mfma_f32_16x16x16_bf16 v[16:19], v[48:49], v[36:37], v[16:19]\n"
      "v_add_f32_dpp v32, v29, v32 quad_perm:[1,1,1,1] row_mask:0xf bank_mask:0xf\n"
      "v_add_f32_dpp v33, v31, v33 quad_perm:[3,3,3,3] row_mask:0xf bank_mask:0xf\n"
      "v_mfma_f32_16x16x16_bf16 v[20:23], v[50:51], v[36:37], v[20:23]\n"
      "ds_read_b128 v[48:51], v3 offset:29696\n"
      "v_add_f32 v32, v32, v33\n"
      "v_exp_f32 v34, v32\n"
      "v_max_f32 v35, v35, v32\n"
      "v_add_f32 v7, v7, v34\n"
      "v_cvt_pk_bf16_f32 v34, v34, v34\n"
      "v_and_b32 v38, v4, v34\n"
      "v_and_b32 v39, v5, v34\n"
      "s_waitcnt lgkmcnt(3)\n"
      "v_mfma_f32_16x16x32_bf16 v[28:31], v[44:47], v[12:15], 0\n"
      "ds_read_b128 v[44:47], v3 offset:34816\n"
      "v_sub_f32_dpp v32, v24, v6 quad_perm:[0,0,0,0] row_mask:0xf bank_mask:0xf\n"
      "v_mov_b32_dpp v33, v26 quad_perm:[2,2,2,2] row_mask:0xf bank_mask:0xf\n"
      "s_waitcnt lgkmcnt(3)\n"
      "v_mfma_f32_16x16x16_bf16 v[16:19], v[52:53], v[38:39], v[16:19]\n"
      "v_add_f32_dpp v32, v25, v32 quad_perm:[1,1,1,1] row_mask:0xf bank_mask:0xf\n"
      "v_add_f32_dpp v33, v27, v33 quad_perm:[3,3,3,3] row_mask:0xf bank_mask:0xf\n"
      "v_mfma_f32_16x16x16_bf16 v[20:23], v[54:55], v[38:39], v[20:23]\n"
      "ds_read_b128 v[52:55], v3 offset:31744\n"
      "v_add_f32 v32, v32, v33\n"
      "v_exp_f32 v34, v32\n"
      "v_max_f32 v35, v35, v32\n"
      "v_add_f32 v7, v7, v34\n"
      "v_cvt_pk_bf16_f32 v34, v34, v34\n"
      "v_and_b32 v36, v4, v34\n"
      "v_and_b32 v37, v5, v34\n"
      "s_waitcnt lgkmcnt(3)\n"
      "v_mfma_f32_16x16x32_bf16 v[24:27], v[40:43], v[12:15], 0\n"
      "ds_read_b128 v[40:43], v3 offset:36864\n"
      "v_sub_f32_dpp v32, v28, v6 quad_perm:[0,0,0,0] row_mask:0xf bank_mask:0xf\n"
      "v_mov_b32_dpp v33, v30 quad_perm:[2,2,2,2] row_mask:0xf bank_mask:0xf\n"
      "s_waitcnt lgkmcnt(3)\n"
      "v_mfma_f32_16x16x16_bf16 v[16:19], v[48:49], v[36:37], v[16:19]\n"
      "v_add_f32_dpp v32, v29, v32 quad_perm:[1,1,1,1] row_mask:0xf bank_mask:0xf\n"
      "v_add_f32_dpp v33, v31, v33 quad_perm:[3,3,3,3] row_mask:0xf bank_mask:0xf\n"
      "v_mfma_f32_16x16x16_bf16 v[20:23], v[50:51], v[36:37], v[20:23]\n"
      "ds_read_b128 v[48:51], v3 offset:33792\n"
      "v_add_f32 v32, v32, v33\n"
      "v_exp_f32 v34, v32\n"
      "v_max_f32 v35, v35, v32\n"
      "v_add_f32 v7, v7, v34\n"
      "v_cvt_pk_bf16_f32 v34, v34, v34\n"
      "v_and_b32 v38, v4, v34\n"
      "v_and_b32 v39, v5, v34\n"
      "s_waitcnt vmcnt(8)\n"
      "global_load_dwordx4 v[236:239], v0, s[42:43] nt\n"
      "s_add_u32 s42, s42, 110592\n"
      "s_addc_u32 s43, s43, 0\n"
      "s_waitcnt lgkmcnt(3)\n"
      "v_mfma_f32_16x16x32_bf16 v[28:31], v[44:47], v[12:15], 0\n"
      "v_sub_f32_dpp v32, v24, v6 quad_perm:[0,0,0,0] row_mask:0xf bank_mask:0xf\n"
      "v_mov_b32_dpp v33, v26 quad_perm:[2,2,2,2] row_mask:0xf bank_mask:0xf\n"
      "s_waitcnt lgkmcnt(2)\n"
      "v_mfma_f32_16x16x16_bf16 v[16:19], v[52:53], v[38:39], v[16:19]\n"
      "v_add_f32_dpp v32, v25, v32 quad_perm:[1,1,1,1] row_mask:0xf bank_mask:0xf\n"
      "v_add_f32_dpp v33, v27, v33 quad_perm:[3,3,3,3] row_mask:0xf bank_mask:0xf\n"
      "v_mfma_f32_16x16x16_bf16 v[20:23], v[54:55], v[38:39], v[20:23]\n"
      "ds_read_b128 v[52:55], v3 offset:35840\n"
      "v_add_f32 v32, v32, v33\n"
      "v_exp_f32 v34, v32\n"
      "v_max_f32 v35, v35, v32\n"
      "v_add_f32 v7, v7, v34\n"
      "v_cvt_pk_bf16_f32 v34, v34, v34\n"
      "v_and_b32 v36, v4, v34\n"
      "v_and_b32 v37, v5, v34\n"
      "s_waitcnt lgkmcnt(2)\n"
      "v_mfma_f32_16x16x32_bf16 v[24:27], v[40:43], v[12:15], 0\n"
      "v_sub_f32_dpp v32, v28, v6 quad_perm:[0,0,0,0] row_mask:0xf bank_mask:0xf\n"
      "v_mov_b32_dpp v33, v30 quad_perm:[2,2,2,2] row_mask:0xf bank_mask:0xf\n"
      "s_waitcnt lgkmcnt(1)\n"
      "v_mfma_f32_16x16x16_bf16 v[16:19], v[48:49], v[36:37], v[16:19]\n"
      "v_add_f32_dpp v32, v29, v32 quad_perm:[1,1,1,1] row_mask:0xf bank_mask:0xf\n"
      "v_add_f32_dpp v33, v31, v33 quad_perm:[3,3,3,3] row_mask:0xf bank_mask:0xf\n"
      "v_mfma_f32_16x16x16_bf16 v[20:23], v[50:51], v[36:37], v[20:23]\n"
      "ds_read_b128 v[48:51], v3 offset:37888\n"
      "v_add_f32 v32, v32, v33\n"
      "v_exp_f32 v34, v32\n"
      "v_max_f32 v35, v35, v32\n"
      "v_add_f32 v7, v7, v34\n"
      "v_cvt_pk_bf16_f32 v34, v34, v34\n"
      "v_and_b32 v38, v4, v34\n"
      "v_and_b32 v39, v5, v34\n"
      "s_waitcnt vmcnt(8)\n"
      "global_load_dwordx4 v[240:243], v1, s[40:41] nt\n"
      "s_add_u32 s40, s40, s51\n"
      "s_addc_u32 s41, s41, 0\n"
      "v_sub_f32_dpp v32, v24, v6 quad_perm:[0,0,0,0] row_mask:0xf bank_mask:0xf\n"
      "v_mov_b32_dpp v33, v26 quad_perm:[2,2,2,2] row_mask:0xf bank_mask:0xf\n"
      "s_waitcnt lgkmcnt(1)\n"
      "v_mfma_f32_16x16x16_bf16 v[16:19], v[52:53], v[38:39], v[16:19]\n"
      "v_add_f32_dpp v32, v25, v32 quad_perm:[1,1,1,1] row_mask:0xf bank_mask:0xf\n"
      "v_add_f32_dpp v33, v27, v33 quad_perm:[3,3,3,3] row_mask:0xf bank_mask:0xf\n"
      "v_mfma_f32_16x16x16_bf16 v[20:23], v[54:55], v[38:39], v[20:23]\n"
      "v_add_f32 v32, v32, v33\n"
      "v_add_f32 v32, s69, v32\n"
      "v_exp_f32 v34, v32\n"
      "v_max_f32 v35, v35, v32\n"
      "v_add_f32 v7, v7, v34\n"
      "v_cvt_pk_bf16_f32 v34, v34, v34\n"
      "v_and_b32 v36, v4, v34\n"
      "v_and_b32 v37, v5, v34\n"
      "s_waitcnt lgkmcnt(0)\n"
      "s_nop 0\n"
      "v_mfma_f32_16x16x16_bf16 v[16:19], v[48:49], v[36:37], v[16:19]\n"
      "v_mfma_f32_16x16x16_bf16 v[20:23], v[50:51], v[36:37], v[20:23]\n"
      "s_waitcnt vmcnt(8)\n"
      "global_load_dwordx4 v[244:247], v0, s[42:43] nt\n"
      "s_add_u32 s42, s42, s51\n"
      "s_addc_u32 s43, s43, 0\n"
      "v_cmp_lt_f32 vcc, 0x42800000, v35\n"
      "s_nop 0\n"
      "s_cbranch_vccnz L_fb3_%=\n"
      "L_ret3_%=:\n"
      "v_mov_b32 v49, v7\n"
      "v_lshlrev_b32 v41, 2, v0\n"
      "v_and_b32 v41, 0x300, v41\n"
      "v_permlane32_swap_b32 v49, v7\n"
      "v_lshrrev_b32 v42, 4, v0\n"
      "v_add_f32 v7, v49, v7\n"
      "v_and_b32 v42, 0x30, v42\n"
      "v_mov_b32 v49, v7\n"
      "v_add_u32 v41, v41, v42\n"
      "v_lshrrev_b32 v40, 2, v0\n"
      "v_permlane16_swap_b32 v49, v7\n"
      "v_and_b32 v42, 12, v40\n"
      "v_add_f32 v7, v49, v7\n"
      "v_add_u32 v41, v41, v42\n"
      "s_lshl_b32 s62, s60, 10\n"
      "s_add_u32 s62, s62, 155648\n"
      "v_add_u32 v41, s62, v41\n"
      "v_lshrrev_b32 v43, 3, v0\n"
      "v_and_b32 v43, 24, v43\n"
      "s_lshl_b32 s62, s60, 5\n"
      "s_add_u32 s62, s62, 159744\n"
      "v_add_u32 v43, s62, v43\n"
      "v_cvt_pk_bf16_f32 v36, v16, v17\n"
      "v_cvt_pk_bf16_f32 v37, v18, v19\n"
      "v_cvt_pk_bf16_f32 v38, v20, v21\n"
      "v_cvt_pk_bf16_f32 v39, v22, v23\n"
      "s_mov_b64 exec, 0x1111\n"
      "ds_write_b64 v43, v[6:7]\n"
      "s_mov_b64 exec, -1\n"
      "ds_write_b32 v41, v36 offset:0\n"
      "ds_write_b32 v41, v37 offset:64\n"
      "ds_write_b32 v41, v38 offset:128\n"
      "ds_write_b32 v41, v39 offset:192\n"
      "s_lshl_b32 s62, s60, 8\n"
      "s_add_u32 s62, s62, 155648\n"
      "v_add_u32 v42, s62, v40\n"
      "s_lshl_b32 s62, s60, 3\n"
      "s_add_u32 s62, s62, 159744\n"
      "v_mov_b32 v50, s62\n"
      "s_waitcnt lgkmcnt(0)\n"
      "s_barrier\n"
      "ds_read_b64 v[28:29], v50 offset:0\n"
      "ds_read_b64 v[30:31], v50 offset:32\n"
      "ds_read_b64 v[32:33], v50 offset:64\n"
      "ds_read_b64 v[34:35], v50 offset:96\n"
      "ds_read_b32 v24, v42 offset:0\n"
      "ds_read_b32 v25, v42 offset:1024\n"
      "ds_read_b32 v26, v42 offset:2048\n"
      "ds_read_b32 v27, v42 offset:3072\n"
      "s_waitcnt lgkmcnt(4)\n"
      "v_max3_f32 v43, v28, v30, v32\n"
      "v_max_f32 v43, v43, v34\n"
      "v_sub_f32 v28, v28, v43\n"
      "v_sub_f32 v30, v30, v43\n"
      "v_sub_f32 v32, v32, v43\n"
      "v_sub_f32 v34, v34, v43\n"
      "v_exp_f32 v28, v28\n"
      "v_exp_f32 v30, v30\n"
      "v_exp_f32 v32, v32\n"
      "v_exp_f32 v34, v34\n"
      "s_waitcnt lgkmcnt(0)\n"
      "v_lshlrev_b32 v44, 16, v24\n"
      "v_and_b32 v45, 0xffff0000, v24\n"
      "v_lshlrev_b32 v46, 16, v25\n"
      "v_and_b32 v47, 0xffff0000, v25\n"
      "v_lshlrev_b32 v48, 16, v26\n"
      "v_and_b32 v49, 0xffff0000, v26\n"
      "v_lshlrev_b32 v50, 16, v27\n"
      "v_and_b32 v51, 0xffff0000, v27\n"
      "v_mul_f32 v38, v28, v29\n"
      "v_pk_mul_f32 v[36:37], v[44:45], v[28:29] op_sel_hi:[1,0]\n"
      "v_fmac_f32 v38, v30, v31\n"
      "v_pk_fma_f32 v[36:37], v[46:47], v[30:31], v[36:37] op_sel_hi:[1,0,1]\n"
      "v_fmac_f32 v38, v32, v33\n"
      "v_pk_fma_f32 v[36:37], v[48:49], v[32:33], v[36:37] op_sel_hi:[1,0,1]\n"
      "v_fmac_f32 v38, v34, v35\n"
      "v_pk_fma_f32 v[36:37], v[50:51], v[34:35], v[36:37] op_sel_hi:[1,0,1]\n"
      "v_rcp_f32 v39, v38\n"
      "v_log_f32 v38, v38\n"
      "v_mul_f32 v36, v39, v36\n"
      "v_mul_f32 v37, v39, v37\n"
      "v_add_f32 v38, v38, v43\n"
      "v_cvt_pk_bf16_f32 v36, v36, v37\n"
      "v_min_u32 v38, -2, v38\n"
      "v_min_u32 v36, -2, v36\n"
      "v_mov_b32 v12, v36\n"
      "v_mov_b32 v13, v38\n"
      "s_waitcnt vmcnt(8)\n"
      "global_load_dwordx4 v[248:251], v1, s[40:41] nt\n"
      "s_add_u32 s40, s40, s52\n"
      "s_addc_u32 s41, s41, 0\n"
      "s_waitcnt vmcnt(8)\n"
      "global_load_dwordx4 v[252:255], v0, s[42:43] nt\n"
      "s_add_u32 s42, s42, s52\n"
      "s_addc_u32 s43, s43, 0\n"
      "s_waitcnt vmcnt(8)\n"
      "s_add_u32 m0, s61, 0\n"
      "s_nop 0\n"
      "global_load_lds_dwordx4 v1, s[40:41] nt\n"
      "s_add_u32 s40, s40, 110592\n"
      "s_addc_u32 s41, s41, 0\n"
      "s_waitcnt vmcnt(8)\n"
      "s_add_u32 m0, s61, 1024\n"
      "s_nop 0\n"
      "global_load_lds_dwordx4 v0, s[42:43] nt\n"
      "s_add_u32 s42, s42, 110592\n"
      "s_addc_u32 s43, s43, 0\n"
      "s_barrier\n"
      "s_mov_b64 exec, 0xffff\n"
      "s_mov_b32 s53, 0\n"
      "L_qc11_%=:\n"
      "v_max3_u32 v49, v8, v9, v10\n"
      "v_max_u32 v49, v49, v11\n"
      "v_cmp_eq_u32 vcc, -1, v49\n"
      "s_nop 0\n"
      "s_cbranch_vccz L_qo12_%=\n"
      "s_cmp_lg_u32 s54, 0\n"
      "s_cbranch_scc1 L_qo12_%=\n"
      "s_add_u32 s56, s48, 24576\n"
      "s_addc_u32 s57, s49, 0\n"
      "global_load_dwordx4 v[8:11], v0, s[56:57] sc1\n"
      "s_add_u32 s53, s53, 1\n"
      "s_waitcnt vmcnt(0)\n"
      "s_cmp_lt_u32 s53, 30000\n"
      "s_cbranch_scc1 L_qc11_%=\n"
      "s_mov_b32 s54, 103\n"
      "v_mov_b32 v49, 103\n"
      "v_mov_b32 v50, 256\n"
      "global_store_dword v50, v49, s[58:59] sc1\n"
      "L_qo12_%=:\n"
      "s_lshl_b32 s62, s60, 8\n"
      "s_add_u32 s62, s62, 159872\n"
      "v_add_u32 v49, s62, v0\n"
      "ds_write_b128 v49, v[8:11]\n"
      "s_mov_b64 exec, -1\n"
      "s_waitcnt lgkmcnt(0)\n"
      "s_barrier\n"
      "ds_read_b128 v[8:11], v2 offset:0\n"
      "s_waitcnt lgkmcnt(0)\n"
      "v_mfma_f32_16x16x32_bf16 v[24:27], v[104:107], v[8:11], 0\n"
      "v_mfma_f32_16x16x32_bf16 v[28:31], v[112:115], v[8:11], 0\n"
      "s_nop 6\n"
      "v_mov_b32_dpp v34, v24 quad_perm:[0,0,0,0] row_mask:0xf bank_mask:0xf\n"
      "v_mov_b32_dpp v33, v26 quad_perm:[2,2,2,2] row_mask:0xf bank_mask:0xf\n"
      "s_nop 0\n"
      "v_add_f32_dpp v34, v25, v34 quad_perm:[1,1,1,1] row_mask:0xf bank_mask:0xf\n"
      "v_add_f32_dpp v33, v27, v33 quad_perm:[3,3,3,3] row_mask:0xf bank_mask:0xf\n"
      "v_add_f32 v34, v34, v33\n"
      "v_mov_b32_dpp v32, v28 quad_perm:[0,0,0,0] row_mask:0xf bank_mask:0xf\n"
      "v_mov_b32_dpp v33, v30 quad_perm:[2,2,2,2] row_mask:0xf bank_mask:0xf\n"
      "s_nop 0\n"
      "v_add_f32_dpp v32, v29, v32 quad_perm:[1,1,1,1] row_mask:0xf bank_mask:0xf\n"
      "v_add_f32_dpp v33, v31, v33 quad_perm:[3,3,3,3] row_mask:0xf bank_mask:0xf\n"
      "v_add_f32 v32, v32, v33\n"
      "v_max_f32 v35, v34, v32\n"
      "v_mov_b32 v33, v35\n"
      "s_nop 1\n"
      "v_permlane32_swap_b32 v33, v35\n"
      "v_max_f32 v35, v33, v35\n"
      "v_mov_b32 v33, v35\n"
      "s_nop 1\n"
      "v_permlane16_swap_b32 v33, v35\n"
      "v_max_f32 v6, v33, v35\n"
      "v_sub_f32 v34, v34, v6\n"
      "v_sub_f32 v32, v32, v6\n"
      "v_mov_b32 v35, 0\n"
      "v_exp_f32 v34, v34\n"
      "s_nop 0\n"
      "v_mov_b32 v7, v34\n"
      "v_cvt_pk_bf16_f32 v34, v34, v34\n"
      "v_and_b32 v36, v4, v34\n"
      "v_and_b32 v37, v5, v34\n"
      "v_mfma_f32_16x16x32_bf16 v[24:27], v[120:123], v[8:11], 0\n"
      "s_nop 0\n"
      "v_mfma_f32_16x16x16_bf16 v[16:19], v[108:109], v[36:37], 0\n"
      "v_mfma_f32_16x16x16_bf16 v[20:23], v[110:111], v[36:37], 0\n"
      "v_exp_f32 v34, v32\n"
      "v_max_f32 v35, v35, v32\n"
      "v_add_f32 v7, v7, v34\n"
      "v_cvt_pk_bf16_f32 v34, v34, v34\n"
      "v_and_b32 v38, v4, v34\n"
      "v_and_b32 v39, v5, v34\n"
      "v_mfma_f32_16x16x32_bf16 v[28:31], v[128:131], v[8:11], 0\n"
      "v_sub_f32_dpp v32, v24, v6 quad_perm:[0,0,0,0] row_mask:0xf bank_mask:0xf\n"
      "v_mov_b32_dpp v33, v26 quad_perm:[2,2,2,2] row_mask:0xf bank_mask:0xf\n"
      "v_mfma_f32_16x16x16_bf16 v[16:19], v[116:117], v[38:39], v[16:19]\n"
      "v_add_f32_dpp v32, v25, v32 quad_perm:[1,1,1,1] row_mask:0xf bank_mask:0xf\n"
      "v_add_f32_dpp v33, v27, v33 quad_perm:[3,3,3,3] row_mask:0xf bank_mask:0xf\n"
      "v_mfma_f32_16x16x16_bf16 v[20:23], v[118:119], v[38:39], v[20:23]\n"
      "v_add_f32 v32, v32, v33\n"
      "v_exp_f32 v34, v32\n"
      "v_max_f32 v35, v35, v32\n"
      "v_add_f32 v7, v7, v34\n"
      "v_cvt_pk_bf16_f32 v34, v34, v34\n"
      "v_and_b32 v36, v4, v34\n"
      "v_and_b32 v37, v5, v34\n"
      "v_mfma_f32_16x16x32_bf16 v[24:27], v[136:139], v[8:11], 0\n"
      "v_sub_f32_dpp v32, v28, v6 quad_perm:[0,0,0,0] row_mask:0xf bank_mask:0xf\n"
      "v_mov_b32_dpp v33, v30 quad_perm:[2,2,2,2] row_mask:0xf bank_mask:0xf\n"
      "v_mfma_f32_16x16x16_bf16 v[16:19], v[124:125], v[36:37], v[16:19]\n"
      "v_add_f32_dpp v32, v29, v32 quad_perm:[1,1,1,1] row_mask:0xf bank_mask:0xf\n"
      "v_add_f32_dpp v33, v31, v33 quad_perm:[3,3,3,3] row_mask:0xf bank_mask:0xf\n"
      "v_mfma_f32_16x16x16_bf16 v[20:23], v[126:127], v[36:37], v[20:23]\n"
      "v_add_f32 v32, v32, v33\n"
      "v_exp_f32 v34, v32\n"
      "v_max_f32 v35, v35, v32\n"
      "v_add_f32 v7, v7, v34\n"
      "v_cvt_pk_bf16_f32 v34, v34, v34\n"
      "v_and_b32 v38, v4, v34\n"
      "v_and_b32 v39, v5, v34\n"
      "s_waitcnt vmcnt(8)\n"
      "s_add_u32 m0, s61, 2048\n"
      "s_nop 0\n"
      "global_load_lds_dwordx4 v1, s[40:41] nt\n"
      "s_add_u32 s40, s40, 110592\n"
      "s_addc_u32 s41, s41, 0\n"
      "v_mfma_f32_16x16x32_bf16 v[28:31], v[144:147], v[8:11], 0\n"
      "v_sub_f32_dpp v32, v24, v6 quad_perm:[0,0,0,0] row_mask:0xf bank_mask:0xf\n"
      "v_mov_b32_dpp v33, v26 quad_perm:[2,2,2,2] row_mask:0xf bank_mask:0xf\n"
      "v_mfma_f32_16x16x16_bf16 v[16:19], v[132:133], v[38:39], v[16:19]\n"
      "v_add_f32_dpp v32, v25, v32 quad_perm:[1,1,1,1] row_mask:0xf bank_mask:0xf\n"
      "v_add_f32_dpp v33, v27, v33 quad_perm:[3,3,3,3] row_mask:0xf bank_mask:0xf\n"
      "v_mfma_f32_16x16x16_bf16 v[20:23], v[134:135], v[38:39], v[20:23]\n"
      "v_add_f32 v32, v32, v33\n"
      "v_exp_f32 v34, v32\n"
      "v_max_f32 v35, v35, v32\n"
      "v_add_f32 v7, v7, v34\n"
      "v_cvt_pk_bf16_f32 v34, v34, v34\n"
      "v_and_b32 v36, v4, v34\n"
      "v_and_b32 v37, v5, v34\n"
      "v_mfma_f32_16x16x32_bf16 v[24:27], v[152:155], v[8:11], 0\n"
      "v_sub_f32_dpp v32, v28, v6 quad_perm:[0,0,0,0] row_mask:0xf bank_mask:0xf\n"
      "v_mov_b32_dpp v33, v30 quad_perm:[2,2,2,2] row_mask:0xf bank_mask:0xf\n"
      "v_mfma_f32_16x16x16_bf16 v[16:19], v[140:141], v[36:37], v[16:19]\n"
      "v_add_f32_dpp v32, v29, v32 quad_perm:[1,1,1,1] row_mask:0xf bank_mask:0xf\n"
      "v_add_f32_dpp v33, v31, v33 quad_perm:[3,3,3,3] row_mask:0xf bank_mask:0xf\n"
      "v_mfma_f32_16x16x16_bf16 v[20:23], v[142:143], v[36:37], v[20:23]\n"
      "v_add_f32 v32, v32, v33\n"
      "v_exp_f32 v34, v32\n"
      "v_max_f32 v35, v35, v32\n"
      "v_add_f32 v7, v7, v34\n"
      "v_cvt_pk_bf16_f32 v34, v34, v34\n"
      "v_and_b32 v38, v4, v34\n"
      "v_and_b32 v39, v5, v34\n"
      "s_waitcnt vmcnt(8)\n"
      "s_add_u32 m0, s61, 3072\n"
      "s_nop 0\n"
      "global_load_lds_dwordx4 v0, s[42:43] nt\n"
      "s_add_u32 s42, s42, 110592\n"
      "s_addc_u32 s43, s43, 0\n"
      "v_mfma_f32_16x16x32_bf16 v[28:31], v[160:163], v[8:11], 0\n"
      "v_sub_f32_dpp v32, v24, v6 quad_perm:[0,0,0,0] row_mask:0xf bank_mask:0xf\n"
      "v_mov_b32_dpp v33, v26 quad_perm:[2,2,2,2] row_mask:0xf bank_mask:0xf\n"
      "v_mfma_f32_16x16x16_bf16 v[16:19], v[148:149], v[38:39], v[16:19]\n"
      "v_add_f32_dpp v32, v25, v32 quad_perm:[1,1,1,1] row_mask:0xf bank_mask:0xf\n"
      "v_add_f32_dpp v33, v27, v33 quad_perm:[3,3,3,3] row_mask:0xf bank_mask:0xf\n"
      "v_mfma_f32_16x16x16_bf16 v[20:23], v[150:151], v[38:39], v[20:23]\n"
      "v_add_f32 v32, v32, v33\n"
      "v_exp_f32 v34, v32\n"
      "v_max_f32 v35, v35, v32\n"
      "v_add_f32 v7, v7, v34\n"
      "v_cvt_pk_bf16_f32 v34, v34, v34\n"
      "v_and_b32 v36, v4, v34\n"
      "v_and_b32 v37, v5, v34\n"
      "v_mfma_f32_16x16x32_bf16 v[24:27], v[168:171], v[8:11], 0\n"
      "v_sub_f32_dpp v32, v28, v6 quad_perm:[0,0,0,0] row_mask:0xf bank_mask:0xf\n"
      "v_mov_b32_dpp v33, v30 quad_perm:[2,2,2,2] row_mask:0xf bank_mask:0xf\n"
      "v_mfma_f32_16x16x16_bf16 v[16:19], v[156:157], v[36:37], v[16:19]\n"
      "v_add_f32_dpp v32, v29, v32 quad_perm:[1,1,1,1] row_mask:0xf bank_mask:0xf\n"
      "v_add_f32_dpp v33, v31, v33 quad_perm:[3,3,3,3] row_mask:0xf bank_mask:0xf\n"
      "v_mfma_f32_16x16x16_bf16 v[20:23], v[158:159], v[36:37], v[20:23]\n"
      "v_add_f32 v32, v32, v33\n"
      "v_exp_f32 v34, v32\n"
      "v_max_f32 v35, v35, v32\n"
      "v_add_f32 v7, v7, v34\n"
      "v_cvt_pk_bf16_f32 v34, v34, v34\n"
      "v_and_b32 v38, v4, v34\n"
      "v_and_b32 v39, v5, v34\n"
      "v_mfma_f32_16x16x32_bf16 v[28:31], v[176:179], v[8:11], 0\n"
      "v_sub_f32_dpp v32, v24, v6 quad_perm:[0,0,0,0] row_mask:0xf bank_mask:0xf\n"
      "v_mov_b32_dpp v33, v26 quad_perm:[2,2,2,2] row_mask:0xf bank_mask:0xf\n"
      "v_mfma_f32_16x16x16_bf16 v[16:19], v[164:165], v[38:39], v[16:19]\n"
      "v_add_f32_dpp v32, v25, v32 quad_perm:[1,1,1,1] row_mask:0xf bank_mask:0xf\n"
      "v_add_f32_dpp v33, v27, v33 quad_perm:[3,3,3,3] row_mask:0xf bank_mask:0xf\n"
      "v_mfma_f32_16x16x16_bf16 v[20:23], v[166:167], v[38:39], v[20:23]\n"
      "v_add_f32 v32, v32, v33\n"
      "v_exp_f32 v34, v32\n"
      "v_max_f32 v35, v35, v32\n"
      "v_add_f32 v7, v7, v34\n"
      "v_cvt_pk_bf16_f32 v34, v34, v34\n"
      "v_and_b32 v36, v4, v34\n"
      "v_and_b32 v37, v5, v34\n"
      "s_waitcnt vmcnt(8)\n"
      "s_add_u32 m0, s61, 4096\n"
      "s_nop 0\n"
      "global_load_lds_dwordx4 v1, s[40:41] nt\n"
      "s_add_u32 s40, s40, 110592\n"
      "s_addc_u32 s41, s41, 0\n"
      "v_mfma_f32_16x16x32_bf16 v[24:27], v[184:187], v[8:11], 0\n"
      "v_sub_f32_dpp v32, v28, v6 quad_perm:[0,0,0,0] row_mask:0xf bank_mask:0xf\n"
      "v_mov_b32_dpp v33, v30 quad_perm:[2,2,2,2] row_mask:0xf bank_mask:0xf\n"
      "v_mfma_f32_16x16x16_bf16 v[16:19], v[172:173], v[36:37], v[16:19]\n"
      "v_add_f32_dpp v32, v29, v32 quad_perm:[1,1,1,1] row_mask:0xf bank_mask:0xf\n"
      "v_add_f32_dpp v33, v31, v33 quad_perm:[3,3,3,3] row_mask:0xf bank_mask:0xf\n"
      "v_mfma_f32_16x16x16_bf16 v[20:23], v[174:175], v[36:37], v[20:23]\n"
      "v_add_f32 v32, v32, v33\n"
      "v_exp_f32 v34, v32\n"
      "v_max_f32 v35, v35, v32\n"
      "v_add_f32 v7, v7, v34\n"
      "v_cvt_pk_bf16_f32 v34, v34, v34\n"
      "v_and_b32 v38, v4, v34\n"
      "v_and_b32 v39, v5, v34\n"
      "v_mfma_f32_16x16x32_bf16 v[28:31], v[192:195], v[8:11], 0\n"
      "v_sub_f32_dpp v32, v24, v6 quad_perm:[0,0,0,0] row_mask:0xf bank_mask:0xf\n"
      "v_mov_b32_dpp v33, v26 quad_perm:[2,2,2,2] row_mask:0xf bank_mask:0xf\n"
      "v_mfma_f32_16x16x16_bf16 v[16:19], v[180:181], v[38:39], v[16:19]\n"
      "v_add_f32_dpp v32, v25, v32 quad_perm:[1,1,1,1] row_mask:0xf bank_mask:0xf\n"
      "v_add_f32_dpp v33, v27, v33 quad_perm:[3,3,3,3] row_mask:0xf bank_mask:0xf\n"
      "v_mfma_f32_16x16x16_bf16 v[20:23], v[182:183], v[38:39], v[20:23]\n"
      "v_add_f32 v32, v32, v33\n"
      "v_exp_f32 v34, v32\n"
      "v_max_f32 v35, v35, v32\n"
      "v_add_f32 v7, v7, v34\n"
      "v_cvt_pk_bf16_f32 v34, v34, v34\n"
      "v_and_b32 v36, v4, v34\n"
      "v_and_b32 v37, v5, v34\n"
      "s_waitcnt vmcnt(8)\n"
      "s_add_u32 m0, s61, 5120\n"
      "s_nop 0\n"
      "global_load_lds_dwordx4 v0, s[42:43] nt\n"
      "s_add_u32 s42, s42, 110592\n"
      "s_addc_u32 s43, s43, 0\n"
      "v_mfma_f32_16x16x32_bf16 v[24:27], v[200:203], v[8:11], 0\n"
      "v_sub_f32_dpp v32, v28, v6 quad_perm:[0,0,0,0] row_mask:0xf bank_mask:0xf\n"
      "v_mov_b32_dpp v33, v30 quad_perm:[2,2,2,2] row_mask:0xf bank_mask:0xf\n"
      "v_mfma_f32_16x16x16_bf16 v[16:19], v[188:189], v[36:37], v[16:19]\n"
      "v_add_f32_dpp v32, v29, v32 quad_perm:[1,1,1,1] row_mask:0xf bank_mask:0xf\n"
      "v_add_f32_dpp v33, v31, v33 quad_perm:[3,3,3,3] row_mask:0xf bank_mask:0xf\n"
      "v_mfma_f32_16x16x16_bf16 v[20:23], v[190:191], v[36:37], v[20:23]\n"
      "v_add_f32 v32, v32, v33\n"
      "v_exp_f32 v34, v32\n"
      "v_max_f32 v35, v35, v32\n"
      "v_add_f32 v7, v7, v34\n"
      "v_cvt_pk_bf16_f32 v34, v34, v34\n"
      "v_and_b32 v38, v4, v34\n"
      "v_and_b32 v39, v5, v34\n"
      "v_mfma_f32_16x16x32_bf16 v[28:31], v[208:211], v[8:11], 0\n"
      "v_sub_f32_dpp v32, v24, v6 quad_perm:[0,0,0,0] row_mask:0xf bank_mask:0xf\n"
      "v_mov_b32_dpp v33, v26 quad_perm:[2,2,2,2] row_mask:0xf bank_mask:0xf\n"
      "v_mfma_f32_16x16x16_bf16 v[16:19], v[196:197], v[38:39], v[16:19]\n"
      "v_add_f32_dpp v32, v25, v32 quad_perm:[1,1,1,1] row_mask:0xf bank_mask:0xf\n"
      "v_add_f32_dpp v33, v27, v33 quad_perm:[3,3,3,3] row_mask:0xf bank_mask:0xf\n"
      "v_mfma_f32_16x16x16_bf16 v[20:23], v[198:199], v[38:39], v[20:23]\n"
      "v_add_f32 v32, v32, v33\n"
      "v_exp_f32 v34, v32\n"
      "v_max_f32 v35, v35, v32\n"
      "v_add_f32 v7, v7, v34\n"
      "v_cvt_pk_bf16_f32 v34, v34, v34\n"
      "v_and_b32 v36, v4, v34\n"
      "v_and_b32 v37, v5, v34\n"
      "s_waitcnt vmcnt(8)\n"
      "s_add_u32 m0, s61, 6144\n"
      "s_nop 0\n"
      "global_load_lds_dwordx4 v1, s[40:41] nt\n"
      "s_add_u32 s40, s40, 110592\n"
      "s_addc_u32 s41, s41, 0\n"
      "v_mfma_f32_16x16x32_bf16 v[24:27], v[216:219], v[8:11], 0\n"
      "v_sub_f32_dpp v32, v28, v6 quad_perm:[0,0,0,0] row_mask:0xf bank_mask:0xf\n"
      "v_mov_b32_dpp v33, v30 quad_perm:[2,2,2,2] row_mask:0xf bank_mask:0xf\n"
      "v_mfma_f32_16x16x16_bf16 v[16:19], v[204:205], v[36:37], v[16:19]\n"
      "v_add_f32_dpp v32, v29, v32 quad_perm:[1,1,1,1] row_mask:0xf bank_mask:0xf\n"
      "v_add_f32_dpp v33, v31, v33 quad_perm:[3,3,3,3] row_mask:0xf bank_mask:0xf\n"
      "v_mfma_f32_16x16x16_bf16 v[20:23], v[206:207], v[36:37], v[20:23]\n"
      "v_add_f32 v32, v32, v33\n"
      "v_exp_f32 v34, v32\n"
      "v_max_f32 v35, v35, v32\n"
      "v_add_f32 v7, v7, v34\n"
      "v_cvt_pk_bf16_f32 v34, v34, v34\n"
      "v_and_b32 v38, v4, v34\n"
      "v_and_b32 v39, v5, v34\n"
      "v_mfma_f32_16x16x32_bf16 v[28:31], v[224:227], v[8:11], 0\n"
      "v_sub_f32_dpp v32, v24, v6 quad_perm:[0,0,0,0] row_mask:0xf bank_mask:0xf\n"
      "v_mov_b32_dpp v33, v26 quad_perm:[2,2,2,2] row_mask:0xf bank_mask:0xf\n"
      "v_mfma_f32_16x16x16_bf16 v[16:19], v[212:213], v[38:39], v[16:19]\n"
      "v_add_f32_dpp v32, v25, v32 quad_perm:[1,1,1,1] row_mask:0xf bank_mask:0xf\n"
      "v_add_f32_dpp v33, v27, v33 quad_perm:[3,3,3,3] row_mask:0xf bank_mask:0xf\n"
      "v_mfma_f32_16x16x16_bf16 v[20:23], v[214:215], v[38:39], v[20:23]\n"
      "v_add_f32 v32, v32, v33\n"
      "v_exp_f32 v34, v32\n"
      "v_max_f32 v35, v35, v32\n"
      "v_add_f32 v7, v7, v34\n"
      "v_cvt_pk_bf16_f32 v34, v34, v34\n"
      "v_and_b32 v36, v4, v34\n"
      "v_and_b32 v37, v5, v34\n"
      "v_mfma_f32_16x16x32_bf16 v[24:27], v[232:235], v[8:11], 0\n"
      "v_sub_f32_dpp v32, v28, v6 quad_perm:[0,0,0,0] row_mask:0xf bank_mask:0xf\n"
      "v_mov_b32_dpp v33, v30 quad_perm:[2,2,2,2] row_mask:0xf bank_mask:0xf\n"
      "v_mfma_f32_16x16x16_bf16 v[16:19], v[220:221], v[36:37], v[16:19]\n"
      "v_add_f32_dpp v32, v29, v32 quad_perm:[1,1,1,1] row_mask:0xf bank_mask:0xf\n"
      "v_add_f32_dpp v33, v31, v33 quad_perm:[3,3,3,3] row_mask:0xf bank_mask:0xf\n"
      "v_mfma_f32_16x16x16_bf16 v[20:23], v[222:223], v[36:37], v[20:23]\n"
      "v_add_f32 v32, v32, v33\n"
      "v_exp_f32 v34, v32\n"
      "v_max_f32 v35, v35, v32\n"
      "v_add_f32 v7, v7, v34\n"
      "v_cvt_pk_bf16_f32 v34, v34, v34\n"
      "v_and_b32 v38, v4, v34\n"
      "v_and_b32 v39, v5, v34\n"
      "s_waitcnt vmcnt(8)\n"
      "s_add_u32 m0, s61, 7168\n"
      "s_nop 0\n"
      "global_load_lds_dwordx4 v0, s[42:43] nt\n"
      "s_add_u32 s42, s42, 110592\n"
      "s_addc_u32 s43, s43, 0\n"
      "v_mfma_f32_16x16x32_bf16 v[28:31], v[240:243], v[8:11], 0\n"
      "v_sub_f32_dpp v32, v24, v6 quad_perm:[0,0,0,0] row_mask:0xf bank_mask:0xf\n"
      "v_mov_b32_dpp v33, v26 quad_perm:[2,2,2,2] row_mask:0xf bank_mask:0xf\n"
      "v_mfma_f32_16x16x16_bf16 v[16:19], v[228:229], v[38:39], v[16:19]\n"
      "v_add_f32_dpp v32, v25, v32 quad_perm:[1,1,1,1] row_mask:0xf bank_mask:0xf\n"
      "v_add_f32_dpp v33, v27, v33 quad_perm:[3,3,3,3] row_mask:0xf bank_mask:0xf\n"
      "v_mfma_f32_16x16x16_bf16 v[20:23], v[230:231], v[38:39], v[20:23]\n"
      "v_add_f32 v32, v32, v33\n"
      "v_exp_f32 v34, v32\n"
      "v_max_f32 v35, v35, v32\n"
      "v_add_f32 v7, v7, v34\n"
      "v_cvt_pk_bf16_f32 v34, v34, v34\n"
      "v_and_b32 v36, v4, v34\n"
      "v_and_b32 v37, v5, v34\n"
      "v_mfma_f32_16x16x32_bf16 v[24:27], v[248:251], v[8:11], 0\n"
      "v_sub_f32_dpp v32, v28, v6 quad_perm:[0,0,0,0] row_mask:0xf bank_mask:0xf\n"
      "v_mov_b32_dpp v33, v30 quad_perm:[2,2,2,2] row_mask:0xf bank_mask:0xf\n"
      "v_mfma_f32_16x16x16_bf16 v[16:19], v[236:237], v[36:37], v[16:19]\n"
      "v_add_f32_dpp v32, v29, v32 quad_perm:[1,1,1,1] row_mask:0xf bank_mask:0xf\n"
      "v_add_f32_dpp v33, v31, v33 quad_perm:[3,3,3,3] row_mask:0xf bank_mask:0xf\n"
      "v_mfma_f32_16x16x16_bf16 v[20:23], v[238:239], v[36:37], v[20:23]\n"
      "v_add_f32 v32, v32, v33\n"
      "v_exp_f32 v34, v32\n"
      "v_max_f32 v35, v35, v32\n"
      "v_add_f32 v7, v7, v34\n"
      "v_cvt_pk_bf16_f32 v34, v34, v34\n"
      "v_and_b32 v38, v4, v34\n"
      "v_and_b32 v39, v5, v34\n"
      "s_waitcnt vmcnt(8)\n"
      "s_add_u32 m0, s61, 8192\n"
      "s_nop 0\n"
      "global_load_lds_dwordx4 v1, s[40:41] nt\n"
      "s_add_u32 s40, s40, 110592\n"
      "s_addc_u32 s41, s41, 0\n"
      "v_sub_f32_dpp v32, v24, v6 quad_perm:[0,0,0,0] row_mask:0xf bank_mask:0xf\n"
      "v_mov_b32_dpp v33, v26 quad_perm:[2,2,2,2] row_mask:0xf bank_mask:0xf\n"
      "v_mfma_f32_16x16x16_bf16 v[16:19], v[244:245], v[38:39], v[16:19]\n"
      "v_add_f32_dpp v32, v25, v32 quad_perm:[1,1,1,1] row_mask:0xf bank_mask:0xf\n"
      "v_add_f32_dpp v33, v27, v33 quad_perm:[3,3,3,3] row_mask:0xf bank_mask:0xf\n"
      "v_mfma_f32_16x16x16_bf16 v[20:23], v[246:247], v[38:39], v[20:23]\n"
      "v_add_f32 v32, v32, v33\n"
      "v_add_f32 v32, s69, v32\n"
      "v_exp_f32 v34, v32\n"
      "v_max_f32 v35, v35, v32\n"
      "v_add_f32 v7, v7, v34\n"
      "v_cvt_pk_bf16_f32 v34, v34, v34\n"
      "v_and_b32 v36, v4, v34\n"
      "v_and_b32 v37, v5, v34\n"
      "s_nop 1\n"
      "v_mfma_f32_16x16x16_bf16 v[16:19], v[252:253], v[36:37], v[16:19]\n"
      "v_mfma_f32_16x16x16_bf16 v[20:23], v[254:255], v[36:37], v[20:23]\n"
      "s_waitcnt vmcnt(8)\n"
      "s_add_u32 m0, s61, 9216\n"
      "s_nop 0\n"
      "global_load_lds_dwordx4 v0, s[42:43] nt\n"
      "s_add_u32 s42, s42, 110592\n"
      "s_addc_u32 s43, s43, 0\n"
      "v_cmp_lt_f32 vcc, 0x42800000, v35\n"
      "s_nop 0\n"
      "s_cbranch_vccnz L_fb4_%=\n"
      "L_ret4_%=:\n"
      "v_mov_b32 v49, v7\n"
      "v_lshlrev_b32 v41, 2, v0\n"
      "v_and_b32 v41, 0x300, v41\n"
      "v_permlane32_swap_b32 v49, v7\n"
      "v_lshrrev_b32 v42, 4, v0\n"
      "v_add_f32 v7, v49, v7\n"
      "v_and_b32 v42, 0x30, v42\n"
      "v_mov_b32 v49, v7\n"
      "v_add_u32 v41, v41, v42\n"
      "v_lshrrev_b32 v40, 2, v0\n"
      "v_permlane16_swap_b32 v49, v7\n"
      "v_and_b32 v42, 12, v40\n"
      "v_add_f32 v7, v49, v7\n"
      "v_add_u32 v41, v41, v42\n"
      "s_lshl_b32 s62, s60, 10\n"
      "s_add_u32 s62, s62, 155648\n"
      "v_add_u32 v41, s62, v41\n"
      "v_lshrrev_b32 v43, 3, v0\n"
      "v_and_b32 v43, 24, v43\n"
      "s_lshl_b32 s62, s60, 5\n"
      "s_add_u32 s62, s62, 159744\n"
      "v_add_u32 v43, s62, v43\n"
      "v_cvt_pk_bf16_f32 v36, v16, v17\n"
      "v_cvt_pk_bf16_f32 v37, v18, v19\n"
      "v_cvt_pk_bf16_f32 v38, v20, v21\n"
      "v_cvt_pk_bf16_f32 v39, v22, v23\n"
      "s_mov_b64 exec, 0x1111\n"
      "ds_write_b64 v43, v[6:7]\n"
      "s_mov_b64 exec, -1\n"
      "ds_write_b32 v41, v36 offset:0\n"
      "ds_write_b32 v41, v37 offset:64\n"
      "ds_write_b32 v41, v38 offset:128\n"
      "ds_write_b32 v41, v39 offset:192\n"
      "s_lshl_b32 s62, s60, 8\n"
      "s_add_u32 s62, s62, 155648\n"
      "v_add_u32 v42, s62, v40\n"
      "s_lshl_b32 s62, s60, 3\n"
      "s_add_u32 s62, s62, 159744\n"
      "v_mov_b32 v50, s62\n"
      "s_waitcnt lgkmcnt(0)\n"
      "s_barrier\n"
      "ds_read_b64 v[28:29], v50 offset:0\n"
      "ds_read_b64 v[30:31], v50 offset:32\n"
      "ds_read_b64 v[32:33], v50 offset:64\n"
      "ds_read_b64 v[34:35], v50 offset:96\n"
      "ds_read_b32 v24, v42 offset:0\n"
      "ds_read_b32 v25, v42 offset:1024\n"
      "ds_read_b32 v26, v42 offset:2048\n"
      "ds_read_b32 v27, v42 offset:3072\n"
      "s_add_u32 s56, s44, 995328\n"
      "s_addc_u32 s57, s45, 0\n"
      "s_waitcnt lgkmcnt(4)\n"
      "v_max3_f32 v43, v28, v30, v32\n"
      "v_max_f32 v43, v43, v34\n"
      "v_sub_f32 v28, v28, v43\n"
      "v_sub_f32 v30, v30, v43\n"
      "v_sub_f32 v32, v32, v43\n"
      "v_sub_f32 v34, v34, v43\n"
      "v_exp_f32 v28, v28\n"
      "v_exp_f32 v30, v30\n"
      "v_exp_f32 v32, v32\n"
      "v_exp_f32 v34, v34\n"
      "s_waitcnt lgkmcnt(0)\n"
      "v_lshlrev_b32 v44, 16, v24\n"
      "v_and_b32 v45, 0xffff0000, v24\n"
      "v_lshlrev_b32 v46, 16, v25\n"
      "v_and_b32 v47, 0xffff0000, v25\n"
      "v_lshlrev_b32 v48, 16, v26\n"
      "v_and_b32 v49, 0xffff0000, v26\n"
      "v_lshlrev_b32 v50, 16, v27\n"
      "v_and_b32 v51, 0xffff0000, v27\n"
      "v_mul_f32 v38, v28, v29\n"
      "v_pk_mul_f32 v[36:37], v[44:45], v[28:29] op_sel_hi:[1,0]\n"
      "v_fmac_f32 v38, v30, v31\n"
      "v_pk_fma_f32 v[36:37], v[46:47], v[30:31], v[36:37] op_sel_hi:[1,0,1]\n"
      "v_fmac_f32 v38, v32, v33\n"
      "v_pk_fma_f32 v[36:37], v[48:49], v[32:33], v[36:37] op_sel_hi:[1,0,1]\n"
      "v_fmac_f32 v38, v34, v35\n"
      "v_pk_fma_f32 v[36:37], v[50:51], v[34:35], v[36:37] op_sel_hi:[1,0,1]\n"
      "v_rcp_f32 v39, v38\n"
      "v_log_f32 v38, v38\n"
      "v_mul_f32 v36, v39, v36\n"
      "v_mul_f32 v37, v39, v37\n"
      "v_add_f32 v38, v38, v43\n"
      "v_cvt_pk_bf16_f32 v36, v36, v37\n"
      "v_min_u32 v38, -2, v38\n"
      "v_min_u32 v36, -2, v36\n"
      "global_store_dword v40, v36, s[56:57] sc1\n"
      "s_mov_b32 exec_hi, 0\n"
      "global_store_dword v40, v38, s[56:57] offset:256 sc1\n"
      "s_mov_b64 exec, -1\n"
      "s_add_u32 s56, s44, 2654208\n"
      "s_addc_u32 s57, s45, 0\n"
      "global_store_dword v40, v12, s[56:57] sc1\n"
      "s_mov_b32 exec_hi, 0\n"
      "global_store_dword v40, v13, s[56:57] offset:256 sc1\n"
      "s_mov_b64 exec, -1\n"
      "s_waitcnt vmcnt(8)\n"
      "s_add_u32 m0, s61, 10240\n"
      "s_nop 0\n"
      "global_load_lds_dwordx4 v1, s[40:41] nt\n"
      "s_add_u32 s40, s40, 110592\n"
      "s_addc_u32 s41, s41, 0\n"
      "s_waitcnt vmcnt(8)\n"
      "s_add_u32 m0, s61, 11264\n"
      "s_nop 0\n"
      "global_load_lds_dwordx4 v0, s[42:43] nt\n"
      "s_add_u32 s42, s42, 110592\n"
      "s_addc_u32 s43, s43, 0\n"
      "s_waitcnt vmcnt(8)\n"
      "s_add_u32 m0, s61, 12288\n"
      "s_nop 0\n"
      "global_load_lds_dwordx4 v1, s[40:41] nt\n"
      "s_add_u32 s40, s40, 110592\n"
      "s_addc_u32 s41, s41, 0\n"
      "s_waitcnt vmcnt(8)\n"
      "s_add_u32 m0, s61, 13312\n"
      "s_nop 0\n"
      "global_load_lds_dwordx4 v0, s[42:43] nt\n"
      "s_add_u32 s42, s42, 110592\n"
      "s_addc_u32 s43, s43, 0\n"
      "s_barrier\n"
      "L_per4_%=:\n"
      "s_add_u32 s56, s87, L_per5_%=-L_begin_%=\n"
      "s_min_u32 s56, s56, L_end_%=-L_begin_%=-1024\n"
      "s_add_u32 s56, s84, s56\n"
      "s_addc_u32 s57, s85, 0\n"
      "s_mov_b32 m0, s86\n"
      "s_nop 0\n"
      "global_load_lds_dword v0, s[56:57]\n"
      "s_waitcnt vmcnt(9)\n"
      "s_add_u32 m0, s61, 14336\n"
      "s_nop 0\n"
      "global_load_lds_dwordx4 v1, s[40:41] nt\n"
      "s_add_u32 s40, s40, 110592\n"
      "s_addc_u32 s41, s41, 0\n"
      "s_waitcnt vmcnt(9)\n"
      "s_add_u32 m0, s61, 15360\n"
      "s_nop 0\n"
      "global_load_lds_dwordx4 v0, s[42:43] nt\n"
      "s_add_u32 s42, s42, 110592\n"
      "s_addc_u32 s43, s43, 0\n"
      "s_waitcnt vmcnt(9)\n"
      "s_add_u32 m0, s61, 16384\n"
      "s_nop 0\n"
      "global_load_lds_dwordx4 v1, s[40:41] nt\n"
      "s_add_u32 s40, s40, 110592\n"
      "s_addc_u32 s41, s41, 0\n"
      "s_waitcnt vmcnt(9)\n"
      "s_add_u32 m0, s61, 17408\n"
      "s_nop 0\n"
      "global_load_lds_dwordx4 v0, s[42:43] nt\n"
      "s_add_u32 s42, s42, 110592\n"
      "s_addc_u32 s43, s43, 0\n"
      "s_waitcnt vmcnt(9)\n"
      "s_add_u32 m0, s61, 18432\n"
      "s_nop 0\n"
      "global_load_lds_dwordx4 v1, s[40:41] nt\n"
      "s_add_u32 s40, s40, 110592\n"
      "s_addc_u32 s41, s41, 0\n"
      "s_add_u32 s56, s48, 32768\n"
      "s_addc_u32 s57, s49, 0\n"
      "s_mov_b64 exec, 0xffff\n"
      "global_load_dwordx4 v[8:11], v0, s[56:57] sc1\n"
      "s_mov_b64 exec, -1\n"
      "s_add_u32 s56, s48, 73728\n"
      "s_addc_u32 s57, s49, 0\n"
      "s_mov_b64 exec, 0xffff\n"
      "global_load_dwordx4 v[12:15], v0, s[56:57] sc1\n"
      "s_mov_b64 exec, -1\n"
      "s_waitcnt vmcnt(11)\n"
      "s_add_u32 m0, s61, 19456\n"
      "s_nop 0\n"
      "global_load_lds_dwordx4 v0, s[42:43] nt\n"
      "s_add_u32 s42, s42, 110592\n"
      "s_addc_u32 s43, s43, 0\n"
      "s_waitcnt vmcnt(11)\n"
      "s_add_u32 m0, s61, 20480\n"
      "s_nop 0\n"
      "global_load_lds_dwordx4 v1, s[40:41] nt\n"
      "s_add_u32 s40, s40, 110592\n"
      "s_addc_u32 s41, s41, 0\n"
      "s_waitcnt vmcnt(11)\n"
      "s_add_u32 m0, s61, 21504\n"
      "s_nop 0\n"
      "global_load_lds_dwordx4 v0, s[42:43] nt\n"
      "s_add_u32 s42, s42, 110592\n"
      "s_addc_u32 s43, s43, 0\n"
      "s_waitcnt vmcnt(11)\n"
      "s_add_u32 m0, s61, 22528\n"
      "s_nop 0\n"
      "global_load_lds_dwordx4 v1, s[40:41] nt\n"
      "s_add_u32 s40, s40, 110592\n"
      "s_addc_u32 s41, s41, 0\n"
      "s_waitcnt vmcnt(10)\n"
      "s_add_u32 m0, s61, 23552\n"
      "s_nop 0\n"
      "global_load_lds_dwordx4 v0, s[42:43] nt\n"
      "s_add_u32 s42, s42, 110592\n"
      "s_addc_u32 s43, s43, 0\n"
      "s_waitcnt vmcnt(10)\n"
      "s_add_u32 m0, s61, 24576\n"
      "s_nop 0\n"
      "global_load_lds_dwordx4 v1, s[40:41] nt\n"
      "s_add_u32 s40, s40, 110592\n"
      "s_addc_u32 s41, s41, 0\n"
      "s_waitcnt vmcnt(10)\n"
      "s_add_u32 m0, s61, 25600\n"
      "s_nop 0\n"
      "global_load_lds_dwordx4 v0, s[42:43] nt\n"
      "s_add_u32 s42, s42, 110592\n"
      "s_addc_u32 s43, s43, 0\n"
      "s_waitcnt vmcnt(10)\n"
      "s_add_u32 m0, s61, 26624\n"
      "s_nop 0\n"
      "global_load_lds_dwordx4 v1, s[40:41] nt\n"
      "s_add_u32 s40, s40, 110592\n"
      "s_addc_u32 s41, s41, 0\n"
      "s_waitcnt vmcnt(10)\n"
      "s_add_u32 m0, s61, 27648\n"
      "s_nop 0\n"
      "global_load_lds_dwordx4 v0, s[42:43] nt\n"
      "s_add_u32 s42, s42, 110592\n"
      "s_addc_u32 s43, s43, 0\n"
      "s_waitcnt vmcnt(9)\n"
      "s_mov_b64 exec, 0xffff\n"
      "s_mov_b32 s53, 0\n"
      "L_qc13_%=:\n"
      "v_max3_u32 v49, v12, v13, v14\n"
      "v_max_u32 v49, v49, v15\n"
      "v_cmp_eq_u32 vcc, -1, v49\n"
      "s_nop 0\n"
      "s_cbranch_vccz L_qo14_%=\n"
      "s_cmp_lg_u32 s54, 0\n"
      "s_cbranch_scc1 L_qo14_%=\n"
      "s_add_u32 s56, s48, 73728\n"
      "s_addc_u32 s57, s49, 0\n"
      "global_load_dwordx4 v[12:15], v0, s[56:57] sc1\n"
      "s_add_u32 s53, s53, 1\n"
      "s_waitcnt vmcnt(0)\n"
      "s_cmp_lt_u32 s53, 30000\n"
      "s_cbranch_scc1 L_qc13_%=\n"
      "s_mov_b32 s54, 201\n"
      "v_mov_b32 v49, 201\n"
      "v_mov_b32 v50, 256\n"
      "global_store_dword v50, v49, s[58:59] sc1\n"
      "L_qo14_%=:\n"
      "s_lshl_b32 s62, s60, 8\n"
      "s_add_u32 s62, s62, 160896\n"
      "v_add_u32 v49, s62, v0\n"
      "ds_write_b128 v49, v[12:15]\n"
      "s_mov_b64 exec, -1\n"
      "s_waitcnt lgkmcnt(0)\n"
      "s_barrier\n"
      "ds_read_b128 v[12:15], v2 offset:1024\n"
      "s_waitcnt lgkmcnt(0)\n"
      "v_mfma_f32_16x16x32_bf16 v[24:27], a[0:3], v[12:15], 0\n"
      "v_mfma_f32_16x16x32_bf16 v[28:31], a[8:11], v[12:15], 0\n"
      "s_nop 6\n"
      "v_mov_b32_dpp v34, v24 quad_perm:[0,0,0,0] row_mask:0xf bank_mask:0xf\n"
      "v_mov_b32_dpp v33, v26 quad_perm:[2,2,2,2] row_mask:0xf bank_mask:0xf\n"
      "s_nop 0\n"
      "v_add_f32_dpp v34, v25, v34 quad_perm:[1,1,1,1] row_mask:0xf bank_mask:0xf\n"
      "v_add_f32_dpp v33, v27, v33 quad_perm:[3,3,3,3] row_mask:0xf bank_mask:0xf\n"
      "v_add_f32 v34, v34, v33\n"
      "v_mov_b32_dpp v32, v28 quad_perm:[0,0,0,0] row_mask:0xf bank_mask:0xf\n"
      "v_mov_b32_dpp v33, v30 quad_perm:[2,2,2,2] row_mask:0xf bank_mask:0xf\n"
      "s_nop 0\n"
      "v_add_f32_dpp v32, v29, v32 quad_perm:[1,1,1,1] row_mask:0xf bank_mask:0xf\n"
      "v_add_f32_dpp v33, v31, v33 quad_perm:[3,3,3,3] row_mask:0xf bank_mask:0xf\n"
      "v_add_f32 v32, v32, v33\n"
      "v_max_f32 v35, v34, v32\n"
      "v_mov_b32 v33, v35\n"
      "s_nop 1\n"
      "v_permlane32_swap_b32 v33, v35\n"
      "v_max_f32 v35, v33, v35\n"
      "v_mov_b32 v33, v35\n"
      "s_nop 1\n"
      "v_permlane16_swap_b32 v33, v35\n"
      "v_max_f32 v6, v33, v35\n"
      "v_sub_f32 v34, v34, v6\n"
      "v_sub_f32 v32, v32, v6\n"
      "v_mov_b32 v35, 0\n"
      "v_exp_f32 v34, v34\n"
      "s_nop 0\n"
      "v_mov_b32 v7, v34\n"
      "v_cvt_pk_bf16_f32 v34, v34, v34\n"
      "v_and_b32 v36, v4, v34\n"
      "v_and_b32 v37, v5, v34\n"
      "v_mfma_f32_16x16x32_bf16 v[24:27], a[16:19], v[12:15], 0\n"
      "s_nop 0\n"
      "v_mfma_f32_16x16x16_bf16 v[16:19], a[4:5], v[36:37], 0\n"
      "v_mfma_f32_16x16x16_bf16 v[20:23], a[6:7], v[36:37], 0\n"
      "v_exp_f32 v34, v32\n"
      "v_max_f32 v35, v35, v32\n"
      "v_add_f32 v7, v7, v34\n"
      "v_cvt_pk_bf16_f32 v34, v34, v34\n"
      "v_and_b32 v38, v4, v34\n"
      "v_and_b32 v39, v5, v34\n"
      "v_mfma_f32_16x16x32_bf16 v[28:31], a[24:27], v[12:15], 0\n"
      "v_sub_f32_dpp v32, v24, v6 quad_perm:[0,0,0,0] row_mask:0xf bank_mask:0xf\n"
      "v_mov_b32_dpp v33, v26 quad_perm:[2,2,2,2] row_mask:0xf bank_mask:0xf\n"
      "v_mfma_f32_16x16x16_bf16 v[16:19], a[12:13], v[38:39], v[16:19]\n"
      "v_add_f32_dpp v32, v25, v32 quad_perm:[1,1,1,1] row_mask:0xf bank_mask:0xf\n"
      "v_add_f32_dpp v33, v27, v33 quad_perm:[3,3,3,3] row_mask:0xf bank_mask:0xf\n"
      "v_mfma_f32_16x16x16_bf16 v[20:23], a[14:15], v[38:39], v[20:23]\n"
      "v_add_f32 v32, v32, v33\n"
      "v_exp_f32 v34, v32\n"
      "v_max_f32 v35, v35, v32\n"
      "v_add_f32 v7, v7, v34\n"
      "v_cvt_pk_bf16_f32 v34, v34, v34\n"
      "v_and_b32 v36, v4, v34\n"
      "v_and_b32 v37, v5, v34\n"
      "v_mfma_f32_16x16x32_bf16 v[24:27], a[32:35], v[12:15], 0\n"
      "v_sub_f32_dpp v32, v28, v6 quad_perm:[0,0,0,0] row_mask:0xf bank_mask:0xf\n"
      "v_mov_b32_dpp v33, v30 quad_perm:[2,2,2,2] row_mask:0xf bank_mask:0xf\n"
      "v_mfma_f32_16x16x16_bf16 v[16:19], a[20:21], v[36:37], v[16:19]\n"
      "v_add_f32_dpp v32, v29, v32 quad_perm:[1,1,1,1] row_mask:0xf bank_mask:0xf\n"
      "v_add_f32_dpp v33, v31, v33 quad_perm:[3,3,3,3] row_mask:0xf bank_mask:0xf\n"
      "v_mfma_f32_16x16x16_bf16 v[20:23], a[22:23], v[36:37], v[20:23]\n"
      "v_add_f32 v32, v32, v33\n"
      "v_exp_f32 v34, v32\n"
      "v_max_f32 v35, v35, v32\n"
      "v_add_f32 v7, v7, v34\n"
      "v_cvt_pk_bf16_f32 v34, v34, v34\n"
      "v_and_b32 v38, v4, v34\n"
      "v_and_b32 v39, v5, v34\n"
      "s_waitcnt vmcnt(8)\n"
      "s_add_u32 m0, s61, 28672\n"
      "s_nop 0\n"
      "global_load_lds_dwordx4 v1, s[40:41] nt\n"
      "s_add_u32 s40, s40, 110592\n"
      "s_addc_u32 s41, s41, 0\n"
      "v_mfma_f32_16x16x32_bf16 v[28:31], a[40:43], v[12:15], 0\n"
      "v_sub_f32_dpp v32, v24, v6 quad_perm:[0,0,0,0] row_mask:0xf bank_mask:0xf\n"
      "v_mov_b32_dpp v33, v26 quad_perm:[2,2,2,2] row_mask:0xf bank_mask:0xf\n"
      "v_mfma_f32_16x16x16_bf16 v[16:19], a[28:29], v[38:39], v[16:19]\n"
      "v_add_f32_dpp v32, v25, v32 quad_perm:[1,1,1,1] row_mask:0xf bank_mask:0xf\n"
      "v_add_f32_dpp v33, v27, v33 quad_perm:[3,3,3,3] row_mask:0xf bank_mask:0xf\n"
      "v_mfma_f32_16x16x16_bf16 v[20:23], a[30:31], v[38:39], v[20:23]\n"
      "v_add_f32 v32, v32, v33\n"
      "v_exp_f32 v34, v32\n"
      "v_max_f32 v35, v35, v32\n"
      "v_add_f32 v7, v7, v34\n"
      "v_cvt_pk_bf16_f32 v34, v34, v34\n"
      "v_and_b32 v36, v4, v34\n"
      "v_and_b32 v37, v5, v34\n"
      "v_mfma_f32_16x16x32_bf16 v[24:27], a[48:51], v[12:15], 0\n"
      "v_sub_f32_dpp v32, v28, v6 quad_perm:[0,0,0,0] row_mask:0xf bank_mask:0xf\n"
      "v_mov_b32_dpp v33, v30 quad_perm:[2,2,2,2] row_mask:0xf bank_mask:0xf\n"
      "v_mfma_f32_16x16x16_bf16 v[16:19], a[36:37], v[36:37], v[16:19]\n"
      "v_add_f32_dpp v32, v29, v32 quad_perm:[1,1,1,1] row_mask:0xf bank_mask:0xf\n"
      "v_add_f32_dpp v33, v31, v33 quad_perm:[3,3,3,3] row_mask:0xf bank_mask:0xf\n"
      "v_mfma_f32_16x16x16_bf16 v[20:23], a[38:39], v[36:37], v[20:23]\n"
      "v_add_f32 v32, v32, v33\n"
      "v_exp_f32 v34, v32\n"
      "v_max_f32 v35, v35, v32\n"
      "v_add_f32 v7, v7, v34\n"
      "v_cvt_pk_bf16_f32 v34, v34, v34\n"
      "v_and_b32 v38, v4, v34\n"
      "v_and_b32 v39, v5, v34\n"
      "s_waitcnt vmcnt(8)\n"
      "s_add_u32 m0, s61, 29696\n"
      "s_nop 0\n"
      "global_load_lds_dwordx4 v0, s[42:43] nt\n"
      "s_add_u32 s42, s42, 110592\n"
      "s_addc_u32 s43, s43, 0\n"
      "v_mfma_f32_16x16x32_bf16 v[28:31], a[56:59], v[12:15], 0\n"
      "v_sub_f32_dpp v32, v24, v6 quad_perm:[0,0,0,0] row_mask:0xf bank_mask:0xf\n"
      "v_mov_b32_dpp v33, v26 quad_perm:[2,2,2,2] row_mask:0xf bank_mask:0xf\n"
      "v_mfma_f32_16x16x16_bf16 v[16:19], a[44:45], v[38:39], v[16:19]\n"
      "v_add_f32_dpp v32, v25, v32 quad_perm:[1,1,1,1] row_mask:0xf bank_mask:0xf\n"
      "v_add_f32_dpp v33, v27, v33 quad_perm:[3,3,3,3] row_mask:0xf bank_mask:0xf\n"
      "v_mfma_f32_16x16x16_bf16 v[20:23], a[46:47], v[38:39], v[20:23]\n"
      "v_add_f32 v32, v32, v33\n"
      "v_exp_f32 v34, v32\n"
      "v_max_f32 v35, v35, v32\n"
      "v_add_f32 v7, v7, v34\n"
      "v_cvt_pk_bf16_f32 v34, v34, v34\n"
      "v_and_b32 v36, v4, v34\n"
      "v_and_b32 v37, v5, v34\n"
      "v_mfma_f32_16x16x32_bf16 v[24:27], a[64:67], v[12:15], 0\n"
      "v_sub_f32_dpp v32, v28, v6 quad_perm:[0,0,0,0] row_mask:0xf bank_mask:0xf\n"
      "v_mov_b32_dpp v33, v30 quad_perm:[2,2,2,2] row_mask:0xf bank_mask:0xf\n"
      "v_mfma_f32_16x16x16_bf16 v[16:19], a[52:53], v[36:37], v[16:19]\n"
      "v_add_f32_dpp v32, v29, v32 quad_perm:[1,1,1,1] row_mask:0xf bank_mask:0xf\n"
      "v_add_f32_dpp v33, v31, v33 quad_perm:[3,3,3,3] row_mask:0xf bank_mask:0xf\n"
      "v_mfma_f32_16x16x16_bf16 v[20:23], a[54:55], v[36:37], v[20:23]\n"
      "v_add_f32 v32, v32, v33\n"
      "v_exp_f32 v34, v32\n"
      "v_max_f32 v35, v35, v32\n"
      "v_add_f32 v7, v7, v34\n"
      "v_cvt_pk_bf16_f32 v34, v34, v34\n"
      "v_and_b32 v38, v4, v34\n"
      "v_and_b32 v39, v5, v34\n"
      "v_mfma_f32_16x16x32_bf16 v[28:31], a[72:75], v[12:15], 0\n"
      "v_sub_f32_dpp v32, v24, v6 quad_perm:[0,0,0,0] row_mask:0xf bank_mask:0xf\n"
      "v_mov_b32_dpp v33, v26 quad_perm:[2,2,2,2] row_mask:0xf bank_mask:0xf\n"
      "v_mfma_f32_16x16x16_bf16 v[16:19], a[60:61], v[38:39], v[16:19]\n"
      "v_add_f32_dpp v32, v25, v32 quad_perm:[1,1,1,1] row_mask:0xf bank_mask:0xf\n"
      "v_add_f32_dpp v33, v27, v33 quad_perm:[3,3,3,3] row_mask:0xf bank_mask:0xf\n"
      "v_mfma_f32_16x16x16_bf16 v[20:23], a[62:63], v[38:39], v[20:23]\n"
      "v_add_f32 v32, v32, v33\n"
      "v_exp_f32 v34, v32\n"
      "v_max_f32 v35, v35, v32\n"
      "v_add_f32 v7, v7, v34\n"
      "v_cvt_pk_bf16_f32 v34, v34, v34\n"
      "v_and_b32 v36, v4, v34\n"
      "v_and_b32 v37, v5, v34\n"
      "s_waitcnt vmcnt(8)\n"
      "s_add_u32 m0, s61, 30720\n"
      "s_nop 0\n"
      "global_load_lds_dwordx4 v1, s[40:41] nt\n"
      "s_add_u32 s40, s40, 110592\n"
      "s_addc_u32 s41, s41, 0\n"
      "v_mfma_f32_16x16x32_bf16 v[24:27], a[80:83], v[12:15], 0\n"
      "v_sub_f32_dpp v32, v28, v6 quad_perm:[0,0,0,0] row_mask:0xf bank_mask:0xf\n"
      "v_mov_b32_dpp v33, v30 quad_perm:[2,2,2,2] row_mask:0xf bank_mask:0xf\n"
      "v_mfma_f32_16x16x16_bf16 v[16:19], a[68:69], v[36:37], v[16:19]\n"
      "v_add_f32_dpp v32, v29, v32 quad_perm:[1,1,1,1] row_mask:0xf bank_mask:0xf\n"
      "v_add_f32_dpp v33, v31, v33 quad_perm:[3,3,3,3] row_mask:0xf bank_mask:0xf\n"
      "v_mfma_f32_16x16x16_bf16 v[20:23], a[70:71], v[36:37], v[20:23]\n"
      "v_add_f32 v32, v32, v33\n"
      "v_exp_f32 v34, v32\n"
      "v_max_f32 v35, v35, v32\n"
      "v_add_f32 v7, v7, v34\n"
      "v_cvt_pk_bf16_f32 v34, v34, v34\n"
      "v_and_b32 v38, v4, v34\n"
      "v_and_b32 v39, v5, v34\n"
      "v_mfma_f32_16x16x32_bf16 v[28:31], a[88:91], v[12:15], 0\n"
      "v_sub_f32_dpp v32, v24, v6 quad_perm:[0,0,0,0] row_mask:0xf bank_mask:0xf\n"
      "v_mov_b32_dpp v33, v26 quad_perm:[2,2,2,2] row_mask:0xf bank_mask:0xf\n"
      "v_mfma_f32_16x16x16_bf16 v[16:19], a[76:77], v[38:39], v[16:19]\n"
      "v_add_f32_dpp v32, v25, v32 quad_perm:[1,1,1,1] row_mask:0xf bank_mask:0xf\n"
      "v_add_f32_dpp v33, v27, v33 quad_perm:[3,3,3,3] row_mask:0xf bank_mask:0xf\n"
      "v_mfma_f32_16x16x16_bf16 v[20:23], a[78:79], v[38:39], v[20:23]\n"
      "v_add_f32 v32, v32, v33\n"
      "v_exp_f32 v34, v32\n"
      "v_max_f32 v35, v35, v32\n"
      "v_add_f32 v7, v7, v34\n"
      "v_cvt_pk_bf16_f32 v34, v34, v34\n"
      "v_and_b32 v36, v4, v34\n"
      "v_and_b32 v37, v5, v34\n"
      "s_waitcnt vmcnt(8)\n"
      "s_add_u32 m0, s61, 31744\n"
      "s_nop 0\n"
      "global_load_lds_dwordx4 v0, s[42:43] nt\n"
      "s_add_u32 s42, s42, 110592\n"
      "s_addc_u32 s43, s43, 0\n"
      "v_mfma_f32_16x16x32_bf16 v[24:27], a[96:99], v[12:15], 0\n"
      "v_sub_f32_dpp v32, v28, v6 quad_perm:[0,0,0,0] row_mask:0xf bank_mask:0xf\n"
      "v_mov_b32_dpp v33, v30 quad_perm:[2,2,2,2] row_mask:0xf bank_mask:0xf\n"
      "v_mfma_f32_16x16x16_bf16 v[16:19], a[84:85], v[36:37], v[16:19]\n"
      "v_add_f32_dpp v32, v29, v32 quad_perm:[1,1,1,1] row_mask:0xf bank_mask:0xf\n"
      "v_add_f32_dpp v33, v31, v33 quad_perm:[3,3,3,3] row_mask:0xf bank_mask:0xf\n"
      "v_mfma_f32_16x16x16_bf16 v[20:23], a[86:87], v[36:37], v[20:23]\n"
      "v_add_f32 v32, v32, v33\n"
      "v_exp_f32 v34, v32\n"
      "v_max_f32 v35, v35, v32\n"
      "v_add_f32 v7, v7, v34\n"
      "v_cvt_pk_bf16_f32 v34, v34, v34\n"
      "v_and_b32 v38, v4, v34\n"
      "v_and_b32 v39, v5, v34\n"
      "v_mfma_f32_16x16x32_bf16 v[28:31], a[104:107], v[12:15], 0\n"
      "v_sub_f32_dpp v32, v24, v6 quad_perm:[0,0,0,0] row_mask:0xf bank_mask:0xf\n"
      "v_mov_b32_dpp v33, v26 quad_perm:[2,2,2,2] row_mask:0xf bank_mask:0xf\n"
      "v_mfma_f32_16x16x16_bf16 v[16:19], a[92:93], v[38:39], v[16:19]\n"
      "v_add_f32_dpp v32, v25, v32 quad_perm:[1,1,1,1] row_mask:0xf bank_mask:0xf\n"
      "v_add_f32_dpp v33, v27, v33 quad_perm:[3,3,3,3] row_mask:0xf bank_mask:0xf\n"
      "v_mfma_f32_16x16x16_bf16 v[20:23], a[94:95], v[38:39], v[20:23]\n"
      "v_add_f32 v32, v32, v33\n"
      "v_exp_f32 v34, v32\n"
      "v_max_f32 v35, v35, v32\n"
      "v_add_f32 v7, v7, v34\n"
      "v_cvt_pk_bf16_f32 v34, v34, v34\n"
      "v_and_b32 v36, v4, v34\n"
      "v_and_b32 v37, v5, v34\n"
      "s_waitcnt vmcnt(8)\n"
      "s_add_u32 m0, s61, 32768\n"
      "s_nop 0\n"
      "global_load_lds_dwordx4 v1, s[40:41] nt\n"
      "s_add_u32 s40, s40, 110592\n"
      "s_addc_u32 s41, s41, 0\n"
      "v_mfma_f32_16x16x32_bf16 v[24:27], a[112:115], v[12:15], 0\n"
      "v_sub_f32_dpp v32, v28, v6 quad_perm:[0,0,0,0] row_mask:0xf bank_mask:0xf\n"
      "v_mov_b32_dpp v33, v30 quad_perm:[2,2,2,2] row_mask:0xf bank_mask:0xf\n"
      "v_mfma_f32_16x16x16_bf16 v[16:19], a[100:101], v[36:37], v[16:19]\n"
      "v_add_f32_dpp v32, v29, v32 quad_perm:[1,1,1,1] row_mask:0xf bank_mask:0xf\n"
      "v_add_f32_dpp v33, v31, v33 quad_perm:[3,3,3,3] row_mask:0xf bank_mask:0xf\n"
      "v_mfma_f32_16x16x16_bf16 v[20:23], a[102:103], v[36:37], v[20:23]\n"
      "v_add_f32 v32, v32, v33\n"
      "v_exp_f32 v34, v32\n"
      "v_max_f32 v35, v35, v32\n"
      "v_add_f32 v7, v7, v34\n"
      "v_cvt_pk_bf16_f32 v34, v34, v34\n"
      "v_and_b32 v38, v4, v34\n"
      "v_and_b32 v39, v5, v34\n"
      "v_mfma_f32_16x16x32_bf16 v[28:31], a[120:123], v[12:15], 0\n"
      "v_sub_f32_dpp v32, v24, v6 quad_perm:[0,0,0,0] row_mask:0xf bank_mask:0xf\n"
      "v_mov_b32_dpp v33, v26 quad_perm:[2,2,2,2] row_mask:0xf bank_mask:0xf\n"
      "v_mfma_f32_16x16x16_bf16 v[16:19], a[108:109], v[38:39], v[16:19]\n"
      "v_add_f32_dpp v32, v25, v32 quad_perm:[1,1,1,1] row_mask:0xf bank_mask:0xf\n"
      "v_add_f32_dpp v33, v27, v33 quad_perm:[3,3,3,3] row_mask:0xf bank_mask:0xf\n"
      "v_mfma_f32_16x16x16_bf16 v[20:23], a[110:111], v[38:39], v[20:23]\n"
      "v_add_f32 v32, v32, v33\n"
      "v_exp_f32 v34, v32\n"
      "v_max_f32 v35, v35, v32\n"
      "v_add_f32 v7, v7, v34\n"
      "v_cvt_pk_bf16_f32 v34, v34, v34\n"
      "v_and_b32 v36, v4, v34\n"
      "v_and_b32 v37, v5, v34\n"
      "v_mfma_f32_16x16x32_bf16 v[24:27], a[128:131], v[12:15], 0\n"
      "v_sub_f32_dpp v32, v28, v6 quad_perm:[0,0,0,0] row_mask:0xf bank_mask:0xf\n"
      "v_mov_b32_dpp v33, v30 quad_perm:[2,2,2,2] row_mask:0xf bank_mask:0xf\n"
      "v_mfma_f32_16x16x16_bf16 v[16:19], a[116:117], v[36:37], v[16:19]\n"
      "v_add_f32_dpp v32, v29, v32 quad_perm:[1,1,1,1] row_mask:0xf bank_mask:0xf\n"
      "v_add_f32_dpp v33, v31, v33 quad_perm:[3,3,3,3] row_mask:0xf bank_mask:0xf\n"
      "v_mfma_f32_16x16x16_bf16 v[20:23], a[118:119], v[36:37], v[20:23]\n"
      "v_add_f32 v32, v32, v33\n"
      "v_exp_f32 v34, v32\n"
      "v_max_f32 v35, v35, v32\n"
      "v_add_f32 v7, v7, v34\n"
      "v_cvt_pk_bf16_f32 v34, v34, v34\n"
      "v_and_b32 v38, v4, v34\n"
      "v_and_b32 v39, v5, v34\n"
      "s_waitcnt vmcnt(8)\n"
      "s_add_u32 m0, s61, 33792\n"
      "s_nop 0\n"
      "global_load_lds_dwordx4 v0, s[42:43] nt\n"
      "s_add_u32 s42, s42, 110592\n"
      "s_addc_u32 s43, s43, 0\n"
      "v_mfma_f32_16x16x32_bf16 v[28:31], a[136:139], v[12:15], 0\n"
      "v_sub_f32_dpp v32, v24, v6 quad_perm:[0,0,0,0] row_mask:0xf bank_mask:0xf\n"
      "v_mov_b32_dpp v33, v26 quad_perm:[2,2,2,2] row_mask:0xf bank_mask:0xf\n"
      "v_mfma_f32_16x16x16_bf16 v[16:19], a[124:125], v[38:39], v[16:19]\n"
      "v_add_f32_dpp v32, v25, v32 quad_perm:[1,1,1,1] row_mask:0xf bank_mask:0xf\n"
      "v_add_f32_dpp v33, v27, v33 quad_perm:[3,3,3,3] row_mask:0xf bank_mask:0xf\n"
      "v_mfma_f32_16x16x16_bf16 v[20:23], a[126:127], v[38:39], v[20:23]\n"
      "v_add_f32 v32, v32, v33\n"
      "v_exp_f32 v34, v32\n"
      "v_max_f32 v35, v35, v32\n"
      "v_add_f32 v7, v7, v34\n"
      "v_cvt_pk_bf16_f32 v34, v34, v34\n"
      "v_and_b32 v36, v4, v34\n"
      "v_and_b32 v37, v5, v34\n"
      "v_mfma_f32_16x16x32_bf16 v[24:27], a[144:147], v[12:15], 0\n"
      "v_sub_f32_dpp v32, v28, v6 quad_perm:[0,0,0,0] row_mask:0xf bank_mask:0xf\n"
      "v_mov_b32_dpp v33, v30 quad_perm:[2,2,2,2] row_mask:0xf bank_mask:0xf\n"
      "v_mfma_f32_16x16x16_bf16 v[16:19], a[132:133], v[36:37], v[16:19]\n"
      "v_add_f32_dpp v32, v29, v32 quad_perm:[1,1,1,1] row_mask:0xf bank_mask:0xf\n"
      "v_add_f32_dpp v33, v31, v33 quad_perm:[3,3,3,3] row_mask:0xf bank_mask:0xf\n"
      "v_mfma_f32_16x16x16_bf16 v[20:23], a[134:135], v[36:37], v[20:23]\n"
      "v_add_f32 v32, v32, v33\n"
      "v_exp_f32 v34, v32\n"
      "v_max_f32 v35, v35, v32\n"
      "v_add_f32 v7, v7, v34\n"
      "v_cvt_pk_bf16_f32 v34, v34, v34\n"
      "v_and_b32 v38, v4, v34\n"
      "v_and_b32 v39, v5, v34\n"
      "s_waitcnt vmcnt(8)\n"
      "s_add_u32 m0, s61, 34816\n"
      "s_nop 0\n"
      "global_load_lds_dwordx4 v1, s[40:41] nt\n"
      "s_add_u32 s40, s40, s51\n"
      "s_addc_u32 s41, s41, 0\n"
      "v_sub_f32_dpp v32, v24, v6 quad_perm:[0,0,0,0] row_mask:0xf bank_mask:0xf\n"
      "v_mov_b32_dpp v33, v26 quad_perm:[2,2,2,2] row_mask:0xf bank_mask:0xf\n"
      "v_mfma_f32_16x16x16_bf16 v[16:19], a[140:141], v[38:39], v[16:19]\n"
      "v_add_f32_dpp v32, v25, v32 quad_perm:[1,1,1,1] row_mask:0xf bank_mask:0xf\n"
      "v_add_f32_dpp v33, v27, v33 quad_perm:[3,3,3,3] row_mask:0xf bank_mask:0xf\n"
      "v_mfma_f32_16x16x16_bf16 v[20:23], a[142:143], v[38:39], v[20:23]\n"
      "v_add_f32 v32, v32, v33\n"
      "v_add_f32 v32, s69, v32\n"
      "v_exp_f32 v34, v32\n"
      "v_max_f32 v35, v35, v32\n"
      "v_add_f32 v7, v7, v34\n"
      "v_cvt_pk_bf16_f32 v34, v34, v34\n"
      "v_and_b32 v36, v4, v34\n"
      "v_and_b32 v37, v5, v34\n"
      "s_nop 1\n"
      "v_mfma_f32_16x16x16_bf16 v[16:19], a[148:149], v[36:37], v[16:19]\n"
      "v_mfma_f32_16x16x16_bf16 v[20:23], a[150:151], v[36:37], v[20:23]\n"
      "s_waitcnt vmcnt(8)\n"
      "s_add_u32 m0, s61, 35840\n"
      "s_nop 0\n"
      "global_load_lds_dwordx4 v0, s[42:43] nt\n"
      "s_add_u32 s42, s42, s51\n"
      "s_addc_u32 s43, s43, 0\n"
      "v_cmp_lt_f32 vcc, 0x42800000, v35\n"
      "s_nop 0\n"
      "s_cbranch_vccnz L_fb5_%=\n"
      "L_ret5_%=:\n"
      "v_mov_b32 v49, v7\n"
      "v_lshlrev_b32 v41, 2, v0\n"
      "v_and_b32 v41, 0x300, v41\n"
      "v_permlane32_swap_b32 v49, v7\n"
      "v_lshrrev_b32 v42, 4, v0\n"
      "v_add_f32 v7, v49, v7\n"
      "v_and_b32 v42, 0x30, v42\n"
      "v_mov_b32 v49, v7\n"
      "v_add_u32 v41, v41, v42\n"
      "v_lshrrev_b32 v40, 2, v0\n"
      "v_permlane16_swap_b32 v49, v7\n"
      "v_and_b32 v42, 12, v40\n"
      "v_add_f32 v7, v49, v7\n"
      "v_add_u32 v41, v41, v42\n"
      "s_lshl_b32 s62, s60, 10\n"
      "s_add_u32 s62, s62, 155648\n"
      "v_add_u32 v41, s62, v41\n"
      "v_lshrrev_b32 v43, 3, v0\n"
      "v_and_b32 v43, 24, v43\n"
      "s_lshl_b32 s62, s60, 5\n"
      "s_add_u32 s62, s62, 159744\n"
      "v_add_u32 v43, s62, v43\n"
      "v_cvt_pk_bf16_f32 v36, v16, v17\n"
      "v_cvt_pk_bf16_f32 v37, v18, v19\n"
      "v_cvt_pk_bf16_f32 v38, v20, v21\n"
      "v_cvt_pk_bf16_f32 v39, v22, v23\n"
      "s_mov_b64 exec, 0x1111\n"
      "ds_write_b64 v43, v[6:7]\n"
      "s_mov_b64 exec, -1\n"
      "ds_write_b32 v41, v36 offset:0\n"
      "ds_write_b32 v41, v37 offset:64\n"
      "ds_write_b32 v41, v38 offset:128\n"
      "ds_write_b32 v41, v39 offset:192\n"
      "s_lshl_b32 s62, s60, 8\n"
      "s_add_u32 s62, s62, 155648\n"
      "v_add_u32 v42, s62, v40\n"
      "s_lshl_b32 s62, s60, 3\n"
      "s_add_u32 s62, s62, 159744\n"
      "v_mov_b32 v50, s62\n"
      "s_waitcnt lgkmcnt(0)\n"
      "s_barrier\n"
      "ds_read_b64 v[28:29], v50 offset:0\n"
      "ds_read_b64 v[30:31], v50 offset:32\n"
      "ds_read_b64 v[32:33], v50 offset:64\n"
      "ds_read_b64 v[34:35], v50 offset:96\n"
      "ds_read_b32 v24, v42 offset:0\n"
      "ds_read_b32 v25, v42 offset:1024\n"
      "ds_read_b32 v26, v42 offset:2048\n"
      "ds_read_b32 v27, v42 offset:3072\n"
      "s_waitcnt lgkmcnt(4)\n"
      "v_max3_f32 v43, v28, v30, v32\n"
      "v_max_f32 v43, v43, v34\n"
      "v_sub_f32 v28, v28, v43\n"
      "v_sub_f32 v30, v30, v43\n"
      "v_sub_f32 v32, v32, v43\n"
      "v_sub_f32 v34, v34, v43\n"
      "v_exp_f32 v28, v28\n"
      "v_exp_f32 v30, v30\n"
      "v_exp_f32 v32, v32\n"
      "v_exp_f32 v34, v34\n"
      "s_waitcnt lgkmcnt(0)\n"
      "v_lshlrev_b32 v44, 16, v24\n"
      "v_and_b32 v45, 0xffff0000, v24\n"
      "v_lshlrev_b32 v46, 16, v25\n"
      "v_and_b32 v47, 0xffff0000, v25\n"
      "v_lshlrev_b32 v48, 16, v26\n"
      "v_and_b32 v49, 0xffff0000, v26\n"
      "v_lshlrev_b32 v50, 16, v27\n"
      "v_and_b32 v51, 0xffff0000, v27\n"
      "v_mul_f32 v38, v28, v29\n"
      "v_pk_mul_f32 v[36:37], v[44:45], v[28:29] op_sel_hi:[1,0]\n"
      "v_fmac_f32 v38, v30, v31\n"
      "v_pk_fma_f32 v[36:37], v[46:47], v[30:31], v[36:37] op_sel_hi:[1,0,1]\n"
      "v_fmac_f32 v38, v32, v33\n"
      "v_pk_fma_f32 v[36:37], v[48:49], v[32:33], v[36:37] op_sel_hi:[1,0,1]\n"
      "v_fmac_f32 v38, v34, v35\n"
      "v_pk_fma_f32 v[36:37], v[50:51], v[34:35], v[36:37] op_sel_hi:[1,0,1]\n"
      "v_rcp_f32 v39, v38\n"
      "v_log_f32 v38, v38\n"
      "v_mul_f32 v36, v39, v36\n"
      "v_mul_f32 v37, v39, v37\n"
      "v_add_f32 v38, v38, v43\n"
      "v_cvt_pk_bf16_f32 v36, v36, v37\n"
      "v_min_u32 v38, -2, v38\n"
      "v_min_u32 v36, -2, v36\n"
      "v_mov_b32 v12, v36\n"
      "v_mov_b32 v13, v38\n"
      "s_waitcnt vmcnt(8)\n"
      "s_add_u32 m0, s61, 36864\n"
      "s_nop 0\n"
      "global_load_lds_dwordx4 v1, s[40:41] nt\n"
      "s_add_u32 s40, s40, s52\n"
      "s_addc_u32 s41, s41, 0\n"
      "s_waitcnt vmcnt(8)\n"
      "s_add_u32 m0, s61, 37888\n"
      "s_nop 0\n"
      "global_load_lds_dwordx4 v0, s[42:43] nt\n"
      "s_add_u32 s42, s42, s52\n"
      "s_addc_u32 s43, s43, 0\n"
      "s_waitcnt vmcnt(8)\n"
      "global_load_dwordx4 a[0:3], v1, s[40:41] nt\n"
      "s_add_u32 s40, s40, 110592\n"
      "s_addc_u32 s41, s41, 0\n"
      "s_waitcnt vmcnt(8)\n"
      "global_load_dwordx4 a[4:7], v0, s[42:43] nt\n"
      "s_add_u32 s42, s42, 110592\n"
      "s_addc_u32 s43, s43, 0\n"
      "s_barrier\n"
      "s_mov_b64 exec, 0xffff\n"
      "s_mov_b32 s53, 0\n"
      "L_qc15_%=:\n"
      "v_max3_u32 v49, v8, v9, v10\n"
      "v_max_u32 v49, v49, v11\n"
      "v_cmp_eq_u32 vcc, -1, v49\n"
      "s_nop 0\n"
      "s_cbranch_vccz L_qo16_%=\n"
      "s_cmp_lg_u32 s54, 0\n"
      "s_cbranch_scc1 L_qo16_%=\n"
      "s_add_u32 s56, s48, 32768\n"
      "s_addc_u32 s57, s49, 0\n"
      "global_load_dwordx4 v[8:11], v0, s[56:57] sc1\n"
      "s_add_u32 s53, s53, 1\n"
      "s_waitcnt vmcnt(0)\n"
      "s_cmp_lt_u32 s53, 30000\n"
      "s_cbranch_scc1 L_qc15_%=\n"
      "s_mov_b32 s54, 104\n"
      "v_mov_b32 v49, 104\n"
      "v_mov_b32 v50, 256\n"
      "global_store_dword v50, v49, s[58:59] sc1\n"
      "L_qo16_%=:\n"
      "s_lshl_b32 s62, s60, 8\n"
      "s_add_u32 s62, s62, 159872\n"
      "v_add_u32 v49, s62, v0\n"
      "ds_write_b128 v49, v[8:11]\n"
      "s_mov_b64 exec, -1\n"
      "s_waitcnt lgkmcnt(0)\n"
      "s_barrier\n"
      "ds_read_b128 v[8:11], v2 offset:0\n"
      "s_waitcnt lgkmcnt(0)\n"
      "ds_read_b128 v[40:43], v3 offset:0\n"
      "ds_read_b128 v[44:47], v3 offset:2048\n"
      "ds_read_b128 v[48:51], v3 offset:1024\n"
      "ds_read_b128 v[52:55], v3 offset:3072\n"
      "s_waitcnt lgkmcnt(3)\n"
      "v_mfma_f32_16x16x32_bf16 v[24:27], v[40:43], v[8:11], 0\n"
      "ds_read_b128 v[40:43], v3 offset:4096\n"
      "s_waitcnt lgkmcnt(3)\n"
      "v_mfma_f32_16x16x32_bf16 v[28:31], v[44:47], v[8:11], 0\n"
      "ds_read_b128 v[44:47], v3 offset:6144\n"
      "s_nop 3\n"
      "v_mov_b32_dpp v34, v24 quad_perm:[0,0,0,0] row_mask:0xf bank_mask:0xf\n"
      "v_mov_b32_dpp v33, v26 quad_perm:[2,2,2,2] row_mask:0xf bank_mask:0xf\n"
      "s_nop 0\n"
      "v_add_f32_dpp v34, v25, v34 quad_perm:[1,1,1,1] row_mask:0xf bank_mask:0xf\n"
      "v_add_f32_dpp v33, v27, v33 quad_perm:[3,3,3,3] row_mask:0xf bank_mask:0xf\n"
      "v_add_f32 v34, v34, v33\n"
      "v_mov_b32_dpp v32, v28 quad_perm:[0,0,0,0] row_mask:0xf bank_mask:0xf\n"
      "v_mov_b32_dpp v33, v30 quad_perm:[2,2,2,2] row_mask:0xf bank_mask:0xf\n"
      "s_nop 0\n"
      "v_add_f32_dpp v32, v29, v32 quad_perm:[1,1,1,1] row_mask:0xf bank_mask:0xf\n"
      "v_add_f32_dpp v33, v31, v33 quad_perm:[3,3,3,3] row_mask:0xf bank_mask:0xf\n"
      "v_add_f32 v32, v32, v33\n"
      "v_max_f32 v35, v34, v32\n"
      "v_mov_b32 v33, v35\n"
      "s_nop 1\n"
      "v_permlane32_swap_b32 v33, v35\n"
      "v_max_f32 v35, v33, v35\n"
      "v_mov_b32 v33, v35\n"
      "s_nop 1\n"
      "v_permlane16_swap_b32 v33, v35\n"
      "v_max_f32 v6, v33, v35\n"
      "v_sub_f32 v34, v34, v6\n"
      "v_sub_f32 v32, v32, v6\n"
      "v_mov_b32 v35, 0\n"
      "v_exp_f32 v34, v34\n"
      "s_nop 0\n"
      "v_mov_b32 v7, v34\n"
      "v_cvt_pk_bf16_f32 v34, v34, v34\n"
      "v_and_b32 v36, v4, v34\n"
      "v_and_b32 v37, v5, v34\n"
      "s_waitcnt lgkmcnt(1)\n"
      "v_mfma_f32_16x16x32_bf16 v[24:27], v[40:43], v[8:11], 0\n"
      "ds_read_b128 v[40:43], v3 offset:8192\n"
      "v_mfma_f32_16x16x16_bf16 v[16:19], v[48:49], v[36:37], 0\n"
      "v_mfma_f32_16x16x16_bf16 v[20:23], v[50:51], v[36:37], 0\n"
      "ds_read_b128 v[48:51], v3 offset:5120\n"
      "v_exp_f32 v34, v32\n"
      "v_max_f32 v35, v35, v32\n"
      "v_add_f32 v7, v7, v34\n"
      "v_cvt_pk_bf16_f32 v34, v34, v34\n"
      "v_and_b32 v38, v4, v34\n"
      "v_and_b32 v39, v5, v34\n"
      "s_waitcnt lgkmcnt(2)\n"
      "v_mfma_f32_16x16x32_bf16 v[28:31], v[44:47], v[8:11], 0\n"
      "ds_read_b128 v[44:47], v3 offset:10240\n"
      "v_sub_f32_dpp v32, v24, v6 quad_perm:[0,0,0,0] row_mask:0xf bank_mask:0xf\n"
      "v_mov_b32_dpp v33, v26 quad_perm:[2,2,2,2] row_mask:0xf bank_mask:0xf\n"
      "v_mfma_f32_16x16x16_bf16 v[16:19], v[52:53], v[38:39], v[16:19]\n"
      "v_add_f32_dpp v32, v25, v32 quad_perm:[1,1,1,1] row_mask:0xf bank_mask:0xf\n"
      "v_add_f32_dpp v33, v27, v33 quad_perm:[3,3,3,3] row_mask:0xf bank_mask:0xf\n"
      "v_mfma_f32_16x16x16_bf16 v[20:23], v[54:55], v[38:39], v[20:23]\n"
      "ds_read_b128 v[52:55], v3 offset:7168\n"
      "v_add_f32 v32, v32, v33\n"
      "v_exp_f32 v34, v32\n"
      "v_max_f32 v35, v35, v32\n"
      "v_add_f32 v7, v7, v34\n"
      "v_cvt_pk_bf16_f32 v34, v34, v34\n"
      "v_and_b32 v36, v4, v34\n"
      "v_and_b32 v37, v5, v34\n"
      "s_waitcnt lgkmcnt(3)\n"
      "v_mfma_f32_16x16x32_bf16 v[24:27], v[40:43], v[8:11], 0\n"
      "ds_read_b128 v[40:43], v3 offset:12288\n"
      "v_sub_f32_dpp v32, v28, v6 quad_perm:[0,0,0,0] row_mask:0xf bank_mask:0xf\n"
      "v_mov_b32_dpp v33, v30 quad_perm:[2,2,2,2] row_mask:0xf bank_mask:0xf\n"
      "s_waitcnt lgkmcnt(3)\n"
      "v_mfma_f32_16x16x16_bf16 v[16:19], v[48:49], v[36:37], v[16:19]\n"
      "v_add_f32_dpp v32, v29, v32 quad_perm:[1,1,1,1] row_mask:0xf bank_mask:0xf\n"
      "v_add_f32_dpp v33, v31, v33 quad_perm:[3,3,3,3] row_mask:0xf bank_mask:0xf\n"
      "v_mfma_f32_16x16x16_bf16 v[20:23], v[50:51], v[36:37], v[20:23]\n"
      "ds_read_b128 v[48:51], v3 offset:9216\n"
      "v_add_f32 v32, v32, v33\n"
      "v_exp_f32 v34, v32\n"
      "v_max_f32 v35, v35, v32\n"
      "v_add_f32 v7, v7, v34\n"
      "v_cvt_pk_bf16_f32 v34, v34, v34\n"
      "v_and_b32 v38, v4, v34\n"
      "v_and_b32 v39, v5, v34\n"
      "s_waitcnt vmcnt(8)\n"
      "global_load_dwordx4 a[8:11], v1, s[40:41] nt\n"
      "s_add_u32 s40, s40, 110592\n"
      "s_addc_u32 s41, s41, 0\n"
      "s_waitcnt lgkmcnt(3)\n"
      "v_mfma_f32_16x16x32_bf16 v[28:31], v[44:47], v[8:11], 0\n"
      "ds_read_b128 v[44:47], v3 offset:14336\n"
      "v_sub_f32_dpp v32, v24, v6 quad_perm:[0,0,0,0] row_mask:0xf bank_mask:0xf\n"
      "v_mov_b32_dpp v33, v26 quad_perm:[2,2,2,2] row_mask:0xf bank_mask:0xf\n"
      "s_waitcnt lgkmcnt(3)\n"
      "v_mfma_f32_16x16x16_bf16 v[16:19], v[52:53], v[38:39], v[16:19]\n"
      "v_add_f32_dpp v32, v25, v32 quad_perm:[1,1,1,1] row_mask:0xf bank_mask:0xf\n"
      "v_add_f32_dpp v33, v27, v33 quad_perm:[3,3,3,3] row_mask:0xf bank_mask:0xf\n"
      "v_mfma_f32_16x16x16_bf16 v[20:23], v[54:55], v[38:39], v[20:23]\n"
      "ds_read_b128 v[52:55], v3 offset:11264\n"
      "v_add_f32 v32, v32, v33\n"
      "v_exp_f32 v34, v32\n"
      "v_max_f32 v35, v35, v32\n"
      "v_add_f32 v7, v7, v34\n"
      "v_cvt_pk_bf16_f32 v34, v34, v34\n"
      "v_and_b32 v36, v4, v34\n"
      "v_and_b32 v37, v5, v34\n"
      "s_waitcnt lgkmcnt(3)\n"
      "v_mfma_f32_16x16x32_bf16 v[24:27], v[40:43], v[8:11], 0\n"
      "ds_read_b128 v[40:43], v3 offset:16384\n"
      "v_sub_f32_dpp v32, v28, v6 quad_perm:[0,0,0,0] row_mask:0xf bank_mask:0xf\n"
      "v_mov_b32_dpp v33, v30 quad_perm:[2,2,2,2] row_mask:0xf bank_mask:0xf\n"
      "s_waitcnt lgkmcnt(3)\n"
      "v_mfma_f32_16x16x16_bf16 v[16:19], v[48:49], v[36:37], v[16:19]\n"
      "v_add_f32_dpp v32, v29, v32 quad_perm:[1,1,1,1] row_mask:0xf bank_mask:0xf\n"
      "v_add_f32_dpp v33, v31, v33 quad_perm:[3,3,3,3] row_mask:0xf bank_mask:0xf\n"
      "v_mfma_f32_16x16x16_bf16 v[20:23], v[50:51], v[36:37], v[20:23]\n"
      "ds_read_b128 v[48:51], v3 offset:13312\n"
      "v_add_f32 v32, v32, v33\n"
      "v_exp_f32 v34, v32\n"
      "v_max_f32 v35, v35, v32\n"
      "v_add_f32 v7, v7, v34\n"
      "v_cvt_pk_bf16_f32 v34, v34, v34\n"
      "v_and_b32 v38, v4, v34\n"
      "v_and_b32 v39, v5, v34\n"
      "s_waitcnt vmcnt(8)\n"
      "global_load_dwordx4 a[12:15], v0, s[42:43] nt\n"
      "s_add_u32 s42, s42, 110592\n"
      "s_addc_u32 s43, s43, 0\n"
      "s_waitcnt lgkmcnt(3)\n"
      "v_mfma_f32_16x16x32_bf16 v[28:31], v[44:47], v[8:11], 0\n"
      "ds_read_b128 v[44:47], v3 offset:18432\n"
      "v_sub_f32_dpp v32, v24, v6 quad_perm:[0,0,0,0] row_mask:0xf bank_mask:0xf\n"
      "v_mov_b32_dpp v33, v26 quad_perm:[2,2,2,2] row_mask:0xf bank_mask:0xf\n"
      "s_waitcnt lgkmcnt(3)\n"
      "v_mfma_f32_16x16x16_bf16 v[16:19], v[52:53], v[38:39], v[16:19]\n"
      "v_add_f32_dpp v32, v25, v32 quad_perm:[1,1,1,1] row_mask:0xf bank_mask:0xf\n"
      "v_add_f32_dpp v33, v27, v33 quad_perm:[3,3,3,3] row_mask:0xf bank_mask:0xf\n"
      "v_mfma_f32_16x16x16_bf16 v[20:23], v[54:55], v[38:39], v[20:23]\n"
      "ds_read_b128 v[52:55], v3 offset:15360\n"
      "v_add_f32 v32, v32, v33\n"
      "v_exp_f32 v34, v32\n"
      "v_max_f32 v35, v35, v32\n"
      "v_add_f32 v7, v7, v34\n"
      "v_cvt_pk_bf16_f32 v34, v34, v34\n"
      "v_and_b32 v36, v4, v34\n"
      "v_and_b32 v37, v5, v34\n"
      "s_waitcnt lgkmcnt(3)\n"
      "v_mfma_f32_16x16x32_bf16 v[24:27], v[40:43], v[8:11], 0\n"
      "ds_read_b128 v[40:43], v3 offset:20480\n"
      "v_sub_f32_dpp v32, v28, v6 quad_perm:[0,0,0,0] row_mask:0xf bank_mask:0xf\n"
      "v_mov_b32_dpp v33, v30 quad_perm:[2,2,2,2] row_mask:0xf bank_mask:0xf\n"
      "s_waitcnt lgkmcnt(3)\n"
      "v_mfma_f32_16x16x16_bf16 v[16:19], v[48:49], v[36:37], v[16:19]\n"
      "v_add_f32_dpp v32, v29, v32 quad_perm:[1,1,1,1] row_mask:0xf bank_mask:0xf\n"
      "v_add_f32_dpp v33, v31, v33 quad_perm:[3,3,3,3] row_mask:0xf bank_mask:0xf\n"
      "v_mfma_f32_16x16x16_bf16 v[20:23], v[50:51], v[36:37], v[20:23]\n"
      "ds_read_b128 v[48:51], v3 offset:17408\n"
      "v_add_f32 v32, v32, v33\n"
      "v_exp_f32 v34, v32\n"
      "v_max_f32 v35, v35, v32\n"
      "v_add_f32 v7, v7, v34\n"
      "v_cvt_pk_bf16_f32 v34, v34, v34\n"
      "v_and_b32 v38, v4, v34\n"
      "v_and_b32 v39, v5, v34\n"
      "s_waitcnt lgkmcnt(3)\n"
      "v_mfma_f32_16x16x32_bf16 v[28:31], v[44:47], v[8:11], 0\n"
      "ds_read_b128 v[44:47], v3 offset:22528\n"
      "v_sub_f32_dpp v32, v24, v6 quad_perm:[0,0,0,0] row_mask:0xf bank_mask:0xf\n"
      "v_mov_b32_dpp v33, v26 quad_perm:[2,2,2,2] row_mask:0xf bank_mask:0xf\n"
      "s_waitcnt lgkmcnt(3)\n"
      "v_mfma_f32_16x16x16_bf16 v[16:19], v[52:53], v[38:39], v[16:19]\n"
      "v_add_f32_dpp v32, v25, v32 quad_perm:[1,1,1,1] row_mask:0xf bank_mask:0xf\n"
      "v_add_f32_dpp v33, v27, v33 quad_perm:[3,3,3,3] row_mask:0xf bank_mask:0xf\n"
      "v_mfma_f32_16x16x16_bf16 v[20:23], v[54:55], v[38:39], v[20:23]\n"
      "ds_read_b128 v[52:55], v3 offset:19456\n"
      "v_add_f32 v32, v32, v33\n"
      "v_exp_f32 v34, v32\n"
      "v_max_f32 v35, v35, v32\n"
      "v_add_f32 v7, v7, v34\n"
      "v_cvt_pk_bf16_f32 v34, v34, v34\n"
      "v_and_b32 v36, v4, v34\n"
      "v_and_b32 v37, v5, v34\n"
      "s_waitcnt vmcnt(8)\n"
      "global_load_dwordx4 a[16:19], v1, s[40:41] nt\n"
      "s_add_u32 s40, s40, 110592\n"
      "s_addc_u32 s41, s41, 0\n"
      "s_waitcnt lgkmcnt(3)\n"
      "v_mfma_f32_16x16x32_bf16 v[24:27], v[40:43], v[8:11], 0\n"
      "ds_read_b128 v[40:43], v3 offset:24576\n"
      "v_sub_f32_dpp v32, v28, v6 quad_perm:[0,0,0,0] row_mask:0xf bank_mask:0xf\n"
      "v_mov_b32_dpp v33, v30 quad_perm:[2,2,2,2] row_mask:0xf bank_mask:0xf\n"
      "s_waitcnt lgkmcnt(3)\n"
      "v_mfma_f32_16x16x16_bf16 v[16:19], v[48:49], v[36:37], v[16:19]\n"
      "v_add_f32_dpp v32, v29, v32 quad_perm:[1,1,1,1] row_mask:0xf bank_mask:0xf\n"
      "v_add_f32_dpp v33, v31, v33 quad_perm:[3,3,3,3] row_mask:0xf bank_mask:0xf\n"
      "v_mfma_f32_16x16x16_bf16 v[20:23], v[50:51], v[36:37], v[20:23]\n"
      "ds_read_b128 v[48:51], v3 offset:21504\n"
      "v_add_f32 v32, v32, v33\n"
      "v_exp_f32 v34, v32\n"
      "v_max_f32 v35, v35, v32\n"
      "v_add_f32 v7, v7, v34\n"
      "v_cvt_pk_bf16_f32 v34, v34, v34\n"
      "v_and_b32 v38, v4, v34\n"
      "v_and_b32 v39, v5, v34\n"
      "s_waitcnt lgkmcnt(3)\n"
      "v_mfma_f32_16x16x32_bf16 v[28:31], v[44:47], v[8:11], 0\n"
      "ds_read_b128 v[44:47], v3 offset:26624\n"
      "v_sub_f32_dpp v32, v24, v6 quad_perm:[0,0,0,0] row_mask:0xf bank_mask:0xf\n"
      "v_mov_b32_dpp v33, v26 quad_perm:[2,2,2,2] row_mask:0xf bank_mask:0xf\n"
      "s_waitcnt lgkmcnt(3)\n"
      "v_mfma_f32_16x16x16_bf16 v[16:19], v[52:53], v[38:39], v[16:19]\n"
      "v_add_f32_dpp v32, v25, v32 quad_perm:[1,1,1,1] row_mask:0xf bank_mask:0xf\n"
      "v_add_f32_dpp v33, v27, v33 quad_perm:[3,3,3,3] row_mask:0xf bank_mask:0xf\n"
      "v_mfma_f32_16x16x16_bf16 v[20:23], v[54:55], v[38:39], v[20:23]\n"
      "ds_read_b128 v[52:55], v3 offset:23552\n"
      "v_add_f32 v32, v32, v33\n"
      "v_exp_f32 v34, v32\n"
      "v_max_f32 v35, v35, v32\n"
      "v_add_f32 v7, v7, v34\n"
      "v_cvt_pk_bf16_f32 v34, v34, v34\n"
      "v_and_b32 v36, v4, v34\n"
      "v_and_b32 v37, v5, v34\n"
      "s_waitcnt vmcnt(8)\n"
      "global_load_dwordx4 a[20:23], v0, s[42:43] nt\n"
      "s_add_u32 s42, s42, 110592\n"
      "s_addc_u32 s43, s43, 0\n"
      "s_waitcnt lgkmcnt(3)\n"
      "v_mfma_f32_16x16x32_bf16 v[24:27], v[40:43], v[8:11], 0\n"
      "ds_read_b128 v[40:43], v3 offset:28672\n"
      "v_sub_f32_dpp v32, v28, v6 quad_perm:[0,0,0,0] row_mask:0xf bank_mask:0xf\n"
      "v_mov_b32_dpp v33, v30 quad_perm:[2,2,2,2] row_mask:0xf bank_mask:0xf\n"
      "s_waitcnt lgkmcnt(3)\n"
      "v_mfma_f32_16x16x16_bf16 v[16:19], v[48:49], v[36:37], v[16:19]\n"
      "v_add_f32_dpp v32, v29, v32 quad_perm:[1,1,1,1] row_mask:0xf bank_mask:0xf\n"
      "v_add_f32_dpp v33, v31, v33 quad_perm:[3,3,3,3] row_mask:0xf bank_mask:0xf\n"
      "v_mfma_f32_16x16x16_bf16 v[20:23], v[50:51], v[36:37], v[20:23]\n"
      "ds_read_b128 v[48:51], v3 offset:25600\n"
      "v_add_f32 v32, v32, v33\n"
      "v_exp_f32 v34, v32\n"
      "v_max_f32 v35, v35, v32\n"
      "v_add_f32 v7, v7, v34\n"
      "v_cvt_pk_bf16_f32 v34, v34, v34\n"
      "v_and_b32 v38, v4, v34\n"
      "v_and_b32 v39, v5, v34\n"
      "s_waitcnt lgkmcnt(3)\n"
      "v_mfma_f32_16x16x32_bf16 v[28:31], v[44:47], v[8:11], 0\n"
      "ds_read_b128 v[44:47], v3 offset:30720\n"
      "v_sub_f32_dpp v32, v24, v6 quad_perm:[0,0,0,0] row_mask:0xf bank_mask:0xf\n"
      "v_mov_b32_dpp v33, v26 quad_perm:[2,2,2,2] row_mask:0xf bank_mask:0xf\n"
      "s_waitcnt lgkmcnt(3)\n"
      "v_mfma_f32_16x16x16_bf16 v[16:19], v[52:53], v[38:39], v[16:19]\n"
      "v_add_f32_dpp v32, v25, v32 quad_perm:[1,1,1,1] row_mask:0xf bank_mask:0xf\n"
      "v_add_f32_dpp v33, v27, v33 quad_perm:[3,3,3,3] row_mask:0xf bank_mask:0xf\n"
      "v_mfma_f32_16x16x16_bf16 v[20:23], v[54:55], v[38:39], v[20:23]\n"
      "ds_read_b128 v[52:55], v3 offset:27648\n"
      "v_add_f32 v32, v32, v33\n"
      "v_exp_f32 v34, v32\n"
      "v_max_f32 v35, v35, v32\n"
      "v_add_f32 v7, v7, v34\n"
      "v_cvt_pk_bf16_f32 v34, v34, v34\n"
      "v_and_b32 v36, v4, v34\n"
      "v_and_b32 v37, v5, v34\n"
      "s_waitcnt vmcnt(8)\n"
      "global_load_dwordx4 a[24:27], v1, s[40:41] nt\n"
      "s_add_u32 s40, s40, 110592\n"
      "s_addc_u32 s41, s41, 0\n"
      "s_waitcnt lgkmcnt(3)\n"
      "v_mfma_f32_16x16x32_bf16 v[24:27], v[40:43], v[8:11], 0\n"
      "ds_read_b128 v[40:43], v3 offset:32768\n"
      "v_sub_f32_dpp v32, v28, v6 quad_perm:[0,0,0,0] row_mask:0xf bank_mask:0xf\n"
      "v_mov_b32_dpp v33, v30 quad_perm:[2,2,2,2] row_mask:0xf bank_mask:0xf\n"
      "s_waitcnt lgkmcnt(3)\n"
      "v_mfma_f32_16x16x16_bf16 v[16:19], v[48:49], v[36:37], v[16:19]\n"
      "v_add_f32_dpp v32, v29, v32 quad_perm:[1,1,1,1] row_mask:0xf bank_mask:0xf\n"
      "v_add_f32_dpp v33, v31, v33 quad_perm:[3,3,3,3] row_mask:0xf bank_mask:0xf\n"
      "v_mfma_f32_16x16x16_bf16 v[20:23], v[50:51], v[36:37], v[20:23]\n"
      "ds_read_b128 v[48:51], v3 offset:29696\n"
      "v_add_f32 v32, v32, v33\n"
      "v_exp_f32 v34, v32\n"
      "v_max_f32 v35, v35, v32\n"
      "v_add_f32 v7, v7, v34\n"
      "v_cvt_pk_bf16_f32 v34, v34, v34\n"
      "v_and_b32 v38, v4, v34\n"
      "v_and_b32 v39, v5, v34\n"
      "s_waitcnt lgkmcnt(3)\n"
      "v_mfma_f32_16x16x32_bf16 v[28:31], v[44:47], v[8:11], 0\n"
      "ds_read_b128 v[44:47], v3 offset:34816\n"
      "v_sub_f32_dpp v32, v24, v6 quad_perm:[0,0,0,0] row_mask:0xf bank_mask:0xf\n"
      "v_mov_b32_dpp v33, v26 quad_perm:[2,2,2,2] row_mask:0xf bank_mask:0xf\n"
      "s_waitcnt lgkmcnt(3)\n"
      "v_mfma_f32_16x16x16_bf16 v[16:19], v[52:53], v[38:39], v[16:19]\n"
      "v_add_f32_dpp v32, v25, v32 quad_perm:[1,1,1,1] row_mask:0xf bank_mask:0xf\n"
      "v_add_f32_dpp v33, v27, v33 quad_perm:[3,3,3,3] row_mask:0xf bank_mask:0xf\n"
      "v_mfma_f32_16x16x16_bf16 v[20:23], v[54:55], v[38:39], v[20:23]\n"
      "ds_read_b128 v[52:55], v3 offset:31744\n"
      "v_add_f32 v32, v32, v33\n"
      "v_exp_f32 v34, v32\n"
      "v_max_f32 v35, v35, v32\n"
      "v_add_f32 v7, v7, v34\n"
      "v_cvt_pk_bf16_f32 v34, v34, v34\n"
      "v_and_b32 v36, v4, v34\n"
      "v_and_b32 v37, v5, v34\n"
      "s_waitcnt lgkmcnt(3)\n"
      "v_mfma_f32_16x16x32_bf16 v[24:27], v[40:43], v[8:11], 0\n"
      "s_waitcnt vmcnt(8)\n"
      "ds_read_b128 v[40:43], v3 offset:36864\n"
      "v_sub_f32_dpp v32, v28, v6 quad_perm:[0,0,0,0] row_mask:0xf bank_mask:0xf\n"
      "v_mov_b32_dpp v33, v30 quad_perm:[2,2,2,2] row_mask:0xf bank_mask:0xf\n"
      "s_waitcnt lgkmcnt(3)\n"
      "v_mfma_f32_16x16x16_bf16 v[16:19], v[48:49], v[36:37], v[16:19]\n"
      "v_add_f32_dpp v32, v29, v32 quad_perm:[1,1,1,1] row_mask:0xf bank_mask:0xf\n"
      "v_add_f32_dpp v33, v31, v33 quad_perm:[3,3,3,3] row_mask:0xf bank_mask:0xf\n"
      "v_mfma_f32_16x16x16_bf16 v[20:23], v[50:51], v[36:37], v[20:23]\n"
      "ds_read_b128 v[48:51], v3 offset:33792\n"
      "v_add_f32 v32, v32, v33\n"
      "v_exp_f32 v34, v32\n"
      "v_max_f32 v35, v35, v32\n"
      "v_add_f32 v7, v7, v34\n"
      "v_cvt_pk_bf16_f32 v34, v34, v34\n"
      "v_and_b32 v38, v4, v34\n"
      "v_and_b32 v39, v5, v34\n"
      "global_load_dwordx4 a[28:31], v0, s[42:43] nt\n"
      "s_add_u32 s42, s42, 110592\n"
      "s_addc_u32 s43, s43, 0\n"
      "s_waitcnt lgkmcnt(3)\n"
      "v_mfma_f32_16x16x32_bf16 v[28:31], v[44:47], v[8:11], 0\n"
      "v_sub_f32_dpp v32, v24, v6 quad_perm:[0,0,0,0] row_mask:0xf bank_mask:0xf\n"
      "v_mov_b32_dpp v33, v26 quad_perm:[2,2,2,2] row_mask:0xf bank_mask:0xf\n"
      "s_waitcnt lgkmcnt(2)\n"
      "v_mfma_f32_16x16x16_bf16 v[16:19], v[52:53], v[38:39], v[16:19]\n"
      "v_add_f32_dpp v32, v25, v32 quad_perm:[1,1,1,1] row_mask:0xf bank_mask:0xf\n"
      "v_add_f32_dpp v33, v27, v33 quad_perm:[3,3,3,3] row_mask:0xf bank_mask:0xf\n"
      "v_mfma_f32_16x16x16_bf16 v[20:23], v[54:55], v[38:39], v[20:23]\n"
      "ds_read_b128 v[52:55], v3 offset:35840\n"
      "v_add_f32 v32, v32, v33\n"
      "v_exp_f32 v34, v32\n"
      "v_max_f32 v35, v35, v32\n"
      "v_add_f32 v7, v7, v34\n"
      "v_cvt_pk_bf16_f32 v34, v34, v34\n"
      "v_and_b32 v36, v4, v34\n"
      "v_and_b32 v37, v5, v34\n"
      "s_waitcnt lgkmcnt(2)\n"
      "v_mfma_f32_16x16x32_bf16 v[24:27], v[40:43], v[8:11], 0\n"
      "v_sub_f32_dpp v32, v28, v6 quad_perm:[0,0,0,0] row_mask:0xf bank_mask:0xf\n"
      "v_mov_b32_dpp v33, v30 quad_perm:[2,2,2,2] row_mask:0xf bank_mask:0xf\n"
      "s_waitcnt lgkmcnt(1)\n"
      "v_mfma_f32_16x16x16_bf16 v[16:19], v[48:49], v[36:37], v[16:19]\n"
      "v_add_f32_dpp v32, v29, v32 quad_perm:[1,1,1,1] row_mask:0xf bank_mask:0xf\n"
      "v_add_f32_dpp v33, v31, v33 quad_perm:[3,3,3,3] row_mask:0xf bank_mask:0xf\n"
      "v_mfma_f32_16x16x16_bf16 v[20:23], v[50:51], v[36:37], v[20:23]\n"
      "s_waitcnt vmcnt(8)\n"
      "ds_read_b128 v[48:51], v3 offset:37888\n"
      "v_add_f32 v32, v32, v33\n"
      "v_exp_f32 v34, v32\n"
      "v_max_f32 v35, v35, v32\n"
      "v_add_f32 v7, v7, v34\n"
      "v_cvt_pk_bf16_f32 v34, v34, v34\n"
      "v_and_b32 v38, v4, v34\n"
      "v_and_b32 v39, v5, v34\n"
      "global_load_dwordx4 a[32:35], v1, s[40:41] nt\n"
      "s_add_u32 s40, s40, 110592\n"
      "s_addc_u32 s41, s41, 0\n"
      "v_sub_f32_dpp v32, v24, v6 quad_perm:[0,0,0,0] row_mask:0xf bank_mask:0xf\n"
      "v_mov_b32_dpp v33, v26 quad_perm:[2,2,2,2] row_mask:0xf bank_mask:0xf\n"
      "s_waitcnt lgkmcnt(1)\n"
      "v_mfma_f32_16x16x16_bf16 v[16:19], v[52:53], v[38:39], v[16:19]\n"
      "v_add_f32_dpp v32, v25, v32 quad_perm:[1,1,1,1] row_mask:0xf bank_mask:0xf\n"
      "v_add_f32_dpp v33, v27, v33 quad_perm:[3,3,3,3] row_mask:0xf bank_mask:0xf\n"
      "v_mfma_f32_16x16x16_bf16 v[20:23], v[54:55], v[38:39], v[20:23]\n"
      "v_add_f32 v32, v32, v33\n"
      "v_add_f32 v32, s69, v32\n"
      "v_exp_f32 v34, v32\n"
      "v_max_f32 v35, v35, v32\n"
      "v_add_f32 v7, v7, v34\n"
      "v_cvt_pk_bf16_f32 v34, v34, v34\n"
      "v_and_b32 v36, v4, v34\n"
      "v_and_b32 v37, v5, v34\n"
      "s_waitcnt lgkmcnt(0)\n"
      "s_nop 0\n"
      "v_mfma_f32_16x16x16_bf16 v[16:19], v[48:49], v[36:37], v[16:19]\n"
      "v_mfma_f32_16x16x16_bf16 v[20:23], v[50:51], v[36:37], v[20:23]\n"
      "s_waitcnt vmcnt(8)\n"
      "global_load_dwordx4 a[36:39], v0, s[42:43] nt\n"
      "s_add_u32 s42, s42, 110592\n"
      "s_addc_u32 s43, s43, 0\n"
      "v_cmp_lt_f32 vcc, 0x42800000, v35\n"
      "s_nop 0\n"
      "s_cbranch_vccnz L_fb6_%=\n"
      "L_ret6_%=:\n"
      "v_mov_b32 v49, v7\n"
      "v_lshlrev_b32 v41, 2, v0\n"
      "v_and_b32 v41, 0x300, v41\n"
      "v_permlane32_swap_b32 v49, v7\n"
      "v_lshrrev_b32 v42, 4, v0\n"
      "v_add_f32 v7, v49, v7\n"
      "v_and_b32 v42, 0x30, v42\n"
      "v_mov_b32 v49, v7\n"
      "v_add_u32 v41, v41, v42\n"
      "v_lshrrev_b32 v40, 2, v0\n"
      "v_permlane16_swap_b32 v49, v7\n"
      "v_and_b32 v42, 12, v40\n"
      "v_add_f32 v7, v49, v7\n"
      "v_add_u32 v41, v41, v42\n"
      "s_lshl_b32 s62, s60, 10\n"
      "s_add_u32 s62, s62, 155648\n"
      "v_add_u32 v41, s62, v41\n"
      "v_lshrrev_b32 v43, 3, v0\n"
      "v_and_b32 v43, 24, v43\n"
      "s_lshl_b32 s62, s60, 5\n"
      "s_add_u32 s62, s62, 159744\n"
      "v_add_u32 v43, s62, v43\n"
      "v_cvt_pk_bf16_f32 v36, v16, v17\n"
      "v_cvt_pk_bf16_f32 v37, v18, v19\n"
      "v_cvt_pk_bf16_f32 v38, v20, v21\n"
      "v_cvt_pk_bf16_f32 v39, v22, v23\n"
      "s_mov_b64 exec, 0x1111\n"
      "ds_write_b64 v43, v[6:7]\n"
      "s_mov_b64 exec, -1\n"
      "ds_write_b32 v41, v36 offset:0\n"
      "ds_write_b32 v41, v37 offset:64\n"
      "ds_write_b32 v41, v38 offset:128\n"
      "ds_write_b32 v41, v39 offset:192\n"
      "s_lshl_b32 s62, s60, 8\n"
      "s_add_u32 s62, s62, 155648\n"
      "v_add_u32 v42, s62, v40\n"
      "s_lshl_b32 s62, s60, 3\n"
      "s_add_u32 s62, s62, 159744\n"
      "v_mov_b32 v50, s62\n"
      "s_waitcnt lgkmcnt(0)\n"
      "s_barrier\n"
      "ds_read_b64 v[28:29], v50 offset:0\n"
      "ds_read_b64 v[30:31], v50 offset:32\n"
      "ds_read_b64 v[32:33], v50 offset:64\n"
      "ds_read_b64 v[34:35], v50 offset:96\n"
      "ds_read_b32 v24, v42 offset:0\n"
      "ds_read_b32 v25, v42 offset:1024\n"
      "ds_read_b32 v26, v42 offset:2048\n"
      "ds_read_b32 v27, v42 offset:3072\n"
      "s_add_u32 s56, s44, 1327104\n"
      "s_addc_u32 s57, s45, 0\n"
      "s_waitcnt lgkmcnt(4)\n"
      "v_max3_f32 v43, v28, v30, v32\n"
      "v_max_f32 v43, v43, v34\n"
      "v_sub_f32 v28, v28, v43\n"
      "v_sub_f32 v30, v30, v43\n"
      "v_sub_f32 v32, v32, v43\n"
      "v_sub_f32 v34, v34, v43\n"
      "v_exp_f32 v28, v28\n"
      "v_exp_f32 v30, v30\n"
      "v_exp_f32 v32, v32\n"
      "v_exp_f32 v34, v34\n"
      "s_waitcnt lgkmcnt(0)\n"
      "v_lshlrev_b32 v44, 16, v24\n"
      "v_and_b32 v45, 0xffff0000, v24\n"
      "v_lshlrev_b32 v46, 16, v25\n"
      "v_and_b32 v47, 0xffff0000, v25\n"
      "v_lshlrev_b32 v48, 16, v26\n"
      "v_and_b32 v49, 0xffff0000, v26\n"
      "v_lshlrev_b32 v50, 16, v27\n"
      "v_and_b32 v51, 0xffff0000, v27\n"
      "v_mul_f32 v38, v28, v29\n"
      "v_pk_mul_f32 v[36:37], v[44:45], v[28:29] op_sel_hi:[1,0]\n"
      "v_fmac_f32 v38, v30, v31\n"
      "v_pk_fma_f32 v[36:37], v[46:47], v[30:31], v[36:37] op_sel_hi:[1,0,1]\n"
      "v_fmac_f32 v38, v32, v33\n"
      "v_pk_fma_f32 v[36:37], v[48:49], v[32:33], v[36:37] op_sel_hi:[1,0,1]\n"
      "v_fmac_f32 v38, v34, v35\n"
      "v_pk_fma_f32 v[36:37], v[50:51], v[34:35], v[36:37] op_sel_hi:[1,0,1]\n"
      "v_rcp_f32 v39, v38\n"
      "v_log_f32 v38, v38\n"
      "v_mul_f32 v36, v39, v36\n"
      "v_mul_f32 v37, v39, v37\n"
      "v_add_f32 v38, v38, v43\n"
      "v_cvt_pk_bf16_f32 v36, v36, v37\n"
      "v_min_u32 v38, -2, v38\n"
      "v_min_u32 v36, -2, v36\n"
      "global_store_dword v40, v36, s[56:57] sc1\n"
      "s_mov_b32 exec_hi, 0\n"
      "global_store_dword v40, v38, s[56:57] offset:256 sc1\n"
      "s_mov_b64 exec, -1\n"
      "s_add_u32 s56, s44, 2985984\n"
      "s_addc_u32 s57, s45, 0\n"
      "global_store_dword v40, v12, s[56:57] sc1\n"
      "s_mov_b32 exec_hi, 0\n"
      "global_store_dword v40, v13, s[56:57] offset:256 sc1\n"
      "s_mov_b64 exec, -1\n"
      "s_waitcnt vmcnt(8)\n"
      "global_load_dwordx4 a[40:43], v1, s[40:41] nt\n"
      "s_add_u32 s40, s40, 110592\n"
      "s_addc_u32 s41, s41, 0\n"
      "s_waitcnt vmcnt(8)\n"
      "global_load_dwordx4 a[44:47], v0, s[42:43] nt\n"
      "s_add_u32 s42, s42, 110592\n"
      "s_addc_u32 s43, s43, 0\n"
      "s_waitcnt vmcnt(8)\n"
      "global_load_dwordx4 a[48:51], v1, s[40:41] nt\n"
      "s_add_u32 s40, s40, 110592\n"
      "s_addc_u32 s41, s41, 0\n"
      "s_waitcnt vmcnt(8)\n"
      "global_load_dwordx4 a[52:55], v0, s[42:43] nt\n"
      "s_add_u32 s42, s42, 110592\n"
      "s_addc_u32 s43, s43, 0\n"
      "s_barrier\n"
      "L_per5_%=:\n"
      "s_add_u32 s56, s87, L_per6_%=-L_begin_%=\n"
      "s_min_u32 s56, s56, L_end_%=-L_begin_%=-1024\n"
      "s_add_u32 s56, s84, s56\n"
      "s_addc_u32 s57, s85, 0\n"
      "s_mov_b32 m0, s86\n"
      "s_nop 0\n"
      "global_load_lds_dword v0, s[56:57]\n"
      "s_waitcnt vmcnt(9)\n"
      "global_load_dwordx4 a[56:59], v1, s[40:41] nt\n"
      "s_add_u32 s40, s40, 110592\n"
      "s_addc_u32 s41, s41, 0\n"
      "s_waitcnt vmcnt(9)\n"
      "global_load_dwordx4 a[60:63], v0, s[42:43] nt\n"
      "s_add_u32 s42, s42, 110592\n"
      "s_addc_u32 s43, s43, 0\n"
      "s_waitcnt vmcnt(9)\n"
      "global_load_dwordx4 a[64:67], v1, s[40:41] nt\n"
      "s_add_u32 s40, s40, 110592\n"
      "s_addc_u32 s41, s41, 0\n"
      "s_waitcnt vmcnt(9)\n"
      "global_load_dwordx4 a[68:71], v0, s[42:43] nt\n"
      "s_add_u32 s42, s42, 110592\n"
      "s_addc_u32 s43, s43, 0\n"
      "s_waitcnt vmcnt(9)\n"
      "global_load_dwordx4 a[72:75], v1, s[40:41] nt\n"
      "s_add_u32 s40, s40, 110592\n"
      "s_addc_u32 s41, s41, 0\n"
      "s_add_u32 s56, s48, 40960\n"
      "s_addc_u32 s57, s49, 0\n"
      "s_mov_b64 exec, 0xffff\n"
      "global_load_dwordx4 v[8:11], v0, s[56:57] sc1\n"
      "s_mov_b64 exec, -1\n"
      "s_add_u32 s56, s48, 81920\n"
      "s_addc_u32 s57, s49, 0\n"
      "s_mov_b64 exec, 0xffff\n"
      "global_load_dwordx4 v[12:15], v0, s[56:57] sc1\n"
      "s_mov_b64 exec, -1\n"
      "s_waitcnt vmcnt(11)\n"
      "global_load_dwordx4 a[76:79], v0, s[42:43] nt\n"
      "s_add_u32 s42, s42, 110592\n"
      "s_addc_u32 s43, s43, 0\n"
      "s_waitcnt vmcnt(11)\n"
      "global_load_dwordx4 a[80:83], v1, s[40:41] nt\n"
      "s_add_u32 s40, s40, 110592\n"
      "s_addc_u32 s41, s41, 0\n"
      "s_waitcnt vmcnt(11)\n"
      "global_load_dwordx4 a[84:87], v0, s[42:43] nt\n"
      "s_add_u32 s42, s42, 110592\n"
      "s_addc_u32 s43, s43, 0\n"
      "s_waitcnt vmcnt(11)\n"
      "global_load_dwordx4 a[88:91], v1, s[40:41] nt\n"
      "s_add_u32 s40, s40, 110592\n"
      "s_addc_u32 s41, s41, 0\n"
      "s_waitcnt vmcnt(10)\n"
      "global_load_dwordx4 a[92:95], v0, s[42:43] nt\n"
      "s_add_u32 s42, s42, 110592\n"
      "s_addc_u32 s43, s43, 0\n"
      "s_waitcnt vmcnt(10)\n"
      "global_load_dwordx4 a[96:99], v1, s[40:41] nt\n"
      "s_add_u32 s40, s40, 110592\n"
      "s_addc_u32 s41, s41, 0\n"
      "s_waitcnt vmcnt(10)\n"
      "global_load_dwordx4 a[100:103], v0, s[42:43] nt\n"
      "s_add_u32 s42, s42, 110592\n"
      "s_addc_u32 s43, s43, 0\n"
      "s_waitcnt vmcnt(10)\n"
      "global_load_dwordx4 a[104:107], v1, s[40:41] nt\n"
      "s_add_u32 s40, s40, 110592\n"
      "s_addc_u32 s41, s41, 0\n"
      "s_waitcnt vmcnt(10)\n"
      "global_load_dwordx4 a[108:111], v0, s[42:43] nt\n"
      "s_add_u32 s42, s42, 110592\n"
      "s_addc_u32 s43, s43, 0\n"
      "s_waitcnt vmcnt(9)\n"
      "s_mov_b64 exec, 0xffff\n"
      "s_mov_b32 s53, 0\n"
      "L_qc17_%=:\n"
      "v_max3_u32 v49, v12, v13, v14\n"
      "v_max_u32 v49, v49, v15\n"
      "v_cmp_eq_u32 vcc, -1, v49\n"
      "s_nop 0\n"
      "s_cbranch_vccz L_qo18_%=\n"
      "s_cmp_lg_u32 s54, 0\n"
      "s_cbranch_scc1 L_qo18_%=\n"
      "s_add_u32 s56, s48, 81920\n"
      "s_addc_u32 s57, s49, 0\n"
      "global_load_dwordx4 v[12:15], v0, s[56:57] sc1\n"
      "s_add_u32 s53, s53, 1\n"
      "s_waitcnt vmcnt(0)\n"
      "s_cmp_lt_u32 s53, 30000\n"
      "s_cbranch_scc1 L_qc17_%=\n"
      "s_mov_b32 s54, 202\n"
      "v_mov_b32 v49, 202\n"
      "v_mov_b32 v50, 256\n"
      "global_store_dword v50, v49, s[58:59] sc1\n"
      "L_qo18_%=:\n"
      "s_lshl_b32 s62, s60, 8\n"
      "s_add_u32 s62, s62, 160896\n"
      "v_add_u32 v49, s62, v0\n"
      "ds_write_b128 v49, v[12:15]\n"
      "s_mov_b64 exec, -1\n"
      "s_waitcnt lgkmcnt(0)\n"
      "s_barrier\n"
      "ds_read_b128 v[12:15], v2 offset:1024\n"
      "s_waitcnt lgkmcnt(0)\n"
      "v_mfma_f32_16x16x32_bf16 v[24:27], a[152:155], v[12:15], 0\n"
      "v_mfma_f32_16x16x32_bf16 v[28:31], a[160:163], v[12:15], 0\n"
      "s_nop 6\n"
      "v_mov_b32_dpp v34, v24 quad_perm:[0,0,0,0] row_mask:0xf bank_mask:0xf\n"
      "v_mov_b32_dpp v33, v26 quad_perm:[2,2,2,2] row_mask:0xf bank_mask:0xf\n"
      "s_nop 0\n"
      "v_add_f32_dpp v34, v25, v34 quad_perm:[1,1,1,1] row_mask:0xf bank_mask:0xf\n"
      "v_add_f32_dpp v33, v27, v33 quad_perm:[3,3,3,3] row_mask:0xf bank_mask:0xf\n"
      "v_add_f32 v34, v34, v33\n"
      "v_mov_b32_dpp v32, v28 quad_perm:[0,0,0,0] row_mask:0xf bank_mask:0xf\n"
      "v_mov_b32_dpp v33, v30 quad_perm:[2,2,2,2] row_mask:0xf bank_mask:0xf\n"
      "s_nop 0\n"
      "v_add_f32_dpp v32, v29, v32 quad_perm:[1,1,1,1] row_mask:0xf bank_mask:0xf\n"
      "v_add_f32_dpp v33, v31, v33 quad_perm:[3,3,3,3] row_mask:0xf bank_mask:0xf\n"
      "v_add_f32 v32, v32, v33\n"
      "v_max_f32 v35, v34, v32\n"
      "v_mov_b32 v33, v35\n"
      "s_nop 1\n"
      "v_permlane32_swap_b32 v33, v35\n"
      "v_max_f32 v35, v33, v35\n"
      "v_mov_b32 v33, v35\n"
      "s_nop 1\n"
      "v_permlane16_swap_b32 v33, v35\n"
      "v_max_f32 v6, v33, v35\n"
      "v_sub_f32 v34, v34, v6\n"
      "v_sub_f32 v32, v32, v6\n"
      "v_mov_b32 v35, 0\n"
      "v_exp_f32 v34, v34\n"
      "s_nop 0\n"
      "v_mov_b32 v7, v34\n"
      "v_cvt_pk_bf16_f32 v34, v34, v34\n"
      "v_and_b32 v36, v4, v34\n"
      "v_and_b32 v37, v5, v34\n"
      "v_mfma_f32_16x16x32_bf16 v[24:27], a[168:171], v[12:15], 0\n"
      "s_nop 0\n"
      "v_mfma_f32_16x16x16_bf16 v[16:19], a[156:157], v[36:37], 0\n"
      "v_mfma_f32_16x16x16_bf16 v[20:23], a[158:159], v[36:37], 0\n"
      "v_exp_f32 v34, v32\n"
      "v_max_f32 v35, v35, v32\n"
      "v_add_f32 v7, v7, v34\n"
      "v_cvt_pk_bf16_f32 v34, v34, v34\n"
      "v_and_b32 v38, v4, v34\n"
      "v_and_b32 v39, v5, v34\n"
      "v_mfma_f32_16x16x32_bf16 v[28:31], a[176:179], v[12:15], 0\n"
      "v_sub_f32_dpp v32, v24, v6 quad_perm:[0,0,0,0] row_mask:0xf bank_mask:0xf\n"
      "v_mov_b32_dpp v33, v26 quad_perm:[2,2,2,2] row_mask:0xf bank_mask:0xf\n"
      "v_mfma_f32_16x16x16_bf16 v[16:19], a[164:165], v[38:39], v[16:19]\n"
      "v_add_f32_dpp v32, v25, v32 quad_perm:[1,1,1,1] row_mask:0xf bank_mask:0xf\n"
      "v_add_f32_dpp v33, v27, v33 quad_perm:[3,3,3,3] row_mask:0xf bank_mask:0xf\n"
      "v_mfma_f32_16x16x16_bf16 v[20:23], a[166:167], v[38:39], v[20:23]\n"
      "v_add_f32 v32, v32, v33\n"
      "v_exp_f32 v34, v32\n"
      "v_max_f32 v35, v35, v32\n"
      "v_add_f32 v7, v7, v34\n"
      "v_cvt_pk_bf16_f32 v34, v34, v34\n"
      "v_and_b32 v36, v4, v34\n"
      "v_and_b32 v37, v5, v34\n"
      "v_mfma_f32_16x16x32_bf16 v[24:27], a[184:187], v[12:15], 0\n"
      "v_sub_f32_dpp v32, v28, v6 quad_perm:[0,0,0,0] row_mask:0xf bank_mask:0xf\n"
      "v_mov_b32_dpp v33, v30 quad_perm:[2,2,2,2] row_mask:0xf bank_mask:0xf\n"
      "v_mfma_f32_16x16x16_bf16 v[16:19], a[172:173], v[36:37], v[16:19]\n"
      "v_add_f32_dpp v32, v29, v32 quad_perm:[1,1,1,1] row_mask:0xf bank_mask:0xf\n"
      "v_add_f32_dpp v33, v31, v33 quad_perm:[3,3,3,3] row_mask:0xf bank_mask:0xf\n"
      "v_mfma_f32_16x16x16_bf16 v[20:23], a[174:175], v[36:37], v[20:23]\n"
      "v_add_f32 v32, v32, v33\n"
      "v_exp_f32 v34, v32\n"
      "v_max_f32 v35, v35, v32\n"
      "v_add_f32 v7, v7, v34\n"
      "v_cvt_pk_bf16_f32 v34, v34, v34\n"
      "v_and_b32 v38, v4, v34\n"
      "v_and_b32 v39, v5, v34\n"
      "s_waitcnt vmcnt(8)\n"
      "global_load_dwordx4 a[112:115], v1, s[40:41] nt\n"
      "s_add_u32 s40, s40, 110592\n"
      "s_addc_u32 s41, s41, 0\n"
      "v_mfma_f32_16x16x32_bf16 v[28:31], a[192:195], v[12:15], 0\n"
      "v_sub_f32_dpp v32, v24, v6 quad_perm:[0,0,0,0] row_mask:0xf bank_mask:0xf\n"
      "v_mov_b32_dpp v33, v26 quad_perm:[2,2,2,2] row_mask:0xf bank_mask:0xf\n"
      "v_mfma_f32_16x16x16_bf16 v[16:19], a[180:181], v[38:39], v[16:19]\n"
      "v_add_f32_dpp v32, v25, v32 quad_perm:[1,1,1,1] row_mask:0xf bank_mask:0xf\n"
      "v_add_f32_dpp v33, v27, v33 quad_perm:[3,3,3,3] row_mask:0xf bank_mask:0xf\n"
      "v_mfma_f32_16x16x16_bf16 v[20:23], a[182:183], v[38:39], v[20:23]\n"
      "v_add_f32 v32, v32, v33\n"
      "v_exp_f32 v34, v32\n"
      "v_max_f32 v35, v35, v32\n"
      "v_add_f32 v7, v7, v34\n"
      "v_cvt_pk_bf16_f32 v34, v34, v34\n"
      "v_and_b32 v36, v4, v34\n"
      "v_and_b32 v37, v5, v34\n"
      "v_mfma_f32_16x16x32_bf16 v[24:27], a[200:203], v[12:15], 0\n"
      "v_sub_f32_dpp v32, v28, v6 quad_perm:[0,0,0,0] row_mask:0xf bank_mask:0xf\n"
      "v_mov_b32_dpp v33, v30 quad_perm:[2,2,2,2] row_mask:0xf bank_mask:0xf\n"
      "v_mfma_f32_16x16x16_bf16 v[16:19], a[188:189], v[36:37], v[16:19]\n"
      "v_add_f32_dpp v32, v29, v32 quad_perm:[1,1,1,1] row_mask:0xf bank_mask:0xf\n"
      "v_add_f32_dpp v33, v31, v33 quad_perm:[3,3,3,3] row_mask:0xf bank_mask:0xf\n"
      "v_mfma_f32_16x16x16_bf16 v[20:23], a[190:191], v[36:37], v[20:23]\n"
      "v_add_f32 v32, v32, v33\n"
      "v_exp_f32 v34, v32\n"
      "v_max_f32 v35, v35, v32\n"
      "v_add_f32 v7, v7, v34\n"
      "v_cvt_pk_bf16_f32 v34, v34, v34\n"
      "v_and_b32 v38, v4, v34\n"
      "v_and_b32 v39, v5, v34\n"
      "s_waitcnt vmcnt(8)\n"
      "global_load_dwordx4 a[116:119], v0, s[42:43] nt\n"
      "s_add_u32 s42, s42, 110592\n"
      "s_addc_u32 s43, s43, 0\n"
      "v_mfma_f32_16x16x32_bf16 v[28:31], a[208:211], v[12:15], 0\n"
      "v_sub_f32_dpp v32, v24, v6 quad_perm:[0,0,0,0] row_mask:0xf bank_mask:0xf\n"
      "v_mov_b32_dpp v33, v26 quad_perm:[2,2,2,2] row_mask:0xf bank_mask:0xf\n"
      "v_mfma_f32_16x16x16_bf16 v[16:19], a[196:197], v[38:39], v[16:19]\n"
      "v_add_f32_dpp v32, v25, v32 quad_perm:[1,1,1,1] row_mask:0xf bank_mask:0xf\n"
      "v_add_f32_dpp v33, v27, v33 quad_perm:[3,3,3,3] row_mask:0xf bank_mask:0xf\n"
      "v_mfma_f32_16x16x16_bf16 v[20:23], a[198:199], v[38:39], v[20:23]\n"
      "v_add_f32 v32, v32, v33\n"
      "v_exp_f32 v34, v32\n"
      "v_max_f32 v35, v35, v32\n"
      "v_add_f32 v7, v7, v34\n"
      "v_cvt_pk_bf16_f32 v34, v34, v34\n"
      "v_and_b32 v36, v4, v34\n"
      "v_and_b32 v37, v5, v34\n"
      "v_mfma_f32_16x16x32_bf16 v[24:27], a[216:219], v[12:15], 0\n"
      "v_sub_f32_dpp v32, v28, v6 quad_perm:[0,0,0,0] row_mask:0xf bank_mask:0xf\n"
      "v_mov_b32_dpp v33, v30 quad_perm:[2,2,2,2] row_mask:0xf bank_mask:0xf\n"
      "v_mfma_f32_16x16x16_bf16 v[16:19], a[204:205], v[36:37], v[16:19]\n"
      "v_add_f32_dpp v32, v29, v32 quad_perm:[1,1,1,1] row_mask:0xf bank_mask:0xf\n"
      "v_add_f32_dpp v33, v31, v33 quad_perm:[3,3,3,3] row_mask:0xf bank_mask:0xf\n"
      "v_mfma_f32_16x16x16_bf16 v[20:23], a[206:207], v[36:37], v[20:23]\n"
      "v_add_f32 v32, v32, v33\n"
      "v_exp_f32 v34, v32\n"
      "v_max_f32 v35, v35, v32\n"
      "v_add_f32 v7, v7, v34\n"
      "v_cvt_pk_bf16_f32 v34, v34, v34\n"
      "v_and_b32 v38, v4, v34\n"
      "v_and_b32 v39, v5, v34\n"
      "v_mfma_f32_16x16x32_bf16 v[28:31], a[224:227], v[12:15], 0\n"
      "v_sub_f32_dpp v32, v24, v6 quad_perm:[0,0,0,0] row_mask:0xf bank_mask:0xf\n"
      "v_mov_b32_dpp v33, v26 quad_perm:[2,2,2,2] row_mask:0xf bank_mask:0xf\n"
      "v_mfma_f32_16x16x16_bf16 v[16:19], a[212:213], v[38:39], v[16:19]\n"
      "v_add_f32_dpp v32, v25, v32 quad_perm:[1,1,1,1] row_mask:0xf bank_mask:0xf\n"
      "v_add_f32_dpp v33, v27, v33 quad_perm:[3,3,3,3] row_mask:0xf bank_mask:0xf\n"
      "v_mfma_f32_16x16x16_bf16 v[20:23], a[214:215], v[38:39], v[20:23]\n"
      "v_add_f32 v32, v32, v33\n"
      "v_exp_f32 v34, v32\n"
      "v_max_f32 v35, v35, v32\n"
      "v_add_f32 v7, v7, v34\n"
      "v_cvt_pk_bf16_f32 v34, v34, v34\n"
      "v_and_b32 v36, v4, v34\n"
      "v_and_b32 v37, v5, v34\n"
      "s_waitcnt vmcnt(8)\n"
      "global_load_dwordx4 a[120:123], v1, s[40:41] nt\n"
      "s_add_u32 s40, s40, 110592\n"
      "s_addc_u32 s41, s41, 0\n"
      "v_mfma_f32_16x16x32_bf16 v[24:27], a[232:235], v[12:15], 0\n"
      "v_sub_f32_dpp v32, v28, v6 quad_perm:[0,0,0,0] row_mask:0xf bank_mask:0xf\n"
      "v_mov_b32_dpp v33, v30 quad_perm:[2,2,2,2] row_mask:0xf bank_mask:0xf\n"
      "v_mfma_f32_16x16x16_bf16 v[16:19], a[220:221], v[36:37], v[16:19]\n"
      "v_add_f32_dpp v32, v29, v32 quad_perm:[1,1,1,1] row_mask:0xf bank_mask:0xf\n"
      "v_add_f32_dpp v33, v31, v33 quad_perm:[3,3,3,3] row_mask:0xf bank_mask:0xf\n"
      "v_mfma_f32_16x16x16_bf16 v[20:23], a[222:223], v[36:37], v[20:23]\n"
      "v_add_f32 v32, v32, v33\n"
      "v_exp_f32 v34, v32\n"
      "v_max_f32 v35, v35, v32\n"
      "v_add_f32 v7, v7, v34\n"
      "v_cvt_pk_bf16_f32 v34, v34, v34\n"
      "v_and_b32 v38, v4, v34\n"
      "v_and_b32 v39, v5, v34\n"
      "v_mfma_f32_16x16x32_bf16 v[28:31], a[240:243], v[12:15], 0\n"
      "v_sub_f32_dpp v32, v24, v6 quad_perm:[0,0,0,0] row_mask:0xf bank_mask:0xf\n"
      "v_mov_b32_dpp v33, v26 quad_perm:[2,2,2,2] row_mask:0xf bank_mask:0xf\n"
      "v_mfma_f32_16x16x16_bf16 v[16:19], a[228:229], v[38:39], v[16:19]\n"
      "v_add_f32_dpp v32, v25, v32 quad_perm:[1,1,1,1] row_mask:0xf bank_mask:0xf\n"
      "v_add_f32_dpp v33, v27, v33 quad_perm:[3,3,3,3] row_mask:0xf bank_mask:0xf\n"
      "v_mfma_f32_16x16x16_bf16 v[20:23], a[230:231], v[38:39], v[20:23]\n"
      "v_add_f32 v32, v32, v33\n"
      "v_exp_f32 v34, v32\n"
      "v_max_f32 v35, v35, v32\n"
      "v_add_f32 v7, v7, v34\n"
      "v_cvt_pk_bf16_f32 v34, v34, v34\n"
      "v_and_b32 v36, v4, v34\n"
      "v_and_b32 v37, v5, v34\n"
      "s_waitcnt vmcnt(8)\n"
      "global_load_dwordx4 a[124:127], v0, s[42:43] nt\n"
      "s_add_u32 s42, s42, 110592\n"
      "s_addc_u32 s43, s43, 0\n"
      "v_mfma_f32_16x16x32_bf16 v[24:27], a[248:251], v[12:15], 0\n"
      "v_sub_f32_dpp v32, v28, v6 quad_perm:[0,0,0,0] row_mask:0xf bank_mask:0xf\n"
      "v_mov_b32_dpp v33, v30 quad_perm:[2,2,2,2] row_mask:0xf bank_mask:0xf\n"
      "v_mfma_f32_16x16x16_bf16 v[16:19], a[236:237], v[36:37], v[16:19]\n"
      "v_add_f32_dpp v32, v29, v32 quad_perm:[1,1,1,1] row_mask:0xf bank_mask:0xf\n"
      "v_add_f32_dpp v33, v31, v33 quad_perm:[3,3,3,3] row_mask:0xf bank_mask:0xf\n"
      "v_mfma_f32_16x16x16_bf16 v[20:23], a[238:239], v[36:37], v[20:23]\n"
      "v_add_f32 v32, v32, v33\n"
      "v_exp_f32 v34, v32\n"
      "v_max_f32 v35, v35, v32\n"
      "v_add_f32 v7, v7, v34\n"
      "v_cvt_pk_bf16_f32 v34, v34, v34\n"
      "v_and_b32 v38, v4, v34\n"
      "v_and_b32 v39, v5, v34\n"
      "v_mfma_f32_16x16x32_bf16 v[28:31], v[56:59], v[12:15], 0\n"
      "v_sub_f32_dpp v32, v24, v6 quad_perm:[0,0,0,0] row_mask:0xf bank_mask:0xf\n"
      "v_mov_b32_dpp v33, v26 quad_perm:[2,2,2,2] row_mask:0xf bank_mask:0xf\n"
      "v_mfma_f32_16x16x16_bf16 v[16:19], a[244:245], v[38:39], v[16:19]\n"
      "v_add_f32_dpp v32, v25, v32 quad_perm:[1,1,1,1] row_mask:0xf bank_mask:0xf\n"
      "v_add_f32_dpp v33, v27, v33 quad_perm:[3,3,3,3] row_mask:0xf bank_mask:0xf\n"
      "v_mfma_f32_16x16x16_bf16 v[20:23], a[246:247], v[38:39], v[20:23]\n"
      "v_add_f32 v32, v32, v33\n"
      "v_exp_f32 v34, v32\n"
      "v_max_f32 v35, v35, v32\n"
      "v_add_f32 v7, v7, v34\n"
      "v_cvt_pk_bf16_f32 v34, v34, v34\n"
      "v_and_b32 v36, v4, v34\n"
      "v_and_b32 v37, v5, v34\n"
      "s_waitcnt vmcnt(8)\n"
      "global_load_dwordx4 a[128:131], v1, s[40:41] nt\n"
      "s_add_u32 s40, s40, 110592\n"
      "s_addc_u32 s41, s41, 0\n"
      "v_mfma_f32_16x16x32_bf16 v[24:27], v[64:67], v[12:15], 0\n"
      "v_sub_f32_dpp v32, v28, v6 quad_perm:[0,0,0,0] row_mask:0xf bank_mask:0xf\n"
      "v_mov_b32_dpp v33, v30 quad_perm:[2,2,2,2] row_mask:0xf bank_mask:0xf\n"
      "v_mfma_f32_16x16x16_bf16 v[16:19], a[252:253], v[36:37], v[16:19]\n"
      "v_add_f32_dpp v32, v29, v32 quad_perm:[1,1,1,1] row_mask:0xf bank_mask:0xf\n"
      "v_add_f32_dpp v33, v31, v33 quad_perm:[3,3,3,3] row_mask:0xf bank_mask:0xf\n"
      "v_mfma_f32_16x16x16_bf16 v[20:23], a[254:255], v[36:37], v[20:23]\n"
      "v_add_f32 v32, v32, v33\n"
      "v_exp_f32 v34, v32\n"
      "v_max_f32 v35, v35, v32\n"
      "v_add_f32 v7, v7, v34\n"
      "v_cvt_pk_bf16_f32 v34, v34, v34\n"
      "v_and_b32 v38, v4, v34\n"
      "v_and_b32 v39, v5, v34\n"
      "v_mfma_f32_16x16x32_bf16 v[28:31], v[72:75], v[12:15], 0\n"
      "v_sub_f32_dpp v32, v24, v6 quad_perm:[0,0,0,0] row_mask:0xf bank_mask:0xf\n"
      "v_mov_b32_dpp v33, v26 quad_perm:[2,2,2,2] row_mask:0xf bank_mask:0xf\n"
      "v_mfma_f32_16x16x16_bf16 v[16:19], v[60:61], v[38:39], v[16:19]\n"
      "v_add_f32_dpp v32, v25, v32 quad_perm:[1,1,1,1] row_mask:0xf bank_mask:0xf\n"
      "v_add_f32_dpp v33, v27, v33 quad_perm:[3,3,3,3] row_mask:0xf bank_mask:0xf\n"
      "v_mfma_f32_16x16x16_bf16 v[20:23], v[62:63], v[38:39], v[20:23]\n"
      "v_add_f32 v32, v32, v33\n"
      "v_exp_f32 v34, v32\n"
      "v_max_f32 v35, v35, v32\n"
      "v_add_f32 v7, v7, v34\n"
      "v_cvt_pk_bf16_f32 v34, v34, v34\n"
      "v_and_b32 v36, v4, v34\n"
      "v_and_b32 v37, v5, v34\n"
      "v_mfma_f32_16x16x32_bf16 v[24:27], v[80:83], v[12:15], 0\n"
      "v_sub_f32_dpp v32, v28, v6 quad_perm:[0,0,0,0] row_mask:0xf bank_mask:0xf\n"
      "v_mov_b32_dpp v33, v30 quad_perm:[2,2,2,2] row_mask:0xf bank_mask:0xf\n"
      "v_mfma_f32_16x16x16_bf16 v[16:19], v[68:69], v[36:37], v[16:19]\n"
      "v_add_f32_dpp v32, v29, v32 quad_perm:[1,1,1,1] row_mask:0xf bank_mask:0xf\n"
      "v_add_f32_dpp v33, v31, v33 quad_perm:[3,3,3,3] row_mask:0xf bank_mask:0xf\n"
      "v_mfma_f32_16x16x16_bf16 v[20:23], v[70:71], v[36:37], v[20:23]\n"
      "v_add_f32 v32, v32, v33\n"
      "v_exp_f32 v34, v32\n"
      "v_max_f32 v35, v35, v32\n"
      "v_add_f32 v7, v7, v34\n"
      "v_cvt_pk_bf16_f32 v34, v34, v34\n"
      "v_and_b32 v38, v4, v34\n"
      "v_and_b32 v39, v5, v34\n"
      "s_waitcnt vmcnt(8)\n"
      "global_load_dwordx4 a[132:135], v0, s[42:43] nt\n"
      "s_add_u32 s42, s42, 110592\n"
      "s_addc_u32 s43, s43, 0\n"
      "v_mfma_f32_16x16x32_bf16 v[28:31], v[88:91], v[12:15], 0\n"
      "v_sub_f32_dpp v32, v24, v6 quad_perm:[0,0,0,0] row_mask:0xf bank_mask:0xf\n"
      "v_mov_b32_dpp v33, v26 quad_perm:[2,2,2,2] row_mask:0xf bank_mask:0xf\n"
      "v_mfma_f32_16x16x16_bf16 v[16:19], v[76:77], v[38:39], v[16:19]\n"
      "v_add_f32_dpp v32, v25, v32 quad_perm:[1,1,1,1] row_mask:0xf bank_mask:0xf\n"
      "v_add_f32_dpp v33, v27, v33 quad_perm:[3,3,3,3] row_mask:0xf bank_mask:0xf\n"
      "v_mfma_f32_16x16x16_bf16 v[20:23], v[78:79], v[38:39], v[20:23]\n"
      "v_add_f32 v32, v32, v33\n"
      "v_exp_f32 v34, v32\n"
      "v_max_f32 v35, v35, v32\n"
      "v_add_f32 v7, v7, v34\n"
      "v_cvt_pk_bf16_f32 v34, v34, v34\n"
      "v_and_b32 v36, v4, v34\n"
      "v_and_b32 v37, v5, v34\n"
      "v_mfma_f32_16x16x32_bf16 v[24:27], v[96:99], v[12:15], 0\n"
      "v_sub_f32_dpp v32, v28, v6 quad_perm:[0,0,0,0] row_mask:0xf bank_mask:0xf\n"
      "v_mov_b32_dpp v33, v30 quad_perm:[2,2,2,2] row_mask:0xf bank_mask:0xf\n"
      "v_mfma_f32_16x16x16_bf16 v[16:19], v[84:85], v[36:37], v[16:19]\n"
      "v_add_f32_dpp v32, v29, v32 quad_perm:[1,1,1,1] row_mask:0xf bank_mask:0xf\n"
      "v_add_f32_dpp v33, v31, v33 quad_perm:[3,3,3,3] row_mask:0xf bank_mask:0xf\n"
      "v_mfma_f32_16x16x16_bf16 v[20:23], v[86:87], v[36:37], v[20:23]\n"
      "v_add_f32 v32, v32, v33\n"
      "v_exp_f32 v34, v32\n"
      "v_max_f32 v35, v35, v32\n"
      "v_add_f32 v7, v7, v34\n"
      "v_cvt_pk_bf16_f32 v34, v34, v34\n"
      "v_and_b32 v38, v4, v34\n"
      "v_and_b32 v39, v5, v34\n"
      "s_waitcnt vmcnt(8)\n"
      "global_load_dwordx4 a[136:139], v1, s[40:41] nt\n"
      "s_add_u32 s40, s40, s51\n"
      "s_addc_u32 s41, s41, 0\n"
      "v_sub_f32_dpp v32, v24, v6 quad_perm:[0,0,0,0] row_mask:0xf bank_mask:0xf\n"
      "v_mov_b32_dpp v33, v26 quad_perm:[2,2,2,2] row_mask:0xf bank_mask:0xf\n"
      "v_mfma_f32_16x16x16_bf16 v[16:19], v[92:93], v[38:39], v[16:19]\n"
      "v_add_f32_dpp v32, v25, v32 quad_perm:[1,1,1,1] row_mask:0xf bank_mask:0xf\n"
      "v_add_f32_dpp v33, v27, v33 quad_perm:[3,3,3,3] row_mask:0xf bank_mask:0xf\n"
      "v_mfma_f32_16x16x16_bf16 v[20:23], v[94:95], v[38:39], v[20:23]\n"
      "v_add_f32 v32, v32, v33\n"
      "v_add_f32 v32, s69, v32\n"
      "v_exp_f32 v34, v32\n"
      "v_max_f32 v35, v35, v32\n"
      "v_add_f32 v7, v7, v34\n"
      "v_cvt_pk_bf16_f32 v34, v34, v34\n"
      "v_and_b32 v36, v4, v34\n"
      "v_and_b32 v37, v5, v34\n"
      "s_nop 1\n"
      "v_mfma_f32_16x16x16_bf16 v[16:19], v[100:101], v[36:37], v[16:19]\n"
      "v_mfma_f32_16x16x16_bf16 v[20:23], v[102:103], v[36:37], v[20:23]\n"
      "s_waitcnt vmcnt(8)\n"
      "global_load_dwordx4 a[140:143], v0, s[42:43] nt\n"
      "s_add_u32 s42, s42, s51\n"
      "s_addc_u32 s43, s43, 0\n"
      "v_cmp_lt_f32 vcc, 0x42800000, v35\n"
      "s_nop 0\n"
      "s_cbranch_vccnz L_fb7_%=\n"
      "L_ret7_%=:\n"
      "v_mov_b32 v49, v7\n"
      "v_lshlrev_b32 v41, 2, v0\n"
      "v_and_b32 v41, 0x300, v41\n"
      "v_permlane32_swap_b32 v49, v7\n"
      "v_lshrrev_b32 v42, 4, v0\n"
      "v_add_f32 v7, v49, v7\n"
      "v_and_b32 v42, 0x30, v42\n"
      "v_mov_b32 v49, v7\n"
      "v_add_u32 v41, v41, v42\n"
      "v_lshrrev_b32 v40, 2, v0\n"
      "v_permlane16_swap_b32 v49, v7\n"
      "v_and_b32 v42, 12, v40\n"
      "v_add_f32 v7, v49, v7\n"
      "v_add_u32 v41, v41, v42\n"
      "s_lshl_b32 s62, s60, 10\n"
      "s_add_u32 s62, s62, 155648\n"
      "v_add_u32 v41, s62, v41\n"
      "v_lshrrev_b32 v43, 3, v0\n"
      "v_and_b32 v43, 24, v43\n"
      "s_lshl_b32 s62, s60, 5\n"
      "s_add_u32 s62, s62, 159744\n"
      "v_add_u32 v43, s62, v43\n"
      "v_cvt_pk_bf16_f32 v36, v16, v17\n"
      "v_cvt_pk_bf16_f32 v37, v18, v19\n"
      "v_cvt_pk_bf16_f32 v38, v20, v21\n"
      "v_cvt_pk_bf16_f32 v39, v22, v23\n"
      "s_mov_b64 exec, 0x1111\n"
      "ds_write_b64 v43, v[6:7]\n"
      "s_mov_b64 exec, -1\n"
      "ds_write_b32 v41, v36 offset:0\n"
      "ds_write_b32 v41, v37 offset:64\n"
      "ds_write_b32 v41, v38 offset:128\n"
      "ds_write_b32 v41, v39 offset:192\n"
      "s_lshl_b32 s62, s60, 8\n"
      "s_add_u32 s62, s62, 155648\n"
      "v_add_u32 v42, s62, v40\n"
      "s_lshl_b32 s62, s60, 3\n"
      "s_add_u32 s62, s62, 159744\n"
      "v_mov_b32 v50, s62\n"
      "s_waitcnt lgkmcnt(0)\n"
      "s_barrier\n"
      "ds_read_b64 v[28:29], v50 offset:0\n"
      "ds_read_b64 v[30:31], v50 offset:32\n"
      "ds_read_b64 v[32:33], v50 offset:64\n"
      "ds_read_b64 v[34:35], v50 offset:96\n"
      "ds_read_b32 v24, v42 offset:0\n"
      "ds_read_b32 v25, v42 offset:1024\n"
      "ds_read_b32 v26, v42 offset:2048\n"
      "ds_read_b32 v27, v42 offset:3072\n"
      "s_waitcnt lgkmcnt(4)\n"
      "v_max3_f32 v43, v28, v30, v32\n"
      "v_max_f32 v43, v43, v34\n"
      "v_sub_f32 v28, v28, v43\n"
      "v_sub_f32 v30, v30, v43\n"
      "v_sub_f32 v32, v32, v43\n"
      "v_sub_f32 v34, v34, v43\n"
      "v_exp_f32 v28, v28\n"
      "v_exp_f32 v30, v30\n"
      "v_exp_f32 v32, v32\n"
      "v_exp_f32 v34, v34\n"
      "s_waitcnt lgkmcnt(0)\n"
      "v_lshlrev_b32 v44, 16, v24\n"
      "v_and_b32 v45, 0xffff0000, v24\n"
      "v_lshlrev_b32 v46, 16, v25\n"
      "v_and_b32 v47, 0xffff0000, v25\n"
      "v_lshlrev_b32 v48, 16, v26\n"
      "v_and_b32 v49, 0xffff0000, v26\n"
      "v_lshlrev_b32 v50, 16, v27\n"
      "v_and_b32 v51, 0xffff0000, v27\n"
      "v_mul_f32 v38, v28, v29\n"
      "v_pk_mul_f32 v[36:37], v[44:45], v[28:29] op_sel_hi:[1,0]\n"
      "v_fmac_f32 v38, v30, v31\n"
      "v_pk_fma_f32 v[36:37], v[46:47], v[30:31], v[36:37] op_sel_hi:[1,0,1]\n"
      "v_fmac_f32 v38, v32, v33\n"
      "v_pk_fma_f32 v[36:37], v[48:49], v[32:33], v[36:37] op_sel_hi:[1,0,1]\n"
      "v_fmac_f32 v38, v34, v35\n"
      "v_pk_fma_f32 v[36:37], v[50:51], v[34:35], v[36:37] op_sel_hi:[1,0,1]\n"
      "v_rcp_f32 v39, v38\n"
      "v_log_f32 v38, v38\n"
      "v_mul_f32 v36, v39, v36\n"
      "v_mul_f32 v37, v39, v37\n"
      "v_add_f32 v38, v38, v43\n"
      "v_cvt_pk_bf16_f32 v36, v36, v37\n"
      "v_min_u32 v38, -2, v38\n"
      "v_min_u32 v36, -2, v36\n"
      "v_mov_b32 v12, v36\n"
      "v_mov_b32 v13, v38\n"
      "s_waitcnt vmcnt(8)\n"
      "global_load_dwordx4 a[144:147], v1, s[40:41] nt\n"
      "s_add_u32 s40, s40, s52\n"
      "s_addc_u32 s41, s41, 0\n"
      "s_waitcnt vmcnt(8)\n"
      "global_load_dwordx4 a[148:151], v0, s[42:43] nt\n"
      "s_add_u32 s42, s42, s52\n"
      "s_addc_u32 s43, s43, 0\n"
      "s_waitcnt vmcnt(8)\n"
      "global_load_dwordx4 a[152:155], v1, s[40:41] nt\n"
      "s_add_u32 s40, s40, 110592\n"
      "s_addc_u32 s41, s41, 0\n"
      "s_waitcnt vmcnt(8)\n"
      "global_load_dwordx4 a[156:159], v0, s[42:43] nt\n"
      "s_add_u32 s42, s42, 110592\n"
      "s_addc_u32 s43, s43, 0\n"
      "s_barrier\n"
      "s_mov_b64 exec, 0xffff\n"
      "s_mov_b32 s53, 0\n"
      "L_qc19_%=:\n"
      "v_max3_u32 v49, v8, v9, v10\n"
      "v_max_u32 v49, v49, v11\n"
      "v_cmp_eq_u32 vcc, -1, v49\n"
      "s_nop 0\n"
      "s_cbranch_vccz L_qo20_%=\n"
      "s_cmp_lg_u32 s54, 0\n"
      "s_cbranch_scc1 L_qo20_%=\n"
      "s_add_u32 s56, s48, 40960\n"
      "s_addc_u32 s57, s49, 0\n"
      "global_load_dwordx4 v[8:11], v0, s[56:57] sc1\n"
      "s_add_u32 s53, s53, 1\n"
      "s_waitcnt vmcnt(0)\n"
      "s_cmp_lt_u32 s53, 30000\n"
      "s_cbranch_scc1 L_qc19_%=\n"
      "s_mov_b32 s54, 105\n"
      "v_mov_b32 v49, 105\n"
      "v_mov_b32 v50, 256\n"
      "global_store_dword v50, v49, s[58:59] sc1\n"
      "L_qo20_%=:\n"
      "s_lshl_b32 s62, s60, 8\n"
      "s_add_u32 s62, s62, 159872\n"
      "v_add_u32 v49, s62, v0\n"
      "ds_write_b128 v49, v[8:11]\n"
      "s_mov_b64 exec, -1\n"
      "s_waitcnt lgkmcnt(0)\n"
      "s_barrier\n"
      "ds_read_b128 v[8:11], v2 offset:0\n"
      "s_waitcnt lgkmcnt(0)\n"
      "v_mfma_f32_16x16x32_bf16 v[24:27], a[0:3], v[8:11], 0\n"
      "v_mfma_f32_16x16x32_bf16 v[28:31], a[8:11], v[8:11], 0\n"
      "s_nop 6\n"
      "v_mov_b32_dpp v34, v24 quad_perm:[0,0,0,0] row_mask:0xf bank_mask:0xf\n"
      "v_mov_b32_dpp v33, v26 quad_perm:[2,2,2,2] row_mask:0xf bank_mask:0xf\n"
      "s_nop 0\n"
      "v_add_f32_dpp v34, v25, v34 quad_perm:[1,1,1,1] row_mask:0xf bank_mask:0xf\n"
      "v_add_f32_dpp v33, v27, v33 quad_perm:[3,3,3,3] row_mask:0xf bank_mask:0xf\n"
      "v_add_f32 v34, v34, v33\n"
      "v_mov_b32_dpp v32, v28 quad_perm:[0,0,0,0] row_mask:0xf bank_mask:0xf\n"
      "v_mov_b32_dpp v33, v30 quad_perm:[2,2,2,2] row_mask:0xf bank_mask:0xf\n"
      "s_nop 0\n"
      "v_add_f32_dpp v32, v29, v32 quad_perm:[1,1,1,1] row_mask:0xf bank_mask:0xf\n"
      "v_add_f32_dpp v33, v31, v33 quad_perm:[3,3,3,3] row_mask:0xf bank_mask:0xf\n"
      "v_add_f32 v32, v32, v33\n"
      "v_max_f32 v35, v34, v32\n"
      "v_mov_b32 v33, v35\n"
      "s_nop 1\n"
      "v_permlane32_swap_b32 v33, v35\n"
      "v_max_f32 v35, v33, v35\n"
      "v_mov_b32 v33, v35\n"
      "s_nop 1\n"
      "v_permlane16_swap_b32 v33, v35\n"
      "v_max_f32 v6, v33, v35\n"
      "v_sub_f32 v34, v34, v6\n"
      "v_sub_f32 v32, v32, v6\n"
      "v_mov_b32 v35, 0\n"
      "v_exp_f32 v34, v34\n"
      "s_nop 0\n"
      "v_mov_b32 v7, v34\n"
      "v_cvt_pk_bf16_f32 v34, v34, v34\n"
      "v_and_b32 v36, v4, v34\n"
      "v_and_b32 v37, v5, v34\n"
      "v_mfma_f32_16x16x32_bf16 v[24:27], a[16:19], v[8:11], 0\n"
      "s_nop 0\n"
      "v_mfma_f32_16x16x16_bf16 v[16:19], a[4:5], v[36:37], 0\n"
      "v_mfma_f32_16x16x16_bf16 v[20:23], a[6:7], v[36:37], 0\n"
      "v_exp_f32 v34, v32\n"
      "v_max_f32 v35, v35, v32\n"
      "v_add_f32 v7, v7, v34\n"
      "v_cvt_pk_bf16_f32 v34, v34, v34\n"
      "v_and_b32 v38, v4, v34\n"
      "v_and_b32 v39, v5, v34\n"
      "v_mfma_f32_16x16x32_bf16 v[28:31], a[24:27], v[8:11], 0\n"
      "v_sub_f32_dpp v32, v24, v6 quad_perm:[0,0,0,0] row_mask:0xf bank_mask:0xf\n"
      "v_mov_b32_dpp v33, v26 quad_perm:[2,2,2,2] row_mask:0xf bank_mask:0xf\n"
      "v_mfma_f32_16x16x16_bf16 v[16:19], a[12:13], v[38:39], v[16:19]\n"
      "v_add_f32_dpp v32, v25, v32 quad_perm:[1,1,1,1] row_mask:0xf bank_mask:0xf\n"
      "v_add_f32_dpp v33, v27, v33 quad_perm:[3,3,3,3] row_mask:0xf bank_mask:0xf\n"
      "v_mfma_f32_16x16x16_bf16 v[20:23], a[14:15], v[38:39], v[20:23]\n"
      "v_add_f32 v32, v32, v33\n"
      "v_exp_f32 v34, v32\n"
      "v_max_f32 v35, v35, v32\n"
      "v_add_f32 v7, v7, v34\n"
      "v_cvt_pk_bf16_f32 v34, v34, v34\n"
      "v_and_b32 v36, v4, v34\n"
      "v_and_b32 v37, v5, v34\n"
      "v_mfma_f32_16x16x32_bf16 v[24:27], a[32:35], v[8:11], 0\n"
      "v_sub_f32_dpp v32, v28, v6 quad_perm:[0,0,0,0] row_mask:0xf bank_mask:0xf\n"
      "v_mov_b32_dpp v33, v30 quad_perm:[2,2,2,2] row_mask:0xf bank_mask:0xf\n"
      "v_mfma_f32_16x16x16_bf16 v[16:19], a[20:21], v[36:37], v[16:19]\n"
      "v_add_f32_dpp v32, v29, v32 quad_perm:[1,1,1,1] row_mask:0xf bank_mask:0xf\n"
      "v_add_f32_dpp v33, v31, v33 quad_perm:[3,3,3,3] row_mask:0xf bank_mask:0xf\n"
      "v_mfma_f32_16x16x16_bf16 v[20:23], a[22:23], v[36:37], v[20:23]\n"
      "v_add_f32 v32, v32, v33\n"
      "v_exp_f32 v34, v32\n"
      "v_max_f32 v35, v35, v32\n"
      "v_add_f32 v7, v7, v34\n"
      "v_cvt_pk_bf16_f32 v34, v34, v34\n"
      "v_and_b32 v38, v4, v34\n"
      "v_and_b32 v39, v5, v34\n"
      "s_waitcnt vmcnt(8)\n"
      "global_load_dwordx4 a[160:163], v1, s[40:41] nt\n"
      "s_add_u32 s40, s40, 110592\n"
      "s_addc_u32 s41, s41, 0\n"
      "v_mfma_f32_16x16x32_bf16 v[28:31], a[40:43], v[8:11], 0\n"
      "v_sub_f32_dpp v32, v24, v6 quad_perm:[0,0,0,0] row_mask:0xf bank_mask:0xf\n"
      "v_mov_b32_dpp v33, v26 quad_perm:[2,2,2,2] row_mask:0xf bank_mask:0xf\n"
      "v_mfma_f32_16x16x16_bf16 v[16:19], a[28:29], v[38:39], v[16:19]\n"
      "v_add_f32_dpp v32, v25, v32 quad_perm:[1,1,1,1] row_mask:0xf bank_mask:0xf\n"
      "v_add_f32_dpp v33, v27, v33 quad_perm:[3,3,3,3] row_mask:0xf bank_mask:0xf\n"
      "v_mfma_f32_16x16x16_bf16 v[20:23], a[30:31], v[38:39], v[20:23]\n"
      "v_add_f32 v32, v32, v33\n"
      "v_exp_f32 v34, v32\n"
      "v_max_f32 v35, v35, v32\n"
      "v_add_f32 v7, v7, v34\n"
      "v_cvt_pk_bf16_f32 v34, v34, v34\n"
      "v_and_b32 v36, v4, v34\n"
      "v_and_b32 v37, v5, v34\n"
      "v_mfma_f32_16x16x32_bf16 v[24:27], a[48:51], v[8:11], 0\n"
      "v_sub_f32_dpp v32, v28, v6 quad_perm:[0,0,0,0] row_mask:0xf bank_mask:0xf\n"
      "v_mov_b32_dpp v33, v30 quad_perm:[2,2,2,2] row_mask:0xf bank_mask:0xf\n"
      "v_mfma_f32_16x16x16_bf16 v[16:19], a[36:37], v[36:37], v[16:19]\n"
      "v_add_f32_dpp v32, v29, v32 quad_perm:[1,1,1,1] row_mask:0xf bank_mask:0xf\n"
      "v_add_f32_dpp v33, v31, v33 quad_perm:[3,3,3,3] row_mask:0xf bank_mask:0xf\n"
      "v_mfma_f32_16x16x16_bf16 v[20:23], a[38:39], v[36:37], v[20:23]\n"
      "v_add_f32 v32, v32, v33\n"
      "v_exp_f32 v34, v32\n"
      "v_max_f32 v35, v35, v32\n"
      "v_add_f32 v7, v7, v34\n"
      "v_cvt_pk_bf16_f32 v34, v34, v34\n"
      "v_and_b32 v38, v4, v34\n"
      "v_and_b32 v39, v5, v34\n"
      "s_waitcnt vmcnt(8)\n"
      "global_load_dwordx4 a[164:167], v0, s[42:43] nt\n"
      "s_add_u32 s42, s42, 110592\n"
      "s_addc_u32 s43, s43, 0\n"
      "v_mfma_f32_16x16x32_bf16 v[28:31], a[56:59], v[8:11], 0\n"
      "v_sub_f32_dpp v32, v24, v6 quad_perm:[0,0,0,0] row_mask:0xf bank_mask:0xf\n"
      "v_mov_b32_dpp v33, v26 quad_perm:[2,2,2,2] row_mask:0xf bank_mask:0xf\n"
      "v_mfma_f32_16x16x16_bf16 v[16:19], a[44:45], v[38:39], v[16:19]\n"
      "v_add_f32_dpp v32, v25, v32 quad_perm:[1,1,1,1] row_mask:0xf bank_mask:0xf\n"
      "v_add_f32_dpp v33, v27, v33 quad_perm:[3,3,3,3] row_mask:0xf bank_mask:0xf\n"
      "v_mfma_f32_16x16x16_bf16 v[20:23], a[46:47], v[38:39], v[20:23]\n"
      "v_add_f32 v32, v32, v33\n"
      "v_exp_f32 v34, v32\n"
      "v_max_f32 v35, v35, v32\n"
      "v_add_f32 v7, v7, v34\n"
      "v_cvt_pk_bf16_f32 v34, v34, v34\n"
      "v_and_b32 v36, v4, v34\n"
      "v_and_b32 v37, v5, v34\n"
      "v_mfma_f32_16x16x32_bf16 v[24:27], a[64:67], v[8:11], 0\n"
      "v_sub_f32_dpp v32, v28, v6 quad_perm:[0,0,0,0] row_mask:0xf bank_mask:0xf\n"
      "v_mov_b32_dpp v33, v30 quad_perm:[2,2,2,2] row_mask:0xf bank_mask:0xf\n"
      "v_mfma_f32_16x16x16_bf16 v[16:19], a[52:53], v[36:37], v[16:19]\n"
      "v_add_f32_dpp v32, v29, v32 quad_perm:[1,1,1,1] row_mask:0xf bank_mask:0xf\n"
      "v_add_f32_dpp v33, v31, v33 quad_perm:[3,3,3,3] row_mask:0xf bank_mask:0xf\n"
      "v_mfma_f32_16x16x16_bf16 v[20:23], a[54:55], v[36:37], v[20:23]\n"
      "v_add_f32 v32, v32, v33\n"
      "v_exp_f32 v34, v32\n"
      "v_max_f32 v35, v35, v32\n"
      "v_add_f32 v7, v7, v34\n"
      "v_cvt_pk_bf16_f32 v34, v34, v34\n"
      "v_and_b32 v38, v4, v34\n"
      "v_and_b32 v39, v5, v34\n"
      "v_mfma_f32_16x16x32_bf16 v[28:31], a[72:75], v[8:11], 0\n"
      "v_sub_f32_dpp v32, v24, v6 quad_perm:[0,0,0,0] row_mask:0xf bank_mask:0xf\n"
      "v_mov_b32_dpp v33, v26 quad_perm:[2,2,2,2] row_mask:0xf bank_mask:0xf\n"
      "v_mfma_f32_16x16x16_bf16 v[16:19], a[60:61], v[38:39], v[16:19]\n"
      "v_add_f32_dpp v32, v25, v32 quad_perm:[1,1,1,1] row_mask:0xf bank_mask:0xf\n"
      "v_add_f32_dpp v33, v27, v33 quad_perm:[3,3,3,3] row_mask:0xf bank_mask:0xf\n"
      "v_mfma_f32_16x16x16_bf16 v[20:23], a[62:63], v[38:39], v[20:23]\n"
      "v_add_f32 v32, v32, v33\n"
      "v_exp_f32 v34, v32\n"
      "v_max_f32 v35, v35, v32\n"
      "v_add_f32 v7, v7, v34\n"
      "v_cvt_pk_bf16_f32 v34, v34, v34\n"
      "v_and_b32 v36, v4, v34\n"
      "v_and_b32 v37, v5, v34\n"
      "s_waitcnt vmcnt(8)\n"
      "global_load_dwordx4 a[168:171], v1, s[40:41] nt\n"
      "s_add_u32 s40, s40, 110592\n"
      "s_addc_u32 s41, s41, 0\n"
      "v_mfma_f32_16x16x32_bf16 v[24:27], a[80:83], v[8:11], 0\n"
      "v_sub_f32_dpp v32, v28, v6 quad_perm:[0,0,0,0] row_mask:0xf bank_mask:0xf\n"
      "v_mov_b32_dpp v33, v30 quad_perm:[2,2,2,2] row_mask:0xf bank_mask:0xf\n"
      "v_mfma_f32_16x16x16_bf16 v[16:19], a[68:69], v[36:37], v[16:19]\n"
      "v_add_f32_dpp v32, v29, v32 quad_perm:[1,1,1,1] row_mask:0xf bank_mask:0xf\n"
      "v_add_f32_dpp v33, v31, v33 quad_perm:[3,3,3,3] row_mask:0xf bank_mask:0xf\n"
      "v_mfma_f32_16x16x16_bf16 v[20:23], a[70:71], v[36:37], v[20:23]\n"
      "v_add_f32 v32, v32, v33\n"
      "v_exp_f32 v34, v32\n"
      "v_max_f32 v35, v35, v32\n"
      "v_add_f32 v7, v7, v34\n"
      "v_cvt_pk_bf16_f32 v34, v34, v34\n"
      "v_and_b32 v38, v4, v34\n"
      "v_and_b32 v39, v5, v34\n"
      "v_mfma_f32_16x16x32_bf16 v[28:31], a[88:91], v[8:11], 0\n"
      "v_sub_f32_dpp v32, v24, v6 quad_perm:[0,0,0,0] row_mask:0xf bank_mask:0xf\n"
      "v_mov_b32_dpp v33, v26 quad_perm:[2,2,2,2] row_mask:0xf bank_mask:0xf\n"
      "v_mfma_f32_16x16x16_bf16 v[16:19], a[76:77], v[38:39], v[16:19]\n"
      "v_add_f32_dpp v32, v25, v32 quad_perm:[1,1,1,1] row_mask:0xf bank_mask:0xf\n"
      "v_add_f32_dpp v33, v27, v33 quad_perm:[3,3,3,3] row_mask:0xf bank_mask:0xf\n"
      "v_mfma_f32_16x16x16_bf16 v[20:23], a[78:79], v[38:39], v[20:23]\n"
      "v_add_f32 v32, v32, v33\n"
      "v_exp_f32 v34, v32\n"
      "v_max_f32 v35, v35, v32\n"
      "v_add_f32 v7, v7, v34\n"
      "v_cvt_pk_bf16_f32 v34, v34, v34\n"
      "v_and_b32 v36, v4, v34\n"
      "v_and_b32 v37, v5, v34\n"
      "s_waitcnt vmcnt(8)\n"
      "global_load_dwordx4 a[172:175], v0, s[42:43] nt\n"
      "s_add_u32 s42, s42, 110592\n"
      "s_addc_u32 s43, s43, 0\n"
      "v_mfma_f32_16x16x32_bf16 v[24:27], a[96:99], v[8:11], 0\n"
      "v_sub_f32_dpp v32, v28, v6 quad_perm:[0,0,0,0] row_mask:0xf bank_mask:0xf\n"
      "v_mov_b32_dpp v33, v30 quad_perm:[2,2,2,2] row_mask:0xf bank_mask:0xf\n"
      "v_mfma_f32_16x16x16_bf16 v[16:19], a[84:85], v[36:37], v[16:19]\n"
      "v_add_f32_dpp v32, v29, v32 quad_perm:[1,1,1,1] row_mask:0xf bank_mask:0xf\n"
      "v_add_f32_dpp v33, v31, v33 quad_perm:[3,3,3,3] row_mask:0xf bank_mask:0xf\n"
      "v_mfma_f32_16x16x16_bf16 v[20:23], a[86:87], v[36:37], v[20:23]\n"
      "v_add_f32 v32, v32, v33\n"
      "v_exp_f32 v34, v32\n"
      "v_max_f32 v35, v35, v32\n"
      "v_add_f32 v7, v7, v34\n"
      "v_cvt_pk_bf16_f32 v34, v34, v34\n"
      "v_and_b32 v38, v4, v34\n"
      "v_and_b32 v39, v5, v34\n"
      "v_mfma_f32_16x16x32_bf16 v[28:31], a[104:107], v[8:11], 0\n"
      "v_sub_f32_dpp v32, v24, v6 quad_perm:[0,0,0,0] row_mask:0xf bank_mask:0xf\n"
      "v_mov_b32_dpp v33, v26 quad_perm:[2,2,2,2] row_mask:0xf bank_mask:0xf\n"
      "v_mfma_f32_16x16x16_bf16 v[16:19], a[92:93], v[38:39], v[16:19]\n"
      "v_add_f32_dpp v32, v25, v32 quad_perm:[1,1,1,1] row_mask:0xf bank_mask:0xf\n"
      "v_add_f32_dpp v33, v27, v33 quad_perm:[3,3,3,3] row_mask:0xf bank_mask:0xf\n"
      "v_mfma_f32_16x16x16_bf16 v[20:23], a[94:95], v[38:39], v[20:23]\n"
      "v_add_f32 v32, v32, v33\n"
      "v_exp_f32 v34, v32\n"
      "v_max_f32 v35, v35, v32\n"
      "v_add_f32 v7, v7, v34\n"
      "v_cvt_pk_bf16_f32 v34, v34, v34\n"
      "v_and_b32 v36, v4, v34\n"
      "v_and_b32 v37, v5, v34\n"
      "s_waitcnt vmcnt(8)\n"
      "global_load_dwordx4 a[176:179], v1, s[40:41] nt\n"
      "s_add_u32 s40, s40, 110592\n"
      "s_addc_u32 s41, s41, 0\n"
      "v_mfma_f32_16x16x32_bf16 v[24:27], a[112:115], v[8:11], 0\n"
      "v_sub_f32_dpp v32, v28, v6 quad_perm:[0,0,0,0] row_mask:0xf bank_mask:0xf\n"
      "v_mov_b32_dpp v33, v30 quad_perm:[2,2,2,2] row_mask:0xf bank_mask:0xf\n"
      "v_mfma_f32_16x16x16_bf16 v[16:19], a[100:101], v[36:37], v[16:19]\n"
      "v_add_f32_dpp v32, v29, v32 quad_perm:[1,1,1,1] row_mask:0xf bank_mask:0xf\n"
      "v_add_f32_dpp v33, v31, v33 quad_perm:[3,3,3,3] row_mask:0xf bank_mask:0xf\n"
      "v_mfma_f32_16x16x16_bf16 v[20:23], a[102:103], v[36:37], v[20:23]\n"
      "v_add_f32 v32, v32, v33\n"
      "v_exp_f32 v34, v32\n"
      "v_max_f32 v35, v35, v32\n"
      "v_add_f32 v7, v7, v34\n"
      "v_cvt_pk_bf16_f32 v34, v34, v34\n"
      "v_and_b32 v38, v4, v34\n"
      "v_and_b32 v39, v5, v34\n"
      "v_mfma_f32_16x16x32_bf16 v[28:31], a[120:123], v[8:11], 0\n"
      "v_sub_f32_dpp v32, v24, v6 quad_perm:[0,0,0,0] row_mask:0xf bank_mask:0xf\n"
      "v_mov_b32_dpp v33, v26 quad_perm:[2,2,2,2] row_mask:0xf bank_mask:0xf\n"
      "v_mfma_f32_16x16x16_bf16 v[16:19], a[108:109], v[38:39], v[16:19]\n"
      "v_add_f32_dpp v32, v25, v32 quad_perm:[1,1,1,1] row_mask:0xf bank_mask:0xf\n"
      "v_add_f32_dpp v33, v27, v33 quad_perm:[3,3,3,3] row_mask:0xf bank_mask:0xf\n"
      "v_mfma_f32_16x16x16_bf16 v[20:23], a[110:111], v[38:39], v[20:23]\n"
      "v_add_f32 v32, v32, v33\n"
      "v_exp_f32 v34, v32\n"
      "v_max_f32 v35, v35, v32\n"
      "v_add_f32 v7, v7, v34\n"
      "v_cvt_pk_bf16_f32 v34, v34, v34\n"
      "v_and_b32 v36, v4, v34\n"
      "v_and_b32 v37, v5, v34\n"
      "v_mfma_f32_16x16x32_bf16 v[24:27], a[128:131], v[8:11], 0\n"
      "v_sub_f32_dpp v32, v28, v6 quad_perm:[0,0,0,0] row_mask:0xf bank_mask:0xf\n"
      "v_mov_b32_dpp v33, v30 quad_perm:[2,2,2,2] row_mask:0xf bank_mask:0xf\n"
      "v_mfma_f32_16x16x16_bf16 v[16:19], a[116:117], v[36:37], v[16:19]\n"
      "v_add_f32_dpp v32, v29, v32 quad_perm:[1,1,1,1] row_mask:0xf bank_mask:0xf\n"
      "v_add_f32_dpp v33, v31, v33 quad_perm:[3,3,3,3] row_mask:0xf bank_mask:0xf\n"
      "v_mfma_f32_16x16x16_bf16 v[20:23], a[118:119], v[36:37], v[20:23]\n"
      "v_add_f32 v32, v32, v33\n"
      "v_exp_f32 v34, v32\n"
      "v_max_f32 v35, v35, v32\n"
      "v_add_f32 v7, v7, v34\n"
      "v_cvt_pk_bf16_f32 v34, v34, v34\n"
      "v_and_b32 v38, v4, v34\n"
      "v_and_b32 v39, v5, v34\n"
      "s_waitcnt vmcnt(8)\n"
      "global_load_dwordx4 a[180:183], v0, s[42:43] nt\n"
      "s_add_u32 s42, s42, 110592\n"
      "s_addc_u32 s43, s43, 0\n"
      "v_mfma_f32_16x16x32_bf16 v[28:31], a[136:139], v[8:11], 0\n"
      "v_sub_f32_dpp v32, v24, v6 quad_perm:[0,0,0,0] row_mask:0xf bank_mask:0xf\n"
      "v_mov_b32_dpp v33, v26 quad_perm:[2,2,2,2] row_mask:0xf bank_mask:0xf\n"
      "v_mfma_f32_16x16x16_bf16 v[16:19], a[124:125], v[38:39], v[16:19]\n"
      "v_add_f32_dpp v32, v25, v32 quad_perm:[1,1,1,1] row_mask:0xf bank_mask:0xf\n"
      "v_add_f32_dpp v33, v27, v33 quad_perm:[3,3,3,3] row_mask:0xf bank_mask:0xf\n"
      "v_mfma_f32_16x16x16_bf16 v[20:23], a[126:127], v[38:39], v[20:23]\n"
      "v_add_f32 v32, v32, v33\n"
      "v_exp_f32 v34, v32\n"
      "v_max_f32 v35, v35, v32\n"
      "v_add_f32 v7, v7, v34\n"
      "v_cvt_pk_bf16_f32 v34, v34, v34\n"
      "v_and_b32 v36, v4, v34\n"
      "v_and_b32 v37, v5, v34\n"
      "v_mfma_f32_16x16x32_bf16 v[24:27], a[144:147], v[8:11], 0\n"
      "v_sub_f32_dpp v32, v28, v6 quad_perm:[0,0,0,0] row_mask:0xf bank_mask:0xf\n"
      "v_mov_b32_dpp v33, v30 quad_perm:[2,2,2,2] row_mask:0xf bank_mask:0xf\n"
      "v_mfma_f32_16x16x16_bf16 v[16:19], a[132:133], v[36:37], v[16:19]\n"
      "v_add_f32_dpp v32, v29, v32 quad_perm:[1,1,1,1] row_mask:0xf bank_mask:0xf\n"
      "v_add_f32_dpp v33, v31, v33 quad_perm:[3,3,3,3] row_mask:0xf bank_mask:0xf\n"
      "v_mfma_f32_16x16x16_bf16 v[20:23], a[134:135], v[36:37], v[20:23]\n"
      "v_add_f32 v32, v32, v33\n"
      "v_exp_f32 v34, v32\n"
      "v_max_f32 v35, v35, v32\n"
      "v_add_f32 v7, v7, v34\n"
      "v_cvt_pk_bf16_f32 v34, v34, v34\n"
      "v_and_b32 v38, v4, v34\n"
      "v_and_b32 v39, v5, v34\n"
      "s_waitcnt vmcnt(8)\n"
      "global_load_dwordx4 a[184:187], v1, s[40:41] nt\n"
      "s_add_u32 s40, s40, 110592\n"
      "s_addc_u32 s41, s41, 0\n"
      "v_sub_f32_dpp v32, v24, v6 quad_perm:[0,0,0,0] row_mask:0xf bank_mask:0xf\n"
      "v_mov_b32_dpp v33, v26 quad_perm:[2,2,2,2] row_mask:0xf bank_mask:0xf\n"
      "v_mfma_f32_16x16x16_bf16 v[16:19], a[140:141], v[38:39], v[16:19]\n"
      "v_add_f32_dpp v32, v25, v32 quad_perm:[1,1,1,1] row_mask:0xf bank_mask:0xf\n"
      "v_add_f32_dpp v33, v27, v33 quad_perm:[3,3,3,3] row_mask:0xf bank_mask:0xf\n"
      "v_mfma_f32_16x16x16_bf16 v[20:23], a[142:143], v[38:39], v[20:23]\n"
      "v_add_f32 v32, v32, v33\n"
      "v_add_f32 v32, s69, v32\n"
      "v_exp_f32 v34, v32\n"
      "v_max_f32 v35, v35, v32\n"
      "v_add_f32 v7, v7, v34\n"
      "v_cvt_pk_bf16_f32 v34, v34, v34\n"
      "v_and_b32 v36, v4, v34\n"
      "v_and_b32 v37, v5, v34\n"
      "s_nop 1\n"
      "v_mfma_f32_16x16x16_bf16 v[16:19], a[148:149], v[36:37], v[16:19]\n"
      "v_mfma_f32_16x16x16_bf16 v[20:23], a[150:151], v[36:37], v[20:23]\n"
      "s_waitcnt vmcnt(8)\n"
      "global_load_dwordx4 a[188:191], v0, s[42:43] nt\n"
      "s_add_u32 s42, s42, 110592\n"
      "s_addc_u32 s43, s43, 0\n"
      "v_cmp_lt_f32 vcc, 0x42800000, v35\n"
      "s_nop 0\n"
      "s_cbranch_vccnz L_fb8_%=\n"
      "L_ret8_%=:\n"
      "v_mov_b32 v49, v7\n"
      "v_lshlrev_b32 v41, 2, v0\n"
      "v_and_b32 v41, 0x300, v41\n"
      "v_permlane32_swap_b32 v49, v7\n"
      "v_lshrrev_b32 v42, 4, v0\n"
      "v_add_f32 v7, v49, v7\n"
      "v_and_b32 v42, 0x30, v42\n"
      "v_mov_b32 v49, v7\n"
      "v_add_u32 v41, v41, v42\n"
      "v_lshrrev_b32 v40, 2, v0\n"
      "v_permlane16_swap_b32 v49, v7\n"
      "v_and_b32 v42, 12, v40\n"
      "v_add_f32 v7, v49, v7\n"
      "v_add_u32 v41, v41, v42\n"
      "s_lshl_b32 s62, s60, 10\n"
      "s_add_u32 s62, s62, 155648\n"
      "v_add_u32 v41, s62, v41\n"
      "v_lshrrev_b32 v43, 3, v0\n"
      "v_and_b32 v43, 24, v43\n"
      "s_lshl_b32 s62, s60, 5\n"
      "s_add_u32 s62, s62, 159744\n"
      "v_add_u32 v43, s62, v43\n"
      "v_cvt_pk_bf16_f32 v36, v16, v17\n"
      "v_cvt_pk_bf16_f32 v37, v18, v19\n"
      "v_cvt_pk_bf16_f32 v38, v20, v21\n"
      "v_cvt_pk_bf16_f32 v39, v22, v23\n"
      "s_mov_b64 exec, 0x1111\n"
      "ds_write_b64 v43, v[6:7]\n"
      "s_mov_b64 exec, -1\n"
      "ds_write_b32 v41, v36 offset:0\n"
      "ds_write_b32 v41, v37 offset:64\n"
      "ds_write_b32 v41, v38 offset:128\n"
      "ds_write_b32 v41, v39 offset:192\n"
      "s_lshl_b32 s62, s60, 8\n"
      "s_add_u32 s62, s62, 155648\n"
      "v_add_u32 v42, s62, v40\n"
      "s_lshl_b32 s62, s60, 3\n"
      "s_add_u32 s62, s62, 159744\n"
      "v_mov_b32 v50, s62\n"
      "s_waitcnt lgkmcnt(0)\n"
      "s_barrier\n"
      "ds_read_b64 v[28:29], v50 offset:0\n"
      "ds_read_b64 v[30:31], v50 offset:32\n"
      "ds_read_b64 v[32:33], v50 offset:64\n"
      "ds_read_b64 v[34:35], v50 offset:96\n"
      "ds_read_b32 v24, v42 offset:0\n"
      "ds_read_b32 v25, v42 offset:1024\n"
      "ds_read_b32 v26, v42 offset:2048\n"
      "ds_read_b32 v27, v42 offset:3072\n"
      "s_add_u32 s56, s44, 1658880\n"
      "s_addc_u32 s57, s45, 0\n"
      "s_waitcnt lgkmcnt(4)\n"
      "v_max3_f32 v43, v28, v30, v32\n"
      "v_max_f32 v43, v43, v34\n"
      "v_sub_f32 v28, v28, v43\n"
      "v_sub_f32 v30, v30, v43\n"
      "v_sub_f32 v32, v32, v43\n"
      "v_sub_f32 v34, v34, v43\n"
      "v_exp_f32 v28, v28\n"
      "v_exp_f32 v30, v30\n"
      "v_exp_f32 v32, v32\n"
      "v_exp_f32 v34, v34\n"
      "s_waitcnt lgkmcnt(0)\n"
      "v_lshlrev_b32 v44, 16, v24\n"
      "v_and_b32 v45, 0xffff0000, v24\n"
      "v_lshlrev_b32 v46, 16, v25\n"
      "v_and_b32 v47, 0xffff0000, v25\n"
      "v_lshlrev_b32 v48, 16, v26\n"
      "v_and_b32 v49, 0xffff0000, v26\n"
      "v_lshlrev_b32 v50, 16, v27\n"
      "v_and_b32 v51, 0xffff0000, v27\n"
      "v_mul_f32 v38, v28, v29\n"
      "v_pk_mul_f32 v[36:37], v[44:45], v[28:29] op_sel_hi:[1,0]\n"
      "v_fmac_f32 v38, v30, v31\n"
      "v_pk_fma_f32 v[36:37], v[46:47], v[30:31], v[36:37] op_sel_hi:[1,0,1]\n"
      "v_fmac_f32 v38, v32, v33\n"
      "v_pk_fma_f32 v[36:37], v[48:49], v[32:33], v[36:37] op_sel_hi:[1,0,1]\n"
      "v_fmac_f32 v38, v34, v35\n"
      "v_pk_fma_f32 v[36:37], v[50:51], v[34:35], v[36:37] op_sel_hi:[1,0,1]\n"
      "v_rcp_f32 v39, v38\n"
      "v_log_f32 v38, v38\n"
      "v_mul_f32 v36, v39, v36\n"
      "v_mul_f32 v37, v39, v37\n"
      "v_add_f32 v38, v38, v43\n"
      "v_cvt_pk_bf16_f32 v36, v36, v37\n"
      "v_min_u32 v38, -2, v38\n"
      "v_min_u32 v36, -2, v36\n"
      "global_store_dword v40, v36, s[56:57] sc1\n"
      "s_mov_b32 exec_hi, 0\n"
      "global_store_dword v40, v38, s[56:57] offset:256 sc1\n"
      "s_mov_b64 exec, -1\n"
      "s_add_u32 s56, s44, 3317760\n"
      "s_addc_u32 s57, s45, 0\n"
      "global_store_dword v40, v12, s[56:57] sc1\n"
      "s_mov_b32 exec_hi, 0\n"
      "global_store_dword v40, v13, s[56:57] offset:256 sc1\n"
      "s_mov_b64 exec, -1\n"
      "s_waitcnt vmcnt(8)\n"
      "global_load_dwordx4 a[192:195], v1, s[40:41] nt\n"
      "s_add_u32 s40, s40, 110592\n"
      "s_addc_u32 s41, s41, 0\n"
      "s_waitcnt vmcnt(8)\n"
      "global_load_dwordx4 a[196:199], v0, s[42:43] nt\n"
      "s_add_u32 s42, s42, 110592\n"
      "s_addc_u32 s43, s43, 0\n"
      "s_waitcnt vmcnt(8)\n"
      "global_load_dwordx4 a[200:203], v1, s[40:41] nt\n"
      "s_add_u32 s40, s40, 110592\n"
      "s_addc_u32 s41, s41, 0\n"
      "s_waitcnt vmcnt(8)\n"
      "global_load_dwordx4 a[204:207], v0, s[42:43] nt\n"
      "s_add_u32 s42, s42, 110592\n"
      "s_addc_u32 s43, s43, 0\n"
      "s_barrier\n"
      "L_per6_%=:\n"
      "s_add_u32 s56, s87, L_per7_%=-L_begin_%=\n"
      "s_min_u32 s56, s56, L_end_%=-L_begin_%=-1024\n"
      "s_add_u32 s56, s84, s56\n"
      "s_addc_u32 s57, s85, 0\n"
      "s_mov_b32 m0, s86\n"
      "s_nop 0\n"
      "global_load_lds_dword v0, s[56:57]\n"
      "s_waitcnt vmcnt(9)\n"
      "global_load_dwordx4 a[208:211], v1, s[40:41] nt\n"
      "s_add_u32 s40, s40, 110592\n"
      "s_addc_u32 s41, s41, 0\n"
      "s_waitcnt vmcnt(9)\n"
      "global_load_dwordx4 a[212:215], v0, s[42:43] nt\n"
      "s_add_u32 s42, s42, 110592\n"
      "s_addc_u32 s43, s43, 0\n"
      "s_waitcnt vmcnt(9)\n"
      "global_load_dwordx4 a[216:219], v1, s[40:41] nt\n"
      "s_add_u32 s40, s40, 110592\n"
      "s_addc_u32 s41, s41, 0\n"
      "s_waitcnt vmcnt(9)\n"
      "global_load_dwordx4 a[220:223], v0, s[42:43] nt\n"
      "s_add_u32 s42, s42, 110592\n"
      "s_addc_u32 s43, s43, 0\n"
      "s_waitcnt vmcnt(9)\n"
      "global_load_dwordx4 a[224:227], v1, s[40:41] nt\n"
      "s_add_u32 s40, s40, 110592\n"
      "s_addc_u32 s41, s41, 0\n"
      "s_add_u32 s56, s48, 49152\n"
      "s_addc_u32 s57, s49, 0\n"
      "s_mov_b64 exec, 0xffff\n"
      "global_load_dwordx4 v[8:11], v0, s[56:57] sc1\n"
      "s_mov_b64 exec, -1\n"
      "s_add_u32 s56, s48, 90112\n"
      "s_addc_u32 s57, s49, 0\n"
      "s_mov_b64 exec, 0xffff\n"
      "global_load_dwordx4 v[12:15], v0, s[56:57] sc1\n"
      "s_mov_b64 exec, -1\n"
      "s_waitcnt vmcnt(11)\n"
      "global_load_dwordx4 a[228:231], v0, s[42:43] nt\n"
      "s_add_u32 s42, s42, 110592\n"
      "s_addc_u32 s43, s43, 0\n"
      "s_waitcnt vmcnt(11)\n"
      "global_load_dwordx4 a[232:235], v1, s[40:41] nt\n"
      "s_add_u32 s40, s40, 110592\n"
      "s_addc_u32 s41, s41, 0\n"
      "s_waitcnt vmcnt(11)\n"
      "global_load_dwordx4 a[236:239], v0, s[42:43] nt\n"
      "s_add_u32 s42, s42, 110592\n"
      "s_addc_u32 s43, s43, 0\n"
      "s_waitcnt vmcnt(11)\n"
      "global_load_dwordx4 a[240:243], v1, s[40:41] nt\n"
      "s_add_u32 s40, s40, 110592\n"
      "s_addc_u32 s41, s41, 0\n"
      "s_waitcnt vmcnt(10)\n"
      "global_load_dwordx4 a[244:247], v0, s[42:43] nt\n"
      "s_add_u32 s42, s42, 110592\n"
      "s_addc_u32 s43, s43, 0\n"
      "s_waitcnt vmcnt(10)\n"
      "global_load_dwordx4 a[248:251], v1, s[40:41] nt\n"
      "s_add_u32 s40, s40, 110592\n"
      "s_addc_u32 s41, s41, 0\n"
      "s_waitcnt vmcnt(10)\n"
      "global_load_dwordx4 a[252:255], v0, s[42:43] nt\n"
      "s_add_u32 s42, s42, 110592\n"
      "s_addc_u32 s43, s43, 0\n"
      "s_waitcnt vmcnt(10)\n"
      "global_load_dwordx4 v[56:59], v1, s[40:41] nt\n"
      "s_add_u32 s40, s40, 110592\n"
      "s_addc_u32 s41, s41, 0\n"
      "s_waitcnt vmcnt(10)\n"
      "global_load_dwordx4 v[60:63], v0, s[42:43] nt\n"
      "s_add_u32 s42, s42, 110592\n"
      "s_addc_u32 s43, s43, 0\n"
      "s_waitcnt vmcnt(9)\n"
      "s_mov_b64 exec, 0xffff\n"
      "s_mov_b32 s53, 0\n"
      "L_qc21_%=:\n"
      "v_max3_u32 v49, v12, v13, v14\n"
      "v_max_u32 v49, v49, v15\n"
      "v_cmp_eq_u32 vcc, -1, v49\n"
      "s_nop 0\n"
      "s_cbranch_vccz L_qo22_%=\n"
      "s_cmp_lg_u32 s54, 0\n"
      "s_cbranch_scc1 L_qo22_%=\n"
      "s_add_u32 s56, s48, 90112\n"
      "s_addc_u32 s57, s49, 0\n"
      "global_load_dwordx4 v[12:15], v0, s[56:57] sc1\n"
      "s_add_u32 s53, s53, 1\n"
      "s_waitcnt vmcnt(0)\n"
      "s_cmp_lt_u32 s53, 30000\n"
      "s_cbranch_scc1 L_qc21_%=\n"
      "s_mov_b32 s54, 203\n"
      "v_mov_b32 v49, 203\n"
      "v_mov_b32 v50, 256\n"
      "global_store_dword v50, v49, s[58:59] sc1\n"
      "L_qo22_%=:\n"
      "s_lshl_b32 s62, s60, 8\n"
      "s_add_u32 s62, s62, 160896\n"
      "v_add_u32 v49, s62, v0\n"
      "ds_write_b128 v49, v[12:15]\n"
      "s_mov_b64 exec, -1\n"
      "s_waitcnt lgkmcnt(0)\n"
      "s_barrier\n"
      "ds_read_b128 v[12:15], v2 offset:1024\n"
      "s_waitcnt lgkmcnt(0)\n"
      "v_mfma_f32_16x16x32_bf16 v[24:27], v[104:107], v[12:15], 0\n"
      "v_mfma_f32_16x16x32_bf16 v[28:31], v[112:115], v[12:15], 0\n"
      "s_nop 6\n"
      "v_mov_b32_dpp v34, v24 quad_perm:[0,0,0,0] row_mask:0xf bank_mask:0xf\n"
      "v_mov_b32_dpp v33, v26 quad_perm:[2,2,2,2] row_mask:0xf bank_mask:0xf\n"
      "s_nop 0\n"
      "v_add_f32_dpp v34, v25, v34 quad_perm:[1,1,1,1] row_mask:0xf bank_mask:0xf\n"
      "v_add_f32_dpp v33, v27, v33 quad_perm:[3,3,3,3] row_mask:0xf bank_mask:0xf\n"
      "v_add_f32 v34, v34, v33\n"
      "v_mov_b32_dpp v32, v28 quad_perm:[0,0,0,0] row_mask:0xf bank_mask:0xf\n"
      "v_mov_b32_dpp v33, v30 quad_perm:[2,2,2,2] row_mask:0xf bank_mask:0xf\n"
      "s_nop 0\n"
      "v_add_f32_dpp v32, v29, v32 quad_perm:[1,1,1,1] row_mask:0xf bank_mask:0xf\n"
      "v_add_f32_dpp v33, v31, v33 quad_perm:[3,3,3,3] row_mask:0xf bank_mask:0xf\n"
      "v_add_f32 v32, v32, v33\n"
      "v_max_f32 v35, v34, v32\n"
      "v_mov_b32 v33, v35\n"
      "s_nop 1\n"
      "v_permlane32_swap_b32 v33, v35\n"
      "v_max_f32 v35, v33, v35\n"
      "v_mov_b32 v33, v35\n"
      "s_nop 1\n"
      "v_permlane16_swap_b32 v33, v35\n"
      "v_max_f32 v6, v33, v35\n"
      "v_sub_f32 v34, v34, v6\n"
      "v_sub_f32 v32, v32, v6\n"
      "v_mov_b32 v35, 0\n"
      "v_exp_f32 v34, v34\n"
      "s_nop 0\n"
      "v_mov_b32 v7, v34\n"
      "v_cvt_pk_bf16_f32 v34, v34, v34\n"
      "v_and_b32 v36, v4, v34\n"
      "v_and_b32 v37, v5, v34\n"
      "v_mfma_f32_16x16x32_bf16 v[24:27], v[120:123], v[12:15], 0\n"
      "s_nop 0\n"
      "v_mfma_f32_16x16x16_bf16 v[16:19], v[108:109], v[36:37], 0\n"
      "v_mfma_f32_16x16x16_bf16 v[20:23], v[110:111], v[36:37], 0\n"
      "v_exp_f32 v34, v32\n"
      "v_max_f32 v35, v35, v32\n"
      "v_add_f32 v7, v7, v34\n"
      "v_cvt_pk_bf16_f32 v34, v34, v34\n"
      "v_and_b32 v38, v4, v34\n"
      "v_and_b32 v39, v5, v34\n"
      "v_mfma_f32_16x16x32_bf16 v[28:31], v[128:131], v[12:15], 0\n"
      "v_sub_f32_dpp v32, v24, v6 quad_perm:[0,0,0,0] row_mask:0xf bank_mask:0xf\n"
      "v_mov_b32_dpp v33, v26 quad_perm:[2,2,2,2] row_mask:0xf bank_mask:0xf\n"
      "v_mfma_f32_16x16x16_bf16 v[16:19], v[116:117], v[38:39], v[16:19]\n"
      "v_add_f32_dpp v32, v25, v32 quad_perm:[1,1,1,1] row_mask:0xf bank_mask:0xf\n"
      "v_add_f32_dpp v33, v27, v33 quad_perm:[3,3,3,3] row_mask:0xf bank_mask:0xf\n"
      "v_mfma_f32_16x16x16_bf16 v[20:23], v[118:119], v[38:39], v[20:23]\n"
      "v_add_f32 v32, v32, v33\n"
      "v_exp_f32 v34, v32\n"
      "v_max_f32 v35, v35, v32\n"
      "v_add_f32 v7, v7, v34\n"
      "v_cvt_pk_bf16_f32 v34, v34, v34\n"
      "v_and_b32 v36, v4, v34\n"
      "v_and_b32 v37, v5, v34\n"
      "v_mfma_f32_16x16x32_bf16 v[24:27], v[136:139], v[12:15], 0\n"
      "v_sub_f32_dpp v32, v28, v6 quad_perm:[0,0,0,0] row_mask:0xf bank_mask:0xf\n"
      "v_mov_b32_dpp v33, v30 quad_perm:[2,2,2,2] row_mask:0xf bank_mask:0xf\n"
      "v_mfma_f32_16x16x16_bf16 v[16:19], v[124:125], v[36:37], v[16:19]\n"
      "v_add_f32_dpp v32, v29, v32 quad_perm:[1,1,1,1] row_mask:0xf bank_mask:0xf\n"
      "v_add_f32_dpp v33, v31, v33 quad_perm:[3,3,3,3] row_mask:0xf bank_mask:0xf\n"
      "v_mfma_f32_16x16x16_bf16 v[20:23], v[126:127], v[36:37], v[20:23]\n"
      "v_add_f32 v32, v32, v33\n"
      "v_exp_f32 v34, v32\n"
      "v_max_f32 v35, v35, v32\n"
      "v_add_f32 v7, v7, v34\n"
      "v_cvt_pk_bf16_f32 v34, v34, v34\n"
      "v_and_b32 v38, v4, v34\n"
      "v_and_b32 v39, v5, v34\n"
      "s_waitcnt vmcnt(8)\n"
      "global_load_dwordx4 v[64:67], v1, s[40:41] nt\n"
      "s_add_u32 s40, s40, 110592\n"
      "s_addc_u32 s41, s41, 0\n"
      "v_mfma_f32_16x16x32_bf16 v[28:31], v[144:147], v[12:15], 0\n"
      "v_sub_f32_dpp v32, v24, v6 quad_perm:[0,0,0,0] row_mask:0xf bank_mask:0xf\n"
      "v_mov_b32_dpp v33, v26 quad_perm:[2,2,2,2] row_mask:0xf bank_mask:0xf\n"
      "v_mfma_f32_16x16x16_bf16 v[16:19], v[132:133], v[38:39], v[16:19]\n"
      "v_add_f32_dpp v32, v25, v32 quad_perm:[1,1,1,1] row_mask:0xf bank_mask:0xf\n"
      "v_add_f32_dpp v33, v27, v33 quad_perm:[3,3,3,3] row_mask:0xf bank_mask:0xf\n"
      "v_mfma_f32_16x16x16_bf16 v[20:23], v[134:135], v[38:39], v[20:23]\n"
      "v_add_f32 v32, v32, v33\n"
      "v_exp_f32 v34, v32\n"
      "v_max_f32 v35, v35, v32\n"
      "v_add_f32 v7, v7, v34\n"
      "v_cvt_pk_bf16_f32 v34, v34, v34\n"
      "v_and_b32 v36, v4, v34\n"
      "v_and_b32 v37, v5, v34\n"
      "v_mfma_f32_16x16x32_bf16 v[24:27], v[152:155], v[12:15], 0\n"
      "v_sub_f32_dpp v32, v28, v6 quad_perm:[0,0,0,0] row_mask:0xf bank_mask:0xf\n"
      "v_mov_b32_dpp v33, v30 quad_perm:[2,2,2,2] row_mask:0xf bank_mask:0xf\n"
      "v_mfma_f32_16x16x16_bf16 v[16:19], v[140:141], v[36:37], v[16:19]\n"
      "v_add_f32_dpp v32, v29, v32 quad_perm:[1,1,1,1] row_mask:0xf bank_mask:0xf\n"
      "v_add_f32_dpp v33, v31, v33 quad_perm:[3,3,3,3] row_mask:0xf bank_mask:0xf\n"
      "v_mfma_f32_16x16x16_bf16 v[20:23], v[142:143], v[36:37], v[20:23]\n"
      "v_add_f32 v32, v32, v33\n"
      "v_exp_f32 v34, v32\n"
      "v_max_f32 v35, v35, v32\n"
      "v_add_f32 v7, v7, v34\n"
      "v_cvt_pk_bf16_f32 v34, v34, v34\n"
      "v_and_b32 v38, v4, v34\n"
      "v_and_b32 v39, v5, v34\n"
      "s_waitcnt vmcnt(8)\n"
      "global_load_dwordx4 v[68:71], v0, s[42:43] nt\n"
      "s_add_u32 s42, s42, 110592\n"
      "s_addc_u32 s43, s43, 0\n"
      "v_mfma_f32_16x16x32_bf16 v[28:31], v[160:163], v[12:15], 0\n"
      "v_sub_f32_dpp v32, v24, v6 quad_perm:[0,0,0,0] row_mask:0xf bank_mask:0xf\n"
      "v_mov_b32_dpp v33, v26 quad_perm:[2,2,2,2] row_mask:0xf bank_mask:0xf\n"
      "v_mfma_f32_16x16x16_bf16 v[16:19], v[148:149], v[38:39], v[16:19]\n"
      "v_add_f32_dpp v32, v25, v32 quad_perm:[1,1,1,1] row_mask:0xf bank_mask:0xf\n"
      "v_add_f32_dpp v33, v27, v33 quad_perm:[3,3,3,3] row_mask:0xf bank_mask:0xf\n"
      "v_mfma_f32_16x16x16_bf16 v[20:23], v[150:151], v[38:39], v[20:23]\n"
      "v_add_f32 v32, v32, v33\n"
      "v_exp_f32 v34, v32\n"
      "v_max_f32 v35, v35, v32\n"
      "v_add_f32 v7, v7, v34\n"
      "v_cvt_pk_bf16_f32 v34, v34, v34\n"
      "v_and_b32 v36, v4, v34\n"
      "v_and_b32 v37, v5, v34\n"
      "v_mfma_f32_16x16x32_bf16 v[24:27], v[168:171], v[12:15], 0\n"
      "v_sub_f32_dpp v32, v28, v6 quad_perm:[0,0,0,0] row_mask:0xf bank_mask:0xf\n"
      "v_mov_b32_dpp v33, v30 quad_perm:[2,2,2,2] row_mask:0xf bank_mask:0xf\n"
      "v_mfma_f32_16x16x16_bf16 v[16:19], v[156:157], v[36:37], v[16:19]\n"
      "v_add_f32_dpp v32, v29, v32 quad_perm:[1,1,1,1] row_mask:0xf bank_mask:0xf\n"
      "v_add_f32_dpp v33, v31, v33 quad_perm:[3,3,3,3] row_mask:0xf bank_mask:0xf\n"
      "v_mfma_f32_16x16x16_bf16 v[20:23], v[158:159], v[36:37], v[20:23]\n"
      "v_add_f32 v32, v32, v33\n"
      "v_exp_f32 v34, v32\n"
      "v_max_f32 v35, v35, v32\n"
      "v_add_f32 v7, v7, v34\n"
      "v_cvt_pk_bf16_f32 v34, v34, v34\n"
      "v_and_b32 v38, v4, v34\n"
      "v_and_b32 v39, v5, v34\n"
      "v_mfma_f32_16x16x32_bf16 v[28:31], v[176:179], v[12:15], 0\n"
      "v_sub_f32_dpp v32, v24, v6 quad_perm:[0,0,0,0] row_mask:0xf bank_mask:0xf\n"
      "v_mov_b32_dpp v33, v26 quad_perm:[2,2,2,2] row_mask:0xf bank_mask:0xf\n"
      "v_mfma_f32_16x16x16_bf16 v[16:19], v[164:165], v[38:39], v[16:19]\n"
      "v_add_f32_dpp v32, v25, v32 quad_perm:[1,1,1,1] row_mask:0xf bank_mask:0xf\n"
      "v_add_f32_dpp v33, v27, v33 quad_perm:[3,3,3,3] row_mask:0xf bank_mask:0xf\n"
      "v_mfma_f32_16x16x16_bf16 v[20:23], v[166:167], v[38:39], v[20:23]\n"
      "v_add_f32 v32, v32, v33\n"
      "v_exp_f32 v34, v32\n"
      "v_max_f32 v35, v35, v32\n"
      "v_add_f32 v7, v7, v34\n"
      "v_cvt_pk_bf16_f32 v34, v34, v34\n"
      "v_and_b32 v36, v4, v34\n"
      "v_and_b32 v37, v5, v34\n"
      "s_waitcnt vmcnt(8)\n"
      "global_load_dwordx4 v[72:75], v1, s[40:41] nt\n"
      "s_add_u32 s40, s40, 110592\n"
      "s_addc_u32 s41, s41, 0\n"
      "v_mfma_f32_16x16x32_bf16 v[24:27], v[184:187], v[12:15], 0\n"
      "v_sub_f32_dpp v32, v28, v6 quad_perm:[0,0,0,0] row_mask:0xf bank_mask:0xf\n"
      "v_mov_b32_dpp v33, v30 quad_perm:[2,2,2,2] row_mask:0xf bank_mask:0xf\n"
      "v_mfma_f32_16x16x16_bf16 v[16:19], v[172:173], v[36:37], v[16:19]\n"
      "v_add_f32_dpp v32, v29, v32 quad_perm:[1,1,1,1] row_mask:0xf bank_mask:0xf\n"
      "v_add_f32_dpp v33, v31, v33 quad_perm:[3,3,3,3] row_mask:0xf bank_mask:0xf\n"
      "v_mfma_f32_16x16x16_bf16 v[20:23], v[174:175], v[36:37], v[20:23]\n"
      "v_add_f32 v32, v32, v33\n"
      "v_exp_f32 v34, v32\n"
      "v_max_f32 v35, v35, v32\n"
      "v_add_f32 v7, v7, v34\n"
      "v_cvt_pk_bf16_f32 v34, v34, v34\n"
      "v_and_b32 v38, v4, v34\n"
      "v_and_b32 v39, v5, v34\n"
      "v_mfma_f32_16x16x32_bf16 v[28:31], v[192:195], v[12:15], 0\n"
      "v_sub_f32_dpp v32, v24, v6 quad_perm:[0,0,0,0] row_mask:0xf bank_mask:0xf\n"
      "v_mov_b32_dpp v33, v26 quad_perm:[2,2,2,2] row_mask:0xf bank_mask:0xf\n"
      "v_mfma_f32_16x16x16_bf16 v[16:19], v[180:181], v[38:39], v[16:19]\n"
      "v_add_f32_dpp v32, v25, v32 quad_perm:[1,1,1,1] row_mask:0xf bank_mask:0xf\n"
      "v_add_f32_dpp v33, v27, v33 quad_perm:[3,3,3,3] row_mask:0xf bank_mask:0xf\n"
      "v_mfma_f32_16x16x16_bf16 v[20:23], v[182:183], v[38:39], v[20:23]\n"
      "v_add_f32 v32, v32, v33\n"
      "v_exp_f32 v34, v32\n"
      "v_max_f32 v35, v35, v32\n"
      "v_add_f32 v7, v7, v34\n"
      "v_cvt_pk_bf16_f32 v34, v34, v34\n"
      "v_and_b32 v36, v4, v34\n"
      "v_and_b32 v37, v5, v34\n"
      "s_waitcnt vmcnt(8)\n"
      "global_load_dwordx4 v[76:79], v0, s[42:43] nt\n"
      "s_add_u32 s42, s42, 110592\n"
      "s_addc_u32 s43, s43, 0\n"
      "v_mfma_f32_16x16x32_bf16 v[24:27], v[200:203], v[12:15], 0\n"
      "v_sub_f32_dpp v32, v28, v6 quad_perm:[0,0,0,0] row_mask:0xf bank_mask:0xf\n"
      "v_mov_b32_dpp v33, v30 quad_perm:[2,2,2,2] row_mask:0xf bank_mask:0xf\n"
      "v_mfma_f32_16x16x16_bf16 v[16:19], v[188:189], v[36:37], v[16:19]\n"
      "v_add_f32_dpp v32, v29, v32 quad_perm:[1,1,1,1] row_mask:0xf bank_mask:0xf\n"
      "v_add_f32_dpp v33, v31, v33 quad_perm:[3,3,3,3] row_mask:0xf bank_mask:0xf\n"
      "v_mfma_f32_16x16x16_bf16 v[20:23], v[190:191], v[36:37], v[20:23]\n"
      "v_add_f32 v32, v32, v33\n"
      "v_exp_f32 v34, v32\n"
      "v_max_f32 v35, v35, v32\n"
      "v_add_f32 v7, v7, v34\n"
      "v_cvt_pk_bf16_f32 v34, v34, v34\n"
      "v_and_b32 v38, v4, v34\n"
      "v_and_b32 v39, v5, v34\n"
      "v_mfma_f32_16x16x32_bf16 v[28:31], v[208:211], v[12:15], 0\n"
      "v_sub_f32_dpp v32, v24, v6 quad_perm:[0,0,0,0] row_mask:0xf bank_mask:0xf\n"
      "v_mov_b32_dpp v33, v26 quad_perm:[2,2,2,2] row_mask:0xf bank_mask:0xf\n"
      "v_mfma_f32_16x16x16_bf16 v[16:19], v[196:197], v[38:39], v[16:19]\n"
      "v_add_f32_dpp v32, v25, v32 quad_perm:[1,1,1,1] row_mask:0xf bank_mask:0xf\n"
      "v_add_f32_dpp v33, v27, v33 quad_perm:[3,3,3,3] row_mask:0xf bank_mask:0xf\n"
      "v_mfma_f32_16x16x16_bf16 v[20:23], v[198:199], v[38:39], v[20:23]\n"
      "v_add_f32 v32, v32, v33\n"
      "v_exp_f32 v34, v32\n"
      "v_max_f32 v35, v35, v32\n"
      "v_add_f32 v7, v7, v34\n"
      "v_cvt_pk_bf16_f32 v34, v34, v34\n"
      "v_and_b32 v36, v4, v34\n"
      "v_and_b32 v37, v5, v34\n"
      "s_waitcnt vmcnt(8)\n"
      "global_load_dwordx4 v[80:83], v1, s[40:41] nt\n"
      "s_add_u32 s40, s40, 110592\n"
      "s_addc_u32 s41, s41, 0\n"
      "v_mfma_f32_16x16x32_bf16 v[24:27], v[216:219], v[12:15], 0\n"
      "v_sub_f32_dpp v32, v28, v6 quad_perm:[0,0,0,0] row_mask:0xf bank_mask:0xf\n"
      "v_mov_b32_dpp v33, v30 quad_perm:[2,2,2,2] row_mask:0xf bank_mask:0xf\n"
      "v_mfma_f32_16x16x16_bf16 v[16:19], v[204:205], v[36:37], v[16:19]\n"
      "v_add_f32_dpp v32, v29, v32 quad_perm:[1,1,1,1] row_mask:0xf bank_mask:0xf\n"
      "v_add_f32_dpp v33, v31, v33 quad_perm:[3,3,3,3] row_mask:0xf bank_mask:0xf\n"
      "v_mfma_f32_16x16x16_bf16 v[20:23], v[206:207], v[36:37], v[20:23]\n"
      "v_add_f32 v32, v32, v33\n"
      "v_exp_f32 v34, v32\n"
      "v_max_f32 v35, v35, v32\n"
      "v_add_f32 v7, v7, v34\n"
      "v_cvt_pk_bf16_f32 v34, v34, v34\n"
      "v_and_b32 v38, v4, v34\n"
      "v_and_b32 v39, v5, v34\n"
      "v_mfma_f32_16x16x32_bf16 v[28:31], v[224:227], v[12:15], 0\n"
      "v_sub_f32_dpp v32, v24, v6 quad_perm:[0,0,0,0] row_mask:0xf bank_mask:0xf\n"
      "v_mov_b32_dpp v33, v26 quad_perm:[2,2,2,2] row_mask:0xf bank_mask:0xf\n"
      "v_mfma_f32_16x16x16_bf16 v[16:19], v[212:213], v[38:39], v[16:19]\n"
      "v_add_f32_dpp v32, v25, v32 quad_perm:[1,1,1,1] row_mask:0xf bank_mask:0xf\n"
      "v_add_f32_dpp v33, v27, v33 quad_perm:[3,3,3,3] row_mask:0xf bank_mask:0xf\n"
      "v_mfma_f32_16x16x16_bf16 v[20:23], v[214:215], v[38:39], v[20:23]\n"
      "v_add_f32 v32, v32, v33\n"
      "v_exp_f32 v34, v32\n"
      "v_max_f32 v35, v35, v32\n"
      "v_add_f32 v7, v7, v34\n"
      "v_cvt_pk_bf16_f32 v34, v34, v34\n"
      "v_and_b32 v36, v4, v34\n"
      "v_and_b32 v37, v5, v34\n"
      "v_mfma_f32_16x16x32_bf16 v[24:27], v[232:235], v[12:15], 0\n"
      "v_sub_f32_dpp v32, v28, v6 quad_perm:[0,0,0,0] row_mask:0xf bank_mask:0xf\n"
      "v_mov_b32_dpp v33, v30 quad_perm:[2,2,2,2] row_mask:0xf bank_mask:0xf\n"
      "v_mfma_f32_16x16x16_bf16 v[16:19], v[220:221], v[36:37], v[16:19]\n"
      "v_add_f32_dpp v32, v29, v32 quad_perm:[1,1,1,1] row_mask:0xf bank_mask:0xf\n"
      "v_add_f32_dpp v33, v31, v33 quad_perm:[3,3,3,3] row_mask:0xf bank_mask:0xf\n"
      "v_mfma_f32_16x16x16_bf16 v[20:23], v[222:223], v[36:37], v[20:23]\n"
      "v_add_f32 v32, v32, v33\n"
      "v_exp_f32 v34, v32\n"
      "v_max_f32 v35, v35, v32\n"
      "v_add_f32 v7, v7, v34\n"
      "v_cvt_pk_bf16_f32 v34, v34, v34\n"
      "v_and_b32 v38, v4, v34\n"
      "v_and_b32 v39, v5, v34\n"
      "s_waitcnt vmcnt(8)\n"
      "global_load_dwordx4 v[84:87], v0, s[42:43] nt\n"
      "s_add_u32 s42, s42, 110592\n"
      "s_addc_u32 s43, s43, 0\n"
      "v_mfma_f32_16x16x32_bf16 v[28:31], v[240:243], v[12:15], 0\n"
      "v_sub_f32_dpp v32, v24, v6 quad_perm:[0,0,0,0] row_mask:0xf bank_mask:0xf\n"
      "v_mov_b32_dpp v33, v26 quad_perm:[2,2,2,2] row_mask:0xf bank_mask:0xf\n"
      "v_mfma_f32_16x16x16_bf16 v[16:19], v[228:229], v[38:39], v[16:19]\n"
      "v_add_f32_dpp v32, v25, v32 quad_perm:[1,1,1,1] row_mask:0xf bank_mask:0xf\n"
      "v_add_f32_dpp v33, v27, v33 quad_perm:[3,3,3,3] row_mask:0xf bank_mask:0xf\n"
      "v_mfma_f32_16x16x16_bf16 v[20:23], v[230:231], v[38:39], v[20:23]\n"
      "v_add_f32 v32, v32, v33\n"
      "v_exp_f32 v34, v32\n"
      "v_max_f32 v35, v35, v32\n"
      "v_add_f32 v7, v7, v34\n"
      "v_cvt_pk_bf16_f32 v34, v34, v34\n"
      "v_and_b32 v36, v4, v34\n"
      "v_and_b32 v37, v5, v34\n"
      "v_mfma_f32_16x16x32_bf16 v[24:27], v[248:251], v[12:15], 0\n"
      "v_sub_f32_dpp v32, v28, v6 quad_perm:[0,0,0,0] row_mask:0xf bank_mask:0xf\n"
      "v_mov_b32_dpp v33, v30 quad_perm:[2,2,2,2] row_mask:0xf bank_mask:0xf\n"
      "v_mfma_f32_16x16x16_bf16 v[16:19], v[236:237], v[36:37], v[16:19]\n"
      "v_add_f32_dpp v32, v29, v32 quad_perm:[1,1,1,1] row_mask:0xf bank_mask:0xf\n"
      "v_add_f32_dpp v33, v31, v33 quad_perm:[3,3,3,3] row_mask:0xf bank_mask:0xf\n"
      "v_mfma_f32_16x16x16_bf16 v[20:23], v[238:239], v[36:37], v[20:23]\n"
      "v_add_f32 v32, v32, v33\n"
      "v_exp_f32 v34, v32\n"
      "v_max_f32 v35, v35, v32\n"
      "v_add_f32 v7, v7, v34\n"
      "v_cvt_pk_bf16_f32 v34, v34, v34\n"
      "v_and_b32 v38, v4, v34\n"
      "v_and_b32 v39, v5, v34\n"
      "s_waitcnt vmcnt(8)\n"
      "global_load_dwordx4 v[88:91], v1, s[40:41] nt\n"
      "s_add_u32 s40, s40, s51\n"
      "s_addc_u32 s41, s41, 0\n"
      "v_sub_f32_dpp v32, v24, v6 quad_perm:[0,0,0,0] row_mask:0xf bank_mask:0xf\n"
      "v_mov_b32_dpp v33, v26 quad_perm:[2,2,2,2] row_mask:0xf bank_mask:0xf\n"
      "v_mfma_f32_16x16x16_bf16 v[16:19], v[244:245], v[38:39], v[16:19]\n"
      "v_add_f32_dpp v32, v25, v32 quad_perm:[1,1,1,1] row_mask:0xf bank_mask:0xf\n"
      "v_add_f32_dpp v33, v27, v33 quad_perm:[3,3,3,3] row_mask:0xf bank_mask:0xf\n"
      "v_mfma_f32_16x16x16_bf16 v[20:23], v[246:247], v[38:39], v[20:23]\n"
      "v_add_f32 v32, v32, v33\n"
      "v_add_f32 v32, s69, v32\n"
      "v_exp_f32 v34, v32\n"
      "v_max_f32 v35, v35, v32\n"
      "v_add_f32 v7, v7, v34\n"
      "v_cvt_pk_bf16_f32 v34, v34, v34\n"
      "v_and_b32 v36, v4, v34\n"
      "v_and_b32 v37, v5, v34\n"
      "s_nop 1\n"
      "v_mfma_f32_16x16x16_bf16 v[16:19], v[252:253], v[36:37], v[16:19]\n"
      "v_mfma_f32_16x16x16_bf16 v[20:23], v[254:255], v[36:37], v[20:23]\n"
      "s_waitcnt vmcnt(8)\n"
      "global_load_dwordx4 v[92:95], v0, s[42:43] nt\n"
      "s_add_u32 s42, s42, s51\n"
      "s_addc_u32 s43, s43, 0\n"
      "v_cmp_lt_f32 vcc, 0x42800000, v35\n"
      "s_nop 0\n"
      "s_cbranch_vccnz L_fb9_%=\n"
      "L_ret9_%=:\n"
      "v_mov_b32 v49, v7\n"
      "v_lshlrev_b32 v41, 2, v0\n"
      "v_and_b32 v41, 0x300, v41\n"
      "v_permlane32_swap_b32 v49, v7\n"
      "v_lshrrev_b32 v42, 4, v0\n"
      "v_add_f32 v7, v49, v7\n"
      "v_and_b32 v42, 0x30, v42\n"
      "v_mov_b32 v49, v7\n"
      "v_add_u32 v41, v41, v42\n"
      "v_lshrrev_b32 v40, 2, v0\n"
      "v_permlane16_swap_b32 v49, v7\n"
      "v_and_b32 v42, 12, v40\n"
      "v_add_f32 v7, v49, v7\n"
      "v_add_u32 v41, v41, v42\n"
      "s_lshl_b32 s62, s60, 10\n"
      "s_add_u32 s62, s62, 155648\n"
      "v_add_u32 v41, s62, v41\n"
      "v_lshrrev_b32 v43, 3, v0\n"
      "v_and_b32 v43, 24, v43\n"
      "s_lshl_b32 s62, s60, 5\n"
      "s_add_u32 s62, s62, 159744\n"
      "v_add_u32 v43, s62, v43\n"
      "v_cvt_pk_bf16_f32 v36, v16, v17\n"
      "v_cvt_pk_bf16_f32 v37, v18, v19\n"
      "v_cvt_pk_bf16_f32 v38, v20, v21\n"
      "v_cvt_pk_bf16_f32 v39, v22, v23\n"
      "s_mov_b64 exec, 0x1111\n"
      "ds_write_b64 v43, v[6:7]\n"
      "s_mov_b64 exec, -1\n"
      "ds_write_b32 v41, v36 offset:0\n"
      "ds_write_b32 v41, v37 offset:64\n"
      "ds_write_b32 v41, v38 offset:128\n"
      "ds_write_b32 v41, v39 offset:192\n"
      "s_lshl_b32 s62, s60, 8\n"
      "s_add_u32 s62, s62, 155648\n"
      "v_add_u32 v42, s62, v40\n"
      "s_lshl_b32 s62, s60, 3\n"
      "s_add_u32 s62, s62, 159744\n"
      "v_mov_b32 v50, s62\n"
      "s_waitcnt lgkmcnt(0)\n"
      "s_barrier\n"
      "ds_read_b64 v[28:29], v50 offset:0\n"
      "ds_read_b64 v[30:31], v50 offset:32\n"
      "ds_read_b64 v[32:33], v50 offset:64\n"
      "ds_read_b64 v[34:35], v50 offset:96\n"
      "ds_read_b32 v24, v42 offset:0\n"
      "ds_read_b32 v25, v42 offset:1024\n"
      "ds_read_b32 v26, v42 offset:2048\n"
      "ds_read_b32 v27, v42 offset:3072\n"
      "s_waitcnt lgkmcnt(4)\n"
      "v_max3_f32 v43, v28, v30, v32\n"
      "v_max_f32 v43, v43, v34\n"
      "v_sub_f32 v28, v28, v43\n"
      "v_sub_f32 v30, v30, v43\n"
      "v_sub_f32 v32, v32, v43\n"
      "v_sub_f32 v34, v34, v43\n"
      "v_exp_f32 v28, v28\n"
      "v_exp_f32 v30, v30\n"
      "v_exp_f32 v32, v32\n"
      "v_exp_f32 v34, v34\n"
      "s_waitcnt lgkmcnt(0)\n"
      "v_lshlrev_b32 v44, 16, v24\n"
      "v_and_b32 v45, 0xffff0000, v24\n"
      "v_lshlrev_b32 v46, 16, v25\n"
      "v_and_b32 v47, 0xffff0000, v25\n"
      "v_lshlrev_b32 v48, 16, v26\n"
      "v_and_b32 v49, 0xffff0000, v26\n"
      "v_lshlrev_b32 v50, 16, v27\n"
      "v_and_b32 v51, 0xffff0000, v27\n"
      "v_mul_f32 v38, v28, v29\n"
      "v_pk_mul_f32 v[36:37], v[44:45], v[28:29] op_sel_hi:[1,0]\n"
      "v_fmac_f32 v38, v30, v31\n"
      "v_pk_fma_f32 v[36:37], v[46:47], v[30:31], v[36:37] op_sel_hi:[1,0,1]\n"
      "v_fmac_f32 v38, v32, v33\n"
      "v_pk_fma_f32 v[36:37], v[48:49], v[32:33], v[36:37] op_sel_hi:[1,0,1]\n"
      "v_fmac_f32 v38, v34, v35\n"
      "v_pk_fma_f32 v[36:37], v[50:51], v[34:35], v[36:37] op_sel_hi:[1,0,1]\n"
      "v_rcp_f32 v39, v38\n"
      "v_log_f32 v38, v38\n"
      "v_mul_f32 v36, v39, v36\n"
      "v_mul_f32 v37, v39, v37\n"
      "v_add_f32 v38, v38, v43\n"
      "v_cvt_pk_bf16_f32 v36, v36, v37\n"
      "v_min_u32 v38, -2, v38\n"
      "v_min_u32 v36, -2, v36\n"
      "v_mov_b32 v12, v36\n"
      "v_mov_b32 v13, v38\n"
      "s_waitcnt vmcnt(8)\n"
      "global_load_dwordx4 v[96:99], v1, s[40:41] nt\n"
      "s_add_u32 s40, s40, s52\n"
      "s_addc_u32 s41, s41, 0\n"
      "s_waitcnt vmcnt(8)\n"
      "global_load_dwordx4 v[100:103], v0, s[42:43] nt\n"
      "s_add_u32 s42, s42, s52\n"
      "s_addc_u32 s43, s43, 0\n"
      "s_waitcnt vmcnt(8)\n"
      "global_load_dwordx4 v[104:107], v1, s[40:41] nt\n"
      "s_add_u32 s40, s40, 110592\n"
      "s_addc_u32 s41, s41, 0\n"
      "s_waitcnt vmcnt(8)\n"
      "global_load_dwordx4 v[108:111], v0, s[42:43] nt\n"
      "s_add_u32 s42, s42, 110592\n"
      "s_addc_u32 s43, s43, 0\n"
      "s_barrier\n"
      "s_mov_b64 exec, 0xffff\n"
      "s_mov_b32 s53, 0\n"
      "L_qc23_%=:\n"
      "v_max3_u32 v49, v8, v9, v10\n"
      "v_max_u32 v49, v49, v11\n"
      "v_cmp_eq_u32 vcc, -1, v49\n"
      "s_nop 0\n"
      "s_cbranch_vccz L_qo24_%=\n"
      "s_cmp_lg_u32 s54, 0\n"
      "s_cbranch_scc1 L_qo24_%=\n"
      "s_add_u32 s56, s48, 49152\n"
      "s_addc_u32 s57, s49, 0\n"
      "global_load_dwordx4 v[8:11], v0, s[56:57] sc1\n"
      "s_add_u32 s53, s53, 1\n"
      "s_waitcnt vmcnt(0)\n"
      "s_cmp_lt_u32 s53, 30000\n"
      "s_cbranch_scc1 L_qc23_%=\n"
      "s_mov_b32 s54, 106\n"
      "v_mov_b32 v49, 106\n"
      "v_mov_b32 v50, 256\n"
      "global_store_dword v50, v49, s[58:59] sc1\n"
      "L_qo24_%=:\n"
      "s_lshl_b32 s62, s60, 8\n"
      "s_add_u32 s62, s62, 159872\n"
      "v_add_u32 v49, s62, v0\n"
      "ds_write_b128 v49, v[8:11]\n"
      "s_mov_b64 exec, -1\n"
      "s_waitcnt lgkmcnt(0)\n"
      "s_barrier\n"
      "ds_read_b128 v[8:11], v2 offset:0\n"
      "s_waitcnt lgkmcnt(0)\n"
      "v_mfma_f32_16x16x32_bf16 v[24:27], a[152:155], v[8:11], 0\n"
      "v_mfma_f32_16x16x32_bf16 v[28:31], a[160:163], v[8:11], 0\n"
      "s_nop 6\n"
      "v_mov_b32_dpp v34, v24 quad_perm:[0,0,0,0] row_mask:0xf bank_mask:0xf\n"
      "v_mov_b32_dpp v33, v26 quad_perm:[2,2,2,2] row_mask:0xf bank_mask:0xf\n"
      "s_nop 0\n"
      "v_add_f32_dpp v34, v25, v34 quad_perm:[1,1,1,1] row_mask:0xf bank_mask:0xf\n"
      "v_add_f32_dpp v33, v27, v33 quad_perm:[3,3,3,3] row_mask:0xf bank_mask:0xf\n"
      "v_add_f32 v34, v34, v33\n"
      "v_mov_b32_dpp v32, v28 quad_perm:[0,0,0,0] row_mask:0xf bank_mask:0xf\n"
      "v_mov_b32_dpp v33, v30 quad_perm:[2,2,2,2] row_mask:0xf bank_mask:0xf\n"
      "s_nop 0\n"
      "v_add_f32_dpp v32, v29, v32 quad_perm:[1,1,1,1] row_mask:0xf bank_mask:0xf\n"
      "v_add_f32_dpp v33, v31, v33 quad_perm:[3,3,3,3] row_mask:0xf bank_mask:0xf\n"
      "v_add_f32 v32, v32, v33\n"
      "v_max_f32 v35, v34, v32\n"
      "v_mov_b32 v33, v35\n"
      "s_nop 1\n"
      "v_permlane32_swap_b32 v33, v35\n"
      "v_max_f32 v35, v33, v35\n"
      "v_mov_b32 v33, v35\n"
      "s_nop 1\n"
      "v_permlane16_swap_b32 v33, v35\n"
      "v_max_f32 v6, v33, v35\n"
      "v_sub_f32 v34, v34, v6\n"
      "v_sub_f32 v32, v32, v6\n"
      "v_mov_b32 v35, 0\n"
      "v_exp_f32 v34, v34\n"
      "s_nop 0\n"
      "v_mov_b32 v7, v34\n"
      "v_cvt_pk_bf16_f32 v34, v34, v34\n"
      "v_and_b32 v36, v4, v34\n"
      "v_and_b32 v37, v5, v34\n"
      "v_mfma_f32_16x16x32_bf16 v[24:27], a[168:171], v[8:11], 0\n"
      "s_nop 0\n"
      "v_mfma_f32_16x16x16_bf16 v[16:19], a[156:157], v[36:37], 0\n"
      "v_mfma_f32_16x16x16_bf16 v[20:23], a[158:159], v[36:37], 0\n"
      "v_exp_f32 v34, v32\n"
      "v_max_f32 v35, v35, v32\n"
      "v_add_f32 v7, v7, v34\n"
      "v_cvt_pk_bf16_f32 v34, v34, v34\n"
      "v_and_b32 v38, v4, v34\n"
      "v_and_b32 v39, v5, v34\n"
      "v_mfma_f32_16x16x32_bf16 v[28:31], a[176:179], v[8:11], 0\n"
      "v_sub_f32_dpp v32, v24, v6 quad_perm:[0,0,0,0] row_mask:0xf bank_mask:0xf\n"
      "v_mov_b32_dpp v33, v26 quad_perm:[2,2,2,2] row_mask:0xf bank_mask:0xf\n"
      "v_mfma_f32_16x16x16_bf16 v[16:19], a[164:165], v[38:39], v[16:19]\n"
      "v_add_f32_dpp v32, v25, v32 quad_perm:[1,1,1,1] row_mask:0xf bank_mask:0xf\n"
      "v_add_f32_dpp v33, v27, v33 quad_perm:[3,3,3,3] row_mask:0xf bank_mask:0xf\n"
      "v_mfma_f32_16x16x16_bf16 v[20:23], a[166:167], v[38:39], v[20:23]\n"
      "v_add_f32 v32, v32, v33\n"
      "v_exp_f32 v34, v32\n"
      "v_max_f32 v35, v35, v32\n"
      "v_add_f32 v7, v7, v34\n"
      "v_cvt_pk_bf16_f32 v34, v34, v34\n"
      "v_and_b32 v36, v4, v34\n"
      "v_and_b32 v37, v5, v34\n"
      "v_mfma_f32_16x16x32_bf16 v[24:27], a[184:187], v[8:11], 0\n"
      "v_sub_f32_dpp v32, v28, v6 quad_perm:[0,0,0,0] row_mask:0xf bank_mask:0xf\n"
      "v_mov_b32_dpp v33, v30 quad_perm:[2,2,2,2] row_mask:0xf bank_mask:0xf\n"
      "v_mfma_f32_16x16x16_bf16 v[16:19], a[172:173], v[36:37], v[16:19]\n"
      "v_add_f32_dpp v32, v29, v32 quad_perm:[1,1,1,1] row_mask:0xf bank_mask:0xf\n"
      "v_add_f32_dpp v33, v31, v33 quad_perm:[3,3,3,3] row_mask:0xf bank_mask:0xf\n"
      "v_mfma_f32_16x16x16_bf16 v[20:23], a[174:175], v[36:37], v[20:23]\n"
      "v_add_f32 v32, v32, v33\n"
      "v_exp_f32 v34, v32\n"
      "v_max_f32 v35, v35, v32\n"
      "v_add_f32 v7, v7, v34\n"
      "v_cvt_pk_bf16_f32 v34, v34, v34\n"
      "v_and_b32 v38, v4, v34\n"
      "v_and_b32 v39, v5, v34\n"
      "s_waitcnt vmcnt(8)\n"
      "global_load_dwordx4 v[112:115], v1, s[40:41] nt\n"
      "s_add_u32 s40, s40, 110592\n"
      "s_addc_u32 s41, s41, 0\n"
      "v_mfma_f32_16x16x32_bf16 v[28:31], a[192:195], v[8:11], 0\n"
      "v_sub_f32_dpp v32, v24, v6 quad_perm:[0,0,0,0] row_mask:0xf bank_mask:0xf\n"
      "v_mov_b32_dpp v33, v26 quad_perm:[2,2,2,2] row_mask:0xf bank_mask:0xf\n"
      "v_mfma_f32_16x16x16_bf16 v[16:19], a[180:181], v[38:39], v[16:19]\n"
      "v_add_f32_dpp v32, v25, v32 quad_perm:[1,1,1,1] row_mask:0xf bank_mask:0xf\n"
      "v_add_f32_dpp v33, v27, v33 quad_perm:[3,3,3,3] row_mask:0xf bank_mask:0xf\n"
      "v_mfma_f32_16x16x16_bf16 v[20:23], a[182:183], v[38:39], v[20:23]\n"
      "v_add_f32 v32, v32, v33\n"
      "v_exp_f32 v34, v32\n"
      "v_max_f32 v35, v35, v32\n"
      "v_add_f32 v7, v7, v34\n"
      "v_cvt_pk_bf16_f32 v34, v34, v34\n"
      "v_and_b32 v36, v4, v34\n"
      "v_and_b32 v37, v5, v34\n"
      "v_mfma_f32_16x16x32_bf16 v[24:27], a[200:203], v[8:11], 0\n"
      "v_sub_f32_dpp v32, v28, v6 quad_perm:[0,0,0,0] row_mask:0xf bank_mask:0xf\n"
      "v_mov_b32_dpp v33, v30 quad_perm:[2,2,2,2] row_mask:0xf bank_mask:0xf\n"
      "v_mfma_f32_16x16x16_bf16 v[16:19], a[188:189], v[36:37], v[16:19]\n"
      "v_add_f32_dpp v32, v29, v32 quad_perm:[1,1,1,1] row_mask:0xf bank_mask:0xf\n"
      "v_add_f32_dpp v33, v31, v33 quad_perm:[3,3,3,3] row_mask:0xf bank_mask:0xf\n"
      "v_mfma_f32_16x16x16_bf16 v[20:23], a[190:191], v[36:37], v[20:23]\n"
      "v_add_f32 v32, v32, v33\n"
      "v_exp_f32 v34, v32\n"
      "v_max_f32 v35, v35, v32\n"
      "v_add_f32 v7, v7, v34\n"
      "v_cvt_pk_bf16_f32 v34, v34, v34\n"
      "v_and_b32 v38, v4, v34\n"
      "v_and_b32 v39, v5, v34\n"
      "s_waitcnt vmcnt(8)\n"
      "global_load_dwordx4 v[116:119], v0, s[42:43] nt\n"
      "s_add_u32 s42, s42, 110592\n"
      "s_addc_u32 s43, s43, 0\n"
      "v_mfma_f32_16x16x32_bf16 v[28:31], a[208:211], v[8:11], 0\n"
      "v_sub_f32_dpp v32, v24, v6 quad_perm:[0,0,0,0] row_mask:0xf bank_mask:0xf\n"
      "v_mov_b32_dpp v33, v26 quad_perm:[2,2,2,2] row_mask:0xf bank_mask:0xf\n"
      "v_mfma_f32_16x16x16_bf16 v[16:19], a[196:197], v[38:39], v[16:19]\n"
      "v_add_f32_dpp v32, v25, v32 quad_perm:[1,1,1,1] row_mask:0xf bank_mask:0xf\n"
      "v_add_f32_dpp v33, v27, v33 quad_perm:[3,3,3,3] row_mask:0xf bank_mask:0xf\n"
      "v_mfma_f32_16x16x16_bf16 v[20:23], a[198:199], v[38:39], v[20:23]\n"
      "v_add_f32 v32, v32, v33\n"
      "v_exp_f32 v34, v32\n"
      "v_max_f32 v35, v35, v32\n"
      "v_add_f32 v7, v7, v34\n"
      "v_cvt_pk_bf16_f32 v34, v34, v34\n"
      "v_and_b32 v36, v4, v34\n"
      "v_and_b32 v37, v5, v34\n"
      "v_mfma_f32_16x16x32_bf16 v[24:27], a[216:219], v[8:11], 0\n"
      "v_sub_f32_dpp v32, v28, v6 quad_perm:[0,0,0,0] row_mask:0xf bank_mask:0xf\n"
      "v_mov_b32_dpp v33, v30 quad_perm:[2,2,2,2] row_mask:0xf bank_mask:0xf\n"
      "v_mfma_f32_16x16x16_bf16 v[16:19], a[204:205], v[36:37], v[16:19]\n"
      "v_add_f32_dpp v32, v29, v32 quad_perm:[1,1,1,1] row_mask:0xf bank_mask:0xf\n"
      "v_add_f32_dpp v33, v31, v33 quad_perm:[3,3,3,3] row_mask:0xf bank_mask:0xf\n"
      "v_mfma_f32_16x16x16_bf16 v[20:23], a[206:207], v[36:37], v[20:23]\n"
      "v_add_f32 v32, v32, v33\n"
      "v_exp_f32 v34, v32\n"
      "v_max_f32 v35, v35, v32\n"
      "v_add_f32 v7, v7, v34\n"
      "v_cvt_pk_bf16_f32 v34, v34, v34\n"
      "v_and_b32 v38, v4, v34\n"
      "v_and_b32 v39, v5, v34\n"
      "v_mfma_f32_16x16x32_bf16 v[28:31], a[224:227], v[8:11], 0\n"
      "v_sub_f32_dpp v32, v24, v6 quad_perm:[0,0,0,0] row_mask:0xf bank_mask:0xf\n"
      "v_mov_b32_dpp v33, v26 quad_perm:[2,2,2,2] row_mask:0xf bank_mask:0xf\n"
      "v_mfma_f32_16x16x16_bf16 v[16:19], a[212:213], v[38:39], v[16:19]\n"
      "v_add_f32_dpp v32, v25, v32 quad_perm:[1,1,1,1] row_mask:0xf bank_mask:0xf\n"
      "v_add_f32_dpp v33, v27, v33 quad_perm:[3,3,3,3] row_mask:0xf bank_mask:0xf\n"
      "v_mfma_f32_16x16x16_bf16 v[20:23], a[214:215], v[38:39], v[20:23]\n"
      "v_add_f32 v32, v32, v33\n"
      "v_exp_f32 v34, v32\n"
      "v_max_f32 v35, v35, v32\n"
      "v_add_f32 v7, v7, v34\n"
      "v_cvt_pk_bf16_f32 v34, v34, v34\n"
      "v_and_b32 v36, v4, v34\n"
      "v_and_b32 v37, v5, v34\n"
      "s_waitcnt vmcnt(8)\n"
      "global_load_dwordx4 v[120:123], v1, s[40:41] nt\n"
      "s_add_u32 s40, s40, 110592\n"
      "s_addc_u32 s41, s41, 0\n"
      "v_mfma_f32_16x16x32_bf16 v[24:27], a[232:235], v[8:11], 0\n"
      "v_sub_f32_dpp v32, v28, v6 quad_perm:[0,0,0,0] row_mask:0xf bank_mask:0xf\n"
      "v_mov_b32_dpp v33, v30 quad_perm:[2,2,2,2] row_mask:0xf bank_mask:0xf\n"
      "v_mfma_f32_16x16x16_bf16 v[16:19], a[220:221], v[36:37], v[16:19]\n"
      "v_add_f32_dpp v32, v29, v32 quad_perm:[1,1,1,1] row_mask:0xf bank_mask:0xf\n"
      "v_add_f32_dpp v33, v31, v33 quad_perm:[3,3,3,3] row_mask:0xf bank_mask:0xf\n"
      "v_mfma_f32_16x16x16_bf16 v[20:23], a[222:223], v[36:37], v[20:23]\n"
      "v_add_f32 v32, v32, v33\n"
      "v_exp_f32 v34, v32\n"
      "v_max_f32 v35, v35, v32\n"
      "v_add_f32 v7, v7, v34\n"
      "v_cvt_pk_bf16_f32 v34, v34, v34\n"
      "v_and_b32 v38, v4, v34\n"
      "v_and_b32 v39, v5, v34\n"
      "v_mfma_f32_16x16x32_bf16 v[28:31], a[240:243], v[8:11], 0\n"
      "v_sub_f32_dpp v32, v24, v6 quad_perm:[0,0,0,0] row_mask:0xf bank_mask:0xf\n"
      "v_mov_b32_dpp v33, v26 quad_perm:[2,2,2,2] row_mask:0xf bank_mask:0xf\n"
      "v_mfma_f32_16x16x16_bf16 v[16:19], a[228:229], v[38:39], v[16:19]\n"
      "v_add_f32_dpp v32, v25, v32 quad_perm:[1,1,1,1] row_mask:0xf bank_mask:0xf\n"
      "v_add_f32_dpp v33, v27, v33 quad_perm:[3,3,3,3] row_mask:0xf bank_mask:0xf\n"
      "v_mfma_f32_16x16x16_bf16 v[20:23], a[230:231], v[38:39], v[20:23]\n"
      "v_add_f32 v32, v32, v33\n"
      "v_exp_f32 v34, v32\n"
      "v_max_f32 v35, v35, v32\n"
      "v_add_f32 v7, v7, v34\n"
      "v_cvt_pk_bf16_f32 v34, v34, v34\n"
      "v_and_b32 v36, v4, v34\n"
      "v_and_b32 v37, v5, v34\n"
      "s_waitcnt vmcnt(8)\n"
      "global_load_dwordx4 v[124:127], v0, s[42:43] nt\n"
      "s_add_u32 s42, s42, 110592\n"
      "s_addc_u32 s43, s43, 0\n"
      "v_mfma_f32_16x16x32_bf16 v[24:27], a[248:251], v[8:11], 0\n"
      "v_sub_f32_dpp v32, v28, v6 quad_perm:[0,0,0,0] row_mask:0xf bank_mask:0xf\n"
      "v_mov_b32_dpp v33, v30 quad_perm:[2,2,2,2] row_mask:0xf bank_mask:0xf\n"
      "v_mfma_f32_16x16x16_bf16 v[16:19], a[236:237], v[36:37], v[16:19]\n"
      "v_add_f32_dpp v32, v29, v32 quad_perm:[1,1,1,1] row_mask:0xf bank_mask:0xf\n"
      "v_add_f32_dpp v33, v31, v33 quad_perm:[3,3,3,3] row_mask:0xf bank_mask:0xf\n"
      "v_mfma_f32_16x16x16_bf16 v[20:23], a[238:239], v[36:37], v[20:23]\n"
      "v_add_f32 v32, v32, v33\n"
      "v_exp_f32 v34, v32\n"
      "v_max_f32 v35, v35, v32\n"
      "v_add_f32 v7, v7, v34\n"
      "v_cvt_pk_bf16_f32 v34, v34, v34\n"
      "v_and_b32 v38, v4, v34\n"
      "v_and_b32 v39, v5, v34\n"
      "v_mfma_f32_16x16x32_bf16 v[28:31], v[56:59], v[8:11], 0\n"
      "v_sub_f32_dpp v32, v24, v6 quad_perm:[0,0,0,0] row_mask:0xf bank_mask:0xf\n"
      "v_mov_b32_dpp v33, v26 quad_perm:[2,2,2,2] row_mask:0xf bank_mask:0xf\n"
      "v_mfma_f32_16x16x16_bf16 v[16:19], a[244:245], v[38:39], v[16:19]\n"
      "v_add_f32_dpp v32, v25, v32 quad_perm:[1,1,1,1] row_mask:0xf bank_mask:0xf\n"
      "v_add_f32_dpp v33, v27, v33 quad_perm:[3,3,3,3] row_mask:0xf bank_mask:0xf\n"
      "v_mfma_f32_16x16x16_bf16 v[20:23], a[246:247], v[38:39], v[20:23]\n"
      "v_add_f32 v32, v32, v33\n"
      "v_exp_f32 v34, v32\n"
      "v_max_f32 v35, v35, v32\n"
      "v_add_f32 v7, v7, v34\n"
      "v_cvt_pk_bf16_f32 v34, v34, v34\n"
      "v_and_b32 v36, v4, v34\n"
      "v_and_b32 v37, v5, v34\n"
      "s_waitcnt vmcnt(8)\n"
      "global_load_dwordx4 v[128:131], v1, s[40:41] nt\n"
      "s_add_u32 s40, s40, 110592\n"
      "s_addc_u32 s41, s41, 0\n"
      "v_mfma_f32_16x16x32_bf16 v[24:27], v[64:67], v[8:11], 0\n"
      "v_sub_f32_dpp v32, v28, v6 quad_perm:[0,0,0,0] row_mask:0xf bank_mask:0xf\n"
      "v_mov_b32_dpp v33, v30 quad_perm:[2,2,2,2] row_mask:0xf bank_mask:0xf\n"
      "v_mfma_f32_16x16x16_bf16 v[16:19], a[252:253], v[36:37], v[16:19]\n"
      "v_add_f32_dpp v32, v29, v32 quad_perm:[1,1,1,1] row_mask:0xf bank_mask:0xf\n"
      "v_add_f32_dpp v33, v31, v33 quad_perm:[3,3,3,3] row_mask:0xf bank_mask:0xf\n"
      "v_mfma_f32_16x16x16_bf16 v[20:23], a[254:255], v[36:37], v[20:23]\n"
      "v_add_f32 v32, v32, v33\n"
      "v_exp_f32 v34, v32\n"
      "v_max_f32 v35, v35, v32\n"
      "v_add_f32 v7, v7, v34\n"
      "v_cvt_pk_bf16_f32 v34, v34, v34\n"
      "v_and_b32 v38, v4, v34\n"
      "v_and_b32 v39, v5, v34\n"
      "v_mfma_f32_16x16x32_bf16 v[28:31], v[72:75], v[8:11], 0\n"
      "v_sub_f32_dpp v32, v24, v6 quad_perm:[0,0,0,0] row_mask:0xf bank_mask:0xf\n"
      "v_mov_b32_dpp v33, v26 quad_perm:[2,2,2,2] row_mask:0xf bank_mask:0xf\n"
      "v_mfma_f32_16x16x16_bf16 v[16:19], v[60:61], v[38:39], v[16:19]\n"
      "v_add_f32_dpp v32, v25, v32 quad_perm:[1,1,1,1] row_mask:0xf bank_mask:0xf\n"
      "v_add_f32_dpp v33, v27, v33 quad_perm:[3,3,3,3] row_mask:0xf bank_mask:0xf\n"
      "v_mfma_f32_16x16x16_bf16 v[20:23], v[62:63], v[38:39], v[20:23]\n"
      "v_add_f32 v32, v32, v33\n"
      "v_exp_f32 v34, v32\n"
      "v_max_f32 v35, v35, v32\n"
      "v_add_f32 v7, v7, v34\n"
      "v_cvt_pk_bf16_f32 v34, v34, v34\n"
      "v_and_b32 v36, v4, v34\n"
      "v_and_b32 v37, v5, v34\n"
      "v_mfma_f32_16x16x32_bf16 v[24:27], v[80:83], v[8:11], 0\n"
      "v_sub_f32_dpp v32, v28, v6 quad_perm:[0,0,0,0] row_mask:0xf bank_mask:0xf\n"
      "v_mov_b32_dpp v33, v30 quad_perm:[2,2,2,2] row_mask:0xf bank_mask:0xf\n"
      "v_mfma_f32_16x16x16_bf16 v[16:19], v[68:69], v[36:37], v[16:19]\n"
      "v_add_f32_dpp v32, v29, v32 quad_perm:[1,1,1,1] row_mask:0xf bank_mask:0xf\n"
      "v_add_f32_dpp v33, v31, v33 quad_perm:[3,3,3,3] row_mask:0xf bank_mask:0xf\n"
      "v_mfma_f32_16x16x16_bf16 v[20:23], v[70:71], v[36:37], v[20:23]\n"
      "v_add_f32 v32, v32, v33\n"
      "v_exp_f32 v34, v32\n"
      "v_max_f32 v35, v35, v32\n"
      "v_add_f32 v7, v7, v34\n"
      "v_cvt_pk_bf16_f32 v34, v34, v34\n"
      "v_and_b32 v38, v4, v34\n"
      "v_and_b32 v39, v5, v34\n"
      "s_waitcnt vmcnt(8)\n"
      "global_load_dwordx4 v[132:135], v0, s[42:43] nt\n"
      "s_add_u32 s42, s42, 110592\n"
      "s_addc_u32 s43, s43, 0\n"
      "v_mfma_f32_16x16x32_bf16 v[28:31], v[88:91], v[8:11], 0\n"
      "v_sub_f32_dpp v32, v24, v6 quad_perm:[0,0,0,0] row_mask:0xf bank_mask:0xf\n"
      "v_mov_b32_dpp v33, v26 quad_perm:[2,2,2,2] row_mask:0xf bank_mask:0xf\n"
      "v_mfma_f32_16x16x16_bf16 v[16:19], v[76:77], v[38:39], v[16:19]\n"
      "v_add_f32_dpp v32, v25, v32 quad_perm:[1,1,1,1] row_mask:0xf bank_mask:0xf\n"
      "v_add_f32_dpp v33, v27, v33 quad_perm:[3,3,3,3] row_mask:0xf bank_mask:0xf\n"
      "v_mfma_f32_16x16x16_bf16 v[20:23], v[78:79], v[38:39], v[20:23]\n"
      "v_add_f32 v32, v32, v33\n"
      "v_exp_f32 v34, v32\n"
      "v_max_f32 v35, v35, v32\n"
      "v_add_f32 v7, v7, v34\n"
      "v_cvt_pk_bf16_f32 v34, v34, v34\n"
      "v_and_b32 v36, v4, v34\n"
      "v_and_b32 v37, v5, v34\n"
      "v_mfma_f32_16x16x32_bf16 v[24:27], v[96:99], v[8:11], 0\n"
      "v_sub_f32_dpp v32, v28, v6 quad_perm:[0,0,0,0] row_mask:0xf bank_mask:0xf\n"
      "v_mov_b32_dpp v33, v30 quad_perm:[2,2,2,2] row_mask:0xf bank_mask:0xf\n"
      "v_mfma_f32_16x16x16_bf16 v[16:19], v[84:85], v[36:37], v[16:19]\n"
      "v_add_f32_dpp v32, v29, v32 quad_perm:[1,1,1,1] row_mask:0xf bank_mask:0xf\n"
      "v_add_f32_dpp v33, v31, v33 quad_perm:[3,3,3,3] row_mask:0xf bank_mask:0xf\n"
      "v_mfma_f32_16x16x16_bf16 v[20:23], v[86:87], v[36:37], v[20:23]\n"
      "v_add_f32 v32, v32, v33\n"
      "v_exp_f32 v34, v32\n"
      "v_max_f32 v35, v35, v32\n"
      "v_add_f32 v7, v7, v34\n"
      "v_cvt_pk_bf16_f32 v34, v34, v34\n"
      "v_and_b32 v38, v4, v34\n"
      "v_and_b32 v39, v5, v34\n"
      "s_waitcnt vmcnt(8)\n"
      "global_load_dwordx4 v[136:139], v1, s[40:41] nt\n"
      "s_add_u32 s40, s40, 110592\n"
      "s_addc_u32 s41, s41, 0\n"
      "v_sub_f32_dpp v32, v24, v6 quad_perm:[0,0,0,0] row_mask:0xf bank_mask:0xf\n"
      "v_mov_b32_dpp v33, v26 quad_perm:[2,2,2,2] row_mask:0xf bank_mask:0xf\n"
      "v_mfma_f32_16x16x16_bf16 v[16:19], v[92:93], v[38:39], v[16:19]\n"
      "v_add_f32_dpp v32, v25, v32 quad_perm:[1,1,1,1] row_mask:0xf bank_mask:0xf\n"
      "v_add_f32_dpp v33, v27, v33 quad_perm:[3,3,3,3] row_mask:0xf bank_mask:0xf\n"
      "v_mfma_f32_16x16x16_bf16 v[20:23], v[94:95], v[38:39], v[20:23]\n"
      "v_add_f32 v32, v32, v33\n"
      "v_add_f32 v32, s69, v32\n"
      "v_exp_f32 v34, v32\n"
      "v_max_f32 v35, v35, v32\n"
      "v_add_f32 v7, v7, v34\n"
      "v_cvt_pk_bf16_f32 v34, v34, v34\n"
      "v_and_b32 v36, v4, v34\n"
      "v_and_b32 v37, v5, v34\n"
      "s_nop 1\n"
      "v_mfma_f32_16x16x16_bf16 v[16:19], v[100:101], v[36:37], v[16:19]\n"
      "v_mfma_f32_16x16x16_bf16 v[20:23], v[102:103], v[36:37], v[20:23]\n"
      "s_waitcnt vmcnt(8)\n"
      "global_load_dwordx4 v[140:143], v0, s[42:43] nt\n"
      "s_add_u32 s42, s42, 110592\n"
      "s_addc_u32 s43, s43, 0\n"
      "v_cmp_lt_f32 vcc, 0x42800000, v35\n"
      "s_nop 0\n"
      "s_cbranch_vccnz L_fb10_%=\n"
      "L_ret10_%=:\n"
      "v_mov_b32 v49, v7\n"
      "v_lshlrev_b32 v41, 2, v0\n"
      "v_and_b32 v41, 0x300, v41\n"
      "v_permlane32_swap_b32 v49, v7\n"
      "v_lshrrev_b32 v42, 4, v0\n"
      "v_add_f32 v7, v49, v7\n"
      "v_and_b32 v42, 0x30, v42\n"
      "v_mov_b32 v49, v7\n"
      "v_add_u32 v41, v41, v42\n"
      "v_lshrrev_b32 v40, 2, v0\n"
      "v_permlane16_swap_b32 v49, v7\n"
      "v_and_b32 v42, 12, v40\n"
      "v_add_f32 v7, v49, v7\n"
      "v_add_u32 v41, v41, v42\n"
      "s_lshl_b32 s62, s60, 10\n"
      "s_add_u32 s62, s62, 155648\n"
      "v_add_u32 v41, s62, v41\n"
      "v_lshrrev_b32 v43, 3, v0\n"
      "v_and_b32 v43, 24, v43\n"
      "s_lshl_b32 s62, s60, 5\n"
      "s_add_u32 s62, s62, 159744\n"
      "v_add_u32 v43, s62, v43\n"
      "v_cvt_pk_bf16_f32 v36, v16, v17\n"
      "v_cvt_pk_bf16_f32 v37, v18, v19\n"
      "v_cvt_pk_bf16_f32 v38, v20, v21\n"
      "v_cvt_pk_bf16_f32 v39, v22, v23\n"
      "s_mov_b64 exec, 0x1111\n"
      "ds_write_b64 v43, v[6:7]\n"
      "s_mov_b64 exec, -1\n"
      "ds_write_b32 v41, v36 offset:0\n"
      "ds_write_b32 v41, v37 offset:64\n"
      "ds_write_b32 v41, v38 offset:128\n"
      "ds_write_b32 v41, v39 offset:192\n"
      "s_lshl_b32 s62, s60, 8\n"
      "s_add_u32 s62, s62, 155648\n"
      "v_add_u32 v42, s62, v40\n"
      "s_lshl_b32 s62, s60, 3\n"
      "s_add_u32 s62, s62, 159744\n"
      "v_mov_b32 v50, s62\n"
      "s_waitcnt lgkmcnt(0)\n"
      "s_barrier\n"
      "ds_read_b64 v[28:29], v50 offset:0\n"
      "ds_read_b64 v[30:31], v50 offset:32\n"
      "ds_read_b64 v[32:33], v50 offset:64\n"
      "ds_read_b64 v[34:35], v50 offset:96\n"
      "ds_read_b32 v24, v42 offset:0\n"
      "ds_read_b32 v25, v42 offset:1024\n"
      "ds_read_b32 v26, v42 offset:2048\n"
      "ds_read_b32 v27, v42 offset:3072\n"
      "s_add_u32 s56, s44, 1990656\n"
      "s_addc_u32 s57, s45, 0\n"
      "s_waitcnt lgkmcnt(4)\n"
      "v_max3_f32 v43, v28, v30, v32\n"
      "v_max_f32 v43, v43, v34\n"
      "v_sub_f32 v28, v28, v43\n"
      "v_sub_f32 v30, v30, v43\n"
      "v_sub_f32 v32, v32, v43\n"
      "v_sub_f32 v34, v34, v43\n"
      "v_exp_f32 v28, v28\n"
      "v_exp_f32 v30, v30\n"
      "v_exp_f32 v32, v32\n"
      "v_exp_f32 v34, v34\n"
      "s_waitcnt lgkmcnt(0)\n"
      "v_lshlrev_b32 v44, 16, v24\n"
      "v_and_b32 v45, 0xffff0000, v24\n"
      "v_lshlrev_b32 v46, 16, v25\n"
      "v_and_b32 v47, 0xffff0000, v25\n"
      "v_lshlrev_b32 v48, 16, v26\n"
      "v_and_b32 v49, 0xffff0000, v26\n"
      "v_lshlrev_b32 v50, 16, v27\n"
      "v_and_b32 v51, 0xffff0000, v27\n"
      "v_mul_f32 v38, v28, v29\n"
      "v_pk_mul_f32 v[36:37], v[44:45], v[28:29] op_sel_hi:[1,0]\n"
      "v_fmac_f32 v38, v30, v31\n"
      "v_pk_fma_f32 v[36:37], v[46:47], v[30:31], v[36:37] op_sel_hi:[1,0,1]\n"
      "v_fmac_f32 v38, v32, v33\n"
      "v_pk_fma_f32 v[36:37], v[48:49], v[32:33], v[36:37] op_sel_hi:[1,0,1]\n"
      "v_fmac_f32 v38, v34, v35\n"
      "v_pk_fma_f32 v[36:37], v[50:51], v[34:35], v[36:37] op_sel_hi:[1,0,1]\n"
      "v_rcp_f32 v39, v38\n"
      "v_log_f32 v38, v38\n"
      "v_mul_f32 v36, v39, v36\n"
      "v_mul_f32 v37, v39, v37\n"
      "v_add_f32 v38, v38, v43\n"
      "v_cvt_pk_bf16_f32 v36, v36, v37\n"
      "v_min_u32 v38, -2, v38\n"
      "v_min_u32 v36, -2, v36\n"
      "global_store_dword v40, v36, s[56:57] sc1\n"
      "s_mov_b32 exec_hi, 0\n"
      "global_store_dword v40, v38, s[56:57] offset:256 sc1\n"
      "s_mov_b64 exec, -1\n"
      "s_add_u32 s56, s44, 3649536\n"
      "s_addc_u32 s57, s45, 0\n"
      "global_store_dword v40, v12, s[56:57] sc1\n"
      "s_mov_b32 exec_hi, 0\n"
      "global_store_dword v40, v13, s[56:57] offset:256 sc1\n"
      "s_mov_b64 exec, -1\n"
      "s_waitcnt vmcnt(8)\n"
      "global_load_dwordx4 v[144:147], v1, s[40:41] nt\n"
      "s_add_u32 s40, s40, 110592\n"
      "s_addc_u32 s41, s41, 0\n"
      "s_waitcnt vmcnt(8)\n"
      "global_load_dwordx4 v[148:151], v0, s[42:43] nt\n"
      "s_add_u32 s42, s42, 110592\n"
      "s_addc_u32 s43, s43, 0\n"
      "s_waitcnt vmcnt(8)\n"
      "global_load_dwordx4 v[152:155], v1, s[40:41] nt\n"
      "s_add_u32 s40, s40, 110592\n"
      "s_addc_u32 s41, s41, 0\n"
      "s_waitcnt vmcnt(8)\n"
      "global_load_dwordx4 v[156:159], v0, s[42:43] nt\n"
      "s_add_u32 s42, s42, 110592\n"
      "s_addc_u32 s43, s43, 0\n"
      "s_barrier\n"
      "L_per7_%=:\n"
      "s_add_u32 s56, s87, L_per8_%=-L_begin_%=\n"
      "s_min_u32 s56, s56, L_end_%=-L_begin_%=-1024\n"
      "s_add_u32 s56, s84, s56\n"
      "s_addc_u32 s57, s85, 0\n"
      "s_mov_b32 m0, s86\n"
      "s_nop 0\n"
      "global_load_lds_dword v0, s[56:57]\n"
      "s_waitcnt vmcnt(9)\n"
      "global_load_dwordx4 v[160:163], v1, s[40:41] nt\n"
      "s_add_u32 s40, s40, 110592\n"
      "s_addc_u32 s41, s41, 0\n"
      "s_waitcnt vmcnt(9)\n"
      "global_load_dwordx4 v[164:167], v0, s[42:43] nt\n"
      "s_add_u32 s42, s42, 110592\n"
      "s_addc_u32 s43, s43, 0\n"
      "s_waitcnt vmcnt(9)\n"
      "global_load_dwordx4 v[168:171], v1, s[40:41] nt\n"
      "s_add_u32 s40, s40, 110592\n"
      "s_addc_u32 s41, s41, 0\n"
      "s_waitcnt vmcnt(9)\n"
      "global_load_dwordx4 v[172:175], v0, s[42:43] nt\n"
      "s_add_u32 s42, s42, 110592\n"
      "s_addc_u32 s43, s43, 0\n"
      "s_waitcnt vmcnt(9)\n"
      "global_load_dwordx4 v[176:179], v1, s[40:41] nt\n"
      "s_add_u32 s40, s40, 110592\n"
      "s_addc_u32 s41, s41, 0\n"
      "s_add_u32 s56, s48, 57344\n"
      "s_addc_u32 s57, s49, 0\n"
      "s_mov_b64 exec, 0xffff\n"
      "global_load_dwordx4 v[8:11], v0, s[56:57] sc1\n"
      "s_mov_b64 exec, -1\n"
      "s_add_u32 s56, s48, 98304\n"
      "s_addc_u32 s57, s49, 0\n"
      "s_mov_b64 exec, 0xffff\n"
      "global_load_dwordx4 v[12:15], v0, s[56:57] sc1\n"
      "s_mov_b64 exec, -1\n"
      "s_waitcnt vmcnt(11)\n"
      "global_load_dwordx4 v[180:183], v0, s[42:43] nt\n"
      "s_add_u32 s42, s42, 110592\n"
      "s_addc_u32 s43, s43, 0\n"
      "s_waitcnt vmcnt(11)\n"
      "global_load_dwordx4 v[184:187], v1, s[40:41] nt\n"
      "s_add_u32 s40, s40, 110592\n"
      "s_addc_u32 s41, s41, 0\n"
      "s_waitcnt vmcnt(11)\n"
      "global_load_dwordx4 v[188:191], v0, s[42:43] nt\n"
      "s_add_u32 s42, s42, 110592\n"
      "s_addc_u32 s43, s43, 0\n"
      "s_waitcnt vmcnt(11)\n"
      "global_load_dwordx4 v[192:195], v1, s[40:41] nt\n"
      "s_add_u32 s40, s40, 110592\n"
      "s_addc_u32 s41, s41, 0\n"
      "s_waitcnt vmcnt(10)\n"
      "global_load_dwordx4 v[196:199], v0, s[42:43] nt\n"
      "s_add_u32 s42, s42, 110592\n"
      "s_addc_u32 s43, s43, 0\n"
      "s_waitcnt vmcnt(10)\n"
      "global_load_dwordx4 v[200:203], v1, s[40:41] nt\n"
      "s_add_u32 s40, s40, 110592\n"
      "s_addc_u32 s41, s41, 0\n"
      "s_waitcnt vmcnt(10)\n"
      "global_load_dwordx4 v[204:207], v0, s[42:43] nt\n"
      "s_add_u32 s42, s42, 110592\n"
      "s_addc_u32 s43, s43, 0\n"
      "s_waitcnt vmcnt(10)\n"
      "global_load_dwordx4 v[208:211], v1, s[40:41] nt\n"
      "s_add_u32 s40, s40, 110592\n"
      "s_addc_u32 s41, s41, 0\n"
      "s_waitcnt vmcnt(10)\n"
      "global_load_dwordx4 v[212:215], v0, s[42:43] nt\n"
      "s_add_u32 s42, s42, 110592\n"
      "s_addc_u32 s43, s43, 0\n"
      "s_waitcnt vmcnt(9)\n"
      "s_mov_b64 exec, 0xffff\n"
      "s_mov_b32 s53, 0\n"
      "L_qc25_%=:\n"
      "v_max3_u32 v49, v12, v13, v14\n"
      "v_max_u32 v49, v49, v15\n"
      "v_cmp_eq_u32 vcc, -1, v49\n"
      "s_nop 0\n"
      "s_cbranch_vccz L_qo26_%=\n"
      "s_cmp_lg_u32 s54, 0\n"
      "s_cbranch_scc1 L_qo26_%=\n"
      "s_add_u32 s56, s48, 98304\n"
      "s_addc_u32 s57, s49, 0\n"
      "global_load_dwordx4 v[12:15], v0, s[56:57] sc1\n"
      "s_add_u32 s53, s53, 1\n"
      "s_waitcnt vmcnt(0)\n"
      "s_cmp_lt_u32 s53, 30000\n"
      "s_cbranch_scc1 L_qc25_%=\n"
      "s_mov_b32 s54, 204\n"
      "v_mov_b32 v49, 204\n"
      "v_mov_b32 v50, 256\n"
      "global_store_dword v50, v49, s[58:59] sc1\n"
      "L_qo26_%=:\n"
      "s_lshl_b32 s62, s60, 8\n"
      "s_add_u32 s62, s62, 160896\n"
      "v_add_u32 v49, s62, v0\n"
      "ds_write_b128 v49, v[12:15]\n"
      "s_mov_b64 exec, -1\n"
      "s_waitcnt lgkmcnt(0)\n"
      "s_barrier\n"
      "ds_read_b128 v[12:15], v2 offset:1024\n"
      "s_waitcnt lgkmcnt(0)\n"
      "ds_read_b128 v[40:43], v3 offset:0\n"
      "ds_read_b128 v[44:47], v3 offset:2048\n"
      "ds_read_b128 v[48:51], v3 offset:1024\n"
      "ds_read_b128 v[52:55], v3 offset:3072\n"
      "s_waitcnt lgkmcnt(3)\n"
      "v_mfma_f32_16x16x32_bf16 v[24:27], v[40:43], v[12:15], 0\n"
      "ds_read_b128 v[40:43], v3 offset:4096\n"
      "s_waitcnt lgkmcnt(3)\n"
      "v_mfma_f32_16x16x32_bf16 v[28:31], v[44:47], v[12:15], 0\n"
      "ds_read_b128 v[44:47], v3 offset:6144\n"
      "s_nop 3\n"
      "v_mov_b32_dpp v34, v24 quad_perm:[0,0,0,0] row_mask:0xf bank_mask:0xf\n"
      "v_mov_b32_dpp v33, v26 quad_perm:[2,2,2,2] row_mask:0xf bank_mask:0xf\n"
      "s_nop 0\n"
      "v_add_f32_dpp v34, v25, v34 quad_perm:[1,1,1,1] row_mask:0xf bank_mask:0xf\n"
      "v_add_f32_dpp v33, v27, v33 quad_perm:[3,3,3,3] row_mask:0xf bank_mask:0xf\n"
      "v_add_f32 v34, v34, v33\n"
      "v_mov_b32_dpp v32, v28 quad_perm:[0,0,0,0] row_mask:0xf bank_mask:0xf\n"
      "v_mov_b32_dpp v33, v30 quad_perm:[2,2,2,2] row_mask:0xf bank_mask:0xf\n"
      "s_nop 0\n"
      "v_add_f32_dpp v32, v29, v32 quad_perm:[1,1,1,1] row_mask:0xf bank_mask:0xf\n"
      "v_add_f32_dpp v33, v31, v33 quad_perm:[3,3,3,3] row_mask:0xf bank_mask:0xf\n"
      "v_add_f32 v32, v32, v33\n"
      "v_max_f32 v35, v34, v32\n"
      "v_mov_b32 v33, v35\n"
      "s_nop 1\n"
      "v_permlane32_swap_b32 v33, v35\n"
      "v_max_f32 v35, v33, v35\n"
      "v_mov_b32 v33, v35\n"
      "s_nop 1\n"
      "v_permlane16_swap_b32 v33, v35\n"
      "v_max_f32 v6, v33, v35\n"
      "v_sub_f32 v34, v34, v6\n"
      "v_sub_f32 v32, v32, v6\n"
      "v_mov_b32 v35, 0\n"
      "v_exp_f32 v34, v34\n"
      "s_nop 0\n"
      "v_mov_b32 v7, v34\n"
      "v_cvt_pk_bf16_f32 v34, v34, v34\n"
      "v_and_b32 v36, v4, v34\n"
      "v_and_b32 v37, v5, v34\n"
      "s_waitcnt lgkmcnt(1)\n"
      "v_mfma_f32_16x16x32_bf16 v[24:27], v[40:43], v[12:15], 0\n"
      "ds_read_b128 v[40:43], v3 offset:8192\n"
      "v_mfma_f32_16x16x16_bf16 v[16:19], v[48:49], v[36:37], 0\n"
      "v_mfma_f32_16x16x16_bf16 v[20:23], v[50:51], v[36:37], 0\n"
      "ds_read_b128 v[48:51], v3 offset:5120\n"
      "v_exp_f32 v34, v32\n"
      "v_max_f32 v35, v35, v32\n"
      "v_add_f32 v7, v7, v34\n"
      "v_cvt_pk_bf16_f32 v34, v34, v34\n"
      "v_and_b32 v38, v4, v34\n"
      "v_and_b32 v39, v5, v34\n"
      "s_waitcnt lgkmcnt(2)\n"
      "v_mfma_f32_16x16x32_bf16 v[28:31], v[44:47], v[12:15], 0\n"
      "ds_read_b128 v[44:47], v3 offset:10240\n"
      "v_sub_f32_dpp v32, v24, v6 quad_perm:[0,0,0,0] row_mask:0xf bank_mask:0xf\n"
      "v_mov_b32_dpp v33, v26 quad_perm:[2,2,2,2] row_mask:0xf bank_mask:0xf\n"
      "v_mfma_f32_16x16x16_bf16 v[16:19], v[52:53], v[38:39], v[16:19]\n"
      "v_add_f32_dpp v32, v25, v32 quad_perm:[1,1,1,1] row_mask:0xf bank_mask:0xf\n"
      "v_add_f32_dpp v33, v27, v33 quad_perm:[3,3,3,3] row_mask:0xf bank_mask:0xf\n"
      "v_mfma_f32_16x16x16_bf16 v[20:23], v[54:55], v[38:39], v[20:23]\n"
      "ds_read_b128 v[52:55], v3 offset:7168\n"
      "v_add_f32 v32, v32, v33\n"
      "v_exp_f32 v34, v32\n"
      "v_max_f32 v35, v35, v32\n"
      "v_add_f32 v7, v7, v34\n"
      "v_cvt_pk_bf16_f32 v34, v34, v34\n"
      "v_and_b32 v36, v4, v34\n"
      "v_and_b32 v37, v5, v34\n"
      "s_waitcnt lgkmcnt(3)\n"
      "v_mfma_f32_16x16x32_bf16 v[24:27], v[40:43], v[12:15], 0\n"
      "ds_read_b128 v[40:43], v3 offset:12288\n"
      "v_sub_f32_dpp v32, v28, v6 quad_perm:[0,0,0,0] row_mask:0xf bank_mask:0xf\n"
      "v_mov_b32_dpp v33, v30 quad_perm:[2,2,2,2] row_mask:0xf bank_mask:0xf\n"
      "s_waitcnt lgkmcnt(3)\n"
      "v_mfma_f32_16x16x16_bf16 v[16:19], v[48:49], v[36:37], v[16:19]\n"
      "v_add_f32_dpp v32, v29, v32 quad_perm:[1,1,1,1] row_mask:0xf bank_mask:0xf\n"
      "v_add_f32_dpp v33, v31, v33 quad_perm:[3,3,3,3] row_mask:0xf bank_mask:0xf\n"
      "v_mfma_f32_16x16x16_bf16 v[20:23], v[50:51], v[36:37], v[20:23]\n"
      "ds_read_b128 v[48:51], v3 offset:9216\n"
      "v_add_f32 v32, v32, v33\n"
      "v_exp_f32 v34, v32\n"
      "v_max_f32 v35, v35, v32\n"
      "v_add_f32 v7, v7, v34\n"
      "v_cvt_pk_bf16_f32 v34, v34, v34\n"
      "v_and_b32 v38, v4, v34\n"
      "v_and_b32 v39, v5, v34\n"
      "s_waitcnt vmcnt(8)\n"
      "global_load_dwordx4 v[216:219], v1, s[40:41] nt\n"
      "s_add_u32 s40, s40, 110592\n"
      "s_addc_u32 s41, s41, 0\n"
      "s_waitcnt lgkmcnt(3)\n"
      "v_mfma_f32_16x16x32_bf16 v[28:31], v[44:47], v[12:15], 0\n"
      "ds_read_b128 v[44:47], v3 offset:14336\n"
      "v_sub_f32_dpp v32, v24, v6 quad_perm:[0,0,0,0] row_mask:0xf bank_mask:0xf\n"
      "v_mov_b32_dpp v33, v26 quad_perm:[2,2,2,2] row_mask:0xf bank_mask:0xf\n"
      "s_waitcnt lgkmcnt(3)\n"
      "v_mfma_f32_16x16x16_bf16 v[16:19], v[52:53], v[38:39], v[16:19]\n"
      "v_add_f32_dpp v32, v25, v32 quad_perm:[1,1,1,1] row_mask:0xf bank_mask:0xf\n"
      "v_add_f32_dpp v33, v27, v33 quad_perm:[3,3,3,3] row_mask:0xf bank_mask:0xf\n"
      "v_mfma_f32_16x16x16_bf16 v[20:23], v[54:55], v[38:39], v[20:23]\n"
      "ds_read_b128 v[52:55], v3 offset:11264\n"
      "v_add_f32 v32, v32, v33\n"
      "v_exp_f32 v34, v32\n"
      "v_max_f32 v35, v35, v32\n"
      "v_add_f32 v7, v7, v34\n"
      "v_cvt_pk_bf16_f32 v34, v34, v34\n"
      "v_and_b32 v36, v4, v34\n"
      "v_and_b32 v37, v5, v34\n"
      "s_waitcnt lgkmcnt(3)\n"
      "v_mfma_f32_16x16x32_bf16 v[24:27], v[40:43], v[12:15], 0\n"
      "ds_read_b128 v[40:43], v3 offset:16384\n"
      "v_sub_f32_dpp v32, v28, v6 quad_perm:[0,0,0,0] row_mask:0xf bank_mask:0xf\n"
      "v_mov_b32_dpp v33, v30 quad_perm:[2,2,2,2] row_mask:0xf bank_mask:0xf\n"
      "s_waitcnt lgkmcnt(3)\n"
      "v_mfma_f32_16x16x16_bf16 v[16:19], v[48:49], v[36:37], v[16:19]\n"
      "v_add_f32_dpp v32, v29, v32 quad_perm:[1,1,1,1] row_mask:0xf bank_mask:0xf\n"
      "v_add_f32_dpp v33, v31, v33 quad_perm:[3,3,3,3] row_mask:0xf bank_mask:0xf\n"
      "v_mfma_f32_16x16x16_bf16 v[20:23], v[50:51], v[36:37], v[20:23]\n"
      "ds_read_b128 v[48:51], v3 offset:13312\n"
      "v_add_f32 v32, v32, v33\n"
      "v_exp_f32 v34, v32\n"
      "v_max_f32 v35, v35, v32\n"
      "v_add_f32 v7, v7, v34\n"
      "v_cvt_pk_bf16_f32 v34, v34, v34\n"
      "v_and_b32 v38, v4, v34\n"
      "v_and_b32 v39, v5, v34\n"
      "s_waitcnt vmcnt(8)\n"
      "global_load_dwordx4 v[220:223], v0, s[42:43] nt\n"
      "s_add_u32 s42, s42, 110592\n"
      "s_addc_u32 s43, s43, 0\n"
      "s_waitcnt lgkmcnt(3)\n"
      "v_mfma_f32_16x16x32_bf16 v[28:31], v[44:47], v[12:15], 0\n"
      "ds_read_b128 v[44:47], v3 offset:18432\n"
      "v_sub_f32_dpp v32, v24, v6 quad_perm:[0,0,0,0] row_mask:0xf bank_mask:0xf\n"
      "v_mov_b32_dpp v33, v26 quad_perm:[2,2,2,2] row_mask:0xf bank_mask:0xf\n"
      "s_waitcnt lgkmcnt(3)\n"
      "v_mfma_f32_16x16x16_bf16 v[16:19], v[52:53], v[38:39], v[16:19]\n"
      "v_add_f32_dpp v32, v25, v32 quad_perm:[1,1,1,1] row_mask:0xf bank_mask:0xf\n"
      "v_add_f32_dpp v33, v27, v33 quad_perm:[3,3,3,3] row_mask:0xf bank_mask:0xf\n"
      "v_mfma_f32_16x16x16_bf16 v[20:23], v[54:55], v[38:39], v[20:23]\n"
      "ds_read_b128 v[52:55], v3 offset:15360\n"
      "v_add_f32 v32, v32, v33\n"
      "v_exp_f32 v34, v32\n"
      "v_max_f32 v35, v35, v32\n"
      "v_add_f32 v7, v7, v34\n"
      "v_cvt_pk_bf16_f32 v34, v34, v34\n"
      "v_and_b32 v36, v4, v34\n"
      "v_and_b32 v37, v5, v34\n"
      "s_waitcnt lgkmcnt(3)\n"
      "v_mfma_f32_16x16x32_bf16 v[24:27], v[40:43], v[12:15], 0\n"
      "ds_read_b128 v[40:43], v3 offset:20480\n"
      "v_sub_f32_dpp v32, v28, v6 quad_perm:[0,0,0,0] row_mask:0xf bank_mask:0xf\n"
      "v_mov_b32_dpp v33, v30 quad_perm:[2,2,2,2] row_mask:0xf bank_mask:0xf\n"
      "s_waitcnt lgkmcnt(3)\n"
      "v_mfma_f32_16x16x16_bf16 v[16:19], v[48:49], v[36:37], v[16:19]\n"
      "v_add_f32_dpp v32, v29, v32 quad_perm:[1,1,1,1] row_mask:0xf bank_mask:0xf\n"
      "v_add_f32_dpp v33, v31, v33 quad_perm:[3,3,3,3] row_mask:0xf bank_mask:0xf\n"
      "v_mfma_f32_16x16x16_bf16 v[20:23], v[50:51], v[36:37], v[20:23]\n"
      "ds_read_b128 v[48:51], v3 offset:17408\n"
      "v_add_f32 v32, v32, v33\n"
      "v_exp_f32 v34, v32\n"
      "v_max_f32 v35, v35, v32\n"
      "v_add_f32 v7, v7, v34\n"
      "v_cvt_pk_bf16_f32 v34, v34, v34\n"
      "v_and_b32 v38, v4, v34\n"
      "v_and_b32 v39, v5, v34\n"
      "s_waitcnt lgkmcnt(3)\n"
      "v_mfma_f32_16x16x32_bf16 v[28:31], v[44:47], v[12:15], 0\n"
      "ds_read_b128 v[44:47], v3 offset:22528\n"
      "v_sub_f32_dpp v32, v24, v6 quad_perm:[0,0,0,0] row_mask:0xf bank_mask:0xf\n"
      "v_mov_b32_dpp v33, v26 quad_perm:[2,2,2,2] row_mask:0xf bank_mask:0xf\n"
      "s_waitcnt lgkmcnt(3)\n"
      "v_mfma_f32_16x16x16_bf16 v[16:19], v[52:53], v[38:39], v[16:19]\n"
      "v_add_f32_dpp v32, v25, v32 quad_perm:[1,1,1,1] row_mask:0xf bank_mask:0xf\n"
      "v_add_f32_dpp v33, v27, v33 quad_perm:[3,3,3,3] row_mask:0xf bank_mask:0xf\n"
      "v_mfma_f32_16x16x16_bf16 v[20:23], v[54:55], v[38:39], v[20:23]\n"
      "ds_read_b128 v[52:55], v3 offset:19456\n"
      "v_add_f32 v32, v32, v33\n"
      "v_exp_f32 v34, v32\n"
      "v_max_f32 v35, v35, v32\n"
      "v_add_f32 v7, v7, v34\n"
      "v_cvt_pk_bf16_f32 v34, v34, v34\n"
      "v_and_b32 v36, v4, v34\n"
      "v_and_b32 v37, v5, v34\n"
      "s_waitcnt vmcnt(8)\n"
      "global_load_dwordx4 v[224:227], v1, s[40:41] nt\n"
      "s_add_u32 s40, s40, 110592\n"
      "s_addc_u32 s41, s41, 0\n"
      "s_waitcnt lgkmcnt(3)\n"
      "v_mfma_f32_16x16x32_bf16 v[24:27], v[40:43], v[12:15], 0\n"
      "ds_read_b128 v[40:43], v3 offset:24576\n"
      "v_sub_f32_dpp v32, v28, v6 quad_perm:[0,0,0,0] row_mask:0xf bank_mask:0xf\n"
      "v_mov_b32_dpp v33, v30 quad_perm:[2,2,2,2] row_mask:0xf bank_mask:0xf\n"
      "s_waitcnt lgkmcnt(3)\n"
      "v_mfma_f32_16x16x16_bf16 v[16:19], v[48:49], v[36:37], v[16:19]\n"
      "v_add_f32_dpp v32, v29, v32 quad_perm:[1,1,1,1] row_mask:0xf bank_mask:0xf\n"
      "v_add_f32_dpp v33, v31, v33 quad_perm:[3,3,3,3] row_mask:0xf bank_mask:0xf\n"
      "v_mfma_f32_16x16x16_bf16 v[20:23], v[50:51], v[36:37], v[20:23]\n"
      "ds_read_b128 v[48:51], v3 offset:21504\n"
      "v_add_f32 v32, v32, v33\n"
      "v_exp_f32 v34, v32\n"
      "v_max_f32 v35, v35, v32\n"
      "v_add_f32 v7, v7, v34\n"
      "v_cvt_pk_bf16_f32 v34, v34, v34\n"
      "v_and_b32 v38, v4, v34\n"
      "v_and_b32 v39, v5, v34\n"
      "s_waitcnt lgkmcnt(3)\n"
      "v_mfma_f32_16x16x32_bf16 v[28:31], v[44:47], v[12:15], 0\n"
      "ds_read_b128 v[44:47], v3 offset:26624\n"
      "v_sub_f32_dpp v32, v24, v6 quad_perm:[0,0,0,0] row_mask:0xf bank_mask:0xf\n"
      "v_mov_b32_dpp v33, v26 quad_perm:[2,2,2,2] row_mask:0xf bank_mask:0xf\n"
      "s_waitcnt lgkmcnt(3)\n"
      "v_mfma_f32_16x16x16_bf16 v[16:19], v[52:53], v[38:39], v[16:19]\n"
      "v_add_f32_dpp v32, v25, v32 quad_perm:[1,1,1,1] row_mask:0xf bank_mask:0xf\n"
      "v_add_f32_dpp v33, v27, v33 quad_perm:[3,3,3,3] row_mask:0xf bank_mask:0xf\n"
      "v_mfma_f32_16x16x16_bf16 v[20:23], v[54:55], v[38:39], v[20:23]\n"
      "ds_read_b128 v[52:55], v3 offset:23552\n"
      "v_add_f32 v32, v32, v33\n"
      "v_exp_f32 v34, v32\n"
      "v_max_f32 v35, v35, v32\n"
      "v_add_f32 v7, v7, v34\n"
      "v_cvt_pk_bf16_f32 v34, v34, v34\n"
      "v_and_b32 v36, v4, v34\n"
      "v_and_b32 v37, v5, v34\n"
      "s_waitcnt vmcnt(8)\n"
      "global_load_dwordx4 v[228:231], v0, s[42:43] nt\n"
      "s_add_u32 s42, s42, 110592\n"
      "s_addc_u32 s43, s43, 0\n"
      "s_waitcnt lgkmcnt(3)\n"
      "v_mfma_f32_16x16x32_bf16 v[24:27], v[40:43], v[12:15], 0\n"
      "ds_read_b128 v[40:43], v3 offset:28672\n"
      "v_sub_f32_dpp v32, v28, v6 quad_perm:[0,0,0,0] row_mask:0xf bank_mask:0xf\n"
      "v_mov_b32_dpp v33, v30 quad_perm:[2,2,2,2] row_mask:0xf bank_mask:0xf\n"
      "s_waitcnt lgkmcnt(3)\n"
      "v_mfma_f32_16x16x16_bf16 v[16:19], v[48:49], v[36:37], v[16:19]\n"
      "v_add_f32_dpp v32, v29, v32 quad_perm:[1,1,1,1] row_mask:0xf bank_mask:0xf\n"
      "v_add_f32_dpp v33, v31, v33 quad_perm:[3,3,3,3] row_mask:0xf bank_mask:0xf\n"
      "v_mfma_f32_16x16x16_bf16 v[20:23], v[50:51], v[36:37], v[20:23]\n"
      "ds_read_b128 v[48:51], v3 offset:25600\n"
      "v_add_f32 v32, v32, v33\n"
      "v_exp_f32 v34, v32\n"
      "v_max_f32 v35, v35, v32\n"
      "v_add_f32 v7, v7, v34\n"
      "v_cvt_pk_bf16_f32 v34, v34, v34\n"
      "v_and_b32 v38, v4, v34\n"
      "v_and_b32 v39, v5, v34\n"
      "s_waitcnt lgkmcnt(3)\n"
      "v_mfma_f32_16x16x32_bf16 v[28:31], v[44:47], v[12:15], 0\n"
      "ds_read_b128 v[44:47], v3 offset:30720\n"
      "v_sub_f32_dpp v32, v24, v6 quad_perm:[0,0,0,0] row_mask:0xf bank_mask:0xf\n"
      "v_mov_b32_dpp v33, v26 quad_perm:[2,2,2,2] row_mask:0xf bank_mask:0xf\n"
      "s_waitcnt lgkmcnt(3)\n"
      "v_mfma_f32_16x16x16_bf16 v[16:19], v[52:53], v[38:39], v[16:19]\n"
      "v_add_f32_dpp v32, v25, v32 quad_perm:[1,1,1,1] row_mask:0xf bank_mask:0xf\n"
      "v_add_f32_dpp v33, v27, v33 quad_perm:[3,3,3,3] row_mask:0xf bank_mask:0xf\n"
      "v_mfma_f32_16x16x16_bf16 v[20:23], v[54:55], v[38:39], v[20:23]\n"
      "ds_read_b128 v[52:55], v3 offset:27648\n"
      "v_add_f32 v32, v32, v33\n"
      "v_exp_f32 v34, v32\n"
      "v_max_f32 v35, v35, v32\n"
      "v_add_f32 v7, v7, v34\n"
      "v_cvt_pk_bf16_f32 v34, v34, v34\n"
      "v_and_b32 v36, v4, v34\n"
      "v_and_b32 v37, v5, v34\n"
      "s_waitcnt vmcnt(8)\n"
      "global_load_dwordx4 v[232:235], v1, s[40:41] nt\n"
      "s_add_u32 s40, s40, 110592\n"
      "s_addc_u32 s41, s41, 0\n"
      "s_waitcnt lgkmcnt(3)\n"
      "v_mfma_f32_16x16x32_bf16 v[24:27], v[40:43], v[12:15], 0\n"
      "ds_read_b128 v[40:43], v3 offset:32768\n"
      "v_sub_f32_dpp v32, v28, v6 quad_perm:[0,0,0,0] row_mask:0xf bank_mask:0xf\n"
      "v_mov_b32_dpp v33, v30 quad_perm:[2,2,2,2] row_mask:0xf bank_mask:0xf\n"
      "s_waitcnt lgkmcnt(3)\n"
      "v_mfma_f32_16x16x16_bf16 v[16:19], v[48:49], v[36:37], v[16:19]\n"
      "v_add_f32_dpp v32, v29, v32 quad_perm:[1,1,1,1] row_mask:0xf bank_mask:0xf\n"
      "v_add_f32_dpp v33, v31, v33 quad_perm:[3,3,3,3] row_mask:0xf bank_mask:0xf\n"
      "v_mfma_f32_16x16x16_bf16 v[20:23], v[50:51], v[36:37], v[20:23]\n"
      "ds_read_b128 v[48:51], v3 offset:29696\n"
      "v_add_f32 v32, v32, v33\n"
      "v_exp_f32 v34, v32\n"
      "v_max_f32 v35, v35, v32\n"
      "v_add_f32 v7, v7, v34\n"
      "v_cvt_pk_bf16_f32 v34, v34, v34\n"
      "v_and_b32 v38, v4, v34\n"
      "v_and_b32 v39, v5, v34\n"
      "s_waitcnt lgkmcnt(3)\n"
      "v_mfma_f32_16x16x32_bf16 v[28:31], v[44:47], v[12:15], 0\n"
      "ds_read_b128 v[44:47], v3 offset:34816\n"
      "v_sub_f32_dpp v32, v24, v6 quad_perm:[0,0,0,0] row_mask:0xf bank_mask:0xf\n"
      "v_mov_b32_dpp v33, v26 quad_perm:[2,2,2,2] row_mask:0xf bank_mask:0xf\n"
      "s_waitcnt lgkmcnt(3)\n"
      "v_mfma_f32_16x16x16_bf16 v[16:19], v[52:53], v[38:39], v[16:19]\n"
      "v_add_f32_dpp v32, v25, v32 quad_perm:[1,1,1,1] row_mask:0xf bank_mask:0xf\n"
      "v_add_f32_dpp v33, v27, v33 quad_perm:[3,3,3,3] row_mask:0xf bank_mask:0xf\n"
      "v_mfma_f32_16x16x16_bf16 v[20:23], v[54:55], v[38:39], v[20:23]\n"
      "ds_read_b128 v[52:55], v3 offset:31744\n"
      "v_add_f32 v32, v32, v33\n"
      "v_exp_f32 v34, v32\n"
      "v_max_f32 v35, v35, v32\n"
      "v_add_f32 v7, v7, v34\n"
      "v_cvt_pk_bf16_f32 v34, v34, v34\n"
      "v_and_b32 v36, v4, v34\n"
      "v_and_b32 v37, v5, v34\n"
      "s_waitcnt lgkmcnt(3)\n"
      "v_mfma_f32_16x16x32_bf16 v[24:27], v[40:43], v[12:15], 0\n"
      "ds_read_b128 v[40:43], v3 offset:36864\n"
      "v_sub_f32_dpp v32, v28, v6 quad_perm:[0,0,0,0] row_mask:0xf bank_mask:0xf\n"
      "v_mov_b32_dpp v33, v30 quad_perm:[2,2,2,2] row_mask:0xf bank_mask:0xf\n"
      "s_waitcnt lgkmcnt(3)\n"
      "v_mfma_f32_16x16x16_bf16 v[16:19], v[48:49], v[36:37], v[16:19]\n"
      "v_add_f32_dpp v32, v29, v32 quad_perm:[1,1,1,1] row_mask:0xf bank_mask:0xf\n"
      "v_add_f32_dpp v33, v31, v33 quad_perm:[3,3,3,3] row_mask:0xf bank_mask:0xf\n"
      "v_mfma_f32_16x16x16_bf16 v[20:23], v[50:51], v[36:37], v[20:23]\n"
      "ds_read_b128 v[48:51], v3 offset:33792\n"
      "v_add_f32 v32, v32, v33\n"
      "v_exp_f32 v34, v32\n"
      "v_max_f32 v35, v35, v32\n"
      "v_add_f32 v7, v7, v34\n"
      "v_cvt_pk_bf16_f32 v34, v34, v34\n"
      "v_and_b32 v38, v4, v34\n"
      "v_and_b32 v39, v5, v34\n"
      "s_waitcnt vmcnt(8)\n"
      "global_load_dwordx4 v[236:239], v0, s[42:43] nt\n"
      "s_add_u32 s42, s42, 110592\n"
      "s_addc_u32 s43, s43, 0\n"
      "s_waitcnt lgkmcnt(3)\n"
      "v_mfma_f32_16x16x32_bf16 v[28:31], v[44:47], v[12:15], 0\n"
      "v_sub_f32_dpp v32, v24, v6 quad_perm:[0,0,0,0] row_mask:0xf bank_mask:0xf\n"
      "v_mov_b32_dpp v33, v26 quad_perm:[2,2,2,2] row_mask:0xf bank_mask:0xf\n"
      "s_waitcnt lgkmcnt(2)\n"
      "v_mfma_f32_16x16x16_bf16 v[16:19], v[52:53], v[38:39], v[16:19]\n"
      "v_add_f32_dpp v32, v25, v32 quad_perm:[1,1,1,1] row_mask:0xf bank_mask:0xf\n"
      "v_add_f32_dpp v33, v27, v33 quad_perm:[3,3,3,3] row_mask:0xf bank_mask:0xf\n"
      "v_mfma_f32_16x16x16_bf16 v[20:23], v[54:55], v[38:39], v[20:23]\n"
      "ds_read_b128 v[52:55], v3 offset:35840\n"
      "v_add_f32 v32, v32, v33\n"
      "v_exp_f32 v34, v32\n"
      "v_max_f32 v35, v35, v32\n"
      "v_add_f32 v7, v7, v34\n"
      "v_cvt_pk_bf16_f32 v34, v34, v34\n"
      "v_and_b32 v36, v4, v34\n"
      "v_and_b32 v37, v5, v34\n"
      "s_waitcnt lgkmcnt(2)\n"
      "v_mfma_f32_16x16x32_bf16 v[24:27], v[40:43], v[12:15], 0\n"
      "v_sub_f32_dpp v32, v28, v6 quad_perm:[0,0,0,0] row_mask:0xf bank_mask:0xf\n"
      "v_mov_b32_dpp v33, v30 quad_perm:[2,2,2,2] row_mask:0xf bank_mask:0xf\n"
      "s_waitcnt lgkmcnt(1)\n"
      "v_mfma_f32_16x16x16_bf16 v[16:19], v[48:49], v[36:37], v[16:19]\n"
      "v_add_f32_dpp v32, v29, v32 quad_perm:[1,1,1,1] row_mask:0xf bank_mask:0xf\n"
      "v_add_f32_dpp v33, v31, v33 quad_perm:[3,3,3,3] row_mask:0xf bank_mask:0xf\n"
      "v_mfma_f32_16x16x16_bf16 v[20:23], v[50:51], v[36:37], v[20:23]\n"
      "ds_read_b128 v[48:51], v3 offset:37888\n"
      "v_add_f32 v32, v32, v33\n"
      "v_exp_f32 v34, v32\n"
      "v_max_f32 v35, v35, v32\n"
      "v_add_f32 v7, v7, v34\n"
      "v_cvt_pk_bf16_f32 v34, v34, v34\n"
      "v_and_b32 v38, v4, v34\n"
      "v_and_b32 v39, v5, v34\n"
      "s_waitcnt vmcnt(8)\n"
      "global_load_dwordx4 v[240:243], v1, s[40:41] nt\n"
      "s_add_u32 s40, s40, s51\n"
      "s_addc_u32 s41, s41, 0\n"
      "v_sub_f32_dpp v32, v24, v6 quad_perm:[0,0,0,0] row_mask:0xf bank_mask:0xf\n"
      "v_mov_b32_dpp v33, v26 quad_perm:[2,2,2,2] row_mask:0xf bank_mask:0xf\n"
      "s_waitcnt lgkmcnt(1)\n"
      "v_mfma_f32_16x16x16_bf16 v[16:19], v[52:53], v[38:39], v[16:19]\n"
      "v_add_f32_dpp v32, v25, v32 quad_perm:[1,1,1,1] row_mask:0xf bank_mask:0xf\n"
      "v_add_f32_dpp v33, v27, v33 quad_perm:[3,3,3,3] row_mask:0xf bank_mask:0xf\n"
      "v_mfma_f32_16x16x16_bf16 v[20:23], v[54:55], v[38:39], v[20:23]\n"
      "v_add_f32 v32, v32, v33\n"
      "v_add_f32 v32, s69, v32\n"
      "v_exp_f32 v34, v32\n"
      "v_max_f32 v35, v35, v32\n"
      "v_add_f32 v7, v7, v34\n"
      "v_cvt_pk_bf16_f32 v34, v34, v34\n"
      "v_and_b32 v36, v4, v34\n"
      "v_and_b32 v37, v5, v34\n"
      "s_waitcnt lgkmcnt(0)\n"
      "s_nop 0\n"
      "v_mfma_f32_16x16x16_bf16 v[16:19], v[48:49], v[36:37], v[16:19]\n"
      "v_mfma_f32_16x16x16_bf16 v[20:23], v[50:51], v[36:37], v[20:23]\n"
      "s_waitcnt vmcnt(8)\n"
      "global_load_dwordx4 v[244:247], v0, s[42:43] nt\n"
      "s_add_u32 s42, s42, s51\n"
      "s_addc_u32 s43, s43, 0\n"
      "v_cmp_lt_f32 vcc, 0x42800000, v35\n"
      "s_nop 0\n"
      "s_cbranch_vccnz L_fb11_%=\n"
      "L_ret11_%=:\n"
      "v_mov_b32 v49, v7\n"
      "v_lshlrev_b32 v41, 2, v0\n"
      "v_and_b32 v41, 0x300, v41\n"
      "v_permlane32_swap_b32 v49, v7\n"
      "v_lshrrev_b32 v42, 4, v0\n"
      "v_add_f32 v7, v49, v7\n"
      "v_and_b32 v42, 0x30, v42\n"
      "v_mov_b32 v49, v7\n"
      "v_add_u32 v41, v41, v42\n"
      "v_lshrrev_b32 v40, 2, v0\n"
      "v_permlane16_swap_b32 v49, v7\n"
      "v_and_b32 v42, 12, v40\n"
      "v_add_f32 v7, v49, v7\n"
      "v_add_u32 v41, v41, v42\n"
      "s_lshl_b32 s62, s60, 10\n"
      "s_add_u32 s62, s62, 155648\n"
      "v_add_u32 v41, s62, v41\n"
      "v_lshrrev_b32 v43, 3, v0\n"
      "v_and_b32 v43, 24, v43\n"
      "s_lshl_b32 s62, s60, 5\n"
      "s_add_u32 s62, s62, 159744\n"
      "v_add_u32 v43, s62, v43\n"
      "v_cvt_pk_bf16_f32 v36, v16, v17\n"
      "v_cvt_pk_bf16_f32 v37, v18, v19\n"
      "v_cvt_pk_bf16_f32 v38, v20, v21\n"
      "v_cvt_pk_bf16_f32 v39, v22, v23\n"
      "s_mov_b64 exec, 0x1111\n"
      "ds_write_b64 v43, v[6:7]\n"
      "s_mov_b64 exec, -1\n"
      "ds_write_b32 v41, v36 offset:0\n"
      "ds_write_b32 v41, v37 offset:64\n"
      "ds_write_b32 v41, v38 offset:128\n"
      "ds_write_b32 v41, v39 offset:192\n"
      "s_lshl_b32 s62, s60, 8\n"
      "s_add_u32 s62, s62, 155648\n"
      "v_add_u32 v42, s62, v40\n"
      "s_lshl_b32 s62, s60, 3\n"
      "s_add_u32 s62, s62, 159744\n"
      "v_mov_b32 v50, s62\n"
      "s_waitcnt lgkmcnt(0)\n"
      "s_barrier\n"
      "ds_read_b64 v[28:29], v50 offset:0\n"
      "ds_read_b64 v[30:31], v50 offset:32\n"
      "ds_read_b64 v[32:33], v50 offset:64\n"
      "ds_read_b64 v[34:35], v50 offset:96\n"
      "ds_read_b32 v24, v42 offset:0\n"
      "ds_read_b32 v25, v42 offset:1024\n"
      "ds_read_b32 v26, v42 offset:2048\n"
      "ds_read_b32 v27, v42 offset:3072\n"
      "s_waitcnt lgkmcnt(4)\n"
      "v_max3_f32 v43, v28, v30, v32\n"
      "v_max_f32 v43, v43, v34\n"
      "v_sub_f32 v28, v28, v43\n"
      "v_sub_f32 v30, v30, v43\n"
      "v_sub_f32 v32, v32, v43\n"
      "v_sub_f32 v34, v34, v43\n"
      "v_exp_f32 v28, v28\n"
      "v_exp_f32 v30, v30\n"
      "v_exp_f32 v32, v32\n"
      "v_exp_f32 v34, v34\n"
      "s_waitcnt lgkmcnt(0)\n"
      "v_lshlrev_b32 v44, 16, v24\n"
      "v_and_b32 v45, 0xffff0000, v24\n"
      "v_lshlrev_b32 v46, 16, v25\n"
      "v_and_b32 v47, 0xffff0000, v25\n"
      "v_lshlrev_b32 v48, 16, v26\n"
      "v_and_b32 v49, 0xffff0000, v26\n"
      "v_lshlrev_b32 v50, 16, v27\n"
      "v_and_b32 v51, 0xffff0000, v27\n"
      "v_mul_f32 v38, v28, v29\n"
      "v_pk_mul_f32 v[36:37], v[44:45], v[28:29] op_sel_hi:[1,0]\n"
      "v_fmac_f32 v38, v30, v31\n"
      "v_pk_fma_f32 v[36:37], v[46:47], v[30:31], v[36:37] op_sel_hi:[1,0,1]\n"
      "v_fmac_f32 v38, v32, v33\n"
      "v_pk_fma_f32 v[36:37], v[48:49], v[32:33], v[36:37] op_sel_hi:[1,0,1]\n"
      "v_fmac_f32 v38, v34, v35\n"
      "v_pk_fma_f32 v[36:37], v[50:51], v[34:35], v[36:37] op_sel_hi:[1,0,1]\n"
      "v_rcp_f32 v39, v38\n"
      "v_log_f32 v38, v38\n"
      "v_mul_f32 v36, v39, v36\n"
      "v_mul_f32 v37, v39, v37\n"
      "v_add_f32 v38, v38, v43\n"
      "v_cvt_pk_bf16_f32 v36, v36, v37\n"
      "v_min_u32 v38, -2, v38\n"
      "v_min_u32 v36, -2, v36\n"
      "v_mov_b32 v12, v36\n"
      "v_mov_b32 v13, v38\n"
      "s_waitcnt vmcnt(8)\n"
      "global_load_dwordx4 v[248:251], v1, s[40:41] nt\n"
      "s_waitcnt vmcnt(8)\n"
      "global_load_dwordx4 v[252:255], v0, s[42:43] nt\n"
      "s_barrier\n"
      "s_mov_b64 exec, 0xffff\n"
      "s_mov_b32 s53, 0\n"
      "L_qc27_%=:\n"
      "v_max3_u32 v49, v8, v9, v10\n"
      "v_max_u32 v49, v49, v11\n"
      "v_cmp_eq_u32 vcc, -1, v49\n"
      "s_nop 0\n"
      "s_cbranch_vccz L_qo28_%=\n"
      "s_cmp_lg_u32 s54, 0\n"
      "s_cbranch_scc1 L_qo28_%=\n"
      "s_add_u32 s56, s48, 57344\n"
      "s_addc_u32 s57, s49, 0\n"
      "global_load_dwordx4 v[8:11], v0, s[56:57] sc1\n"
      "s_add_u32 s53, s53, 1\n"
      "s_waitcnt vmcnt(0)\n"
      "s_cmp_lt_u32 s53, 30000\n"
      "s_cbranch_scc1 L_qc27_%=\n"
      "s_mov_b32 s54, 107\n"
      "v_mov_b32 v49, 107\n"
      "v_mov_b32 v50, 256\n"
      "global_store_dword v50, v49, s[58:59] sc1\n"
      "L_qo28_%=:\n"
      "s_lshl_b32 s62, s60, 8\n"
      "s_add_u32 s62, s62, 159872\n"
      "v_add_u32 v49, s62, v0\n"
      "ds_write_b128 v49, v[8:11]\n"
      "s_mov_b64 exec, -1\n"
      "s_waitcnt lgkmcnt(0)\n"
      "s_barrier\n"
      "ds_read_b128 v[8:11], v2 offset:0\n"
      "s_waitcnt lgkmcnt(0)\n"
      "s_add_u32 s56, s87, L_tail1_%=-L_begin_%=\n"
      "s_min_u32 s56, s56, L_end_%=-L_begin_%=-1024\n"
      "s_add_u32 s56, s84, s56\n"
      "s_addc_u32 s57, s85, 0\n"
      "s_mov_b32 m0, s86\n"
      "s_nop 0\n"
      "global_load_lds_dword v0, s[56:57]\n"
      "v_mfma_f32_16x16x32_bf16 v[24:27], v[104:107], v[8:11], 0\n"
      "v_mfma_f32_16x16x32_bf16 v[28:31], v[112:115], v[8:11], 0\n"
      "s_nop 6\n"
      "v_mov_b32_dpp v34, v24 quad_perm:[0,0,0,0] row_mask:0xf bank_mask:0xf\n"
      "v_mov_b32_dpp v33, v26 quad_perm:[2,2,2,2] row_mask:0xf bank_mask:0xf\n"
      "s_nop 0\n"
      "v_add_f32_dpp v34, v25, v34 quad_perm:[1,1,1,1] row_mask:0xf bank_mask:0xf\n"
      "v_add_f32_dpp v33, v27, v33 quad_perm:[3,3,3,3] row_mask:0xf bank_mask:0xf\n"
      "v_add_f32 v34, v34, v33\n"
      "v_mov_b32_dpp v32, v28 quad_perm:[0,0,0,0] row_mask:0xf bank_mask:0xf\n"
      "v_mov_b32_dpp v33, v30 quad_perm:[2,2,2,2] row_mask:0xf bank_mask:0xf\n"
      "s_nop 0\n"
      "v_add_f32_dpp v32, v29, v32 quad_perm:[1,1,1,1] row_mask:0xf bank_mask:0xf\n"
      "v_add_f32_dpp v33, v31, v33 quad_perm:[3,3,3,3] row_mask:0xf bank_mask:0xf\n"
      "v_add_f32 v32, v32, v33\n"
      "v_max_f32 v35, v34, v32\n"
      "v_mov_b32 v33, v35\n"
      "s_nop 1\n"
      "v_permlane32_swap_b32 v33, v35\n"
      "v_max_f32 v35, v33, v35\n"
      "v_mov_b32 v33, v35\n"
      "s_nop 1\n"
      "v_permlane16_swap_b32 v33, v35\n"
      "v_max_f32 v6, v33, v35\n"
      "v_sub_f32 v34, v34, v6\n"
      "v_sub_f32 v32, v32, v6\n"
      "v_mov_b32 v35, 0\n"
      "v_exp_f32 v34, v34\n"
      "s_nop 0\n"
      "v_mov_b32 v7, v34\n"
      "v_cvt_pk_bf16_f32 v34, v34, v34\n"
      "v_and_b32 v36, v4, v34\n"
      "v_and_b32 v37, v5, v34\n"
      "v_mfma_f32_16x16x32_bf16 v[24:27], v[120:123], v[8:11], 0\n"
      "s_nop 0\n"
      "v_mfma_f32_16x16x16_bf16 v[16:19], v[108:109], v[36:37], 0\n"
      "v_mfma_f32_16x16x16_bf16 v[20:23], v[110:111], v[36:37], 0\n"
      "v_exp_f32 v34, v32\n"
      "v_max_f32 v35, v35, v32\n"
      "v_add_f32 v7, v7, v34\n"
      "v_cvt_pk_bf16_f32 v34, v34, v34\n"
      "v_and_b32 v38, v4, v34\n"
      "v_and_b32 v39, v5, v34\n"
      "v_mfma_f32_16x16x32_bf16 v[28:31], v[128:131], v[8:11], 0\n"
      "v_sub_f32_dpp v32, v24, v6 quad_perm:[0,0,0,0] row_mask:0xf bank_mask:0xf\n"
      "v_mov_b32_dpp v33, v26 quad_perm:[2,2,2,2] row_mask:0xf bank_mask:0xf\n"
      "v_mfma_f32_16x16x16_bf16 v[16:19], v[116:117], v[38:39], v[16:19]\n"
      "v_add_f32_dpp v32, v25, v32 quad_perm:[1,1,1,1] row_mask:0xf bank_mask:0xf\n"
      "v_add_f32_dpp v33, v27, v33 quad_perm:[3,3,3,3] row_mask:0xf bank_mask:0xf\n"
      "v_mfma_f32_16x16x16_bf16 v[20:23], v[118:119], v[38:39], v[20:23]\n"
      "v_add_f32 v32, v32, v33\n"
      "v_exp_f32 v34, v32\n"
      "v_max_f32 v35, v35, v32\n"
      "v_add_f32 v7, v7, v34\n"
      "v_cvt_pk_bf16_f32 v34, v34, v34\n"
      "v_and_b32 v36, v4, v34\n"
      "v_and_b32 v37, v5, v34\n"
      "v_mfma_f32_16x16x32_bf16 v[24:27], v[136:139], v[8:11], 0\n"
      "v_sub_f32_dpp v32, v28, v6 quad_perm:[0,0,0,0] row_mask:0xf bank_mask:0xf\n"
      "v_mov_b32_dpp v33, v30 quad_perm:[2,2,2,2] row_mask:0xf bank_mask:0xf\n"
      "v_mfma_f32_16x16x16_bf16 v[16:19], v[124:125], v[36:37], v[16:19]\n"
      "v_add_f32_dpp v32, v29, v32 quad_perm:[1,1,1,1] row_mask:0xf bank_mask:0xf\n"
      "v_add_f32_dpp v33, v31, v33 quad_perm:[3,3,3,3] row_mask:0xf bank_mask:0xf\n"
      "v_mfma_f32_16x16x16_bf16 v[20:23], v[126:127], v[36:37], v[20:23]\n"
      "v_add_f32 v32, v32, v33\n"
      "v_exp_f32 v34, v32\n"
      "v_max_f32 v35, v35, v32\n"
      "v_add_f32 v7, v7, v34\n"
      "v_cvt_pk_bf16_f32 v34, v34, v34\n"
      "v_and_b32 v38, v4, v34\n"
      "v_and_b32 v39, v5, v34\n"
      "v_mfma_f32_16x16x32_bf16 v[28:31], v[144:147], v[8:11], 0\n"
      "v_sub_f32_dpp v32, v24, v6 quad_perm:[0,0,0,0] row_mask:0xf bank_mask:0xf\n"
      "v_mov_b32_dpp v33, v26 quad_perm:[2,2,2,2] row_mask:0xf bank_mask:0xf\n"
      "v_mfma_f32_16x16x16_bf16 v[16:19], v[132:133], v[38:39], v[16:19]\n"
      "v_add_f32_dpp v32, v25, v32 quad_perm:[1,1,1,1] row_mask:0xf bank_mask:0xf\n"
      "v_add_f32_dpp v33, v27, v33 quad_perm:[3,3,3,3] row_mask:0xf bank_mask:0xf\n"
      "v_mfma_f32_16x16x16_bf16 v[20:23], v[134:135], v[38:39], v[20:23]\n"
      "v_add_f32 v32, v32, v33\n"
      "v_exp_f32 v34, v32\n"
      "v_max_f32 v35, v35, v32\n"
      "v_add_f32 v7, v7, v34\n"
      "v_cvt_pk_bf16_f32 v34, v34, v34\n"
      "v_and_b32 v36, v4, v34\n"
      "v_and_b32 v37, v5, v34\n"
      "v_mfma_f32_16x16x32_bf16 v[24:27], v[152:155], v[8:11], 0\n"
      "v_sub_f32_dpp v32, v28, v6 quad_perm:[0,0,0,0] row_mask:0xf bank_mask:0xf\n"
      "v_mov_b32_dpp v33, v30 quad_perm:[2,2,2,2] row_mask:0xf bank_mask:0xf\n"
      "v_mfma_f32_16x16x16_bf16 v[16:19], v[140:141], v[36:37], v[16:19]\n"
      "v_add_f32_dpp v32, v29, v32 quad_perm:[1,1,1,1] row_mask:0xf bank_mask:0xf\n"
      "v_add_f32_dpp v33, v31, v33 quad_perm:[3,3,3,3] row_mask:0xf bank_mask:0xf\n"
      "v_mfma_f32_16x16x16_bf16 v[20:23], v[142:143], v[36:37], v[20:23]\n"
      "v_add_f32 v32, v32, v33\n"
      "v_exp_f32 v34, v32\n"
      "v_max_f32 v35, v35, v32\n"
      "v_add_f32 v7, v7, v34\n"
      "v_cvt_pk_bf16_f32 v34, v34, v34\n"
      "v_and_b32 v38, v4, v34\n"
      "v_and_b32 v39, v5, v34\n"
      "v_mfma_f32_16x16x32_bf16 v[28:31], v[160:163], v[8:11], 0\n"
      "v_sub_f32_dpp v32, v24, v6 quad_perm:[0,0,0,0] row_mask:0xf bank_mask:0xf\n"
      "v_mov_b32_dpp v33, v26 quad_perm:[2,2,2,2] row_mask:0xf bank_mask:0xf\n"
      "v_mfma_f32_16x16x16_bf16 v[16:19], v[148:149], v[38:39], v[16:19]\n"
      "v_add_f32_dpp v32, v25, v32 quad_perm:[1,1,1,1] row_mask:0xf bank_mask:0xf\n"
      "v_add_f32_dpp v33, v27, v33 quad_perm:[3,3,3,3] row_mask:0xf bank_mask:0xf\n"
      "v_mfma_f32_16x16x16_bf16 v[20:23], v[150:151], v[38:39], v[20:23]\n"
      "v_add_f32 v32, v32, v33\n"
      "v_exp_f32 v34, v32\n"
      "v_max_f32 v35, v35, v32\n"
      "v_add_f32 v7, v7, v34\n"
      "v_cvt_pk_bf16_f32 v34, v34, v34\n"
      "v_and_b32 v36, v4, v34\n"
      "v_and_b32 v37, v5, v34\n"
      "v_mfma_f32_16x16x32_bf16 v[24:27], v[168:171], v[8:11], 0\n"
      "v_sub_f32_dpp v32, v28, v6 quad_perm:[0,0,0,0] row_mask:0xf bank_mask:0xf\n"
      "v_mov_b32_dpp v33, v30 quad_perm:[2,2,2,2] row_mask:0xf bank_mask:0xf\n"
      "v_mfma_f32_16x16x16_bf16 v[16:19], v[156:157], v[36:37], v[16:19]\n"
      "v_add_f32_dpp v32, v29, v32 quad_perm:[1,1,1,1] row_mask:0xf bank_mask:0xf\n"
      "v_add_f32_dpp v33, v31, v33 quad_perm:[3,3,3,3] row_mask:0xf bank_mask:0xf\n"
      "v_mfma_f32_16x16x16_bf16 v[20:23], v[158:159], v[36:37], v[20:23]\n"
      "v_add_f32 v32, v32, v33\n"
      "v_exp_f32 v34, v32\n"
      "v_max_f32 v35, v35, v32\n"
      "v_add_f32 v7, v7, v34\n"
      "v_cvt_pk_bf16_f32 v34, v34, v34\n"
      "v_and_b32 v38, v4, v34\n"
      "v_and_b32 v39, v5, v34\n"
      "v_mfma_f32_16x16x32_bf16 v[28:31], v[176:179], v[8:11], 0\n"
      "v_sub_f32_dpp v32, v24, v6 quad_perm:[0,0,0,0] row_mask:0xf bank_mask:0xf\n"
      "v_mov_b32_dpp v33, v26 quad_perm:[2,2,2,2] row_mask:0xf bank_mask:0xf\n"
      "v_mfma_f32_16x16x16_bf16 v[16:19], v[164:165], v[38:39], v[16:19]\n"
      "v_add_f32_dpp v32, v25, v32 quad_perm:[1,1,1,1] row_mask:0xf bank_mask:0xf\n"
      "v_add_f32_dpp v33, v27, v33 quad_perm:[3,3,3,3] row_mask:0xf bank_mask:0xf\n"
      "v_mfma_f32_16x16x16_bf16 v[20:23], v[166:167], v[38:39], v[20:23]\n"
      "v_add_f32 v32, v32, v33\n"
      "v_exp_f32 v34, v32\n"
      "v_max_f32 v35, v35, v32\n"
      "v_add_f32 v7, v7, v34\n"
      "v_cvt_pk_bf16_f32 v34, v34, v34\n"
      "v_and_b32 v36, v4, v34\n"
      "v_and_b32 v37, v5, v34\n"
      "v_mfma_f32_16x16x32_bf16 v[24:27], v[184:187], v[8:11], 0\n"
      "v_sub_f32_dpp v32, v28, v6 quad_perm:[0,0,0,0] row_mask:0xf bank_mask:0xf\n"
      "v_mov_b32_dpp v33, v30 quad_perm:[2,2,2,2] row_mask:0xf bank_mask:0xf\n"
      "v_mfma_f32_16x16x16_bf16 v[16:19], v[172:173], v[36:37], v[16:19]\n"
      "v_add_f32_dpp v32, v29, v32 quad_perm:[1,1,1,1] row_mask:0xf bank_mask:0xf\n"
      "v_add_f32_dpp v33, v31, v33 quad_perm:[3,3,3,3] row_mask:0xf bank_mask:0xf\n"
      "v_mfma_f32_16x16x16_bf16 v[20:23], v[174:175], v[36:37], v[20:23]\n"
      "v_add_f32 v32, v32, v33\n"
      "v_exp_f32 v34, v32\n"
      "v_max_f32 v35, v35, v32\n"
      "v_add_f32 v7, v7, v34\n"
      "v_cvt_pk_bf16_f32 v34, v34, v34\n"
      "v_and_b32 v38, v4, v34\n"
      "v_and_b32 v39, v5, v34\n"
      "v_mfma_f32_16x16x32_bf16 v[28:31], v[192:195], v[8:11], 0\n"
      "v_sub_f32_dpp v32, v24, v6 quad_perm:[0,0,0,0] row_mask:0xf bank_mask:0xf\n"
      "v_mov_b32_dpp v33, v26 quad_perm:[2,2,2,2] row_mask:0xf bank_mask:0xf\n"
      "v_mfma_f32_16x16x16_bf16 v[16:19], v[180:181], v[38:39], v[16:19]\n"
      "v_add_f32_dpp v32, v25, v32 quad_perm:[1,1,1,1] row_mask:0xf bank_mask:0xf\n"
      "v_add_f32_dpp v33, v27, v33 quad_perm:[3,3,3,3] row_mask:0xf bank_mask:0xf\n"
      "v_mfma_f32_16x16x16_bf16 v[20:23], v[182:183], v[38:39], v[20:23]\n"
      "v_add_f32 v32, v32, v33\n"
      "v_exp_f32 v34, v32\n"
      "v_max_f32 v35, v35, v32\n"
      "v_add_f32 v7, v7, v34\n"
      "v_cvt_pk_bf16_f32 v34, v34, v34\n"
      "v_and_b32 v36, v4, v34\n"
      "v_and_b32 v37, v5, v34\n"
      "v_mfma_f32_16x16x32_bf16 v[24:27], v[200:203], v[8:11], 0\n"
      "v_sub_f32_dpp v32, v28, v6 quad_perm:[0,0,0,0] row_mask:0xf bank_mask:0xf\n"
      "v_mov_b32_dpp v33, v30 quad_perm:[2,2,2,2] row_mask:0xf bank_mask:0xf\n"
      "v_mfma_f32_16x16x16_bf16 v[16:19], v[188:189], v[36:37], v[16:19]\n"
      "v_add_f32_dpp v32, v29, v32 quad_perm:[1,1,1,1] row_mask:0xf bank_mask:0xf\n"
      "v_add_f32_dpp v33, v31, v33 quad_perm:[3,3,3,3] row_mask:0xf bank_mask:0xf\n"
      "v_mfma_f32_16x16x16_bf16 v[20:23], v[190:191], v[36:37], v[20:23]\n"
      "v_add_f32 v32, v32, v33\n"
      "v_exp_f32 v34, v32\n"
      "v_max_f32 v35, v35, v32\n"
      "v_add_f32 v7, v7, v34\n"
      "v_cvt_pk_bf16_f32 v34, v34, v34\n"
      "v_and_b32 v38, v4, v34\n"
      "v_and_b32 v39, v5, v34\n"
      "v_mfma_f32_16x16x32_bf16 v[28:31], v[208:211], v[8:11], 0\n"
      "v_sub_f32_dpp v32, v24, v6 quad_perm:[0,0,0,0] row_mask:0xf bank_mask:0xf\n"
      "v_mov_b32_dpp v33, v26 quad_perm:[2,2,2,2] row_mask:0xf bank_mask:0xf\n"
      "v_mfma_f32_16x16x16_bf16 v[16:19], v[196:197], v[38:39], v[16:19]\n"
      "v_add_f32_dpp v32, v25, v32 quad_perm:[1,1,1,1] row_mask:0xf bank_mask:0xf\n"
      "v_add_f32_dpp v33, v27, v33 quad_perm:[3,3,3,3] row_mask:0xf bank_mask:0xf\n"
      "v_mfma_f32_16x16x16_bf16 v[20:23], v[198:199], v[38:39], v[20:23]\n"
      "v_add_f32 v32, v32, v33\n"
      "v_exp_f32 v34, v32\n"
      "v_max_f32 v35, v35, v32\n"
      "v_add_f32 v7, v7, v34\n"
      "v_cvt_pk_bf16_f32 v34, v34, v34\n"
      "v_and_b32 v36, v4, v34\n"
      "v_and_b32 v37, v5, v34\n"
      "v_mfma_f32_16x16x32_bf16 v[24:27], v[216:219], v[8:11], 0\n"
      "v_sub_f32_dpp v32, v28, v6 quad_perm:[0,0,0,0] row_mask:0xf bank_mask:0xf\n"
      "v_mov_b32_dpp v33, v30 quad_perm:[2,2,2,2] row_mask:0xf bank_mask:0xf\n"
      "v_mfma_f32_16x16x16_bf16 v[16:19], v[204:205], v[36:37], v[16:19]\n"
      "v_add_f32_dpp v32, v29, v32 quad_perm:[1,1,1,1] row_mask:0xf bank_mask:0xf\n"
      "v_add_f32_dpp v33, v31, v33 quad_perm:[3,3,3,3] row_mask:0xf bank_mask:0xf\n"
      "v_mfma_f32_16x16x16_bf16 v[20:23], v[206:207], v[36:37], v[20:23]\n"
      "v_add_f32 v32, v32, v33\n"
      "v_exp_f32 v34, v32\n"
      "v_max_f32 v35, v35, v32\n"
      "v_add_f32 v7, v7, v34\n"
      "v_cvt_pk_bf16_f32 v34, v34, v34\n"
      "v_and_b32 v38, v4, v34\n"
      "v_and_b32 v39, v5, v34\n"
      "s_waitcnt vmcnt(8)\n"
      "v_mfma_f32_16x16x32_bf16 v[28:31], v[224:227], v[8:11], 0\n"
      "v_sub_f32_dpp v32, v24, v6 quad_perm:[0,0,0,0] row_mask:0xf bank_mask:0xf\n"
      "v_mov_b32_dpp v33, v26 quad_perm:[2,2,2,2] row_mask:0xf bank_mask:0xf\n"
      "v_mfma_f32_16x16x16_bf16 v[16:19], v[212:213], v[38:39], v[16:19]\n"
      "v_add_f32_dpp v32, v25, v32 quad_perm:[1,1,1,1] row_mask:0xf bank_mask:0xf\n"
      "v_add_f32_dpp v33, v27, v33 quad_perm:[3,3,3,3] row_mask:0xf bank_mask:0xf\n"
      "v_mfma_f32_16x16x16_bf16 v[20:23], v[214:215], v[38:39], v[20:23]\n"
      "v_add_f32 v32, v32, v33\n"
      "v_exp_f32 v34, v32\n"
      "v_max_f32 v35, v35, v32\n"
      "v_add_f32 v7, v7, v34\n"
      "v_cvt_pk_bf16_f32 v34, v34, v34\n"
      "v_and_b32 v36, v4, v34\n"
      "v_and_b32 v37, v5, v34\n"
      "s_waitcnt vmcnt(6)\n"
      "v_mfma_f32_16x16x32_bf16 v[24:27], v[232:235], v[8:11], 0\n"
      "v_sub_f32_dpp v32, v28, v6 quad_perm:[0,0,0,0] row_mask:0xf bank_mask:0xf\n"
      "v_mov_b32_dpp v33, v30 quad_perm:[2,2,2,2] row_mask:0xf bank_mask:0xf\n"
      "v_mfma_f32_16x16x16_bf16 v[16:19], v[220:221], v[36:37], v[16:19]\n"
      "v_add_f32_dpp v32, v29, v32 quad_perm:[1,1,1,1] row_mask:0xf bank_mask:0xf\n"
      "v_add_f32_dpp v33, v31, v33 quad_perm:[3,3,3,3] row_mask:0xf bank_mask:0xf\n"
      "v_mfma_f32_16x16x16_bf16 v[20:23], v[222:223], v[36:37], v[20:23]\n"
      "v_add_f32 v32, v32, v33\n"
      "v_exp_f32 v34, v32\n"
      "v_max_f32 v35, v35, v32\n"
      "v_add_f32 v7, v7, v34\n"
      "v_cvt_pk_bf16_f32 v34, v34, v34\n"
      "v_and_b32 v38, v4, v34\n"
      "v_and_b32 v39, v5, v34\n"
      "s_waitcnt vmcnt(4)\n"
      "v_mfma_f32_16x16x32_bf16 v[28:31], v[240:243], v[8:11], 0\n"
      "v_sub_f32_dpp v32, v24, v6 quad_perm:[0,0,0,0] row_mask:0xf bank_mask:0xf\n"
      "v_mov_b32_dpp v33, v26 quad_perm:[2,2,2,2] row_mask:0xf bank_mask:0xf\n"
      "v_mfma_f32_16x16x16_bf16 v[16:19], v[228:229], v[38:39], v[16:19]\n"
      "v_add_f32_dpp v32, v25, v32 quad_perm:[1,1,1,1] row_mask:0xf bank_mask:0xf\n"
      "v_add_f32_dpp v33, v27, v33 quad_perm:[3,3,3,3] row_mask:0xf bank_mask:0xf\n"
      "v_mfma_f32_16x16x16_bf16 v[20:23], v[230:231], v[38:39], v[20:23]\n"
      "v_add_f32 v32, v32, v33\n"
      "v_exp_f32 v34, v32\n"
      "v_max_f32 v35, v35, v32\n"
      "v_add_f32 v7, v7, v34\n"
      "v_cvt_pk_bf16_f32 v34, v34, v34\n"
      "v_and_b32 v36, v4, v34\n"
      "v_and_b32 v37, v5, v34\n"
      "s_waitcnt vmcnt(2)\n"
      "v_mfma_f32_16x16x32_bf16 v[24:27], v[248:251], v[8:11], 0\n"
      "v_sub_f32_dpp v32, v28, v6 quad_perm:[0,0,0,0] row_mask:0xf bank_mask:0xf\n"
      "v_mov_b32_dpp v33, v30 quad_perm:[2,2,2,2] row_mask:0xf bank_mask:0xf\n"
      "v_mfma_f32_16x16x16_bf16 v[16:19], v[236:237], v[36:37], v[16:19]\n"
      "v_add_f32_dpp v32, v29, v32 quad_perm:[1,1,1,1] row_mask:0xf bank_mask:0xf\n"
      "v_add_f32_dpp v33, v31, v33 quad_perm:[3,3,3,3] row_mask:0xf bank_mask:0xf\n"
      "v_mfma_f32_16x16x16_bf16 v[20:23], v[238:239], v[36:37], v[20:23]\n"
      "v_add_f32 v32, v32, v33\n"
      "v_exp_f32 v34, v32\n"
      "v_max_f32 v35, v35, v32\n"
      "v_add_f32 v7, v7, v34\n"
      "v_cvt_pk_bf16_f32 v34, v34, v34\n"
      "v_and_b32 v38, v4, v34\n"
      "v_and_b32 v39, v5, v34\n"
      "v_sub_f32_dpp v32, v24, v6 quad_perm:[0,0,0,0] row_mask:0xf bank_mask:0xf\n"
      "v_mov_b32_dpp v33, v26 quad_perm:[2,2,2,2] row_mask:0xf bank_mask:0xf\n"
      "v_mfma_f32_16x16x16_bf16 v[16:19], v[244:245], v[38:39], v[16:19]\n"
      "v_add_f32_dpp v32, v25, v32 quad_perm:[1,1,1,1] row_mask:0xf bank_mask:0xf\n"
      "v_add_f32_dpp v33, v27, v33 quad_perm:[3,3,3,3] row_mask:0xf bank_mask:0xf\n"
      "v_mfma_f32_16x16x16_bf16 v[20:23], v[246:247], v[38:39], v[20:23]\n"
      "v_add_f32 v32, v32, v33\n"
      "v_add_f32 v32, s69, v32\n"
      "v_exp_f32 v34, v32\n"
      "v_max_f32 v35, v35, v32\n"
      "v_add_f32 v7, v7, v34\n"
      "v_cvt_pk_bf16_f32 v34, v34, v34\n"
      "v_and_b32 v36, v4, v34\n"
      "v_and_b32 v37, v5, v34\n"
      "s_waitcnt vmcnt(1)\n"
      "s_nop 0\n"
      "v_mfma_f32_16x16x16_bf16 v[16:19], v[252:253], v[36:37], v[16:19]\n"
      "v_mfma_f32_16x16x16_bf16 v[20:23], v[254:255], v[36:37], v[20:23]\n"
      "v_cmp_lt_f32 vcc, 0x42800000, v35\n"
      "s_nop 0\n"
      "s_cbranch_vccnz L_fb12_%=\n"
      "L_ret12_%=:\n"
      "v_mov_b32 v49, v7\n"
      "v_lshlrev_b32 v41, 2, v0\n"
      "v_and_b32 v41, 0x300, v41\n"
      "v_permlane32_swap_b32 v49, v7\n"
      "v_lshrrev_b32 v42, 4, v0\n"
      "v_add_f32 v7, v49, v7\n"
      "v_and_b32 v42, 0x30, v42\n"
      "v_mov_b32 v49, v7\n"
      "v_add_u32 v41, v41, v42\n"
      "v_lshrrev_b32 v40, 2, v0\n"
      "v_permlane16_swap_b32 v49, v7\n"
      "v_and_b32 v42, 12, v40\n"
      "v_add_f32 v7, v49, v7\n"
      "v_add_u32 v41, v41, v42\n"
      "s_lshl_b32 s62, s60, 10\n"
      "s_add_u32 s62, s62, 155648\n"
      "v_add_u32 v41, s62, v41\n"
      "v_lshrrev_b32 v43, 3, v0\n"
      "v_and_b32 v43, 24, v43\n"
      "s_lshl_b32 s62, s60, 5\n"
      "s_add_u32 s62, s62, 159744\n"
      "v_add_u32 v43, s62, v43\n"
      "v_cvt_pk_bf16_f32 v36, v16, v17\n"
      "v_cvt_pk_bf16_f32 v37, v18, v19\n"
      "v_cvt_pk_bf16_f32 v38, v20, v21\n"
      "v_cvt_pk_bf16_f32 v39, v22, v23\n"
      "s_mov_b64 exec, 0x1111\n"
      "ds_write_b64 v43, v[6:7]\n"
      "s_mov_b64 exec, -1\n"
      "ds_write_b32 v41, v36 offset:0\n"
      "ds_write_b32 v41, v37 offset:64\n"
      "ds_write_b32 v41, v38 offset:128\n"
      "ds_write_b32 v41, v39 offset:192\n"
      "s_lshl_b32 s62, s60, 8\n"
      "s_add_u32 s62, s62, 155648\n"
      "v_add_u32 v42, s62, v40\n"
      "s_lshl_b32 s62, s60, 3\n"
      "s_add_u32 s62, s62, 159744\n"
      "v_mov_b32 v50, s62\n"
      "s_waitcnt lgkmcnt(0)\n"
      "s_barrier\n"
      "ds_read_b64 v[28:29], v50 offset:0\n"
      "ds_read_b64 v[30:31], v50 offset:32\n"
      "ds_read_b64 v[32:33], v50 offset:64\n"
      "ds_read_b64 v[34:35], v50 offset:96\n"
      "ds_read_b32 v24, v42 offset:0\n"
      "ds_read_b32 v25, v42 offset:1024\n"
      "ds_read_b32 v26, v42 offset:2048\n"
      "ds_read_b32 v27, v42 offset:3072\n"
      "s_add_u32 s56, s44, 2322432\n"
      "s_addc_u32 s57, s45, 0\n"
      "s_waitcnt lgkmcnt(4)\n"
      "v_max3_f32 v43, v28, v30, v32\n"
      "v_max_f32 v43, v43, v34\n"
      "v_sub_f32 v28, v28, v43\n"
      "v_sub_f32 v30, v30, v43\n"
      "v_sub_f32 v32, v32, v43\n"
      "v_sub_f32 v34, v34, v43\n"
      "v_exp_f32 v28, v28\n"
      "v_exp_f32 v30, v30\n"
      "v_exp_f32 v32, v32\n"
      "v_exp_f32 v34, v34\n"
      "s_waitcnt lgkmcnt(0)\n"
      "v_lshlrev_b32 v44, 16, v24\n"
      "v_and_b32 v45, 0xffff0000, v24\n"
      "v_lshlrev_b32 v46, 16, v25\n"
      "v_and_b32 v47, 0xffff0000, v25\n"
      "v_lshlrev_b32 v48, 16, v26\n"
      "v_and_b32 v49, 0xffff0000, v26\n"
      "v_lshlrev_b32 v50, 16, v27\n"
      "v_and_b32 v51, 0xffff0000, v27\n"
      "v_mul_f32 v38, v28, v29\n"
      "v_pk_mul_f32 v[36:37], v[44:45], v[28:29] op_sel_hi:[1,0]\n"
      "v_fmac_f32 v38, v30, v31\n"
      "v_pk_fma_f32 v[36:37], v[46:47], v[30:31], v[36:37] op_sel_hi:[1,0,1]\n"
      "v_fmac_f32 v38, v32, v33\n"
      "v_pk_fma_f32 v[36:37], v[48:49], v[32:33], v[36:37] op_sel_hi:[1,0,1]\n"
      "v_fmac_f32 v38, v34, v35\n"
      "v_pk_fma_f32 v[36:37], v[50:51], v[34:35], v[36:37] op_sel_hi:[1,0,1]\n"
      "v_rcp_f32 v39, v38\n"
      "v_log_f32 v38, v38\n"
      "v_mul_f32 v36, v39, v36\n"
      "v_mul_f32 v37, v39, v37\n"
      "v_add_f32 v38, v38, v43\n"
      "v_cvt_pk_bf16_f32 v36, v36, v37\n"
      "v_min_u32 v38, -2, v38\n"
      "v_min_u32 v36, -2, v36\n"
      "global_store_dword v40, v36, s[56:57] sc1\n"
      "s_mov_b32 exec_hi, 0\n"
      "global_store_dword v40, v38, s[56:57] offset:256 sc1\n"
      "s_mov_b64 exec, -1\n"
      "s_add_u32 s56, s44, 3981312\n"
      "s_addc_u32 s57, s45, 0\n"
      "global_store_dword v40, v12, s[56:57] sc1\n"
      "s_mov_b32 exec_hi, 0\n"
      "global_store_dword v40, v13, s[56:57] offset:256 sc1\n"
      "s_mov_b64 exec, -1\n"
      "s_barrier\n"
      "L_per8_%=:\n"
      "s_add_u32 s56, s48, 106496\n"
      "s_addc_u32 s57, s49, 0\n"
      "s_mov_b64 exec, 0xffff\n"
      "global_load_dwordx4 v[12:15], v0, s[56:57] sc1\n"
      "s_mov_b64 exec, -1\n"
      "s_waitcnt vmcnt(0)\n"
      "s_mov_b64 exec, 0xffff\n"
      "s_mov_b32 s53, 0\n"
      "L_qc29_%=:\n"
      "v_max3_u32 v49, v12, v13, v14\n"
      "v_max_u32 v49, v49, v15\n"
      "v_cmp_eq_u32 vcc, -1, v49\n"
      "s_nop 0\n"
      "s_cbranch_vccz L_qo30_%=\n"
      "s_cmp_lg_u32 s54, 0\n"
      "s_cbranch_scc1 L_qo30_%=\n"
      "s_add_u32 s56, s48, 106496\n"
      "s_addc_u32 s57, s49, 0\n"
      "global_load_dwordx4 v[12:15], v0, s[56:57] sc1\n"
      "s_add_u32 s53, s53, 1\n"
      "s_waitcnt vmcnt(0)\n"
      "s_cmp_lt_u32 s53, 30000\n"
      "s_cbranch_scc1 L_qc29_%=\n"
      "s_mov_b32 s54, 205\n"
      "v_mov_b32 v49, 205\n"
      "v_mov_b32 v50, 256\n"
      "global_store_dword v50, v49, s[58:59] sc1\n"
      "L_qo30_%=:\n"
      "s_lshl_b32 s62, s60, 8\n"
      "s_add_u32 s62, s62, 160896\n"
      "v_add_u32 v49, s62, v0\n"
      "ds_write_b128 v49, v[12:15]\n"
      "s_mov_b64 exec, -1\n"
      "s_waitcnt lgkmcnt(0)\n"
      "s_barrier\n"
      "ds_read_b128 v[12:15], v2 offset:1024\n"
      "s_waitcnt lgkmcnt(0)\n"
      "v_mfma_f32_16x16x32_bf16 v[24:27], a[0:3], v[12:15], 0\n"
      "v_mfma_f32_16x16x32_bf16 v[28:31], a[8:11], v[12:15], 0\n"
      "s_nop 6\n"
      "v_mov_b32_dpp v34, v24 quad_perm:[0,0,0,0] row_mask:0xf bank_mask:0xf\n"
      "v_mov_b32_dpp v33, v26 quad_perm:[2,2,2,2] row_mask:0xf bank_mask:0xf\n"
      "s_nop 0\n"
      "v_add_f32_dpp v34, v25, v34 quad_perm:[1,1,1,1] row_mask:0xf bank_mask:0xf\n"
      "v_add_f32_dpp v33, v27, v33 quad_perm:[3,3,3,3] row_mask:0xf bank_mask:0xf\n"
      "v_add_f32 v34, v34, v33\n"
      "v_mov_b32_dpp v32, v28 quad_perm:[0,0,0,0] row_mask:0xf bank_mask:0xf\n"
      "v_mov_b32_dpp v33, v30 quad_perm:[2,2,2,2] row_mask:0xf bank_mask:0xf\n"
      "s_nop 0\n"
      "v_add_f32_dpp v32, v29, v32 quad_perm:[1,1,1,1] row_mask:0xf bank_mask:0xf\n"
      "v_add_f32_dpp v33, v31, v33 quad_perm:[3,3,3,3] row_mask:0xf bank_mask:0xf\n"
      "v_add_f32 v32, v32, v33\n"
      "v_max_f32 v35, v34, v32\n"
      "v_mov_b32 v33, v35\n"
      "s_nop 1\n"
      "v_permlane32_swap_b32 v33, v35\n"
      "v_max_f32 v35, v33, v35\n"
      "v_mov_b32 v33, v35\n"
      "s_nop 1\n"
      "v_permlane16_swap_b32 v33, v35\n"
      "v_max_f32 v6, v33, v35\n"
      "v_sub_f32 v34, v34, v6\n"
      "v_sub_f32 v32, v32, v6\n"
      "v_mov_b32 v35, 0\n"
      "v_exp_f32 v34, v34\n"
      "s_nop 0\n"
      "v_mov_b32 v7, v34\n"
      "v_cvt_pk_bf16_f32 v34, v34, v34\n"
      "v_and_b32 v36, v4, v34\n"
      "v_and_b32 v37, v5, v34\n"
      "v_mfma_f32_16x16x32_bf16 v[24:27], a[16:19], v[12:15], 0\n"
      "s_nop 0\n"
      "v_mfma_f32_16x16x16_bf16 v[16:19], a[4:5], v[36:37], 0\n"
      "v_mfma_f32_16x16x16_bf16 v[20:23], a[6:7], v[36:37], 0\n"
      "v_exp_f32 v34, v32\n"
      "v_max_f32 v35, v35, v32\n"
      "v_add_f32 v7, v7, v34\n"
      "v_cvt_pk_bf16_f32 v34, v34, v34\n"
      "v_and_b32 v38, v4, v34\n"
      "v_and_b32 v39, v5, v34\n"
      "v_mfma_f32_16x16x32_bf16 v[28:31], a[24:27], v[12:15], 0\n"
      "v_sub_f32_dpp v32, v24, v6 quad_perm:[0,0,0,0] row_mask:0xf bank_mask:0xf\n"
      "v_mov_b32_dpp v33, v26 quad_perm:[2,2,2,2] row_mask:0xf bank_mask:0xf\n"
      "v_mfma_f32_16x16x16_bf16 v[16:19], a[12:13], v[38:39], v[16:19]\n"
      "v_add_f32_dpp v32, v25, v32 quad_perm:[1,1,1,1] row_mask:0xf bank_mask:0xf\n"
      "v_add_f32_dpp v33, v27, v33 quad_perm:[3,3,3,3] row_mask:0xf bank_mask:0xf\n"
      "v_mfma_f32_16x16x16_bf16 v[20:23], a[14:15], v[38:39], v[20:23]\n"
      "v_add_f32 v32, v32, v33\n"
      "v_exp_f32 v34, v32\n"
      "v_max_f32 v35, v35, v32\n"
      "v_add_f32 v7, v7, v34\n"
      "v_cvt_pk_bf16_f32 v34, v34, v34\n"
      "v_and_b32 v36, v4, v34\n"
      "v_and_b32 v37, v5, v34\n"
      "v_mfma_f32_16x16x32_bf16 v[24:27], a[32:35], v[12:15], 0\n"
      "v_sub_f32_dpp v32, v28, v6 quad_perm:[0,0,0,0] row_mask:0xf bank_mask:0xf\n"
      "v_mov_b32_dpp v33, v30 quad_perm:[2,2,2,2] row_mask:0xf bank_mask:0xf\n"
      "v_mfma_f32_16x16x16_bf16 v[16:19], a[20:21], v[36:37], v[16:19]\n"
      "v_add_f32_dpp v32, v29, v32 quad_perm:[1,1,1,1] row_mask:0xf bank_mask:0xf\n"
      "v_add_f32_dpp v33, v31, v33 quad_perm:[3,3,3,3] row_mask:0xf bank_mask:0xf\n"
      "v_mfma_f32_16x16x16_bf16 v[20:23], a[22:23], v[36:37], v[20:23]\n"
      "v_add_f32 v32, v32, v33\n"
      "v_exp_f32 v34, v32\n"
      "v_max_f32 v35, v35, v32\n"
      "v_add_f32 v7, v7, v34\n"
      "v_cvt_pk_bf16_f32 v34, v34, v34\n"
      "v_and_b32 v38, v4, v34\n"
      "v_and_b32 v39, v5, v34\n"
      "v_mfma_f32_16x16x32_bf16 v[28:31], a[40:43], v[12:15], 0\n"
      "v_sub_f32_dpp v32, v24, v6 quad_perm:[0,0,0,0] row_mask:0xf bank_mask:0xf\n"
      "v_mov_b32_dpp v33, v26 quad_perm:[2,2,2,2] row_mask:0xf bank_mask:0xf\n"
      "v_mfma_f32_16x16x16_bf16 v[16:19], a[28:29], v[38:39], v[16:19]\n"
      "v_add_f32_dpp v32, v25, v32 quad_perm:[1,1,1,1] row_mask:0xf bank_mask:0xf\n"
      "v_add_f32_dpp v33, v27, v33 quad_perm:[3,3,3,3] row_mask:0xf bank_mask:0xf\n"
      "v_mfma_f32_16x16x16_bf16 v[20:23], a[30:31], v[38:39], v[20:23]\n"
      "v_add_f32 v32, v32, v33\n"
      "v_exp_f32 v34, v32\n"
      "v_max_f32 v35, v35, v32\n"
      "v_add_f32 v7, v7, v34\n"
      "v_cvt_pk_bf16_f32 v34, v34, v34\n"
      "v_and_b32 v36, v4, v34\n"
      "v_and_b32 v37, v5, v34\n"
      "v_mfma_f32_16x16x32_bf16 v[24:27], a[48:51], v[12:15], 0\n"
      "v_sub_f32_dpp v32, v28, v6 quad_perm:[0,0,0,0] row_mask:0xf bank_mask:0xf\n"
      "v_mov_b32_dpp v33, v30 quad_perm:[2,2,2,2] row_mask:0xf bank_mask:0xf\n"
      "v_mfma_f32_16x16x16_bf16 v[16:19], a[36:37], v[36:37], v[16:19]\n"
      "v_add_f32_dpp v32, v29, v32 quad_perm:[1,1,1,1] row_mask:0xf bank_mask:0xf\n"
      "v_add_f32_dpp v33, v31, v33 quad_perm:[3,3,3,3] row_mask:0xf bank_mask:0xf\n"
      "v_mfma_f32_16x16x16_bf16 v[20:23], a[38:39], v[36:37], v[20:23]\n"
      "v_add_f32 v32, v32, v33\n"
      "v_exp_f32 v34, v32\n"
      "v_max_f32 v35, v35, v32\n"
      "v_add_f32 v7, v7, v34\n"
      "v_cvt_pk_bf16_f32 v34, v34, v34\n"
      "v_and_b32 v38, v4, v34\n"
      "v_and_b32 v39, v5, v34\n"
      "v_mfma_f32_16x16x32_bf16 v[28:31], a[56:59], v[12:15], 0\n"
      "v_sub_f32_dpp v32, v24, v6 quad_perm:[0,0,0,0] row_mask:0xf bank_mask:0xf\n"
      "v_mov_b32_dpp v33, v26 quad_perm:[2,2,2,2] row_mask:0xf bank_mask:0xf\n"
      "v_mfma_f32_16x16x16_bf16 v[16:19], a[44:45], v[38:39], v[16:19]\n"
      "v_add_f32_dpp v32, v25, v32 quad_perm:[1,1,1,1] row_mask:0xf bank_mask:0xf\n"
      "v_add_f32_dpp v33, v27, v33 quad_perm:[3,3,3,3] row_mask:0xf bank_mask:0xf\n"
      "v_mfma_f32_16x16x16_bf16 v[20:23], a[46:47], v[38:39], v[20:23]\n"
      "v_add_f32 v32, v32, v33\n"
      "v_exp_f32 v34, v32\n"
      "v_max_f32 v35, v35, v32\n"
      "v_add_f32 v7, v7, v34\n"
      "v_cvt_pk_bf16_f32 v34, v34, v34\n"
      "v_and_b32 v36, v4, v34\n"
      "v_and_b32 v37, v5, v34\n"
      "v_mfma_f32_16x16x32_bf16 v[24:27], a[64:67], v[12:15], 0\n"
      "v_sub_f32_dpp v32, v28, v6 quad_perm:[0,0,0,0] row_mask:0xf bank_mask:0xf\n"
      "v_mov_b32_dpp v33, v30 quad_perm:[2,2,2,2] row_mask:0xf bank_mask:0xf\n"
      "v_mfma_f32_16x16x16_bf16 v[16:19], a[52:53], v[36:37], v[16:19]\n"
      "v_add_f32_dpp v32, v29, v32 quad_perm:[1,1,1,1] row_mask:0xf bank_mask:0xf\n"
      "v_add_f32_dpp v33, v31, v33 quad_perm:[3,3,3,3] row_mask:0xf bank_mask:0xf\n"
      "v_mfma_f32_16x16x16_bf16 v[20:23], a[54:55], v[36:37], v[20:23]\n"
      "v_add_f32 v32, v32, v33\n"
      "v_exp_f32 v34, v32\n"
      "v_max_f32 v35, v35, v32\n"
      "v_add_f32 v7, v7, v34\n"
      "v_cvt_pk_bf16_f32 v34, v34, v34\n"
      "v_and_b32 v38, v4, v34\n"
      "v_and_b32 v39, v5, v34\n"
      "v_mfma_f32_16x16x32_bf16 v[28:31], a[72:75], v[12:15], 0\n"
      "v_sub_f32_dpp v32, v24, v6 quad_perm:[0,0,0,0] row_mask:0xf bank_mask:0xf\n"
      "v_mov_b32_dpp v33, v26 quad_perm:[2,2,2,2] row_mask:0xf bank_mask:0xf\n"
      "v_mfma_f32_16x16x16_bf16 v[16:19], a[60:61], v[38:39], v[16:19]\n"
      "v_add_f32_dpp v32, v25, v32 quad_perm:[1,1,1,1] row_mask:0xf bank_mask:0xf\n"
      "v_add_f32_dpp v33, v27, v33 quad_perm:[3,3,3,3] row_mask:0xf bank_mask:0xf\n"
      "v_mfma_f32_16x16x16_bf16 v[20:23], a[62:63], v[38:39], v[20:23]\n"
      "v_add_f32 v32, v32, v33\n"
      "v_exp_f32 v34, v32\n"
      "v_max_f32 v35, v35, v32\n"
      "v_add_f32 v7, v7, v34\n"
      "v_cvt_pk_bf16_f32 v34, v34, v34\n"
      "v_and_b32 v36, v4, v34\n"
      "v_and_b32 v37, v5, v34\n"
      "v_mfma_f32_16x16x32_bf16 v[24:27], a[80:83], v[12:15], 0\n"
      "v_sub_f32_dpp v32, v28, v6 quad_perm:[0,0,0,0] row_mask:0xf bank_mask:0xf\n"
      "v_mov_b32_dpp v33, v30 quad_perm:[2,2,2,2] row_mask:0xf bank_mask:0xf\n"
      "v_mfma_f32_16x16x16_bf16 v[16:19], a[68:69], v[36:37], v[16:19]\n"
      "v_add_f32_dpp v32, v29, v32 quad_perm:[1,1,1,1] row_mask:0xf bank_mask:0xf\n"
      "v_add_f32_dpp v33, v31, v33 quad_perm:[3,3,3,3] row_mask:0xf bank_mask:0xf\n"
      "v_mfma_f32_16x16x16_bf16 v[20:23], a[70:71], v[36:37], v[20:23]\n"
      "v_add_f32 v32, v32, v33\n"
      "v_exp_f32 v34, v32\n"
      "v_max_f32 v35, v35, v32\n"
      "v_add_f32 v7, v7, v34\n"
      "v_cvt_pk_bf16_f32 v34, v34, v34\n"
      "v_and_b32 v38, v4, v34\n"
      "v_and_b32 v39, v5, v34\n"
      "v_mfma_f32_16x16x32_bf16 v[28:31], a[88:91], v[12:15], 0\n"
      "v_sub_f32_dpp v32, v24, v6 quad_perm:[0,0,0,0] row_mask:0xf bank_mask:0xf\n"
      "v_mov_b32_dpp v33, v26 quad_perm:[2,2,2,2] row_mask:0xf bank_mask:0xf\n"
      "v_mfma_f32_16x16x16_bf16 v[16:19], a[76:77], v[38:39], v[16:19]\n"
      "v_add_f32_dpp v32, v25, v32 quad_perm:[1,1,1,1] row_mask:0xf bank_mask:0xf\n"
      "v_add_f32_dpp v33, v27, v33 quad_perm:[3,3,3,3] row_mask:0xf bank_mask:0xf\n"
      "v_mfma_f32_16x16x16_bf16 v[20:23], a[78:79], v[38:39], v[20:23]\n"
      "v_add_f32 v32, v32, v33\n"
      "v_exp_f32 v34, v32\n"
      "v_max_f32 v35, v35, v32\n"
      "v_add_f32 v7, v7, v34\n"
      "v_cvt_pk_bf16_f32 v34, v34, v34\n"
      "v_and_b32 v36, v4, v34\n"
      "v_and_b32 v37, v5, v34\n"
      "v_mfma_f32_16x16x32_bf16 v[24:27], a[96:99], v[12:15], 0\n"
      "v_sub_f32_dpp v32, v28, v6 quad_perm:[0,0,0,0] row_mask:0xf bank_mask:0xf\n"
      "v_mov_b32_dpp v33, v30 quad_perm:[2,2,2,2] row_mask:0xf bank_mask:0xf\n"
      "v_mfma_f32_16x16x16_bf16 v[16:19], a[84:85], v[36:37], v[16:19]\n"
      "v_add_f32_dpp v32, v29, v32 quad_perm:[1,1,1,1] row_mask:0xf bank_mask:0xf\n"
      "v_add_f32_dpp v33, v31, v33 quad_perm:[3,3,3,3] row_mask:0xf bank_mask:0xf\n"
      "v_mfma_f32_16x16x16_bf16 v[20:23], a[86:87], v[36:37], v[20:23]\n"
      "v_add_f32 v32, v32, v33\n"
      "v_exp_f32 v34, v32\n"
      "v_max_f32 v35, v35, v32\n"
      "v_add_f32 v7, v7, v34\n"
      "v_cvt_pk_bf16_f32 v34, v34, v34\n"
      "v_and_b32 v38, v4, v34\n"
      "v_and_b32 v39, v5, v34\n"
      "v_mfma_f32_16x16x32_bf16 v[28:31], a[104:107], v[12:15], 0\n"
      "v_sub_f32_dpp v32, v24, v6 quad_perm:[0,0,0,0] row_mask:0xf bank_mask:0xf\n"
      "v_mov_b32_dpp v33, v26 quad_perm:[2,2,2,2] row_mask:0xf bank_mask:0xf\n"
      "v_mfma_f32_16x16x16_bf16 v[16:19], a[92:93], v[38:39], v[16:19]\n"
      "v_add_f32_dpp v32, v25, v32 quad_perm:[1,1,1,1] row_mask:0xf bank_mask:0xf\n"
      "v_add_f32_dpp v33, v27, v33 quad_perm:[3,3,3,3] row_mask:0xf bank_mask:0xf\n"
      "v_mfma_f32_16x16x16_bf16 v[20:23], a[94:95], v[38:39], v[20:23]\n"
      "v_add_f32 v32, v32, v33\n"
      "v_exp_f32 v34, v32\n"
      "v_max_f32 v35, v35, v32\n"
      "v_add_f32 v7, v7, v34\n"
      "v_cvt_pk_bf16_f32 v34, v34, v34\n"
      "v_and_b32 v36, v4, v34\n"
      "v_and_b32 v37, v5, v34\n"
      "v_mfma_f32_16x16x32_bf16 v[24:27], a[112:115], v[12:15], 0\n"
      "v_sub_f32_dpp v32, v28, v6 quad_perm:[0,0,0,0] row_mask:0xf bank_mask:0xf\n"
      "v_mov_b32_dpp v33, v30 quad_perm:[2,2,2,2] row_mask:0xf bank_mask:0xf\n"
      "v_mfma_f32_16x16x16_bf16 v[16:19], a[100:101], v[36:37], v[16:19]\n"
      "v_add_f32_dpp v32, v29, v32 quad_perm:[1,1,1,1] row_mask:0xf bank_mask:0xf\n"
      "v_add_f32_dpp v33, v31, v33 quad_perm:[3,3,3,3] row_mask:0xf bank_mask:0xf\n"
      "v_mfma_f32_16x16x16_bf16 v[20:23], a[102:103], v[36:37], v[20:23]\n"
      "v_add_f32 v32, v32, v33\n"
      "v_exp_f32 v34, v32\n"
      "v_max_f32 v35, v35, v32\n"
      "v_add_f32 v7, v7, v34\n"
      "v_cvt_pk_bf16_f32 v34, v34, v34\n"
      "v_and_b32 v38, v4, v34\n"
      "v_and_b32 v39, v5, v34\n"
      "v_mfma_f32_16x16x32_bf16 v[28:31], a[120:123], v[12:15], 0\n"
      "v_sub_f32_dpp v32, v24, v6 quad_perm:[0,0,0,0] row_mask:0xf bank_mask:0xf\n"
      "v_mov_b32_dpp v33, v26 quad_perm:[2,2,2,2] row_mask:0xf bank_mask:0xf\n"
      "v_mfma_f32_16x16x16_bf16 v[16:19], a[108:109], v[38:39], v[16:19]\n"
      "v_add_f32_dpp v32, v25, v32 quad_perm:[1,1,1,1] row_mask:0xf bank_mask:0xf\n"
      "v_add_f32_dpp v33, v27, v33 quad_perm:[3,3,3,3] row_mask:0xf bank_mask:0xf\n"
      "v_mfma_f32_16x16x16_bf16 v[20:23], a[110:111], v[38:39], v[20:23]\n"
      "v_add_f32 v32, v32, v33\n"
      "v_exp_f32 v34, v32\n"
      "v_max_f32 v35, v35, v32\n"
      "v_add_f32 v7, v7, v34\n"
      "v_cvt_pk_bf16_f32 v34, v34, v34\n"
      "v_and_b32 v36, v4, v34\n"
      "v_and_b32 v37, v5, v34\n"
      "v_mfma_f32_16x16x32_bf16 v[24:27], a[128:131], v[12:15], 0\n"
      "v_sub_f32_dpp v32, v28, v6 quad_perm:[0,0,0,0] row_mask:0xf bank_mask:0xf\n"
      "v_mov_b32_dpp v33, v30 quad_perm:[2,2,2,2] row_mask:0xf bank_mask:0xf\n"
      "v_mfma_f32_16x16x16_bf16 v[16:19], a[116:117], v[36:37], v[16:19]\n"
      "v_add_f32_dpp v32, v29, v32 quad_perm:[1,1,1,1] row_mask:0xf bank_mask:0xf\n"
      "v_add_f32_dpp v33, v31, v33 quad_perm:[3,3,3,3] row_mask:0xf bank_mask:0xf\n"
      "v_mfma_f32_16x16x16_bf16 v[20:23], a[118:119], v[36:37], v[20:23]\n"
      "v_add_f32 v32, v32, v33\n"
      "v_exp_f32 v34, v32\n"
      "v_max_f32 v35, v35, v32\n"
      "v_add_f32 v7, v7, v34\n"
      "v_cvt_pk_bf16_f32 v34, v34, v34\n"
      "v_and_b32 v38, v4, v34\n"
      "v_and_b32 v39, v5, v34\n"
      "v_mfma_f32_16x16x32_bf16 v[28:31], a[136:139], v[12:15], 0\n"
      "v_sub_f32_dpp v32, v24, v6 quad_perm:[0,0,0,0] row_mask:0xf bank_mask:0xf\n"
      "v_mov_b32_dpp v33, v26 quad_perm:[2,2,2,2] row_mask:0xf bank_mask:0xf\n"
      "v_mfma_f32_16x16x16_bf16 v[16:19], a[124:125], v[38:39], v[16:19]\n"
      "v_add_f32_dpp v32, v25, v32 quad_perm:[1,1,1,1] row_mask:0xf bank_mask:0xf\n"
      "v_add_f32_dpp v33, v27, v33 quad_perm:[3,3,3,3] row_mask:0xf bank_mask:0xf\n"
      "v_mfma_f32_16x16x16_bf16 v[20:23], a[126:127], v[38:39], v[20:23]\n"
      "v_add_f32 v32, v32, v33\n"
      "v_exp_f32 v34, v32\n"
      "v_max_f32 v35, v35, v32\n"
      "v_add_f32 v7, v7, v34\n"
      "v_cvt_pk_bf16_f32 v34, v34, v34\n"
      "v_and_b32 v36, v4, v34\n"
      "v_and_b32 v37, v5, v34\n"
      "v_mfma_f32_16x16x32_bf16 v[24:27], a[144:147], v[12:15], 0\n"
      "v_sub_f32_dpp v32, v28, v6 quad_perm:[0,0,0,0] row_mask:0xf bank_mask:0xf\n"
      "v_mov_b32_dpp v33, v30 quad_perm:[2,2,2,2] row_mask:0xf bank_mask:0xf\n"
      "v_mfma_f32_16x16x16_bf16 v[16:19], a[132:133], v[36:37], v[16:19]\n"
      "v_add_f32_dpp v32, v29, v32 quad_perm:[1,1,1,1] row_mask:0xf bank_mask:0xf\n"
      "v_add_f32_dpp v33, v31, v33 quad_perm:[3,3,3,3] row_mask:0xf bank_mask:0xf\n"
      "v_mfma_f32_16x16x16_bf16 v[20:23], a[134:135], v[36:37], v[20:23]\n"
      "v_add_f32 v32, v32, v33\n"
      "v_exp_f32 v34, v32\n"
      "v_max_f32 v35, v35, v32\n"
      "v_add_f32 v7, v7, v34\n"
      "v_cvt_pk_bf16_f32 v34, v34, v34\n"
      "v_and_b32 v38, v4, v34\n"
      "v_and_b32 v39, v5, v34\n"
      "v_sub_f32_dpp v32, v24, v6 quad_perm:[0,0,0,0] row_mask:0xf bank_mask:0xf\n"
      "v_mov_b32_dpp v33, v26 quad_perm:[2,2,2,2] row_mask:0xf bank_mask:0xf\n"
      "v_mfma_f32_16x16x16_bf16 v[16:19], a[140:141], v[38:39], v[16:19]\n"
      "v_add_f32_dpp v32, v25, v32 quad_perm:[1,1,1,1] row_mask:0xf bank_mask:0xf\n"
      "v_add_f32_dpp v33, v27, v33 quad_perm:[3,3,3,3] row_mask:0xf bank_mask:0xf\n"
      "v_mfma_f32_16x16x16_bf16 v[20:23], a[142:143], v[38:39], v[20:23]\n"
      "v_add_f32 v32, v32, v33\n"
      "v_add_f32 v32, s69, v32\n"
      "v_exp_f32 v34, v32\n"
      "v_max_f32 v35, v35, v32\n"
      "v_add_f32 v7, v7, v34\n"
      "v_cvt_pk_bf16_f32 v34, v34, v34\n"
      "v_and_b32 v36, v4, v34\n"
      "v_and_b32 v37, v5, v34\n"
      "s_nop 1\n"
      "v_mfma_f32_16x16x16_bf16 v[16:19], a[148:149], v[36:37], v[16:19]\n"
      "v_mfma_f32_16x16x16_bf16 v[20:23], a[150:151], v[36:37], v[20:23]\n"
      "v_cmp_lt_f32 vcc, 0x42800000, v35\n"
      "s_nop 0\n"
      "s_cbranch_vccnz L_fb13_%=\n"
      "L_ret13_%=:\n"
      "v_mov_b32 v49, v7\n"
      "v_lshlrev_b32 v41, 2, v0\n"
      "v_and_b32 v41, 0x300, v41\n"
      "v_permlane32_swap_b32 v49, v7\n"
      "v_lshrrev_b32 v42, 4, v0\n"
      "v_add_f32 v7, v49, v7\n"
      "v_and_b32 v42, 0x30, v42\n"
      "v_mov_b32 v49, v7\n"
      "v_add_u32 v41, v41, v42\n"
      "v_lshrrev_b32 v40, 2, v0\n"
      "v_permlane16_swap_b32 v49, v7\n"
      "v_and_b32 v42, 12, v40\n"
      "v_add_f32 v7, v49, v7\n"
      "v_add_u32 v41, v41, v42\n"
      "s_lshl_b32 s62, s60, 10\n"
      "s_add_u32 s62, s62, 155648\n"
      "v_add_u32 v41, s62, v41\n"
      "v_lshrrev_b32 v43, 3, v0\n"
      "v_and_b32 v43, 24, v43\n"
      "s_lshl_b32 s62, s60, 5\n"
      "s_add_u32 s62, s62, 159744\n"
      "v_add_u32 v43, s62, v43\n"
      "v_cvt_pk_bf16_f32 v36, v16, v17\n"
      "v_cvt_pk_bf16_f32 v37, v18, v19\n"
      "v_cvt_pk_bf16_f32 v38, v20, v21\n"
      "v_cvt_pk_bf16_f32 v39, v22, v23\n"
      "s_mov_b64 exec, 0x1111\n"
      "ds_write_b64 v43, v[6:7]\n"
      "s_mov_b64 exec, -1\n"
      "ds_write_b32 v41, v36 offset:0\n"
      "ds_write_b32 v41, v37 offset:64\n"
      "ds_write_b32 v41, v38 offset:128\n"
      "ds_write_b32 v41, v39 offset:192\n"
      "s_lshl_b32 s62, s60, 8\n"
      "s_add_u32 s62, s62, 155648\n"
      "v_add_u32 v42, s62, v40\n"
      "s_lshl_b32 s62, s60, 3\n"
      "s_add_u32 s62, s62, 159744\n"
      "v_mov_b32 v50, s62\n"
      "s_waitcnt lgkmcnt(0)\n"
      "s_barrier\n"
      "ds_read_b64 v[28:29], v50 offset:0\n"
      "ds_read_b64 v[30:31], v50 offset:32\n"
      "ds_read_b64 v[32:33], v50 offset:64\n"
      "ds_read_b64 v[34:35], v50 offset:96\n"
      "ds_read_b32 v24, v42 offset:0\n"
      "ds_read_b32 v25, v42 offset:1024\n"
      "ds_read_b32 v26, v42 offset:2048\n"
      "ds_read_b32 v27, v42 offset:3072\n"
      "s_add_u32 s56, s44, 4313088\n"
      "s_addc_u32 s57, s45, 0\n"
      "s_waitcnt lgkmcnt(4)\n"
      "v_max3_f32 v43, v28, v30, v32\n"
      "v_max_f32 v43, v43, v34\n"
      "v_sub_f32 v28, v28, v43\n"
      "v_sub_f32 v30, v30, v43\n"
      "v_sub_f32 v32, v32, v43\n"
      "v_sub_f32 v34, v34, v43\n"
      "v_exp_f32 v28, v28\n"
      "v_exp_f32 v30, v30\n"
      "v_exp_f32 v32, v32\n"
      "v_exp_f32 v34, v34\n"
      "s_waitcnt lgkmcnt(0)\n"
      "v_lshlrev_b32 v44, 16, v24\n"
      "v_and_b32 v45, 0xffff0000, v24\n"
      "v_lshlrev_b32 v46, 16, v25\n"
      "v_and_b32 v47, 0xffff0000, v25\n"
      "v_lshlrev_b32 v48, 16, v26\n"
      "v_and_b32 v49, 0xffff0000, v26\n"
      "v_lshlrev_b32 v50, 16, v27\n"
      "v_and_b32 v51, 0xffff0000, v27\n"
      "v_mul_f32 v38, v28, v29\n"
      "v_pk_mul_f32 v[36:37], v[44:45], v[28:29] op_sel_hi:[1,0]\n"
      "v_fmac_f32 v38, v30, v31\n"
      "v_pk_fma_f32 v[36:37], v[46:47], v[30:31], v[36:37] op_sel_hi:[1,0,1]\n"
      "v_fmac_f32 v38, v32, v33\n"
      "v_pk_fma_f32 v[36:37], v[48:49], v[32:33], v[36:37] op_sel_hi:[1,0,1]\n"
      "v_fmac_f32 v38, v34, v35\n"
      "v_pk_fma_f32 v[36:37], v[50:51], v[34:35], v[36:37] op_sel_hi:[1,0,1]\n"
      "v_rcp_f32 v39, v38\n"
      "v_log_f32 v38, v38\n"
      "v_mul_f32 v36, v39, v36\n"
      "v_mul_f32 v37, v39, v37\n"
      "v_add_f32 v38, v38, v43\n"
      "v_cvt_pk_bf16_f32 v36, v36, v37\n"
      "v_min_u32 v38, -2, v38\n"
      "v_min_u32 v36, -2, v36\n"
      "global_store_dword v40, v36, s[56:57] sc1\n"
      "s_mov_b32 exec_hi, 0\n"
      "global_store_dword v40, v38, s[56:57] offset:256 sc1\n"
      "s_mov_b64 exec, -1\n"
      "s_barrier\n"
      "L_tail1_%=:\n"
      "s_add_u32 s56, s48, 114688\n"
      "s_addc_u32 s57, s49, 0\n"
      "s_mov_b64 exec, 0xffff\n"
      "global_load_dwordx4 v[12:15], v0, s[56:57] sc1\n"
      "s_mov_b64 exec, -1\n"
      "s_waitcnt vmcnt(0)\n"
      "s_mov_b64 exec, 0xffff\n"
      "s_mov_b32 s53, 0\n"
      "L_qc31_%=:\n"
      "v_max3_u32 v49, v12, v13, v14\n"
      "v_max_u32 v49, v49, v15\n"
      "v_cmp_eq_u32 vcc, -1, v49\n"
      "s_nop 0\n"
      "s_cbranch_vccz L_qo32_%=\n"
      "s_cmp_lg_u32 s54, 0\n"
      "s_cbranch_scc1 L_qo32_%=\n"
      "s_add_u32 s56, s48, 114688\n"
      "s_addc_u32 s57, s49, 0\n"
      "global_load_dwordx4 v[12:15], v0, s[56:57] sc1\n"
      "s_add_u32 s53, s53, 1\n"
      "s_waitcnt vmcnt(0)\n"
      "s_cmp_lt_u32 s53, 30000\n"
      "s_cbranch_scc1 L_qc31_%=\n"
      "s_mov_b32 s54, 206\n"
      "v_mov_b32 v49, 206\n"
      "v_mov_b32 v50, 256\n"
      "global_store_dword v50, v49, s[58:59] sc1\n"
      "L_qo32_%=:\n"
      "s_lshl_b32 s62, s60, 8\n"
      "s_add_u32 s62, s62, 160896\n"
      "v_add_u32 v49, s62, v0\n"
      "ds_write_b128 v49, v[12:15]\n"
      "s_mov_b64 exec, -1\n"
      "s_waitcnt lgkmcnt(0)\n"
      "s_barrier\n"
      "ds_read_b128 v[12:15], v2 offset:1024\n"
      "s_waitcnt lgkmcnt(0)\n"
      "v_mfma_f32_16x16x32_bf16 v[24:27], a[152:155], v[12:15], 0\n"
      "v_mfma_f32_16x16x32_bf16 v[28:31], a[160:163], v[12:15], 0\n"
      "s_nop 6\n"
      "v_mov_b32_dpp v34, v24 quad_perm:[0,0,0,0] row_mask:0xf bank_mask:0xf\n"
      "v_mov_b32_dpp v33, v26 quad_perm:[2,2,2,2] row_mask:0xf bank_mask:0xf\n"
      "s_nop 0\n"
      "v_add_f32_dpp v34, v25, v34 quad_perm:[1,1,1,1] row_mask:0xf bank_mask:0xf\n"
      "v_add_f32_dpp v33, v27, v33 quad_perm:[3,3,3,3] row_mask:0xf bank_mask:0xf\n"
      "v_add_f32 v34, v34, v33\n"
      "v_mov_b32_dpp v32, v28 quad_perm:[0,0,0,0] row_mask:0xf bank_mask:0xf\n"
      "v_mov_b32_dpp v33, v30 quad_perm:[2,2,2,2] row_mask:0xf bank_mask:0xf\n"
      "s_nop 0\n"
      "v_add_f32_dpp v32, v29, v32 quad_perm:[1,1,1,1] row_mask:0xf bank_mask:0xf\n"
      "v_add_f32_dpp v33, v31, v33 quad_perm:[3,3,3,3] row_mask:0xf bank_mask:0xf\n"
      "v_add_f32 v32, v32, v33\n"
      "v_max_f32 v35, v34, v32\n"
      "v_mov_b32 v33, v35\n"
      "s_nop 1\n"
      "v_permlane32_swap_b32 v33, v35\n"
      "v_max_f32 v35, v33, v35\n"
      "v_mov_b32 v33, v35\n"
      "s_nop 1\n"
      "v_permlane16_swap_b32 v33, v35\n"
      "v_max_f32 v6, v33, v35\n"
      "v_sub_f32 v34, v34, v6\n"
      "v_sub_f32 v32, v32, v6\n"
      "v_mov_b32 v35, 0\n"
      "v_exp_f32 v34, v34\n"
      "s_nop 0\n"
      "v_mov_b32 v7, v34\n"
      "v_cvt_pk_bf16_f32 v34, v34, v34\n"
      "v_and_b32 v36, v4, v34\n"
      "v_and_b32 v37, v5, v34\n"
      "v_mfma_f32_16x16x32_bf16 v[24:27], a[168:171], v[12:15], 0\n"
      "s_nop 0\n"
      "v_mfma_f32_16x16x16_bf16 v[16:19], a[156:157], v[36:37], 0\n"
      "v_mfma_f32_16x16x16_bf16 v[20:23], a[158:159], v[36:37], 0\n"
      "v_exp_f32 v34, v32\n"
      "v_max_f32 v35, v35, v32\n"
      "v_add_f32 v7, v7, v34\n"
      "v_cvt_pk_bf16_f32 v34, v34, v34\n"
      "v_and_b32 v38, v4, v34\n"
      "v_and_b32 v39, v5, v34\n"
      "v_mfma_f32_16x16x32_bf16 v[28:31], a[176:179], v[12:15], 0\n"
      "v_sub_f32_dpp v32, v24, v6 quad_perm:[0,0,0,0] row_mask:0xf bank_mask:0xf\n"
      "v_mov_b32_dpp v33, v26 quad_perm:[2,2,2,2] row_mask:0xf bank_mask:0xf\n"
      "v_mfma_f32_16x16x16_bf16 v[16:19], a[164:165], v[38:39], v[16:19]\n"
      "v_add_f32_dpp v32, v25, v32 quad_perm:[1,1,1,1] row_mask:0xf bank_mask:0xf\n"
      "v_add_f32_dpp v33, v27, v33 quad_perm:[3,3,3,3] row_mask:0xf bank_mask:0xf\n"
      "v_mfma_f32_16x16x16_bf16 v[20:23], a[166:167], v[38:39], v[20:23]\n"
      "v_add_f32 v32, v32, v33\n"
      "v_exp_f32 v34, v32\n"
      "v_max_f32 v35, v35, v32\n"
      "v_add_f32 v7, v7, v34\n"
      "v_cvt_pk_bf16_f32 v34, v34, v34\n"
      "v_and_b32 v36, v4, v34\n"
      "v_and_b32 v37, v5, v34\n"
      "v_mfma_f32_16x16x32_bf16 v[24:27], a[184:187], v[12:15], 0\n"
      "v_sub_f32_dpp v32, v28, v6 quad_perm:[0,0,0,0] row_mask:0xf bank_mask:0xf\n"
      "v_mov_b32_dpp v33, v30 quad_perm:[2,2,2,2] row_mask:0xf bank_mask:0xf\n"
      "v_mfma_f32_16x16x16_bf16 v[16:19], a[172:173], v[36:37], v[16:19]\n"
      "v_add_f32_dpp v32, v29, v32 quad_perm:[1,1,1,1] row_mask:0xf bank_mask:0xf\n"
      "v_add_f32_dpp v33, v31, v33 quad_perm:[3,3,3,3] row_mask:0xf bank_mask:0xf\n"
      "v_mfma_f32_16x16x16_bf16 v[20:23], a[174:175], v[36:37], v[20:23]\n"
      "v_add_f32 v32, v32, v33\n"
      "v_exp_f32 v34, v32\n"
      "v_max_f32 v35, v35, v32\n"
      "v_add_f32 v7, v7, v34\n"
      "v_cvt_pk_bf16_f32 v34, v34, v34\n"
      "v_and_b32 v38, v4, v34\n"
      "v_and_b32 v39, v5, v34\n"
      "v_mfma_f32_16x16x32_bf16 v[28:31], a[192:195], v[12:15], 0\n"
      "v_sub_f32_dpp v32, v24, v6 quad_perm:[0,0,0,0] row_mask:0xf bank_mask:0xf\n"
      "v_mov_b32_dpp v33, v26 quad_perm:[2,2,2,2] row_mask:0xf bank_mask:0xf\n"
      "v_mfma_f32_16x16x16_bf16 v[16:19], a[180:181], v[38:39], v[16:19]\n"
      "v_add_f32_dpp v32, v25, v32 quad_perm:[1,1,1,1] row_mask:0xf bank_mask:0xf\n"
      "v_add_f32_dpp v33, v27, v33 quad_perm:[3,3,3,3] row_mask:0xf bank_mask:0xf\n"
      "v_mfma_f32_16x16x16_bf16 v[20:23], a[182:183], v[38:39], v[20:23]\n"
      "v_add_f32 v32, v32, v33\n"
      "v_exp_f32 v34, v32\n"
      "v_max_f32 v35, v35, v32\n"
      "v_add_f32 v7, v7, v34\n"
      "v_cvt_pk_bf16_f32 v34, v34, v34\n"
      "v_and_b32 v36, v4, v34\n"
      "v_and_b32 v37, v5, v34\n"
      "v_mfma_f32_16x16x32_bf16 v[24:27], a[200:203], v[12:15], 0\n"
      "v_sub_f32_dpp v32, v28, v6 quad_perm:[0,0,0,0] row_mask:0xf bank_mask:0xf\n"
      "v_mov_b32_dpp v33, v30 quad_perm:[2,2,2,2] row_mask:0xf bank_mask:0xf\n"
      "v_mfma_f32_16x16x16_bf16 v[16:19], a[188:189], v[36:37], v[16:19]\n"
      "v_add_f32_dpp v32, v29, v32 quad_perm:[1,1,1,1] row_mask:0xf bank_mask:0xf\n"
      "v_add_f32_dpp v33, v31, v33 quad_perm:[3,3,3,3] row_mask:0xf bank_mask:0xf\n"
      "v_mfma_f32_16x16x16_bf16 v[20:23], a[190:191], v[36:37], v[20:23]\n"
      "v_add_f32 v32, v32, v33\n"
      "v_exp_f32 v34, v32\n"
      "v_max_f32 v35, v35, v32\n"
      "v_add_f32 v7, v7, v34\n"
      "v_cvt_pk_bf16_f32 v34, v34, v34\n"
      "v_and_b32 v38, v4, v34\n"
      "v_and_b32 v39, v5, v34\n"
      "v_mfma_f32_16x16x32_bf16 v[28:31], a[208:211], v[12:15], 0\n"
      "v_sub_f32_dpp v32, v24, v6 quad_perm:[0,0,0,0] row_mask:0xf bank_mask:0xf\n"
      "v_mov_b32_dpp v33, v26 quad_perm:[2,2,2,2] row_mask:0xf bank_mask:0xf\n"
      "v_mfma_f32_16x16x16_bf16 v[16:19], a[196:197], v[38:39], v[16:19]\n"
      "v_add_f32_dpp v32, v25, v32 quad_perm:[1,1,1,1] row_mask:0xf bank_mask:0xf\n"
      "v_add_f32_dpp v33, v27, v33 quad_perm:[3,3,3,3] row_mask:0xf bank_mask:0xf\n"
      "v_mfma_f32_16x16x16_bf16 v[20:23], a[198:199], v[38:39], v[20:23]\n"
      "v_add_f32 v32, v32, v33\n"
      "v_exp_f32 v34, v32\n"
      "v_max_f32 v35, v35, v32\n"
      "v_add_f32 v7, v7, v34\n"
      "v_cvt_pk_bf16_f32 v34, v34, v34\n"
      "v_and_b32 v36, v4, v34\n"
      "v_and_b32 v37, v5, v34\n"
      "v_mfma_f32_16x16x32_bf16 v[24:27], a[216:219], v[12:15], 0\n"
      "v_sub_f32_dpp v32, v28, v6 quad_perm:[0,0,0,0] row_mask:0xf bank_mask:0xf\n"
      "v_mov_b32_dpp v33, v30 quad_perm:[2,2,2,2] row_mask:0xf bank_mask:0xf\n"
      "v_mfma_f32_16x16x16_bf16 v[16:19], a[204:205], v[36:37], v[16:19]\n"
      "v_add_f32_dpp v32, v29, v32 quad_perm:[1,1,1,1] row_mask:0xf bank_mask:0xf\n"
      "v_add_f32_dpp v33, v31, v33 quad_perm:[3,3,3,3] row_mask:0xf bank_mask:0xf\n"
      "v_mfma_f32_16x16x16_bf16 v[20:23], a[206:207], v[36:37], v[20:23]\n"
      "v_add_f32 v32, v32, v33\n"
      "v_exp_f32 v34, v32\n"
      "v_max_f32 v35, v35, v32\n"
      "v_add_f32 v7, v7, v34\n"
      "v_cvt_pk_bf16_f32 v34, v34, v34\n"
      "v_and_b32 v38, v4, v34\n"
      "v_and_b32 v39, v5, v34\n"
      "v_mfma_f32_16x16x32_bf16 v[28:31], a[224:227], v[12:15], 0\n"
      "v_sub_f32_dpp v32, v24, v6 quad_perm:[0,0,0,0] row_mask:0xf bank_mask:0xf\n"
      "v_mov_b32_dpp v33, v26 quad_perm:[2,2,2,2] row_mask:0xf bank_mask:0xf\n"
      "v_mfma_f32_16x16x16_bf16 v[16:19], a[212:213], v[38:39], v[16:19]\n"
      "v_add_f32_dpp v32, v25, v32 quad_perm:[1,1,1,1] row_mask:0xf bank_mask:0xf\n"
      "v_add_f32_dpp v33, v27, v33 quad_perm:[3,3,3,3] row_mask:0xf bank_mask:0xf\n"
      "v_mfma_f32_16x16x16_bf16 v[20:23], a[214:215], v[38:39], v[20:23]\n"
      "v_add_f32 v32, v32, v33\n"
      "v_exp_f32 v34, v32\n"
      "v_max_f32 v35, v35, v32\n"
      "v_add_f32 v7, v7, v34\n"
      "v_cvt_pk_bf16_f32 v34, v34, v34\n"
      "v_and_b32 v36, v4, v34\n"
      "v_and_b32 v37, v5, v34\n"
      "v_mfma_f32_16x16x32_bf16 v[24:27], a[232:235], v[12:15], 0\n"
      "v_sub_f32_dpp v32, v28, v6 quad_perm:[0,0,0,0] row_mask:0xf bank_mask:0xf\n"
      "v_mov_b32_dpp v33, v30 quad_perm:[2,2,2,2] row_mask:0xf bank_mask:0xf\n"
      "v_mfma_f32_16x16x16_bf16 v[16:19], a[220:221], v[36:37], v[16:19]\n"
      "v_add_f32_dpp v32, v29, v32 quad_perm:[1,1,1,1] row_mask:0xf bank_mask:0xf\n"
      "v_add_f32_dpp v33, v31, v33 quad_perm:[3,3,3,3] row_mask:0xf bank_mask:0xf\n"
      "v_mfma_f32_16x16x16_bf16 v[20:23], a[222:223], v[36:37], v[20:23]\n"
      "v_add_f32 v32, v32, v33\n"
      "v_exp_f32 v34, v32\n"
      "v_max_f32 v35, v35, v32\n"
      "v_add_f32 v7, v7, v34\n"
      "v_cvt_pk_bf16_f32 v34, v34, v34\n"
      "v_and_b32 v38, v4, v34\n"
      "v_and_b32 v39, v5, v34\n"
      "v_mfma_f32_16x16x32_bf16 v[28:31], a[240:243], v[12:15], 0\n"
      "v_sub_f32_dpp v32, v24, v6 quad_perm:[0,0,0,0] row_mask:0xf bank_mask:0xf\n"
      "v_mov_b32_dpp v33, v26 quad_perm:[2,2,2,2] row_mask:0xf bank_mask:0xf\n"
      "v_mfma_f32_16x16x16_bf16 v[16:19], a[228:229], v[38:39], v[16:19]\n"
      "v_add_f32_dpp v32, v25, v32 quad_perm:[1,1,1,1] row_mask:0xf bank_mask:0xf\n"
      "v_add_f32_dpp v33, v27, v33 quad_perm:[3,3,3,3] row_mask:0xf bank_mask:0xf\n"
      "v_mfma_f32_16x16x16_bf16 v[20:23], a[230:231], v[38:39], v[20:23]\n"
      "v_add_f32 v32, v32, v33\n"
      "v_exp_f32 v34, v32\n"
      "v_max_f32 v35, v35, v32\n"
      "v_add_f32 v7, v7, v34\n"
      "v_cvt_pk_bf16_f32 v34, v34, v34\n"
      "v_and_b32 v36, v4, v34\n"
      "v_and_b32 v37, v5, v34\n"
      "v_mfma_f32_16x16x32_bf16 v[24:27], a[248:251], v[12:15], 0\n"
      "v_sub_f32_dpp v32, v28, v6 quad_perm:[0,0,0,0] row_mask:0xf bank_mask:0xf\n"
      "v_mov_b32_dpp v33, v30 quad_perm:[2,2,2,2] row_mask:0xf bank_mask:0xf\n"
      "v_mfma_f32_16x16x16_bf16 v[16:19], a[236:237], v[36:37], v[16:19]\n"
      "v_add_f32_dpp v32, v29, v32 quad_perm:[1,1,1,1] row_mask:0xf bank_mask:0xf\n"
      "v_add_f32_dpp v33, v31, v33 quad_perm:[3,3,3,3] row_mask:0xf bank_mask:0xf\n"
      "v_mfma_f32_16x16x16_bf16 v[20:23], a[238:239], v[36:37], v[20:23]\n"
      "v_add_f32 v32, v32, v33\n"
      "v_exp_f32 v34, v32\n"
      "v_max_f32 v35, v35, v32\n"
      "v_add_f32 v7, v7, v34\n"
      "v_cvt_pk_bf16_f32 v34, v34, v34\n"
      "v_and_b32 v38, v4, v34\n"
      "v_and_b32 v39, v5, v34\n"
      "v_mfma_f32_16x16x32_bf16 v[28:31], v[56:59], v[12:15], 0\n"
      "v_sub_f32_dpp v32, v24, v6 quad_perm:[0,0,0,0] row_mask:0xf bank_mask:0xf\n"
      "v_mov_b32_dpp v33, v26 quad_perm:[2,2,2,2] row_mask:0xf bank_mask:0xf\n"
      "v_mfma_f32_16x16x16_bf16 v[16:19], a[244:245], v[38:39], v[16:19]\n"
      "v_add_f32_dpp v32, v25, v32 quad_perm:[1,1,1,1] row_mask:0xf bank_mask:0xf\n"
      "v_add_f32_dpp v33, v27, v33 quad_perm:[3,3,3,3] row_mask:0xf bank_mask:0xf\n"
      "v_mfma_f32_16x16x16_bf16 v[20:23], a[246:247], v[38:39], v[20:23]\n"
      "v_add_f32 v32, v32, v33\n"
      "v_exp_f32 v34, v32\n"
      "v_max_f32 v35, v35, v32\n"
      "v_add_f32 v7, v7, v34\n"
      "v_cvt_pk_bf16_f32 v34, v34, v34\n"
      "v_and_b32 v36, v4, v34\n"
      "v_and_b32 v37, v5, v34\n"
      "v_mfma_f32_16x16x32_bf16 v[24:27], v[64:67], v[12:15], 0\n"
      "v_sub_f32_dpp v32, v28, v6 quad_perm:[0,0,0,0] row_mask:0xf bank_mask:0xf\n"
      "v_mov_b32_dpp v33, v30 quad_perm:[2,2,2,2] row_mask:0xf bank_mask:0xf\n"
      "v_mfma_f32_16x16x16_bf16 v[16:19], a[252:253], v[36:37], v[16:19]\n"
      "v_add_f32_dpp v32, v29, v32 quad_perm:[1,1,1,1] row_mask:0xf bank_mask:0xf\n"
      "v_add_f32_dpp v33, v31, v33 quad_perm:[3,3,3,3] row_mask:0xf bank_mask:0xf\n"
      "v_mfma_f32_16x16x16_bf16 v[20:23], a[254:255], v[36:37], v[20:23]\n"
      "v_add_f32 v32, v32, v33\n"
      "v_exp_f32 v34, v32\n"
      "v_max_f32 v35, v35, v32\n"
      "v_add_f32 v7, v7, v34\n"
      "v_cvt_pk_bf16_f32 v34, v34, v34\n"
      "v_and_b32 v38, v4, v34\n"
      "v_and_b32 v39, v5, v34\n"
      "v_mfma_f32_16x16x32_bf16 v[28:31], v[72:75], v[12:15], 0\n"
      "v_sub_f32_dpp v32, v24, v6 quad_perm:[0,0,0,0] row_mask:0xf bank_mask:0xf\n"
      "v_mov_b32_dpp v33, v26 quad_perm:[2,2,2,2] row_mask:0xf bank_mask:0xf\n"
      "v_mfma_f32_16x16x16_bf16 v[16:19], v[60:61], v[38:39], v[16:19]\n"
      "v_add_f32_dpp v32, v25, v32 quad_perm:[1,1,1,1] row_mask:0xf bank_mask:0xf\n"
      "v_add_f32_dpp v33, v27, v33 quad_perm:[3,3,3,3] row_mask:0xf bank_mask:0xf\n"
      "v_mfma_f32_16x16x16_bf16 v[20:23], v[62:63], v[38:39], v[20:23]\n"
      "v_add_f32 v32, v32, v33\n"
      "v_exp_f32 v34, v32\n"
      "v_max_f32 v35, v35, v32\n"
      "v_add_f32 v7, v7, v34\n"
      "v_cvt_pk_bf16_f32 v34, v34, v34\n"
      "v_and_b32 v36, v4, v34\n"
      "v_and_b32 v37, v5, v34\n"
      "v_mfma_f32_16x16x32_bf16 v[24:27], v[80:83], v[12:15], 0\n"
      "v_sub_f32_dpp v32, v28, v6 quad_perm:[0,0,0,0] row_mask:0xf bank_mask:0xf\n"
      "v_mov_b32_dpp v33, v30 quad_perm:[2,2,2,2] row_mask:0xf bank_mask:0xf\n"
      "v_mfma_f32_16x16x16_bf16 v[16:19], v[68:69], v[36:37], v[16:19]\n"
      "v_add_f32_dpp v32, v29, v32 quad_perm:[1,1,1,1] row_mask:0xf bank_mask:0xf\n"
      "v_add_f32_dpp v33, v31, v33 quad_perm:[3,3,3,3] row_mask:0xf bank_mask:0xf\n"
      "v_mfma_f32_16x16x16_bf16 v[20:23], v[70:71], v[36:37], v[20:23]\n"
      "v_add_f32 v32, v32, v33\n"
      "v_exp_f32 v34, v32\n"
      "v_max_f32 v35, v35, v32\n"
      "v_add_f32 v7, v7, v34\n"
      "v_cvt_pk_bf16_f32 v34, v34, v34\n"
      "v_and_b32 v38, v4, v34\n"
      "v_and_b32 v39, v5, v34\n"
      "v_mfma_f32_16x16x32_bf16 v[28:31], v[88:91], v[12:15], 0\n"
      "v_sub_f32_dpp v32, v24, v6 quad_perm:[0,0,0,0] row_mask:0xf bank_mask:0xf\n"
      "v_mov_b32_dpp v33, v26 quad_perm:[2,2,2,2] row_mask:0xf bank_mask:0xf\n"
      "v_mfma_f32_16x16x16_bf16 v[16:19], v[76:77], v[38:39], v[16:19]\n"
      "v_add_f32_dpp v32, v25, v32 quad_perm:[1,1,1,1] row_mask:0xf bank_mask:0xf\n"
      "v_add_f32_dpp v33, v27, v33 quad_perm:[3,3,3,3] row_mask:0xf bank_mask:0xf\n"
      "v_mfma_f32_16x16x16_bf16 v[20:23], v[78:79], v[38:39], v[20:23]\n"
      "v_add_f32 v32, v32, v33\n"
      "v_exp_f32 v34, v32\n"
      "v_max_f32 v35, v35, v32\n"
      "v_add_f32 v7, v7, v34\n"
      "v_cvt_pk_bf16_f32 v34, v34, v34\n"
      "v_and_b32 v36, v4, v34\n"
      "v_and_b32 v37, v5, v34\n"
      "v_mfma_f32_16x16x32_bf16 v[24:27], v[96:99], v[12:15], 0\n"
      "v_sub_f32_dpp v32, v28, v6 quad_perm:[0,0,0,0] row_mask:0xf bank_mask:0xf\n"
      "v_mov_b32_dpp v33, v30 quad_perm:[2,2,2,2] row_mask:0xf bank_mask:0xf\n"
      "v_mfma_f32_16x16x16_bf16 v[16:19], v[84:85], v[36:37], v[16:19]\n"
      "v_add_f32_dpp v32, v29, v32 quad_perm:[1,1,1,1] row_mask:0xf bank_mask:0xf\n"
      "v_add_f32_dpp v33, v31, v33 quad_perm:[3,3,3,3] row_mask:0xf bank_mask:0xf\n"
      "v_mfma_f32_16x16x16_bf16 v[20:23], v[86:87], v[36:37], v[20:23]\n"
      "v_add_f32 v32, v32, v33\n"
      "v_exp_f32 v34, v32\n"
      "v_max_f32 v35, v35, v32\n"
      "v_add_f32 v7, v7, v34\n"
      "v_cvt_pk_bf16_f32 v34, v34, v34\n"
      "v_and_b32 v38, v4, v34\n"
      "v_and_b32 v39, v5, v34\n"
      "v_sub_f32_dpp v32, v24, v6 quad_perm:[0,0,0,0] row_mask:0xf bank_mask:0xf\n"
      "v_mov_b32_dpp v33, v26 quad_perm:[2,2,2,2] row_mask:0xf bank_mask:0xf\n"
      "v_mfma_f32_16x16x16_bf16 v[16:19], v[92:93], v[38:39], v[16:19]\n"
      "v_add_f32_dpp v32, v25, v32 quad_perm:[1,1,1,1] row_mask:0xf bank_mask:0xf\n"
      "v_add_f32_dpp v33, v27, v33 quad_perm:[3,3,3,3] row_mask:0xf bank_mask:0xf\n"
      "v_mfma_f32_16x16x16_bf16 v[20:23], v[94:95], v[38:39], v[20:23]\n"
      "v_add_f32 v32, v32, v33\n"
      "v_add_f32 v32, s69, v32\n"
      "v_exp_f32 v34, v32\n"
      "v_max_f32 v35, v35, v32\n"
      "v_add_f32 v7, v7, v34\n"
      "v_cvt_pk_bf16_f32 v34, v34, v34\n"
      "v_and_b32 v36, v4, v34\n"
      "v_and_b32 v37, v5, v34\n"
      "s_nop 1\n"
      "v_mfma_f32_16x16x16_bf16 v[16:19], v[100:101], v[36:37], v[16:19]\n"
      "v_mfma_f32_16x16x16_bf16 v[20:23], v[102:103], v[36:37], v[20:23]\n"
      "v_cmp_lt_f32 vcc, 0x42800000, v35\n"
      "s_nop 0\n"
      "s_cbranch_vccnz L_fb14_%=\n"
      "L_ret14_%=:\n"
      "v_mov_b32 v49, v7\n"
      "v_lshlrev_b32 v41, 2, v0\n"
      "v_and_b32 v41, 0x300, v41\n"
      "v_permlane32_swap_b32 v49, v7\n"
      "v_lshrrev_b32 v42, 4, v0\n"
      "v_add_f32 v7, v49, v7\n"
      "v_and_b32 v42, 0x30, v42\n"
      "v_mov_b32 v49, v7\n"
      "v_add_u32 v41, v41, v42\n"
      "v_lshrrev_b32 v40, 2, v0\n"
      "v_permlane16_swap_b32 v49, v7\n"
      "v_and_b32 v42, 12, v40\n"
      "v_add_f32 v7, v49, v7\n"
      "v_add_u32 v41, v41, v42\n"
      "s_lshl_b32 s62, s60, 10\n"
      "s_add_u32 s62, s62, 155648\n"
      "v_add_u32 v41, s62, v41\n"
      "v_lshrrev_b32 v43, 3, v0\n"
      "v_and_b32 v43, 24, v43\n"
      "s_lshl_b32 s62, s60, 5\n"
      "s_add_u32 s62, s62, 159744\n"
      "v_add_u32 v43, s62, v43\n"
      "v_cvt_pk_bf16_f32 v36, v16, v17\n"
      "v_cvt_pk_bf16_f32 v37, v18, v19\n"
      "v_cvt_pk_bf16_f32 v38, v20, v21\n"
      "v_cvt_pk_bf16_f32 v39, v22, v23\n"
      "s_mov_b64 exec, 0x1111\n"
      "ds_write_b64 v43, v[6:7]\n"
      "s_mov_b64 exec, -1\n"
      "ds_write_b32 v41, v36 offset:0\n"
      "ds_write_b32 v41, v37 offset:64\n"
      "ds_write_b32 v41, v38 offset:128\n"
      "ds_write_b32 v41, v39 offset:192\n"
      "s_lshl_b32 s62, s60, 8\n"
      "s_add_u32 s62, s62, 155648\n"
      "v_add_u32 v42, s62, v40\n"
      "s_lshl_b32 s62, s60, 3\n"
      "s_add_u32 s62, s62, 159744\n"
      "v_mov_b32 v50, s62\n"
      "s_waitcnt lgkmcnt(0)\n"
      "s_barrier\n"
      "ds_read_b64 v[28:29], v50 offset:0\n"
      "ds_read_b64 v[30:31], v50 offset:32\n"
      "ds_read_b64 v[32:33], v50 offset:64\n"
      "ds_read_b64 v[34:35], v50 offset:96\n"
      "ds_read_b32 v24, v42 offset:0\n"
      "ds_read_b32 v25, v42 offset:1024\n"
      "ds_read_b32 v26, v42 offset:2048\n"
      "ds_read_b32 v27, v42 offset:3072\n"
      "s_add_u32 s56, s44, 4644864\n"
      "s_addc_u32 s57, s45, 0\n"
      "s_waitcnt lgkmcnt(4)\n"
      "v_max3_f32 v43, v28, v30, v32\n"
      "v_max_f32 v43, v43, v34\n"
      "v_sub_f32 v28, v28, v43\n"
      "v_sub_f32 v30, v30, v43\n"
      "v_sub_f32 v32, v32, v43\n"
      "v_sub_f32 v34, v34, v43\n"
      "v_exp_f32 v28, v28\n"
      "v_exp_f32 v30, v30\n"
      "v_exp_f32 v32, v32\n"
      "v_exp_f32 v34, v34\n"
      "s_waitcnt lgkmcnt(0)\n"
      "v_lshlrev_b32 v44, 16, v24\n"
      "v_and_b32 v45, 0xffff0000, v24\n"
      "v_lshlrev_b32 v46, 16, v25\n"
      "v_and_b32 v47, 0xffff0000, v25\n"
      "v_lshlrev_b32 v48, 16, v26\n"
      "v_and_b32 v49, 0xffff0000, v26\n"
      "v_lshlrev_b32 v50, 16, v27\n"
      "v_and_b32 v51, 0xffff0000, v27\n"
      "v_mul_f32 v38, v28, v29\n"
      "v_pk_mul_f32 v[36:37], v[44:45], v[28:29] op_sel_hi:[1,0]\n"
      "v_fmac_f32 v38, v30, v31\n"
      "v_pk_fma_f32 v[36:37], v[46:47], v[30:31], v[36:37] op_sel_hi:[1,0,1]\n"
      "v_fmac_f32 v38, v32, v33\n"
      "v_pk_fma_f32 v[36:37], v[48:49], v[32:33], v[36:37] op_sel_hi:[1,0,1]\n"
      "v_fmac_f32 v38, v34, v35\n"
      "v_pk_fma_f32 v[36:37], v[50:51], v[34:35], v[36:37] op_sel_hi:[1,0,1]\n"
      "v_rcp_f32 v39, v38\n"
      "v_log_f32 v38, v38\n"
      "v_mul_f32 v36, v39, v36\n"
      "v_mul_f32 v37, v39, v37\n"
      "v_add_f32 v38, v38, v43\n"
      "v_cvt_pk_bf16_f32 v36, v36, v37\n"
      "v_min_u32 v38, -2, v38\n"
      "v_min_u32 v36, -2, v36\n"
      "global_store_dword v40, v36, s[56:57] sc1\n"
      "s_mov_b32 exec_hi, 0\n"
      "global_store_dword v40, v38, s[56:57] offset:256 sc1\n"
      "s_mov_b64 exec, -1\n"
      "s_barrier\n"
      "s_add_u32 s56, s48, 122880\n"
      "s_addc_u32 s57, s49, 0\n"
      "s_mov_b64 exec, 0xffff\n"
      "global_load_dwordx4 v[12:15], v0, s[56:57] sc1\n"
      "s_mov_b64 exec, -1\n"
      "s_waitcnt vmcnt(0)\n"
      "s_mov_b64 exec, 0xffff\n"
      "s_mov_b32 s53, 0\n"
      "L_qc33_%=:\n"
      "v_max3_u32 v49, v12, v13, v14\n"
      "v_max_u32 v49, v49, v15\n"
      "v_cmp_eq_u32 vcc, -1, v49\n"
      "s_nop 0\n"
      "s_cbranch_vccz L_qo34_%=\n"
      "s_cmp_lg_u32 s54, 0\n"
      "s_cbranch_scc1 L_qo34_%=\n"
      "s_add_u32 s56, s48, 122880\n"
      "s_addc_u32 s57, s49, 0\n"
      "global_load_dwordx4 v[12:15], v0, s[56:57] sc1\n"
      "s_add_u32 s53, s53, 1\n"
      "s_waitcnt vmcnt(0)\n"
      "s_cmp_lt_u32 s53, 30000\n"
      "s_cbranch_scc1 L_qc33_%=\n"
      "s_mov_b32 s54, 207\n"
      "v_mov_b32 v49, 207\n"
      "v_mov_b32 v50, 256\n"
      "global_store_dword v50, v49, s[58:59] sc1\n"
      "L_qo34_%=:\n"
      "s_lshl_b32 s62, s60, 8\n"
      "s_add_u32 s62, s62, 160896\n"
      "v_add_u32 v49, s62, v0\n"
      "ds_write_b128 v49, v[12:15]\n"
      "s_mov_b64 exec, -1\n"
      "s_waitcnt lgkmcnt(0)\n"
      "s_barrier\n"
      "ds_read_b128 v[12:15], v2 offset:1024\n"
      "s_waitcnt lgkmcnt(0)\n"
      "v_mfma_f32_16x16x32_bf16 v[24:27], v[104:107], v[12:15], 0\n"
      "v_mfma_f32_16x16x32_bf16 v[28:31], v[112:115], v[12:15], 0\n"
      "s_nop 6\n"
      "v_mov_b32_dpp v34, v24 quad_perm:[0,0,0,0] row_mask:0xf bank_mask:0xf\n"
      "v_mov_b32_dpp v33, v26 quad_perm:[2,2,2,2] row_mask:0xf bank_mask:0xf\n"
      "s_nop 0\n"
      "v_add_f32_dpp v34, v25, v34 quad_perm:[1,1,1,1] row_mask:0xf bank_mask:0xf\n"
      "v_add_f32_dpp v33, v27, v33 quad_perm:[3,3,3,3] row_mask:0xf bank_mask:0xf\n"
      "v_add_f32 v34, v34, v33\n"
      "v_mov_b32_dpp v32, v28 quad_perm:[0,0,0,0] row_mask:0xf bank_mask:0xf\n"
      "v_mov_b32_dpp v33, v30 quad_perm:[2,2,2,2] row_mask:0xf bank_mask:0xf\n"
      "s_nop 0\n"
      "v_add_f32_dpp v32, v29, v32 quad_perm:[1,1,1,1] row_mask:0xf bank_mask:0xf\n"
      "v_add_f32_dpp v33, v31, v33 quad_perm:[3,3,3,3] row_mask:0xf bank_mask:0xf\n"
      "v_add_f32 v32, v32, v33\n"
      "v_max_f32 v35, v34, v32\n"
      "v_mov_b32 v33, v35\n"
      "s_nop 1\n"
      "v_permlane32_swap_b32 v33, v35\n"
      "v_max_f32 v35, v33, v35\n"
      "v_mov_b32 v33, v35\n"
      "s_nop 1\n"
      "v_permlane16_swap_b32 v33, v35\n"
      "v_max_f32 v6, v33, v35\n"
      "v_sub_f32 v34, v34, v6\n"
      "v_sub_f32 v32, v32, v6\n"
      "v_mov_b32 v35, 0\n"
      "v_exp_f32 v34, v34\n"
      "s_nop 0\n"
      "v_mov_b32 v7, v34\n"
      "v_cvt_pk_bf16_f32 v34, v34, v34\n"
      "v_and_b32 v36, v4, v34\n"
      "v_and_b32 v37, v5, v34\n"
      "v_mfma_f32_16x16x32_bf16 v[24:27], v[120:123], v[12:15], 0\n"
      "s_nop 0\n"
      "v_mfma_f32_16x16x16_bf16 v[16:19], v[108:109], v[36:37], 0\n"
      "v_mfma_f32_16x16x16_bf16 v[20:23], v[110:111], v[36:37], 0\n"
      "v_exp_f32 v34, v32\n"
      "v_max_f32 v35, v35, v32\n"
      "v_add_f32 v7, v7, v34\n"
      "v_cvt_pk_bf16_f32 v34, v34, v34\n"
      "v_and_b32 v38, v4, v34\n"
      "v_and_b32 v39, v5, v34\n"
      "v_mfma_f32_16x16x32_bf16 v[28:31], v[128:131], v[12:15], 0\n"
      "v_sub_f32_dpp v32, v24, v6 quad_perm:[0,0,0,0] row_mask:0xf bank_mask:0xf\n"
      "v_mov_b32_dpp v33, v26 quad_perm:[2,2,2,2] row_mask:0xf bank_mask:0xf\n"
      "v_mfma_f32_16x16x16_bf16 v[16:19], v[116:117], v[38:39], v[16:19]\n"
      "v_add_f32_dpp v32, v25, v32 quad_perm:[1,1,1,1] row_mask:0xf bank_mask:0xf\n"
      "v_add_f32_dpp v33, v27, v33 quad_perm:[3,3,3,3] row_mask:0xf bank_mask:0xf\n"
      "v_mfma_f32_16x16x16_bf16 v[20:23], v[118:119], v[38:39], v[20:23]\n"
      "v_add_f32 v32, v32, v33\n"
      "v_exp_f32 v34, v32\n"
      "v_max_f32 v35, v35, v32\n"
      "v_add_f32 v7, v7, v34\n"
      "v_cvt_pk_bf16_f32 v34, v34, v34\n"
      "v_and_b32 v36, v4, v34\n"
      "v_and_b32 v37, v5, v34\n"
      "v_mfma_f32_16x16x32_bf16 v[24:27], v[136:139], v[12:15], 0\n"
      "v_sub_f32_dpp v32, v28, v6 quad_perm:[0,0,0,0] row_mask:0xf bank_mask:0xf\n"
      "v_mov_b32_dpp v33, v30 quad_perm:[2,2,2,2] row_mask:0xf bank_mask:0xf\n"
      "v_mfma_f32_16x16x16_bf16 v[16:19], v[124:125], v[36:37], v[16:19]\n"
      "v_add_f32_dpp v32, v29, v32 quad_perm:[1,1,1,1] row_mask:0xf bank_mask:0xf\n"
      "v_add_f32_dpp v33, v31, v33 quad_perm:[3,3,3,3] row_mask:0xf bank_mask:0xf\n"
      "v_mfma_f32_16x16x16_bf16 v[20:23], v[126:127], v[36:37], v[20:23]\n"
      "v_add_f32 v32, v32, v33\n"
      "v_exp_f32 v34, v32\n"
      "v_max_f32 v35, v35, v32\n"
      "v_add_f32 v7, v7, v34\n"
      "v_cvt_pk_bf16_f32 v34, v34, v34\n"
      "v_and_b32 v38, v4, v34\n"
      "v_and_b32 v39, v5, v34\n"
      "v_mfma_f32_16x16x32_bf16 v[28:31], v[144:147], v[12:15], 0\n"
      "v_sub_f32_dpp v32, v24, v6 quad_perm:[0,0,0,0] row_mask:0xf bank_mask:0xf\n"
      "v_mov_b32_dpp v33, v26 quad_perm:[2,2,2,2] row_mask:0xf bank_mask:0xf\n"
      "v_mfma_f32_16x16x16_bf16 v[16:19], v[132:133], v[38:39], v[16:19]\n"
      "v_add_f32_dpp v32, v25, v32 quad_perm:[1,1,1,1] row_mask:0xf bank_mask:0xf\n"
      "v_add_f32_dpp v33, v27, v33 quad_perm:[3,3,3,3] row_mask:0xf bank_mask:0xf\n"
      "v_mfma_f32_16x16x16_bf16 v[20:23], v[134:135], v[38:39], v[20:23]\n"
      "v_add_f32 v32, v32, v33\n"
      "v_exp_f32 v34, v32\n"
      "v_max_f32 v35, v35, v32\n"
      "v_add_f32 v7, v7, v34\n"
      "v_cvt_pk_bf16_f32 v34, v34, v34\n"
      "v_and_b32 v36, v4, v34\n"
      "v_and_b32 v37, v5, v34\n"
      "v_mfma_f32_16x16x32_bf16 v[24:27], v[152:155], v[12:15], 0\n"
      "v_sub_f32_dpp v32, v28, v6 quad_perm:[0,0,0,0] row_mask:0xf bank_mask:0xf\n"
      "v_mov_b32_dpp v33, v30 quad_perm:[2,2,2,2] row_mask:0xf bank_mask:0xf\n"
      "v_mfma_f32_16x16x16_bf16 v[16:19], v[140:141], v[36:37], v[16:19]\n"
      "v_add_f32_dpp v32, v29, v32 quad_perm:[1,1,1,1] row_mask:0xf bank_mask:0xf\n"
      "v_add_f32_dpp v33, v31, v33 quad_perm:[3,3,3,3] row_mask:0xf bank_mask:0xf\n"
      "v_mfma_f32_16x16x16_bf16 v[20:23], v[142:143], v[36:37], v[20:23]\n"
      "v_add_f32 v32, v32, v33\n"
      "v_exp_f32 v34, v32\n"
      "v_max_f32 v35, v35, v32\n"
      "v_add_f32 v7, v7, v34\n"
      "v_cvt_pk_bf16_f32 v34, v34, v34\n"
      "v_and_b32 v38, v4, v34\n"
      "v_and_b32 v39, v5, v34\n"
      "v_mfma_f32_16x16x32_bf16 v[28:31], v[160:163], v[12:15], 0\n"
      "v_sub_f32_dpp v32, v24, v6 quad_perm:[0,0,0,0] row_mask:0xf bank_mask:0xf\n"
      "v_mov_b32_dpp v33, v26 quad_perm:[2,2,2,2] row_mask:0xf bank_mask:0xf\n"
      "v_mfma_f32_16x16x16_bf16 v[16:19], v[148:149], v[38:39], v[16:19]\n"
      "v_add_f32_dpp v32, v25, v32 quad_perm:[1,1,1,1] row_mask:0xf bank_mask:0xf\n"
      "v_add_f32_dpp v33, v27, v33 quad_perm:[3,3,3,3] row_mask:0xf bank_mask:0xf\n"
      "v_mfma_f32_16x16x16_bf16 v[20:23], v[150:151], v[38:39], v[20:23]\n"
      "v_add_f32 v32, v32, v33\n"
      "v_exp_f32 v34, v32\n"
      "v_max_f32 v35, v35, v32\n"
      "v_add_f32 v7, v7, v34\n"
      "v_cvt_pk_bf16_f32 v34, v34, v34\n"
      "v_and_b32 v36, v4, v34\n"
      "v_and_b32 v37, v5, v34\n"
      "v_mfma_f32_16x16x32_bf16 v[24:27], v[168:171], v[12:15], 0\n"
      "v_sub_f32_dpp v32, v28, v6 quad_perm:[0,0,0,0] row_mask:0xf bank_mask:0xf\n"
      "v_mov_b32_dpp v33, v30 quad_perm:[2,2,2,2] row_mask:0xf bank_mask:0xf\n"
      "v_mfma_f32_16x16x16_bf16 v[16:19], v[156:157], v[36:37], v[16:19]\n"
      "v_add_f32_dpp v32, v29, v32 quad_perm:[1,1,1,1] row_mask:0xf bank_mask:0xf\n"
      "v_add_f32_dpp v33, v31, v33 quad_perm:[3,3,3,3] row_mask:0xf bank_mask:0xf\n"
      "v_mfma_f32_16x16x16_bf16 v[20:23], v[158:159], v[36:37], v[20:23]\n"
      "v_add_f32 v32, v32, v33\n"
      "v_exp_f32 v34, v32\n"
      "v_max_f32 v35, v35, v32\n"
      "v_add_f32 v7, v7, v34\n"
      "v_cvt_pk_bf16_f32 v34, v34, v34\n"
      "v_and_b32 v38, v4, v34\n"
      "v_and_b32 v39, v5, v34\n"
      "v_mfma_f32_16x16x32_bf16 v[28:31], v[176:179], v[12:15], 0\n"
      "v_sub_f32_dpp v32, v24, v6 quad_perm:[0,0,0,0] row_mask:0xf bank_mask:0xf\n"
      "v_mov_b32_dpp v33, v26 quad_perm:[2,2,2,2] row_mask:0xf bank_mask:0xf\n"
      "v_mfma_f32_16x16x16_bf16 v[16:19], v[164:165], v[38:39], v[16:19]\n"
      "v_add_f32_dpp v32, v25, v32 quad_perm:[1,1,1,1] row_mask:0xf bank_mask:0xf\n"
      "v_add_f32_dpp v33, v27, v33 quad_perm:[3,3,3,3] row_mask:0xf bank_mask:0xf\n"
      "v_mfma_f32_16x16x16_bf16 v[20:23], v[166:167], v[38:39], v[20:23]\n"
      "v_add_f32 v32, v32, v33\n"
      "v_exp_f32 v34, v32\n"
      "v_max_f32 v35, v35, v32\n"
      "v_add_f32 v7, v7, v34\n"
      "v_cvt_pk_bf16_f32 v34, v34, v34\n"
      "v_and_b32 v36, v4, v34\n"
      "v_and_b32 v37, v5, v34\n"
      "v_mfma_f32_16x16x32_bf16 v[24:27], v[184:187], v[12:15], 0\n"
      "v_sub_f32_dpp v32, v28, v6 quad_perm:[0,0,0,0] row_mask:0xf bank_mask:0xf\n"
      "v_mov_b32_dpp v33, v30 quad_perm:[2,2,2,2] row_mask:0xf bank_mask:0xf\n"
      "v_mfma_f32_16x16x16_bf16 v[16:19], v[172:173], v[36:37], v[16:19]\n"
      "v_add_f32_dpp v32, v29, v32 quad_perm:[1,1,1,1] row_mask:0xf bank_mask:0xf\n"
      "v_add_f32_dpp v33, v31, v33 quad_perm:[3,3,3,3] row_mask:0xf bank_mask:0xf\n"
      "v_mfma_f32_16x16x16_bf16 v[20:23], v[174:175], v[36:37], v[20:23]\n"
      "v_add_f32 v32, v32, v33\n"
      "v_exp_f32 v34, v32\n"
      "v_max_f32 v35, v35, v32\n"
      "v_add_f32 v7, v7, v34\n"
      "v_cvt_pk_bf16_f32 v34, v34, v34\n"
      "v_and_b32 v38, v4, v34\n"
      "v_and_b32 v39, v5, v34\n"
      "v_mfma_f32_16x16x32_bf16 v[28:31], v[192:195], v[12:15], 0\n"
      "v_sub_f32_dpp v32, v24, v6 quad_perm:[0,0,0,0] row_mask:0xf bank_mask:0xf\n"
      "v_mov_b32_dpp v33, v26 quad_perm:[2,2,2,2] row_mask:0xf bank_mask:0xf\n"
      "v_mfma_f32_16x16x16_bf16 v[16:19], v[180:181], v[38:39], v[16:19]\n"
      "v_add_f32_dpp v32, v25, v32 quad_perm:[1,1,1,1] row_mask:0xf bank_mask:0xf\n"
      "v_add_f32_dpp v33, v27, v33 quad_perm:[3,3,3,3] row_mask:0xf bank_mask:0xf\n"
      "v_mfma_f32_16x16x16_bf16 v[20:23], v[182:183], v[38:39], v[20:23]\n"
      "v_add_f32 v32, v32, v33\n"
      "v_exp_f32 v34, v32\n"
      "v_max_f32 v35, v35, v32\n"
      "v_add_f32 v7, v7, v34\n"
      "v_cvt_pk_bf16_f32 v34, v34, v34\n"
      "v_and_b32 v36, v4, v34\n"
      "v_and_b32 v37, v5, v34\n"
      "v_mfma_f32_16x16x32_bf16 v[24:27], v[200:203], v[12:15], 0\n"
      "v_sub_f32_dpp v32, v28, v6 quad_perm:[0,0,0,0] row_mask:0xf bank_mask:0xf\n"
      "v_mov_b32_dpp v33, v30 quad_perm:[2,2,2,2] row_mask:0xf bank_mask:0xf\n"
      "v_mfma_f32_16x16x16_bf16 v[16:19], v[188:189], v[36:37], v[16:19]\n"
      "v_add_f32_dpp v32, v29, v32 quad_perm:[1,1,1,1] row_mask:0xf bank_mask:0xf\n"
      "v_add_f32_dpp v33, v31, v33 quad_perm:[3,3,3,3] row_mask:0xf bank_mask:0xf\n"
      "v_mfma_f32_16x16x16_bf16 v[20:23], v[190:191], v[36:37], v[20:23]\n"
      "v_add_f32 v32, v32, v33\n"
      "v_exp_f32 v34, v32\n"
      "v_max_f32 v35, v35, v32\n"
      "v_add_f32 v7, v7, v34\n"
      "v_cvt_pk_bf16_f32 v34, v34, v34\n"
      "v_and_b32 v38, v4, v34\n"
      "v_and_b32 v39, v5, v34\n"
      "v_mfma_f32_16x16x32_bf16 v[28:31], v[208:211], v[12:15], 0\n"
      "v_sub_f32_dpp v32, v24, v6 quad_perm:[0,0,0,0] row_mask:0xf bank_mask:0xf\n"
      "v_mov_b32_dpp v33, v26 quad_perm:[2,2,2,2] row_mask:0xf bank_mask:0xf\n"
      "v_mfma_f32_16x16x16_bf16 v[16:19], v[196:197], v[38:39], v[16:19]\n"
      "v_add_f32_dpp v32, v25, v32 quad_perm:[1,1,1,1] row_mask:0xf bank_mask:0xf\n"
      "v_add_f32_dpp v33, v27, v33 quad_perm:[3,3,3,3] row_mask:0xf bank_mask:0xf\n"
      "v_mfma_f32_16x16x16_bf16 v[20:23], v[198:199], v[38:39], v[20:23]\n"
      "v_add_f32 v32, v32, v33\n"
      "v_exp_f32 v34, v32\n"
      "v_max_f32 v35, v35, v32\n"
      "v_add_f32 v7, v7, v34\n"
      "v_cvt_pk_bf16_f32 v34, v34, v34\n"
      "v_and_b32 v36, v4, v34\n"
      "v_and_b32 v37, v5, v34\n"
      "v_mfma_f32_16x16x32_bf16 v[24:27], v[216:219], v[12:15], 0\n"
      "v_sub_f32_dpp v32, v28, v6 quad_perm:[0,0,0,0] row_mask:0xf bank_mask:0xf\n"
      "v_mov_b32_dpp v33, v30 quad_perm:[2,2,2,2] row_mask:0xf bank_mask:0xf\n"
      "v_mfma_f32_16x16x16_bf16 v[16:19], v[204:205], v[36:37], v[16:19]\n"
      "v_add_f32_dpp v32, v29, v32 quad_perm:[1,1,1,1] row_mask:0xf bank_mask:0xf\n"
      "v_add_f32_dpp v33, v31, v33 quad_perm:[3,3,3,3] row_mask:0xf bank_mask:0xf\n"
      "v_mfma_f32_16x16x16_bf16 v[20:23], v[206:207], v[36:37], v[20:23]\n"
      "v_add_f32 v32, v32, v33\n"
      "v_exp_f32 v34, v32\n"
      "v_max_f32 v35, v35, v32\n"
      "v_add_f32 v7, v7, v34\n"
      "v_cvt_pk_bf16_f32 v34, v34, v34\n"
      "v_and_b32 v38, v4, v34\n"
      "v_and_b32 v39, v5, v34\n"
      "v_mfma_f32_16x16x32_bf16 v[28:31], v[224:227], v[12:15], 0\n"
      "v_sub_f32_dpp v32, v24, v6 quad_perm:[0,0,0,0] row_mask:0xf bank_mask:0xf\n"
      "v_mov_b32_dpp v33, v26 quad_perm:[2,2,2,2] row_mask:0xf bank_mask:0xf\n"
      "v_mfma_f32_16x16x16_bf16 v[16:19], v[212:213], v[38:39], v[16:19]\n"
      "v_add_f32_dpp v32, v25, v32 quad_perm:[1,1,1,1] row_mask:0xf bank_mask:0xf\n"
      "v_add_f32_dpp v33, v27, v33 quad_perm:[3,3,3,3] row_mask:0xf bank_mask:0xf\n"
      "v_mfma_f32_16x16x16_bf16 v[20:23], v[214:215], v[38:39], v[20:23]\n"
      "v_add_f32 v32, v32, v33\n"
      "v_exp_f32 v34, v32\n"
      "v_max_f32 v35, v35, v32\n"
      "v_add_f32 v7, v7, v34\n"
      "v_cvt_pk_bf16_f32 v34, v34, v34\n"
      "v_and_b32 v36, v4, v34\n"
      "v_and_b32 v37, v5, v34\n"
      "v_mfma_f32_16x16x32_bf16 v[24:27], v[232:235], v[12:15], 0\n"
      "v_sub_f32_dpp v32, v28, v6 quad_perm:[0,0,0,0] row_mask:0xf bank_mask:0xf\n"
      "v_mov_b32_dpp v33, v30 quad_perm:[2,2,2,2] row_mask:0xf bank_mask:0xf\n"
      "v_mfma_f32_16x16x16_bf16 v[16:19], v[220:221], v[36:37], v[16:19]\n"
      "v_add_f32_dpp v32, v29, v32 quad_perm:[1,1,1,1] row_mask:0xf bank_mask:0xf\n"
      "v_add_f32_dpp v33, v31, v33 quad_perm:[3,3,3,3] row_mask:0xf bank_mask:0xf\n"
      "v_mfma_f32_16x16x16_bf16 v[20:23], v[222:223], v[36:37], v[20:23]\n"
      "v_add_f32 v32, v32, v33\n"
      "v_exp_f32 v34, v32\n"
      "v_max_f32 v35, v35, v32\n"
      "v_add_f32 v7, v7, v34\n"
      "v_cvt_pk_bf16_f32 v34, v34, v34\n"
      "v_and_b32 v38, v4, v34\n"
      "v_and_b32 v39, v5, v34\n"
      "v_mfma_f32_16x16x32_bf16 v[28:31], v[240:243], v[12:15], 0\n"
      "v_sub_f32_dpp v32, v24, v6 quad_perm:[0,0,0,0] row_mask:0xf bank_mask:0xf\n"
      "v_mov_b32_dpp v33, v26 quad_perm:[2,2,2,2] row_mask:0xf bank_mask:0xf\n"
      "v_mfma_f32_16x16x16_bf16 v[16:19], v[228:229], v[38:39], v[16:19]\n"
      "v_add_f32_dpp v32, v25, v32 quad_perm:[1,1,1,1] row_mask:0xf bank_mask:0xf\n"
      "v_add_f32_dpp v33, v27, v33 quad_perm:[3,3,3,3] row_mask:0xf bank_mask:0xf\n"
      "v_mfma_f32_16x16x16_bf16 v[20:23], v[230:231], v[38:39], v[20:23]\n"
      "v_add_f32 v32, v32, v33\n"
      "v_exp_f32 v34, v32\n"
      "v_max_f32 v35, v35, v32\n"
      "v_add_f32 v7, v7, v34\n"
      "v_cvt_pk_bf16_f32 v34, v34, v34\n"
      "v_and_b32 v36, v4, v34\n"
      "v_and_b32 v37, v5, v34\n"
      "v_mfma_f32_16x16x32_bf16 v[24:27], v[248:251], v[12:15], 0\n"
      "v_sub_f32_dpp v32, v28, v6 quad_perm:[0,0,0,0] row_mask:0xf bank_mask:0xf\n"
      "v_mov_b32_dpp v33, v30 quad_perm:[2,2,2,2] row_mask:0xf bank_mask:0xf\n"
      "v_mfma_f32_16x16x16_bf16 v[16:19], v[236:237], v[36:37], v[16:19]\n"
      "v_add_f32_dpp v32, v29, v32 quad_perm:[1,1,1,1] row_mask:0xf bank_mask:0xf\n"
      "v_add_f32_dpp v33, v31, v33 quad_perm:[3,3,3,3] row_mask:0xf bank_mask:0xf\n"
      "v_mfma_f32_16x16x16_bf16 v[20:23], v[238:239], v[36:37], v[20:23]\n"
      "v_add_f32 v32, v32, v33\n"
      "v_exp_f32 v34, v32\n"
      "v_max_f32 v35, v35, v32\n"
      "v_add_f32 v7, v7, v34\n"
      "v_cvt_pk_bf16_f32 v34, v34, v34\n"
      "v_and_b32 v38, v4, v34\n"
      "v_and_b32 v39, v5, v34\n"
      "v_sub_f32_dpp v32, v24, v6 quad_perm:[0,0,0,0] row_mask:0xf bank_mask:0xf\n"
      "v_mov_b32_dpp v33, v26 quad_perm:[2,2,2,2] row_mask:0xf bank_mask:0xf\n"
      "v_mfma_f32_16x16x16_bf16 v[16:19], v[244:245], v[38:39], v[16:19]\n"
      "v_add_f32_dpp v32, v25, v32 quad_perm:[1,1,1,1] row_mask:0xf bank_mask:0xf\n"
      "v_add_f32_dpp v33, v27, v33 quad_perm:[3,3,3,3] row_mask:0xf bank_mask:0xf\n"
      "v_mfma_f32_16x16x16_bf16 v[20:23], v[246:247], v[38:39], v[20:23]\n"
      "v_add_f32 v32, v32, v33\n"
      "v_add_f32 v32, s69, v32\n"
      "v_exp_f32 v34, v32\n"
      "v_max_f32 v35, v35, v32\n"
      "v_add_f32 v7, v7, v34\n"
      "v_cvt_pk_bf16_f32 v34, v34, v34\n"
      "v_and_b32 v36, v4, v34\n"
      "v_and_b32 v37, v5, v34\n"
      "s_nop 1\n"
      "v_mfma_f32_16x16x16_bf16 v[16:19], v[252:253], v[36:37], v[16:19]\n"
      "v_mfma_f32_16x16x16_bf16 v[20:23], v[254:255], v[36:37], v[20:23]\n"
      "v_cmp_lt_f32 vcc, 0x42800000, v35\n"
      "s_nop 0\n"
      "s_cbranch_vccnz L_fb15_%=\n"
      "L_ret15_%=:\n"
      "v_mov_b32 v49, v7\n"
      "v_lshlrev_b32 v41, 2, v0\n"
      "v_and_b32 v41, 0x300, v41\n"
      "v_permlane32_swap_b32 v49, v7\n"
      "v_lshrrev_b32 v42, 4, v0\n"
      "v_add_f32 v7, v49, v7\n"
      "v_and_b32 v42, 0x30, v42\n"
      "v_mov_b32 v49, v7\n"
      "v_add_u32 v41, v41, v42\n"
      "v_lshrrev_b32 v40, 2, v0\n"
      "v_permlane16_swap_b32 v49, v7\n"
      "v_and_b32 v42, 12, v40\n"
      "v_add_f32 v7, v49, v7\n"
      "v_add_u32 v41, v41, v42\n"
      "s_lshl_b32 s62, s60, 10\n"
      "s_add_u32 s62, s62, 155648\n"
      "v_add_u32 v41, s62, v41\n"
      "v_lshrrev_b32 v43, 3, v0\n"
      "v_and_b32 v43, 24, v43\n"
      "s_lshl_b32 s62, s60, 5\n"
      "s_add_u32 s62, s62, 159744\n"
      "v_add_u32 v43, s62, v43\n"
      "v_cvt_pk_bf16_f32 v36, v16, v17\n"
      "v_cvt_pk_bf16_f32 v37, v18, v19\n"
      "v_cvt_pk_bf16_f32 v38, v20, v21\n"
      "v_cvt_pk_bf16_f32 v39, v22, v23\n"
      "s_mov_b64 exec, 0x1111\n"
      "ds_write_b64 v43, v[6:7]\n"
      "s_mov_b64 exec, -1\n"
      "ds_write_b32 v41, v36 offset:0\n"
      "ds_write_b32 v41, v37 offset:64\n"
      "ds_write_b32 v41, v38 offset:128\n"
      "ds_write_b32 v41, v39 offset:192\n"
      "s_lshl_b32 s62, s60, 8\n"
      "s_add_u32 s62, s62, 155648\n"
      "v_add_u32 v42, s62, v40\n"
      "s_lshl_b32 s62, s60, 3\n"
      "s_add_u32 s62, s62, 159744\n"
      "v_mov_b32 v50, s62\n"
      "s_waitcnt lgkmcnt(0)\n"
      "s_barrier\n"
      "ds_read_b64 v[28:29], v50 offset:0\n"
      "ds_read_b64 v[30:31], v50 offset:32\n"
      "ds_read_b64 v[32:33], v50 offset:64\n"
      "ds_read_b64 v[34:35], v50 offset:96\n"
      "ds_read_b32 v24, v42 offset:0\n"
      "ds_read_b32 v25, v42 offset:1024\n"
      "ds_read_b32 v26, v42 offset:2048\n"
      "ds_read_b32 v27, v42 offset:3072\n"
      "s_add_u32 s56, s44, 4976640\n"
      "s_addc_u32 s57, s45, 0\n"
      "s_waitcnt lgkmcnt(4)\n"
      "v_max3_f32 v43, v28, v30, v32\n"
      "v_max_f32 v43, v43, v34\n"
      "v_sub_f32 v28, v28, v43\n"
      "v_sub_f32 v30, v30, v43\n"
      "v_sub_f32 v32, v32, v43\n"
      "v_sub_f32 v34, v34, v43\n"
      "v_exp_f32 v28, v28\n"
      "v_exp_f32 v30, v30\n"
      "v_exp_f32 v32, v32\n"
      "v_exp_f32 v34, v34\n"
      "s_waitcnt lgkmcnt(0)\n"
      "v_lshlrev_b32 v44, 16, v24\n"
      "v_and_b32 v45, 0xffff0000, v24\n"
      "v_lshlrev_b32 v46, 16, v25\n"
      "v_and_b32 v47, 0xffff0000, v25\n"
      "v_lshlrev_b32 v48, 16, v26\n"
      "v_and_b32 v49, 0xffff0000, v26\n"
      "v_lshlrev_b32 v50, 16, v27\n"
      "v_and_b32 v51, 0xffff0000, v27\n"
      "v_mul_f32 v38, v28, v29\n"
      "v_pk_mul_f32 v[36:37], v[44:45], v[28:29] op_sel_hi:[1,0]\n"
      "v_fmac_f32 v38, v30, v31\n"
      "v_pk_fma_f32 v[36:37], v[46:47], v[30:31], v[36:37] op_sel_hi:[1,0,1]\n"
      "v_fmac_f32 v38, v32, v33\n"
      "v_pk_fma_f32 v[36:37], v[48:49], v[32:33], v[36:37] op_sel_hi:[1,0,1]\n"
      "v_fmac_f32 v38, v34, v35\n"
      "v_pk_fma_f32 v[36:37], v[50:51], v[34:35], v[36:37] op_sel_hi:[1,0,1]\n"
      "v_rcp_f32 v39, v38\n"
      "v_log_f32 v38, v38\n"
      "v_mul_f32 v36, v39, v36\n"
      "v_mul_f32 v37, v39, v37\n"
      "v_add_f32 v38, v38, v43\n"
      "v_cvt_pk_bf16_f32 v36, v36, v37\n"
      "v_min_u32 v38, -2, v38\n"
      "v_min_u32 v36, -2, v36\n"
      "global_store_dword v40, v36, s[56:57] sc1\n"
      "s_mov_b32 exec_hi, 0\n"
      "global_store_dword v40, v38, s[56:57] offset:256 sc1\n"
      "s_mov_b64 exec, -1\n"
      "s_barrier\n"
      "s_waitcnt vmcnt(0)\n"
      "s_branch L_endfb_%=\n"
      "L_fb0_%=:\n"
      "s_waitcnt vmcnt(0) lgkmcnt(0)\n"
      "s_getpc_b64 s[82:83]\n"
      "L_fbpc0_%=:\n"
      "s_add_u32 s82, s82, L_safeL_%=-L_fbpc0_%=\n"
      "s_addc_u32 s83, s83, 0\n"
      "s_swappc_b64 s[80:81], s[82:83]\n"
      "s_branch L_ret0_%=\n"
      "L_fb1_%=:\n"
      "s_waitcnt vmcnt(0) lgkmcnt(0)\n"
      "s_getpc_b64 s[82:83]\n"
      "L_fbpc1_%=:\n"
      "s_add_u32 s82, s82, L_safeA_%=-L_fbpc1_%=\n"
      "s_addc_u32 s83, s83, 0\n"
      "s_swappc_b64 s[80:81], s[82:83]\n"
      "s_branch L_ret1_%=\n"
      "L_fb2_%=:\n"
      "s_waitcnt vmcnt(0) lgkmcnt(0)\n"
      "s_getpc_b64 s[82:83]\n"
      "L_fbpc2_%=:\n"
      "s_add_u32 s82, s82, L_safeB_%=-L_fbpc2_%=\n"
      "s_addc_u32 s83, s83, 0\n"
      "s_swappc_b64 s[80:81], s[82:83]\n"
      "s_branch L_ret2_%=\n"
      "L_fb3_%=:\n"
      "s_waitcnt vmcnt(0) lgkmcnt(0)\n"
      "v_swap_b32 v8, v12\n"
      "v_swap_b32 v9, v13\n"
      "v_swap_b32 v10, v14\n"
      "v_swap_b32 v11, v15\n"
      "s_getpc_b64 s[82:83]\n"
      "L_fbpc3_%=:\n"
      "s_add_u32 s82, s82, L_safeL_%=-L_fbpc3_%=\n"
      "s_addc_u32 s83, s83, 0\n"
      "s_swappc_b64 s[80:81], s[82:83]\n"
      "v_swap_b32 v8, v12\n"
      "v_swap_b32 v9, v13\n"
      "v_swap_b32 v10, v14\n"
      "v_swap_b32 v11, v15\n"
      "s_branch L_ret3_%=\n"
      "L_fb4_%=:\n"
      "s_waitcnt vmcnt(0) lgkmcnt(0)\n"
      "s_getpc_b64 s[82:83]\n"
      "L_fbpc4_%=:\n"
      "s_add_u32 s82, s82, L_safeC_%=-L_fbpc4_%=\n"
      "s_addc_u32 s83, s83, 0\n"
      "s_swappc_b64 s[80:81], s[82:83]\n"
      "s_branch L_ret4_%=\n"
      "L_fb5_%=:\n"
      "s_waitcnt vmcnt(0) lgkmcnt(0)\n"
      "v_swap_b32 v8, v12\n"
      "v_swap_b32 v9, v13\n"
      "v_swap_b32 v10, v14\n"
      "v_swap_b32 v11, v15\n"
      "s_getpc_b64 s[82:83]\n"
      "L_fbpc5_%=:\n"
      "s_add_u32 s82, s82, L_safeA_%=-L_fbpc5_%=\n"
      "s_addc_u32 s83, s83, 0\n"
      "s_swappc_b64 s[80:81], s[82:83]\n"
      "v_swap_b32 v8, v12\n"
      "v_swap_b32 v9, v13\n"
      "v_swap_b32 v10, v14\n"
      "v_swap_b32 v11, v15\n"
      "s_branch L_ret5_%=\n"
      "L_fb6_%=:\n"
      "s_waitcnt vmcnt(0) lgkmcnt(0)\n"
      "s_getpc_b64 s[82:83]\n"
      "L_fbpc6_%=:\n"
      "s_add_u32 s82, s82, L_safeL_%=-L_fbpc6_%=\n"
      "s_addc_u32 s83, s83, 0\n"
      "s_swappc_b64 s[80:81], s[82:83]\n"
      "s_branch L_ret6_%=\n"
      "L_fb7_%=:\n"
      "s_waitcnt vmcnt(0) lgkmcnt(0)\n"
      "v_swap_b32 v8, v12\n"
      "v_swap_b32 v9, v13\n"
      "v_swap_b32 v10, v14\n"
      "v_swap_b32 v11, v15\n"
      "s_getpc_b64 s[82:83]\n"
      "L_fbpc7_%=:\n"
      "s_add_u32 s82, s82, L_safeB_%=-L_fbpc7_%=\n"
      "s_addc_u32 s83, s83, 0\n"
      "s_swappc_b64 s[80:81], s[82:83]\n"
      "v_swap_b32 v8, v12\n"
      "v_swap_b32 v9, v13\n"
      "v_swap_b32 v10, v14\n"
      "v_swap_b32 v11, v15\n"
      "s_branch L_ret7_%=\n"
      "L_fb8_%=:\n"
      "s_waitcnt vmcnt(0) lgkmcnt(0)\n"
      "s_getpc_b64 s[82:83]\n"
      "L_fbpc8_%=:\n"
      "s_add_u32 s82, s82, L_safeA_%=-L_fbpc8_%=\n"
      "s_addc_u32 s83, s83, 0\n"
      "s_swappc_b64 s[80:81], s[82:83]\n"
      "s_branch L_ret8_%=\n"
      "L_fb9_%=:\n"
      "s_waitcnt vmcnt(0) lgkmcnt(0)\n"
      "v_swap_b32 v8, v12\n"
      "v_swap_b32 v9, v13\n"
      "v_swap_b32 v10, v14\n"
      "v_swap_b32 v11, v15\n"
      "s_getpc_b64 s[82:83]\n"
      "L_fbpc9_%=:\n"
      "s_add_u32 s82, s82, L_safeC_%=-L_fbpc9_%=\n"
      "s_addc_u32 s83, s83, 0\n"
      "s_swappc_b64 s[80:81], s[82:83]\n"
      "v_swap_b32 v8, v12\n"
      "v_swap_b32 v9, v13\n"
      "v_swap_b32 v10, v14\n"
      "v_swap_b32 v11, v15\n"
      "s_branch L_ret9_%=\n"
      "L_fb10_%=:\n"
      "s_waitcnt vmcnt(0) lgkmcnt(0)\n"
      "s_getpc_b64 s[82:83]\n"
      "L_fbpc10_%=:\n"
      "s_add_u32 s82, s82, L_safeB_%=-L_fbpc10_%=\n"
      "s_addc_u32 s83, s83, 0\n"
      "s_swappc_b64 s[80:81], s[82:83]\n"
      "s_branch L_ret10_%=\n"
      "L_fb11_%=:\n"
      "s_waitcnt vmcnt(0) lgkmcnt(0)\n"
      "v_swap_b32 v8, v12\n"
      "v_swap_b32 v9, v13\n"
      "v_swap_b32 v10, v14\n"
      "v_swap_b32 v11, v15\n"
      "s_getpc_b64 s[82:83]\n"
      "L_fbpc11_%=:\n"
      "s_add_u32 s82, s82, L_safeL_%=-L_fbpc11_%=\n"
      "s_addc_u32 s83, s83, 0\n"
      "s_swappc_b64 s[80:81], s[82:83]\n"
      "v_swap_b32 v8, v12\n"
      "v_swap_b32 v9, v13\n"
      "v_swap_b32 v10, v14\n"
      "v_swap_b32 v11, v15\n"
      "s_branch L_ret11_%=\n"
      "L_fb12_%=:\n"
      "s_waitcnt vmcnt(0) lgkmcnt(0)\n"
      "s_getpc_b64 s[82:83]\n"
      "L_fbpc12_%=:\n"
      "s_add_u32 s82, s82, L_safeC_%=-L_fbpc12_%=\n"
      "s_addc_u32 s83, s83, 0\n"
      "s_swappc_b64 s[80:81], s[82:83]\n"
      "s_branch L_ret12_%=\n"
      "L_fb13_%=:\n"
      "s_waitcnt vmcnt(0) lgkmcnt(0)\n"
      "v_swap_b32 v8, v12\n"
      "v_swap_b32 v9, v13\n"
      "v_swap_b32 v10, v14\n"
      "v_swap_b32 v11, v15\n"
      "s_getpc_b64 s[82:83]\n"
      "L_fbpc13_%=:\n"
      "s_add_u32 s82, s82, L_safeA_%=-L_fbpc13_%=\n"
      "s_addc_u32 s83, s83, 0\n"
      "s_swappc_b64 s[80:81], s[82:83]\n"
      "v_swap_b32 v8, v12\n"
      "v_swap_b32 v9, v13\n"
      "v_swap_b32 v10, v14\n"
      "v_swap_b32 v11, v15\n"
      "s_branch L_ret13_%=\n"
      "L_fb14_%=:\n"
      "s_waitcnt vmcnt(0) lgkmcnt(0)\n"
      "v_swap_b32 v8, v12\n"
      "v_swap_b32 v9, v13\n"
      "v_swap_b32 v10, v14\n"
      "v_swap_b32 v11, v15\n"
      "s_getpc_b64 s[82:83]\n"
      "L_fbpc14_%=:\n"
      "s_add_u32 s82, s82, L_safeB_%=-L_fbpc14_%=\n"
      "s_addc_u32 s83, s83, 0\n"
      "s_swappc_b64 s[80:81], s[82:83]\n"
      "v_swap_b32 v8, v12\n"
      "v_swap_b32 v9, v13\n"
      "v_swap_b32 v10, v14\n"
      "v_swap_b32 v11, v15\n"
      "s_branch L_ret14_%=\n"
      "L_fb15_%=:\n"
      "s_waitcnt vmcnt(0) lgkmcnt(0)\n"
      "v_swap_b32 v8, v12\n"
      "v_swap_b32 v9, v13\n"
      "v_swap_b32 v10, v14\n"
      "v_swap_b32 v11, v15\n"
      "s_getpc_b64 s[82:83]\n"
      "L_fbpc15_%=:\n"
      "s_add_u32 s82, s82, L_safeC_%=-L_fbpc15_%=\n"
      "s_addc_u32 s83, s83, 0\n"
      "s_swappc_b64 s[80:81], s[82:83]\n"
      "v_swap_b32 v8, v12\n"
      "v_swap_b32 v9, v13\n"
      "v_swap_b32 v10, v14\n"
      "v_swap_b32 v11, v15\n"
      "s_branch L_ret15_%=\n"
      "L_safeA_%=:\n"
      "s_nop 7\n"
      "v_mov_b32 v16, 0\n"
      "v_mov_b32 v20, 0\n"
      "v_mov_b32 v17, 0\n"
      "v_mov_b32 v21, 0\n"
      "v_mov_b32 v18, 0\n"
      "v_mov_b32 v22, 0\n"
      "v_mov_b32 v19, 0\n"
      "v_mov_b32 v23, 0\n"
      "v_mov_b32 v7, 0\n"
      "v_mov_b32 v6, 0xff61b1e6\n"
      "v_mfma_f32_16x16x32_bf16 v[24:27], a[0:3], v[8:11], 0\n"
      "v_mfma_f32_16x16x32_bf16 v[28:31], a[8:11], v[8:11], 0\n"
      "s_nop 6\n"
      "v_mov_b32_dpp v36, v24 quad_perm:[0,0,0,0] row_mask:0xf bank_mask:0xf\n"
      "v_mov_b32_dpp v43, v26 quad_perm:[2,2,2,2] row_mask:0xf bank_mask:0xf\n"
      "v_mfma_f32_16x16x32_bf16 v[32:35], a[16:19], v[8:11], 0\n"
      "v_add_f32_dpp v36, v25, v36 quad_perm:[1,1,1,1] row_mask:0xf bank_mask:0xf\n"
      "v_add_f32_dpp v43, v27, v43 quad_perm:[3,3,3,3] row_mask:0xf bank_mask:0xf\n"
      "v_add_f32 v36, v36, v43\n"
      "v_mov_b32_dpp v37, v28 quad_perm:[0,0,0,0] row_mask:0xf bank_mask:0xf\n"
      "v_mov_b32_dpp v43, v30 quad_perm:[2,2,2,2] row_mask:0xf bank_mask:0xf\n"
      "v_mfma_f32_16x16x32_bf16 v[24:27], a[24:27], v[8:11], 0\n"
      "v_add_f32_dpp v37, v29, v37 quad_perm:[1,1,1,1] row_mask:0xf bank_mask:0xf\n"
      "v_add_f32_dpp v43, v31, v43 quad_perm:[3,3,3,3] row_mask:0xf bank_mask:0xf\n"
      "v_add_f32 v37, v37, v43\n"
      "v_mov_b32_dpp v38, v32 quad_perm:[0,0,0,0] row_mask:0xf bank_mask:0xf\n"
      "v_mov_b32_dpp v43, v34 quad_perm:[2,2,2,2] row_mask:0xf bank_mask:0xf\n"
      "v_mfma_f32_16x16x32_bf16 v[28:31], a[32:35], v[8:11], 0\n"
      "v_add_f32_dpp v38, v33, v38 quad_perm:[1,1,1,1] row_mask:0xf bank_mask:0xf\n"
      "v_add_f32_dpp v43, v35, v43 quad_perm:[3,3,3,3] row_mask:0xf bank_mask:0xf\n"
      "v_add_f32 v38, v38, v43\n"
      "v_mov_b32_dpp v39, v24 quad_perm:[0,0,0,0] row_mask:0xf bank_mask:0xf\n"
      "v_mov_b32_dpp v43, v26 quad_perm:[2,2,2,2] row_mask:0xf bank_mask:0xf\n"
      "v_mfma_f32_16x16x32_bf16 v[32:35], a[40:43], v[8:11], 0\n"
      "v_add_f32_dpp v39, v25, v39 quad_perm:[1,1,1,1] row_mask:0xf bank_mask:0xf\n"
      "v_add_f32_dpp v43, v27, v43 quad_perm:[3,3,3,3] row_mask:0xf bank_mask:0xf\n"
      "v_add_f32 v39, v39, v43\n"
      "v_mov_b32_dpp v40, v28 quad_perm:[0,0,0,0] row_mask:0xf bank_mask:0xf\n"
      "v_mov_b32_dpp v43, v30 quad_perm:[2,2,2,2] row_mask:0xf bank_mask:0xf\n"
      "v_mfma_f32_16x16x32_bf16 v[24:27], a[48:51], v[8:11], 0\n"
      "v_add_f32_dpp v40, v29, v40 quad_perm:[1,1,1,1] row_mask:0xf bank_mask:0xf\n"
      "v_add_f32_dpp v43, v31, v43 quad_perm:[3,3,3,3] row_mask:0xf bank_mask:0xf\n"
      "v_add_f32 v40, v40, v43\n"
      "v_mov_b32_dpp v41, v32 quad_perm:[0,0,0,0] row_mask:0xf bank_mask:0xf\n"
      "v_mov_b32_dpp v43, v34 quad_perm:[2,2,2,2] row_mask:0xf bank_mask:0xf\n"
      "s_nop 0\n"
      "v_add_f32_dpp v41, v33, v41 quad_perm:[1,1,1,1] row_mask:0xf bank_mask:0xf\n"
      "v_add_f32_dpp v43, v35, v43 quad_perm:[3,3,3,3] row_mask:0xf bank_mask:0xf\n"
      "v_add_f32 v41, v41, v43\n"
      "v_mov_b32_dpp v42, v24 quad_perm:[0,0,0,0] row_mask:0xf bank_mask:0xf\n"
      "v_mov_b32_dpp v43, v26 quad_perm:[2,2,2,2] row_mask:0xf bank_mask:0xf\n"
      "s_nop 0\n"
      "v_add_f32_dpp v42, v25, v42 quad_perm:[1,1,1,1] row_mask:0xf bank_mask:0xf\n"
      "v_add_f32_dpp v43, v27, v43 quad_perm:[3,3,3,3] row_mask:0xf bank_mask:0xf\n"
      "v_add_f32 v42, v42, v43\n"
      "v_max3_f32 v49, v36, v37, v38\n"
      "v_max3_f32 v49, v49, v39, v40\n"
      "v_max3_f32 v49, v49, v41, v42\n"
      "v_max_f32 v49, v49, v6\n"
      "v_mov_b32 v50, v49\n"
      "s_nop 1\n"
      "v_permlane32_swap_b32 v50, v49\n"
      "v_max_f32 v49, v50, v49\n"
      "v_mov_b32 v50, v49\n"
      "s_nop 1\n"
      "v_permlane16_swap_b32 v50, v49\n"
      "v_max_f32 v49, v50, v49\n"
      "v_sub_f32 v50, v6, v49\n"
      "v_exp_f32 v50, v50\n"
      "v_mov_b32 v6, v49\n"
      "v_mul_f32 v7, v50, v7\n"
      "v_mul_f32 v16, v50, v16\n"
      "v_mul_f32 v20, v50, v20\n"
      "v_mul_f32 v17, v50, v17\n"
      "v_mul_f32 v21, v50, v21\n"
      "v_mul_f32 v18, v50, v18\n"
      "v_mul_f32 v22, v50, v22\n"
      "v_mul_f32 v19, v50, v19\n"
      "v_mul_f32 v23, v50, v23\n"
      "v_sub_f32 v36, v36, v6\n"
      "v_exp_f32 v36, v36\n"
      "v_sub_f32 v37, v37, v6\n"
      "v_exp_f32 v37, v37\n"
      "v_cvt_pk_bf16_f32 v48, v36, v36\n"
      "v_add_f32 v7, v7, v36\n"
      "v_and_b32 v44, v4, v48\n"
      "v_and_b32 v45, v5, v48\n"
      "v_sub_f32 v38, v38, v6\n"
      "v_exp_f32 v38, v38\n"
      "v_cvt_pk_bf16_f32 v48, v37, v37\n"
      "v_add_f32 v7, v7, v37\n"
      "v_and_b32 v46, v4, v48\n"
      "v_and_b32 v47, v5, v48\n"
      "v_mfma_f32_16x16x16_bf16 v[16:19], a[4:5], v[44:45], v[16:19]\n"
      "v_mfma_f32_16x16x16_bf16 v[20:23], a[6:7], v[44:45], v[20:23]\n"
      "v_sub_f32 v39, v39, v6\n"
      "v_exp_f32 v39, v39\n"
      "v_cvt_pk_bf16_f32 v48, v38, v38\n"
      "v_add_f32 v7, v7, v38\n"
      "v_and_b32 v44, v4, v48\n"
      "v_and_b32 v45, v5, v48\n"
      "v_mfma_f32_16x16x16_bf16 v[16:19], a[12:13], v[46:47], v[16:19]\n"
      "v_mfma_f32_16x16x16_bf16 v[20:23], a[14:15], v[46:47], v[20:23]\n"
      "v_sub_f32 v40, v40, v6\n"
      "v_exp_f32 v40, v40\n"
      "v_cvt_pk_bf16_f32 v48, v39, v39\n"
      "v_add_f32 v7, v7, v39\n"
      "v_and_b32 v46, v4, v48\n"
      "v_and_b32 v47, v5, v48\n"
      "v_mfma_f32_16x16x16_bf16 v[16:19], a[20:21], v[44:45], v[16:19]\n"
      "v_mfma_f32_16x16x16_bf16 v[20:23], a[22:23], v[44:45], v[20:23]\n"
      "v_sub_f32 v41, v41, v6\n"
      "v_exp_f32 v41, v41\n"
      "v_cvt_pk_bf16_f32 v48, v40, v40\n"
      "v_add_f32 v7, v7, v40\n"
      "v_and_b32 v44, v4, v48\n"
      "v_and_b32 v45, v5, v48\n"
      "v_mfma_f32_16x16x16_bf16 v[16:19], a[28:29], v[46:47], v[16:19]\n"
      "v_mfma_f32_16x16x16_bf16 v[20:23], a[30:31], v[46:47], v[20:23]\n"
      "v_sub_f32 v42, v42, v6\n"
      "v_exp_f32 v42, v42\n"
      "v_cvt_pk_bf16_f32 v48, v41, v41\n"
      "v_add_f32 v7, v7, v41\n"
      "v_and_b32 v46, v4, v48\n"
      "v_and_b32 v47, v5, v48\n"
      "v_mfma_f32_16x16x16_bf16 v[16:19], a[36:37], v[44:45], v[16:19]\n"
      "v_mfma_f32_16x16x16_bf16 v[20:23], a[38:39], v[44:45], v[20:23]\n"
      "v_cvt_pk_bf16_f32 v48, v42, v42\n"
      "v_add_f32 v7, v7, v42\n"
      "v_and_b32 v44, v4, v48\n"
      "v_and_b32 v45, v5, v48\n"
      "v_mfma_f32_16x16x16_bf16 v[16:19], a[44:45], v[46:47], v[16:19]\n"
      "v_mfma_f32_16x16x16_bf16 v[20:23], a[46:47], v[46:47], v[20:23]\n"
      "v_mfma_f32_16x16x16_bf16 v[16:19], a[52:53], v[44:45], v[16:19]\n"
      "v_mfma_f32_16x16x16_bf16 v[20:23], a[54:55], v[44:45], v[20:23]\n"
      "v_mfma_f32_16x16x32_bf16 v[24:27], a[56:59], v[8:11], 0\n"
      "v_mfma_f32_16x16x32_bf16 v[28:31], a[64:67], v[8:11], 0\n"
      "s_nop 6\n"
      "v_mov_b32_dpp v36, v24 quad_perm:[0,0,0,0] row_mask:0xf bank_mask:0xf\n"
      "v_mov_b32_dpp v43, v26 quad_perm:[2,2,2,2] row_mask:0xf bank_mask:0xf\n"
      "v_mfma_f32_16x16x32_bf16 v[32:35], a[72:75], v[8:11], 0\n"
      "v_add_f32_dpp v36, v25, v36 quad_perm:[1,1,1,1] row_mask:0xf bank_mask:0xf\n"
      "v_add_f32_dpp v43, v27, v43 quad_perm:[3,3,3,3] row_mask:0xf bank_mask:0xf\n"
      "v_add_f32 v36, v36, v43\n"
      "v_mov_b32_dpp v37, v28 quad_perm:[0,0,0,0] row_mask:0xf bank_mask:0xf\n"
      "v_mov_b32_dpp v43, v30 quad_perm:[2,2,2,2] row_mask:0xf bank_mask:0xf\n"
      "v_mfma_f32_16x16x32_bf16 v[24:27], a[80:83], v[8:11], 0\n"
      "v_add_f32_dpp v37, v29, v37 quad_perm:[1,1,1,1] row_mask:0xf bank_mask:0xf\n"
      "v_add_f32_dpp v43, v31, v43 quad_perm:[3,3,3,3] row_mask:0xf bank_mask:0xf\n"
      "v_add_f32 v37, v37, v43\n"
      "v_mov_b32_dpp v38, v32 quad_perm:[0,0,0,0] row_mask:0xf bank_mask:0xf\n"
      "v_mov_b32_dpp v43, v34 quad_perm:[2,2,2,2] row_mask:0xf bank_mask:0xf\n"
      "v_mfma_f32_16x16x32_bf16 v[28:31], a[88:91], v[8:11], 0\n"
      "v_add_f32_dpp v38, v33, v38 quad_perm:[1,1,1,1] row_mask:0xf bank_mask:0xf\n"
      "v_add_f32_dpp v43, v35, v43 quad_perm:[3,3,3,3] row_mask:0xf bank_mask:0xf\n"
      "v_add_f32 v38, v38, v43\n"
      "v_mov_b32_dpp v39, v24 quad_perm:[0,0,0,0] row_mask:0xf bank_mask:0xf\n"
      "v_mov_b32_dpp v43, v26 quad_perm:[2,2,2,2] row_mask:0xf bank_mask:0xf\n"
      "v_mfma_f32_16x16x32_bf16 v[32:35], a[96:99], v[8:11], 0\n"
      "v_add_f32_dpp v39, v25, v39 quad_perm:[1,1,1,1] row_mask:0xf bank_mask:0xf\n"
      "v_add_f32_dpp v43, v27, v43 quad_perm:[3,3,3,3] row_mask:0xf bank_mask:0xf\n"
      "v_add_f32 v39, v39, v43\n"
      "v_mov_b32_dpp v40, v28 quad_perm:[0,0,0,0] row_mask:0xf bank_mask:0xf\n"
      "v_mov_b32_dpp v43, v30 quad_perm:[2,2,2,2] row_mask:0xf bank_mask:0xf\n"
      "v_mfma_f32_16x16x32_bf16 v[24:27], a[104:107], v[8:11], 0\n"
      "v_add_f32_dpp v40, v29, v40 quad_perm:[1,1,1,1] row_mask:0xf bank_mask:0xf\n"
      "v_add_f32_dpp v43, v31, v43 quad_perm:[3,3,3,3] row_mask:0xf bank_mask:0xf\n"
      "v_add_f32 v40, v40, v43\n"
      "v_mov_b32_dpp v41, v32 quad_perm:[0,0,0,0] row_mask:0xf bank_mask:0xf\n"
      "v_mov_b32_dpp v43, v34 quad_perm:[2,2,2,2] row_mask:0xf bank_mask:0xf\n"
      "s_nop 0\n"
      "v_add_f32_dpp v41, v33, v41 quad_perm:[1,1,1,1] row_mask:0xf bank_mask:0xf\n"
      "v_add_f32_dpp v43, v35, v43 quad_perm:[3,3,3,3] row_mask:0xf bank_mask:0xf\n"
      "v_add_f32 v41, v41, v43\n"
      "v_mov_b32_dpp v42, v24 quad_perm:[0,0,0,0] row_mask:0xf bank_mask:0xf\n"
      "v_mov_b32_dpp v43, v26 quad_perm:[2,2,2,2] row_mask:0xf bank_mask:0xf\n"
      "s_nop 0\n"
      "v_add_f32_dpp v42, v25, v42 quad_perm:[1,1,1,1] row_mask:0xf bank_mask:0xf\n"
      "v_add_f32_dpp v43, v27, v43 quad_perm:[3,3,3,3] row_mask:0xf bank_mask:0xf\n"
      "v_add_f32 v42, v42, v43\n"
      "v_max3_f32 v49, v36, v37, v38\n"
      "v_max3_f32 v49, v49, v39, v40\n"
      "v_max3_f32 v49, v49, v41, v42\n"
      "v_max_f32 v49, v49, v6\n"
      "v_mov_b32 v50, v49\n"
      "s_nop 1\n"
      "v_permlane32_swap_b32 v50, v49\n"
      "v_max_f32 v49, v50, v49\n"
      "v_mov_b32 v50, v49\n"
      "s_nop 1\n"
      "v_permlane16_swap_b32 v50, v49\n"
      "v_max_f32 v49, v50, v49\n"
      "v_sub_f32 v50, v6, v49\n"
      "v_exp_f32 v50, v50\n"
      "v_mov_b32 v6, v49\n"
      "v_mul_f32 v7, v50, v7\n"
      "v_mul_f32 v16, v50, v16\n"
      "v_mul_f32 v20, v50, v20\n"
      "v_mul_f32 v17, v50, v17\n"
      "v_mul_f32 v21, v50, v21\n"
      "v_mul_f32 v18, v50, v18\n"
      "v_mul_f32 v22, v50, v22\n"
      "v_mul_f32 v19, v50, v19\n"
      "v_mul_f32 v23, v50, v23\n"
      "v_sub_f32 v36, v36, v6\n"
      "v_exp_f32 v36, v36\n"
      "v_sub_f32 v37, v37, v6\n"
      "v_exp_f32 v37, v37\n"
      "v_cvt_pk_bf16_f32 v48, v36, v36\n"
      "v_add_f32 v7, v7, v36\n"
      "v_and_b32 v44, v4, v48\n"
      "v_and_b32 v45, v5, v48\n"
      "v_sub_f32 v38, v38, v6\n"
      "v_exp_f32 v38, v38\n"
      "v_cvt_pk_bf16_f32 v48, v37, v37\n"
      "v_add_f32 v7, v7, v37\n"
      "v_and_b32 v46, v4, v48\n"
      "v_and_b32 v47, v5, v48\n"
      "v_mfma_f32_16x16x16_bf16 v[16:19], a[60:61], v[44:45], v[16:19]\n"
      "v_mfma_f32_16x16x16_bf16 v[20:23], a[62:63], v[44:45], v[20:23]\n"
      "v_sub_f32 v39, v39, v6\n"
      "v_exp_f32 v39, v39\n"
      "v_cvt_pk_bf16_f32 v48, v38, v38\n"
      "v_add_f32 v7, v7, v38\n"
      "v_and_b32 v44, v4, v48\n"
      "v_and_b32 v45, v5, v48\n"
      "v_mfma_f32_16x16x16_bf16 v[16:19], a[68:69], v[46:47], v[16:19]\n"
      "v_mfma_f32_16x16x16_bf16 v[20:23], a[70:71], v[46:47], v[20:23]\n"
      "v_sub_f32 v40, v40, v6\n"
      "v_exp_f32 v40, v40\n"
      "v_cvt_pk_bf16_f32 v48, v39, v39\n"
      "v_add_f32 v7, v7, v39\n"
      "v_and_b32 v46, v4, v48\n"
      "v_and_b32 v47, v5, v48\n"
      "v_mfma_f32_16x16x16_bf16 v[16:19], a[76:77], v[44:45], v[16:19]\n"
      "v_mfma_f32_16x16x16_bf16 v[20:23], a[78:79], v[44:45], v[20:23]\n"
      "v_sub_f32 v41, v41, v6\n"
      "v_exp_f32 v41, v41\n"
      "v_cvt_pk_bf16_f32 v48, v40, v40\n"
      "v_add_f32 v7, v7, v40\n"
      "v_and_b32 v44, v4, v48\n"
      "v_and_b32 v45, v5, v48\n"
      "v_mfma_f32_16x16x16_bf16 v[16:19], a[84:85], v[46:47], v[16:19]\n"
      "v_mfma_f32_16x16x16_bf16 v[20:23], a[86:87], v[46:47], v[20:23]\n"
      "v_sub_f32 v42, v42, v6\n"
      "v_exp_f32 v42, v42\n"
      "v_cvt_pk_bf16_f32 v48, v41, v41\n"
      "v_add_f32 v7, v7, v41\n"
      "v_and_b32 v46, v4, v48\n"
      "v_and_b32 v47, v5, v48\n"
      "v_mfma_f32_16x16x16_bf16 v[16:19], a[92:93], v[44:45], v[16:19]\n"
      "v_mfma_f32_16x16x16_bf16 v[20:23], a[94:95], v[44:45], v[20:23]\n"
      "v_cvt_pk_bf16_f32 v48, v42, v42\n"
      "v_add_f32 v7, v7, v42\n"
      "v_and_b32 v44, v4, v48\n"
      "v_and_b32 v45, v5, v48\n"
      "v_mfma_f32_16x16x16_bf16 v[16:19], a[100:101], v[46:47], v[16:19]\n"
      "v_mfma_f32_16x16x16_bf16 v[20:23], a[102:103], v[46:47], v[20:23]\n"
      "v_mfma_f32_16x16x16_bf16 v[16:19], a[108:109], v[44:45], v[16:19]\n"
      "v_mfma_f32_16x16x16_bf16 v[20:23], a[110:111], v[44:45], v[20:23]\n"
      "v_mfma_f32_16x16x32_bf16 v[24:27], a[112:115], v[8:11], 0\n"
      "v_mfma_f32_16x16x32_bf16 v[28:31], a[120:123], v[8:11], 0\n"
      "s_nop 6\n"
      "v_mov_b32_dpp v36, v24 quad_perm:[0,0,0,0] row_mask:0xf bank_mask:0xf\n"
      "v_mov_b32_dpp v43, v26 quad_perm:[2,2,2,2] row_mask:0xf bank_mask:0xf\n"
      "v_mfma_f32_16x16x32_bf16 v[32:35], a[128:131], v[8:11], 0\n"
      "v_add_f32_dpp v36, v25, v36 quad_perm:[1,1,1,1] row_mask:0xf bank_mask:0xf\n"
      "v_add_f32_dpp v43, v27, v43 quad_perm:[3,3,3,3] row_mask:0xf bank_mask:0xf\n"
      "v_add_f32 v36, v36, v43\n"
      "v_mov_b32_dpp v37, v28 quad_perm:[0,0,0,0] row_mask:0xf bank_mask:0xf\n"
      "v_mov_b32_dpp v43, v30 quad_perm:[2,2,2,2] row_mask:0xf bank_mask:0xf\n"
      "v_mfma_f32_16x16x32_bf16 v[24:27], a[136:139], v[8:11], 0\n"
      "v_add_f32_dpp v37, v29, v37 quad_perm:[1,1,1,1] row_mask:0xf bank_mask:0xf\n"
      "v_add_f32_dpp v43, v31, v43 quad_perm:[3,3,3,3] row_mask:0xf bank_mask:0xf\n"
      "v_add_f32 v37, v37, v43\n"
      "v_mov_b32_dpp v38, v32 quad_perm:[0,0,0,0] row_mask:0xf bank_mask:0xf\n"
      "v_mov_b32_dpp v43, v34 quad_perm:[2,2,2,2] row_mask:0xf bank_mask:0xf\n"
      "v_mfma_f32_16x16x32_bf16 v[28:31], a[144:147], v[8:11], 0\n"
      "v_add_f32_dpp v38, v33, v38 quad_perm:[1,1,1,1] row_mask:0xf bank_mask:0xf\n"
      "v_add_f32_dpp v43, v35, v43 quad_perm:[3,3,3,3] row_mask:0xf bank_mask:0xf\n"
      "v_add_f32 v38, v38, v43\n"
      "v_mov_b32_dpp v39, v24 quad_perm:[0,0,0,0] row_mask:0xf bank_mask:0xf\n"
      "v_mov_b32_dpp v43, v26 quad_perm:[2,2,2,2] row_mask:0xf bank_mask:0xf\n"
      "s_nop 0\n"
      "v_add_f32_dpp v39, v25, v39 quad_perm:[1,1,1,1] row_mask:0xf bank_mask:0xf\n"
      "v_add_f32_dpp v43, v27, v43 quad_perm:[3,3,3,3] row_mask:0xf bank_mask:0xf\n"
      "v_add_f32 v39, v39, v43\n"
      "v_mov_b32_dpp v40, v28 quad_perm:[0,0,0,0] row_mask:0xf bank_mask:0xf\n"
      "v_mov_b32_dpp v43, v30 quad_perm:[2,2,2,2] row_mask:0xf bank_mask:0xf\n"
      "s_nop 0\n"
      "v_add_f32_dpp v40, v29, v40 quad_perm:[1,1,1,1] row_mask:0xf bank_mask:0xf\n"
      "v_add_f32_dpp v43, v31, v43 quad_perm:[3,3,3,3] row_mask:0xf bank_mask:0xf\n"
      "v_add_f32 v40, v40, v43\n"
      "v_add_f32 v40, s69, v40\n"
      "v_max3_f32 v49, v36, v37, v38\n"
      "v_max3_f32 v49, v49, v39, v40\n"
      "v_max_f32 v49, v49, v6\n"
      "v_mov_b32 v50, v49\n"
      "s_nop 1\n"
      "v_permlane32_swap_b32 v50, v49\n"
      "v_max_f32 v49, v50, v49\n"
      "v_mov_b32 v50, v49\n"
      "s_nop 1\n"
      "v_permlane16_swap_b32 v50, v49\n"
      "v_max_f32 v49, v50, v49\n"
      "v_sub_f32 v50, v6, v49\n"
      "v_exp_f32 v50, v50\n"
      "v_mov_b32 v6, v49\n"
      "v_mul_f32 v7, v50, v7\n"
      "v_mul_f32 v16, v50, v16\n"
      "v_mul_f32 v20, v50, v20\n"
      "v_mul_f32 v17, v50, v17\n"
      "v_mul_f32 v21, v50, v21\n"
      "v_mul_f32 v18, v50, v18\n"
      "v_mul_f32 v22, v50, v22\n"
      "v_mul_f32 v19, v50, v19\n"
      "v_mul_f32 v23, v50, v23\n"
      "v_sub_f32 v36, v36, v6\n"
      "v_exp_f32 v36, v36\n"
      "v_sub_f32 v37, v37, v6\n"
      "v_exp_f32 v37, v37\n"
      "v_cvt_pk_bf16_f32 v48, v36, v36\n"
      "v_add_f32 v7, v7, v36\n"
      "v_and_b32 v44, v4, v48\n"
      "v_and_b32 v45, v5, v48\n"
      "v_sub_f32 v38, v38, v6\n"
      "v_exp_f32 v38, v38\n"
      "v_cvt_pk_bf16_f32 v48, v37, v37\n"
      "v_add_f32 v7, v7, v37\n"
      "v_and_b32 v46, v4, v48\n"
      "v_and_b32 v47, v5, v48\n"
      "v_mfma_f32_16x16x16_bf16 v[16:19], a[116:117], v[44:45], v[16:19]\n"
      "v_mfma_f32_16x16x16_bf16 v[20:23], a[118:119], v[44:45], v[20:23]\n"
      "v_sub_f32 v39, v39, v6\n"
      "v_exp_f32 v39, v39\n"
      "v_cvt_pk_bf16_f32 v48, v38, v38\n"
      "v_add_f32 v7, v7, v38\n"
      "v_and_b32 v44, v4, v48\n"
      "v_and_b32 v45, v5, v48\n"
      "v_mfma_f32_16x16x16_bf16 v[16:19], a[124:125], v[46:47], v[16:19]\n"
      "v_mfma_f32_16x16x16_bf16 v[20:23], a[126:127], v[46:47], v[20:23]\n"
      "v_sub_f32 v40, v40, v6\n"
      "v_exp_f32 v40, v40\n"
      "v_cvt_pk_bf16_f32 v48, v39, v39\n"
      "v_add_f32 v7, v7, v39\n"
      "v_and_b32 v46, v4, v48\n"
      "v_and_b32 v47, v5, v48\n"
      "v_mfma_f32_16x16x16_bf16 v[16:19], a[132:133], v[44:45], v[16:19]\n"
      "v_mfma_f32_16x16x16_bf16 v[20:23], a[134:135], v[44:45], v[20:23]\n"
      "v_cvt_pk_bf16_f32 v48, v40, v40\n"
      "v_add_f32 v7, v7, v40\n"
      "v_and_b32 v44, v4, v48\n"
      "v_and_b32 v45, v5, v48\n"
      "v_mfma_f32_16x16x16_bf16 v[16:19], a[140:141], v[46:47], v[16:19]\n"
      "v_mfma_f32_16x16x16_bf16 v[20:23], a[142:143], v[46:47], v[20:23]\n"
      "v_mfma_f32_16x16x16_bf16 v[16:19], a[148:149], v[44:45], v[16:19]\n"
      "v_mfma_f32_16x16x16_bf16 v[20:23], a[150:151], v[44:45], v[20:23]\n"
      "s_nop 7\n"
      "s_setpc_b64 s[80:81]\n"
      "L_safeB_%=:\n"
      "s_nop 7\n"
      "v_mov_b32 v16, 0\n"
      "v_mov_b32 v20, 0\n"
      "v_mov_b32 v17, 0\n"
      "v_mov_b32 v21, 0\n"
      "v_mov_b32 v18, 0\n"
      "v_mov_b32 v22, 0\n"
      "v_mov_b32 v19, 0\n"
      "v_mov_b32 v23, 0\n"
      "v_mov_b32 v7, 0\n"
      "v_mov_b32 v6, 0xff61b1e6\n"
      "v_mfma_f32_16x16x32_bf16 v[24:27], a[152:155], v[8:11], 0\n"
      "v_mfma_f32_16x16x32_bf16 v[28:31], a[160:163], v[8:11], 0\n"
      "s_nop 6\n"
      "v_mov_b32_dpp v36, v24 quad_perm:[0,0,0,0] row_mask:0xf bank_mask:0xf\n"
      "v_mov_b32_dpp v43, v26 quad_perm:[2,2,2,2] row_mask:0xf bank_mask:0xf\n"
      "v_mfma_f32_16x16x32_bf16 v[32:35], a[168:171], v[8:11], 0\n"
      "v_add_f32_dpp v36, v25, v36 quad_perm:[1,1,1,1] row_mask:0xf bank_mask:0xf\n"
      "v_add_f32_dpp v43, v27, v43 quad_perm:[3,3,3,3] row_mask:0xf bank_mask:0xf\n"
      "v_add_f32 v36, v36, v43\n"
      "v_mov_b32_dpp v37, v28 quad_perm:[0,0,0,0] row_mask:0xf bank_mask:0xf\n"
      "v_mov_b32_dpp v43, v30 quad_perm:[2,2,2,2] row_mask:0xf bank_mask:0xf\n"
      "v_mfma_f32_16x16x32_bf16 v[24:27], a[176:179], v[8:11], 0\n"
      "v_add_f32_dpp v37, v29, v37 quad_perm:[1,1,1,1] row_mask:0xf bank_mask:0xf\n"
      "v_add_f32_dpp v43, v31, v43 quad_perm:[3,3,3,3] row_mask:0xf bank_mask:0xf\n"
      "v_add_f32 v37, v37, v43\n"
      "v_mov_b32_dpp v38, v32 quad_perm:[0,0,0,0] row_mask:0xf bank_mask:0xf\n"
      "v_mov_b32_dpp v43, v34 quad_perm:[2,2,2,2] row_mask:0xf bank_mask:0xf\n"
      "v_mfma_f32_16x16x32_bf16 v[28:31], a[184:187], v[8:11], 0\n"
      "v_add_f32_dpp v38, v33, v38 quad_perm:[1,1,1,1] row_mask:0xf bank_mask:0xf\n"
      "v_add_f32_dpp v43, v35, v43 quad_perm:[3,3,3,3] row_mask:0xf bank_mask:0xf\n"
      "v_add_f32 v38, v38, v43\n"
      "v_mov_b32_dpp v39, v24 quad_perm:[0,0,0,0] row_mask:0xf bank_mask:0xf\n"
      "v_mov_b32_dpp v43, v26 quad_perm:[2,2,2,2] row_mask:0xf bank_mask:0xf\n"
      "v_mfma_f32_16x16x32_bf16 v[32:35], a[192:195], v[8:11], 0\n"
      "v_add_f32_dpp v39, v25, v39 quad_perm:[1,1,1,1] row_mask:0xf bank_mask:0xf\n"
      "v_add_f32_dpp v43, v27, v43 quad_perm:[3,3,3,3] row_mask:0xf bank_mask:0xf\n"
      "v_add_f32 v39, v39, v43\n"
      "v_mov_b32_dpp v40, v28 quad_perm:[0,0,0,0] row_mask:0xf bank_mask:0xf\n"
      "v_mov_b32_dpp v43, v30 quad_perm:[2,2,2,2] row_mask:0xf bank_mask:0xf\n"
      "v_mfma_f32_16x16x32_bf16 v[24:27], a[200:203], v[8:11], 0\n"
      "v_add_f32_dpp v40, v29, v40 quad_perm:[1,1,1,1] row_mask:0xf bank_mask:0xf\n"
      "v_add_f32_dpp v43, v31, v43 quad_perm:[3,3,3,3] row_mask:0xf bank_mask:0xf\n"
      "v_add_f32 v40, v40, v43\n"
      "v_mov_b32_dpp v41, v32 quad_perm:[0,0,0,0] row_mask:0xf bank_mask:0xf\n"
      "v_mov_b32_dpp v43, v34 quad_perm:[2,2,2,2] row_mask:0xf bank_mask:0xf\n"
      "s_nop 0\n"
      "v_add_f32_dpp v41, v33, v41 quad_perm:[1,1,1,1] row_mask:0xf bank_mask:0xf\n"
      "v_add_f32_dpp v43, v35, v43 quad_perm:[3,3,3,3] row_mask:0xf bank_mask:0xf\n"
      "v_add_f32 v41, v41, v43\n"
      "v_mov_b32_dpp v42, v24 quad_perm:[0,0,0,0] row_mask:0xf bank_mask:0xf\n"
      "v_mov_b32_dpp v43, v26 quad_perm:[2,2,2,2] row_mask:0xf bank_mask:0xf\n"
      "s_nop 0\n"
      "v_add_f32_dpp v42, v25, v42 quad_perm:[1,1,1,1] row_mask:0xf bank_mask:0xf\n"
      "v_add_f32_dpp v43, v27, v43 quad_perm:[3,3,3,3] row_mask:0xf bank_mask:0xf\n"
      "v_add_f32 v42, v42, v43\n"
      "v_max3_f32 v49, v36, v37, v38\n"
      "v_max3_f32 v49, v49, v39, v40\n"
      "v_max3_f32 v49, v49, v41, v42\n"
      "v_max_f32 v49, v49, v6\n"
      "v_mov_b32 v50, v49\n"
      "s_nop 1\n"
      "v_permlane32_swap_b32 v50, v49\n"
      "v_max_f32 v49, v50, v49\n"
      "v_mov_b32 v50, v49\n"
      "s_nop 1\n"
      "v_permlane16_swap_b32 v50, v49\n"
      "v_max_f32 v49, v50, v49\n"
      "v_sub_f32 v50, v6, v49\n"
      "v_exp_f32 v50, v50\n"
      "v_mov_b32 v6, v49\n"
      "v_mul_f32 v7, v50, v7\n"
      "v_mul_f32 v16, v50, v16\n"
      "v_mul_f32 v20, v50, v20\n"
      "v_mul_f32 v17, v50, v17\n"
      "v_mul_f32 v21, v50, v21\n"
      "v_mul_f32 v18, v50, v18\n"
      "v_mul_f32 v22, v50, v22\n"
      "v_mul_f32 v19, v50, v19\n"
      "v_mul_f32 v23, v50, v23\n"
      "v_sub_f32 v36, v36, v6\n"
      "v_exp_f32 v36, v36\n"
      "v_sub_f32 v37, v37, v6\n"
      "v_exp_f32 v37, v37\n"
      "v_cvt_pk_bf16_f32 v48, v36, v36\n"
      "v_add_f32 v7, v7, v36\n"
      "v_and_b32 v44, v4, v48\n"
      "v_and_b32 v45, v5, v48\n"
      "v_sub_f32 v38, v38, v6\n"
      "v_exp_f32 v38, v38\n"
      "v_cvt_pk_bf16_f32 v48, v37, v37\n"
      "v_add_f32 v7, v7, v37\n"
      "v_and_b32 v46, v4, v48\n"
      "v_and_b32 v47, v5, v48\n"
      "v_mfma_f32_16x16x16_bf16 v[16:19], a[156:157], v[44:45], v[16:19]\n"
      "v_mfma_f32_16x16x16_bf16 v[20:23], a[158:159], v[44:45], v[20:23]\n"
      "v_sub_f32 v39, v39, v6\n"
      "v_exp_f32 v39, v39\n"
      "v_cvt_pk_bf16_f32 v48, v38, v38\n"
      "v_add_f32 v7, v7, v38\n"
      "v_and_b32 v44, v4, v48\n"
      "v_and_b32 v45, v5, v48\n"
      "v_mfma_f32_16x16x16_bf16 v[16:19], a[164:165], v[46:47], v[16:19]\n"
      "v_mfma_f32_16x16x16_bf16 v[20:23], a[166:167], v[46:47], v[20:23]\n"
      "v_sub_f32 v40, v40, v6\n"
      "v_exp_f32 v40, v40\n"
      "v_cvt_pk_bf16_f32 v48, v39, v39\n"
      "v_add_f32 v7, v7, v39\n"
      "v_and_b32 v46, v4, v48\n"
      "v_and_b32 v47, v5, v48\n"
      "v_mfma_f32_16x16x16_bf16 v[16:19], a[172:173], v[44:45], v[16:19]\n"
      "v_mfma_f32_16x16x16_bf16 v[20:23], a[174:175], v[44:45], v[20:23]\n"
      "v_sub_f32 v41, v41, v6\n"
      "v_exp_f32 v41, v41\n"
      "v_cvt_pk_bf16_f32 v48, v40, v40\n"
      "v_add_f32 v7, v7, v40\n"
      "v_and_b32 v44, v4, v48\n"
      "v_and_b32 v45, v5, v48\n"
      "v_mfma_f32_16x16x16_bf16 v[16:19], a[180:181], v[46:47], v[16:19]\n"
      "v_mfma_f32_16x16x16_bf16 v[20:23], a[182:183], v[46:47], v[20:23]\n"
      "v_sub_f32 v42, v42, v6\n"
      "v_exp_f32 v42, v42\n"
      "v_cvt_pk_bf16_f32 v48, v41, v41\n"
      "v_add_f32 v7, v7, v41\n"
      "v_and_b32 v46, v4, v48\n"
      "v_and_b32 v47, v5, v48\n"
      "v_mfma_f32_16x16x16_bf16 v[16:19], a[188:189], v[44:45], v[16:19]\n"
      "v_mfma_f32_16x16x16_bf16 v[20:23], a[190:191], v[44:45], v[20:23]\n"
      "v_cvt_pk_bf16_f32 v48, v42, v42\n"
      "v_add_f32 v7, v7, v42\n"
      "v_and_b32 v44, v4, v48\n"
      "v_and_b32 v45, v5, v48\n"
      "v_mfma_f32_16x16x16_bf16 v[16:19], a[196:197], v[46:47], v[16:19]\n"
      "v_mfma_f32_16x16x16_bf16 v[20:23], a[198:199], v[46:47], v[20:23]\n"
      "v_mfma_f32_16x16x16_bf16 v[16:19], a[204:205], v[44:45], v[16:19]\n"
      "v_mfma_f32_16x16x16_bf16 v[20:23], a[206:207], v[44:45], v[20:23]\n"
      "v_mfma_f32_16x16x32_bf16 v[24:27], a[208:211], v[8:11], 0\n"
      "v_mfma_f32_16x16x32_bf16 v[28:31], a[216:219], v[8:11], 0\n"
      "s_nop 6\n"
      "v_mov_b32_dpp v36, v24 quad_perm:[0,0,0,0] row_mask:0xf bank_mask:0xf\n"
      "v_mov_b32_dpp v43, v26 quad_perm:[2,2,2,2] row_mask:0xf bank_mask:0xf\n"
      "v_mfma_f32_16x16x32_bf16 v[32:35], a[224:227], v[8:11], 0\n"
      "v_add_f32_dpp v36, v25, v36 quad_perm:[1,1,1,1] row_mask:0xf bank_mask:0xf\n"
      "v_add_f32_dpp v43, v27, v43 quad_perm:[3,3,3,3] row_mask:0xf bank_mask:0xf\n"
      "v_add_f32 v36, v36, v43\n"
      "v_mov_b32_dpp v37, v28 quad_perm:[0,0,0,0] row_mask:0xf bank_mask:0xf\n"
      "v_mov_b32_dpp v43, v30 quad_perm:[2,2,2,2] row_mask:0xf bank_mask:0xf\n"
      "v_mfma_f32_16x16x32_bf16 v[24:27], a[232:235], v[8:11], 0\n"
      "v_add_f32_dpp v37, v29, v37 quad_perm:[1,1,1,1] row_mask:0xf bank_mask:0xf\n"
      "v_add_f32_dpp v43, v31, v43 quad_perm:[3,3,3,3] row_mask:0xf bank_mask:0xf\n"
      "v_add_f32 v37, v37, v43\n"
      "v_mov_b32_dpp v38, v32 quad_perm:[0,0,0,0] row_mask:0xf bank_mask:0xf\n"
      "v_mov_b32_dpp v43, v34 quad_perm:[2,2,2,2] row_mask:0xf bank_mask:0xf\n"
      "v_mfma_f32_16x16x32_bf16 v[28:31], a[240:243], v[8:11], 0\n"
      "v_add_f32_dpp v38, v33, v38 quad_perm:[1,1,1,1] row_mask:0xf bank_mask:0xf\n"
      "v_add_f32_dpp v43, v35, v43 quad_perm:[3,3,3,3] row_mask:0xf bank_mask:0xf\n"
      "v_add_f32 v38, v38, v43\n"
      "v_mov_b32_dpp v39, v24 quad_perm:[0,0,0,0] row_mask:0xf bank_mask:0xf\n"
      "v_mov_b32_dpp v43, v26 quad_perm:[2,2,2,2] row_mask:0xf bank_mask:0xf\n"
      "v_mfma_f32_16x16x32_bf16 v[32:35], a[248:251], v[8:11], 0\n"
      "v_add_f32_dpp v39, v25, v39 quad_perm:[1,1,1,1] row_mask:0xf bank_mask:0xf\n"
      "v_add_f32_dpp v43, v27, v43 quad_perm:[3,3,3,3] row_mask:0xf bank_mask:0xf\n"
      "v_add_f32 v39, v39, v43\n"
      "v_mov_b32_dpp v40, v28 quad_perm:[0,0,0,0] row_mask:0xf bank_mask:0xf\n"
      "v_mov_b32_dpp v43, v30 quad_perm:[2,2,2,2] row_mask:0xf bank_mask:0xf\n"
      "v_mfma_f32_16x16x32_bf16 v[24:27], v[56:59], v[8:11], 0\n"
      "v_add_f32_dpp v40, v29, v40 quad_perm:[1,1,1,1] row_mask:0xf bank_mask:0xf\n"
      "v_add_f32_dpp v43, v31, v43 quad_perm:[3,3,3,3] row_mask:0xf bank_mask:0xf\n"
      "v_add_f32 v40, v40, v43\n"
      "v_mov_b32_dpp v41, v32 quad_perm:[0,0,0,0] row_mask:0xf bank_mask:0xf\n"
      "v_mov_b32_dpp v43, v34 quad_perm:[2,2,2,2] row_mask:0xf bank_mask:0xf\n"
      "s_nop 0\n"
      "v_add_f32_dpp v41, v33, v41 quad_perm:[1,1,1,1] row_mask:0xf bank_mask:0xf\n"
      "v_add_f32_dpp v43, v35, v43 quad_perm:[3,3,3,3] row_mask:0xf bank_mask:0xf\n"
      "v_add_f32 v41, v41, v43\n"
      "v_mov_b32_dpp v42, v24 quad_perm:[0,0,0,0] row_mask:0xf bank_mask:0xf\n"
      "v_mov_b32_dpp v43, v26 quad_perm:[2,2,2,2] row_mask:0xf bank_mask:0xf\n"
      "s_nop 0\n"
      "v_add_f32_dpp v42, v25, v42 quad_perm:[1,1,1,1] row_mask:0xf bank_mask:0xf\n"
      "v_add_f32_dpp v43, v27, v43 quad_perm:[3,3,3,3] row_mask:0xf bank_mask:0xf\n"
      "v_add_f32 v42, v42, v43\n"
      "v_max3_f32 v49, v36, v37, v38\n"
      "v_max3_f32 v49, v49, v39, v40\n"
      "v_max3_f32 v49, v49, v41, v42\n"
      "v_max_f32 v49, v49, v6\n"
      "v_mov_b32 v50, v49\n"
      "s_nop 1\n"
      "v_permlane32_swap_b32 v50, v49\n"
      "v_max_f32 v49, v50, v49\n"
      "v_mov_b32 v50, v49\n"
      "s_nop 1\n"
      "v_permlane16_swap_b32 v50, v49\n"
      "v_max_f32 v49, v50, v49\n"
      "v_sub_f32 v50, v6, v49\n"
      "v_exp_f32 v50, v50\n"
      "v_mov_b32 v6, v49\n"
      "v_mul_f32 v7, v50, v7\n"
      "v_mul_f32 v16, v50, v16\n"
      "v_mul_f32 v20, v50, v20\n"
      "v_mul_f32 v17, v50, v17\n"
      "v_mul_f32 v21, v50, v21\n"
      "v_mul_f32 v18, v50, v18\n"
      "v_mul_f32 v22, v50, v22\n"
      "v_mul_f32 v19, v50, v19\n"
      "v_mul_f32 v23, v50, v23\n"
      "v_sub_f32 v36, v36, v6\n"
      "v_exp_f32 v36, v36\n"
      "v_sub_f32 v37, v37, v6\n"
      "v_exp_f32 v37, v37\n"
      "v_cvt_pk_bf16_f32 v48, v36, v36\n"
      "v_add_f32 v7, v7, v36\n"
      "v_and_b32 v44, v4, v48\n"
      "v_and_b32 v45, v5, v48\n"
      "v_sub_f32 v38, v38, v6\n"
      "v_exp_f32 v38, v38\n"
      "v_cvt_pk_bf16_f32 v48, v37, v37\n"
      "v_add_f32 v7, v7, v37\n"
      "v_and_b32 v46, v4, v48\n"
      "v_and_b32 v47, v5, v48\n"
      "v_mfma_f32_16x16x16_bf16 v[16:19], a[212:213], v[44:45], v[16:19]\n"
      "v_mfma_f32_16x16x16_bf16 v[20:23], a[214:215], v[44:45], v[20:23]\n"
      "v_sub_f32 v39, v39, v6\n"
      "v_exp_f32 v39, v39\n"
      "v_cvt_pk_bf16_f32 v48, v38, v38\n"
      "v_add_f32 v7, v7, v38\n"
      "v_and_b32 v44, v4, v48\n"
      "v_and_b32 v45, v5, v48\n"
      "v_mfma_f32_16x16x16_bf16 v[16:19], a[220:221], v[46:47], v[16:19]\n"
      "v_mfma_f32_16x16x16_bf16 v[20:23], a[222:223], v[46:47], v[20:23]\n"
      "v_sub_f32 v40, v40, v6\n"
      "v_exp_f32 v40, v40\n"
      "v_cvt_pk_bf16_f32 v48, v39, v39\n"
      "v_add_f32 v7, v7, v39\n"
      "v_and_b32 v46, v4, v48\n"
      "v_and_b32 v47, v5, v48\n"
      "v_mfma_f32_16x16x16_bf16 v[16:19], a[228:229], v[44:45], v[16:19]\n"
      "v_mfma_f32_16x16x16_bf16 v[20:23], a[230:231], v[44:45], v[20:23]\n"
      "v_sub_f32 v41, v41, v6\n"
      "v_exp_f32 v41, v41\n"
      "v_cvt_pk_bf16_f32 v48, v40, v40\n"
      "v_add_f32 v7, v7, v40\n"
      "v_and_b32 v44, v4, v48\n"
      "v_and_b32 v45, v5, v48\n"
      "v_mfma_f32_16x16x16_bf16 v[16:19], a[236:237], v[46:47], v[16:19]\n"
      "v_mfma_f32_16x16x16_bf16 v[20:23], a[238:239], v[46:47], v[20:23]\n"
      "v_sub_f32 v42, v42, v6\n"
      "v_exp_f32 v42, v42\n"
      "v_cvt_pk_bf16_f32 v48, v41, v41\n"
      "v_add_f32 v7, v7, v41\n"
      "v_and_b32 v46, v4, v48\n"
      "v_and_b32 v47, v5, v48\n"
      "v_mfma_f32_16x16x16_bf16 v[16:19], a[244:245], v[44:45], v[16:19]\n"
      "v_mfma_f32_16x16x16_bf16 v[20:23], a[246:247], v[44:45], v[20:23]\n"
      "v_cvt_pk_bf16_f32 v48, v42, v42\n"
      "v_add_f32 v7, v7, v42\n"
      "v_and_b32 v44, v4, v48\n"
      "v_and_b32 v45, v5, v48\n"
      "v_mfma_f32_16x16x16_bf16 v[16:19], a[252:253], v[46:47], v[16:19]\n"
      "v_mfma_f32_16x16x16_bf16 v[20:23], a[254:255], v[46:47], v[20:23]\n"
      "v_mfma_f32_16x16x16_bf16 v[16:19], v[60:61], v[44:45], v[16:19]\n"
      "v_mfma_f32_16x16x16_bf16 v[20:23], v[62:63], v[44:45], v[20:23]\n"
      "v_mfma_f32_16x16x32_bf16 v[24:27], v[64:67], v[8:11], 0\n"
      "v_mfma_f32_16x16x32_bf16 v[28:31], v[72:75], v[8:11], 0\n"
      "s_nop 6\n"
      "v_mov_b32_dpp v36, v24 quad_perm:[0,0,0,0] row_mask:0xf bank_mask:0xf\n"
      "v_mov_b32_dpp v43, v26 quad_perm:[2,2,2,2] row_mask:0xf bank_mask:0xf\n"
      "v_mfma_f32_16x16x32_bf16 v[32:35], v[80:83], v[8:11], 0\n"
      "v_add_f32_dpp v36, v25, v36 quad_perm:[1,1,1,1] row_mask:0xf bank_mask:0xf\n"
      "v_add_f32_dpp v43, v27, v43 quad_perm:[3,3,3,3] row_mask:0xf bank_mask:0xf\n"
      "v_add_f32 v36, v36, v43\n"
      "v_mov_b32_dpp v37, v28 quad_perm:[0,0,0,0] row_mask:0xf bank_mask:0xf\n"
      "v_mov_b32_dpp v43, v30 quad_perm:[2,2,2,2] row_mask:0xf bank_mask:0xf\n"
      "v_mfma_f32_16x16x32_bf16 v[24:27], v[88:91], v[8:11], 0\n"
      "v_add_f32_dpp v37, v29, v37 quad_perm:[1,1,1,1] row_mask:0xf bank_mask:0xf\n"
      "v_add_f32_dpp v43, v31, v43 quad_perm:[3,3,3,3] row_mask:0xf bank_mask:0xf\n"
      "v_add_f32 v37, v37, v43\n"
      "v_mov_b32_dpp v38, v32 quad_perm:[0,0,0,0] row_mask:0xf bank_mask:0xf\n"
      "v_mov_b32_dpp v43, v34 quad_perm:[2,2,2,2] row_mask:0xf bank_mask:0xf\n"
      "v_mfma_f32_16x16x32_bf16 v[28:31], v[96:99], v[8:11], 0\n"
      "v_add_f32_dpp v38, v33, v38 quad_perm:[1,1,1,1] row_mask:0xf bank_mask:0xf\n"
      "v_add_f32_dpp v43, v35, v43 quad_perm:[3,3,3,3] row_mask:0xf bank_mask:0xf\n"
      "v_add_f32 v38, v38, v43\n"
      "v_mov_b32_dpp v39, v24 quad_perm:[0,0,0,0] row_mask:0xf bank_mask:0xf\n"
      "v_mov_b32_dpp v43, v26 quad_perm:[2,2,2,2] row_mask:0xf bank_mask:0xf\n"
      "s_nop 0\n"
      "v_add_f32_dpp v39, v25, v39 quad_perm:[1,1,1,1] row_mask:0xf bank_mask:0xf\n"
      "v_add_f32_dpp v43, v27, v43 quad_perm:[3,3,3,3] row_mask:0xf bank_mask:0xf\n"
      "v_add_f32 v39, v39, v43\n"
      "v_mov_b32_dpp v40, v28 quad_perm:[0,0,0,0] row_mask:0xf bank_mask:0xf\n"
      "v_mov_b32_dpp v43, v30 quad_perm:[2,2,2,2] row_mask:0xf bank_mask:0xf\n"
      "s_nop 0\n"
      "v_add_f32_dpp v40, v29, v40 quad_perm:[1,1,1,1] row_mask:0xf bank_mask:0xf\n"
      "v_add_f32_dpp v43, v31, v43 quad_perm:[3,3,3,3] row_mask:0xf bank_mask:0xf\n"
      "v_add_f32 v40, v40, v43\n"
      "v_add_f32 v40, s69, v40\n"
      "v_max3_f32 v49, v36, v37, v38\n"
      "v_max3_f32 v49, v49, v39, v40\n"
      "v_max_f32 v49, v49, v6\n"
      "v_mov_b32 v50, v49\n"
      "s_nop 1\n"
      "v_permlane32_swap_b32 v50, v49\n"
      "v_max_f32 v49, v50, v49\n"
      "v_mov_b32 v50, v49\n"
      "s_nop 1\n"
      "v_permlane16_swap_b32 v50, v49\n"
      "v_max_f32 v49, v50, v49\n"
      "v_sub_f32 v50, v6, v49\n"
      "v_exp_f32 v50, v50\n"
      "v_mov_b32 v6, v49\n"
      "v_mul_f32 v7, v50, v7\n"
      "v_mul_f32 v16, v50, v16\n"
      "v_mul_f32 v20, v50, v20\n"
      "v_mul_f32 v17, v50, v17\n"
      "v_mul_f32 v21, v50, v21\n"
      "v_mul_f32 v18, v50, v18\n"
      "v_mul_f32 v22, v50, v22\n"
      "v_mul_f32 v19, v50, v19\n"
      "v_mul_f32 v23, v50, v23\n"
      "v_sub_f32 v36, v36, v6\n"
      "v_exp_f32 v36, v36\n"
      "v_sub_f32 v37, v37, v6\n"
      "v_exp_f32 v37, v37\n"
      "v_cvt_pk_bf16_f32 v48, v36, v36\n"
      "v_add_f32 v7, v7, v36\n"
      "v_and_b32 v44, v4, v48\n"
      "v_and_b32 v45, v5, v48\n"
      "v_sub_f32 v38, v38, v6\n"
      "v_exp_f32 v38, v38\n"
      "v_cvt_pk_bf16_f32 v48, v37, v37\n"
      "v_add_f32 v7, v7, v37\n"
      "v_and_b32 v46, v4, v48\n"
      "v_and_b32 v47, v5, v48\n"
      "v_mfma_f32_16x16x16_bf16 v[16:19], v[68:69], v[44:45], v[16:19]\n"
      "v_mfma_f32_16x16x16_bf16 v[20:23], v[70:71], v[44:45], v[20:23]\n"
      "v_sub_f32 v39, v39, v6\n"
      "v_exp_f32 v39, v39\n"
      "v_cvt_pk_bf16_f32 v48, v38, v38\n"
      "v_add_f32 v7, v7, v38\n"
      "v_and_b32 v44, v4, v48\n"
      "v_and_b32 v45, v5, v48\n"
      "v_mfma_f32_16x16x16_bf16 v[16:19], v[76:77], v[46:47], v[16:19]\n"
      "v_mfma_f32_16x16x16_bf16 v[20:23], v[78:79], v[46:47], v[20:23]\n"
      "v_sub_f32 v40, v40, v6\n"
      "v_exp_f32 v40, v40\n"
      "v_cvt_pk_bf16_f32 v48, v39, v39\n"
      "v_add_f32 v7, v7, v39\n"
      "v_and_b32 v46, v4, v48\n"
      "v_and_b32 v47, v5, v48\n"
      "v_mfma_f32_16x16x16_bf16 v[16:19], v[84:85], v[44:45], v[16:19]\n"
      "v_mfma_f32_16x16x16_bf16 v[20:23], v[86:87], v[44:45], v[20:23]\n"
      "v_cvt_pk_bf16_f32 v48, v40, v40\n"
      "v_add_f32 v7, v7, v40\n"
      "v_and_b32 v44, v4, v48\n"
      "v_and_b32 v45, v5, v48\n"
      "v_mfma_f32_16x16x16_bf16 v[16:19], v[92:93], v[46:47], v[16:19]\n"
      "v_mfma_f32_16x16x16_bf16 v[20:23], v[94:95], v[46:47], v[20:23]\n"
      "v_mfma_f32_16x16x16_bf16 v[16:19], v[100:101], v[44:45], v[16:19]\n"
      "v_mfma_f32_16x16x16_bf16 v[20:23], v[102:103], v[44:45], v[20:23]\n"
      "s_nop 7\n"
      "s_setpc_b64 s[80:81]\n"
      "L_safeC_%=:\n"
      "s_nop 7\n"
      "v_mov_b32 v16, 0\n"
      "v_mov_b32 v20, 0\n"
      "v_mov_b32 v17, 0\n"
      "v_mov_b32 v21, 0\n"
      "v_mov_b32 v18, 0\n"
      "v_mov_b32 v22, 0\n"
      "v_mov_b32 v19, 0\n"
      "v_mov_b32 v23, 0\n"
      "v_mov_b32 v7, 0\n"
      "v_mov_b32 v6, 0xff61b1e6\n"
      "v_mfma_f32_16x16x32_bf16 v[24:27], v[104:107], v[8:11], 0\n"
      "v_mfma_f32_16x16x32_bf16 v[28:31], v[112:115], v[8:11], 0\n"
      "s_nop 6\n"
      "v_mov_b32_dpp v36, v24 quad_perm:[0,0,0,0] row_mask:0xf bank_mask:0xf\n"
      "v_mov_b32_dpp v43, v26 quad_perm:[2,2,2,2] row_mask:0xf bank_mask:0xf\n"
      "v_mfma_f32_16x16x32_bf16 v[32:35], v[120:123], v[8:11], 0\n"
      "v_add_f32_dpp v36, v25, v36 quad_perm:[1,1,1,1] row_mask:0xf bank_mask:0xf\n"
      "v_add_f32_dpp v43, v27, v43 quad_perm:[3,3,3,3] row_mask:0xf bank_mask:0xf\n"
      "v_add_f32 v36, v36, v43\n"
      "v_mov_b32_dpp v37, v28 quad_perm:[0,0,0,0] row_mask:0xf bank_mask:0xf\n"
      "v_mov_b32_dpp v43, v30 quad_perm:[2,2,2,2] row_mask:0xf bank_mask:0xf\n"
      "v_mfma_f32_16x16x32_bf16 v[24:27], v[128:131], v[8:11], 0\n"
      "v_add_f32_dpp v37, v29, v37 quad_perm:[1,1,1,1] row_mask:0xf bank_mask:0xf\n"
      "v_add_f32_dpp v43, v31, v43 quad_perm:[3,3,3,3] row_mask:0xf bank_mask:0xf\n"
      "v_add_f32 v37, v37, v43\n"
      "v_mov_b32_dpp v38, v32 quad_perm:[0,0,0,0] row_mask:0xf bank_mask:0xf\n"
      "v_mov_b32_dpp v43, v34 quad_perm:[2,2,2,2] row_mask:0xf bank_mask:0xf\n"
      "v_mfma_f32_16x16x32_bf16 v[28:31], v[136:139], v[8:11], 0\n"
      "v_add_f32_dpp v38, v33, v38 quad_perm:[1,1,1,1] row_mask:0xf bank_mask:0xf\n"
      "v_add_f32_dpp v43, v35, v43 quad_perm:[3,3,3,3] row_mask:0xf bank_mask:0xf\n"
      "v_add_f32 v38, v38, v43\n"
      "v_mov_b32_dpp v39, v24 quad_perm:[0,0,0,0] row_mask:0xf bank_mask:0xf\n"
      "v_mov_b32_dpp v43, v26 quad_perm:[2,2,2,2] row_mask:0xf bank_mask:0xf\n"
      "v_mfma_f32_16x16x32_bf16 v[32:35], v[144:147], v[8:11], 0\n"
      "v_add_f32_dpp v39, v25, v39 quad_perm:[1,1,1,1] row_mask:0xf bank_mask:0xf\n"
      "v_add_f32_dpp v43, v27, v43 quad_perm:[3,3,3,3] row_mask:0xf bank_mask:0xf\n"
      "v_add_f32 v39, v39, v43\n"
      "v_mov_b32_dpp v40, v28 quad_perm:[0,0,0,0] row_mask:0xf bank_mask:0xf\n"
      "v_mov_b32_dpp v43, v30 quad_perm:[2,2,2,2] row_mask:0xf bank_mask:0xf\n"
      "v_mfma_f32_16x16x32_bf16 v[24:27], v[152:155], v[8:11], 0\n"
      "v_add_f32_dpp v40, v29, v40 quad_perm:[1,1,1,1] row_mask:0xf bank_mask:0xf\n"
      "v_add_f32_dpp v43, v31, v43 quad_perm:[3,3,3,3] row_mask:0xf bank_mask:0xf\n"
      "v_add_f32 v40, v40, v43\n"
      "v_mov_b32_dpp v41, v32 quad_perm:[0,0,0,0] row_mask:0xf bank_mask:0xf\n"
      "v_mov_b32_dpp v43, v34 quad_perm:[2,2,2,2] row_mask:0xf bank_mask:0xf\n"
      "s_nop 0\n"
      "v_add_f32_dpp v41, v33, v41 quad_perm:[1,1,1,1] row_mask:0xf bank_mask:0xf\n"
      "v_add_f32_dpp v43, v35, v43 quad_perm:[3,3,3,3] row_mask:0xf bank_mask:0xf\n"
      "v_add_f32 v41, v41, v43\n"
      "v_mov_b32_dpp v42, v24 quad_perm:[0,0,0,0] row_mask:0xf bank_mask:0xf\n"
      "v_mov_b32_dpp v43, v26 quad_perm:[2,2,2,2] row_mask:0xf bank_mask:0xf\n"
      "s_nop 0\n"
      "v_add_f32_dpp v42, v25, v42 quad_perm:[1,1,1,1] row_mask:0xf bank_mask:0xf\n"
      "v_add_f32_dpp v43, v27, v43 quad_perm:[3,3,3,3] row_mask:0xf bank_mask:0xf\n"
      "v_add_f32 v42, v42, v43\n"
      "v_max3_f32 v49, v36, v37, v38\n"
      "v_max3_f32 v49, v49, v39, v40\n"
      "v_max3_f32 v49, v49, v41, v42\n"
      "v_max_f32 v49, v49, v6\n"
      "v_mov_b32 v50, v49\n"
      "s_nop 1\n"
      "v_permlane32_swap_b32 v50, v49\n"
      "v_max_f32 v49, v50, v49\n"
      "v_mov_b32 v50, v49\n"
      "s_nop 1\n"
      "v_permlane16_swap_b32 v50, v49\n"
      "v_max_f32 v49, v50, v49\n"
      "v_sub_f32 v50, v6, v49\n"
      "v_exp_f32 v50, v50\n"
      "v_mov_b32 v6, v49\n"
      "v_mul_f32 v7, v50, v7\n"
      "v_mul_f32 v16, v50, v16\n"
      "v_mul_f32 v20, v50, v20\n"
      "v_mul_f32 v17, v50, v17\n"
      "v_mul_f32 v21, v50, v21\n"
      "v_mul_f32 v18, v50, v18\n"
      "v_mul_f32 v22, v50, v22\n"
      "v_mul_f32 v19, v50, v19\n"
      "v_mul_f32 v23, v50, v23\n"
      "v_sub_f32 v36, v36, v6\n"
      "v_exp_f32 v36, v36\n"
      "v_sub_f32 v37, v37, v6\n"
      "v_exp_f32 v37, v37\n"
      "v_cvt_pk_bf16_f32 v48, v36, v36\n"
      "v_add_f32 v7, v7, v36\n"
      "v_and_b32 v44, v4, v48\n"
      "v_and_b32 v45, v5, v48\n"
      "v_sub_f32 v38, v38, v6\n"
      "v_exp_f32 v38, v38\n"
      "v_cvt_pk_bf16_f32 v48, v37, v37\n"
      "v_add_f32 v7, v7, v37\n"
      "v_and_b32 v46, v4, v48\n"
      "v_and_b32 v47, v5, v48\n"
      "v_mfma_f32_16x16x16_bf16 v[16:19], v[108:109], v[44:45], v[16:19]\n"
      "v_mfma_f32_16x16x16_bf16 v[20:23], v[110:111], v[44:45], v[20:23]\n"
      "v_sub_f32 v39, v39, v6\n"
      "v_exp_f32 v39, v39\n"
      "v_cvt_pk_bf16_f32 v48, v38, v38\n"
      "v_add_f32 v7, v7, v38\n"
      "v_and_b32 v44, v4, v48\n"
      "v_and_b32 v45, v5, v48\n"
      "v_mfma_f32_16x16x16_bf16 v[16:19], v[116:117], v[46:47], v[16:19]\n"
      "v_mfma_f32_16x16x16_bf16 v[20:23], v[118:119], v[46:47], v[20:23]\n"
      "v_sub_f32 v40, v40, v6\n"
      "v_exp_f32 v40, v40\n"
      "v_cvt_pk_bf16_f32 v48, v39, v39\n"
      "v_add_f32 v7, v7, v39\n"
      "v_and_b32 v46, v4, v48\n"
      "v_and_b32 v47, v5, v48\n"
      "v_mfma_f32_16x16x16_bf16 v[16:19], v[124:125], v[44:45], v[16:19]\n"
      "v_mfma_f32_16x16x16_bf16 v[20:23], v[126:127], v[44:45], v[20:23]\n"
      "v_sub_f32 v41, v41, v6\n"
      "v_exp_f32 v41, v41\n"
      "v_cvt_pk_bf16_f32 v48, v40, v40\n"
      "v_add_f32 v7, v7, v40\n"
      "v_and_b32 v44, v4, v48\n"
      "v_and_b32 v45, v5, v48\n"
      "v_mfma_f32_16x16x16_bf16 v[16:19], v[132:133], v[46:47], v[16:19]\n"
      "v_mfma_f32_16x16x16_bf16 v[20:23], v[134:135], v[46:47], v[20:23]\n"
      "v_sub_f32 v42, v42, v6\n"
      "v_exp_f32 v42, v42\n"
      "v_cvt_pk_bf16_f32 v48, v41, v41\n"
      "v_add_f32 v7, v7, v41\n"
      "v_and_b32 v46, v4, v48\n"
      "v_and_b32 v47, v5, v48\n"
      "v_mfma_f32_16x16x16_bf16 v[16:19], v[140:141], v[44:45], v[16:19]\n"
      "v_mfma_f32_16x16x16_bf16 v[20:23], v[142:143], v[44:45], v[20:23]\n"
      "v_cvt_pk_bf16_f32 v48, v42, v42\n"
      "v_add_f32 v7, v7, v42\n"
      "v_and_b32 v44, v4, v48\n"
      "v_and_b32 v45, v5, v48\n"
      "v_mfma_f32_16x16x16_bf16 v[16:19], v[148:149], v[46:47], v[16:19]\n"
      "v_mfma_f32_16x16x16_bf16 v[20:23], v[150:151], v[46:47], v[20:23]\n"
      "v_mfma_f32_16x16x16_bf16 v[16:19], v[156:157], v[44:45], v[16:19]\n"
      "v_mfma_f32_16x16x16_bf16 v[20:23], v[158:159], v[44:45], v[20:23]\n"
      "v_mfma_f32_16x16x32_bf16 v[24:27], v[160:163], v[8:11], 0\n"
      "v_mfma_f32_16x16x32_bf16 v[28:31], v[168:171], v[8:11], 0\n"
      "s_nop 6\n"
      "v_mov_b32_dpp v36, v24 quad_perm:[0,0,0,0] row_mask:0xf bank_mask:0xf\n"
      "v_mov_b32_dpp v43, v26 quad_perm:[2,2,2,2] row_mask:0xf bank_mask:0xf\n"
      "v_mfma_f32_16x16x32_bf16 v[32:35], v[176:179], v[8:11], 0\n"
      "v_add_f32_dpp v36, v25, v36 quad_perm:[1,1,1,1] row_mask:0xf bank_mask:0xf\n"
      "v_add_f32_dpp v43, v27, v43 quad_perm:[3,3,3,3] row_mask:0xf bank_mask:0xf\n"
      "v_add_f32 v36, v36, v43\n"
      "v_mov_b32_dpp v37, v28 quad_perm:[0,0,0,0] row_mask:0xf bank_mask:0xf\n"
      "v_mov_b32_dpp v43, v30 quad_perm:[2,2,2,2] row_mask:0xf bank_mask:0xf\n"
      "v_mfma_f32_16x16x32_bf16 v[24:27], v[184:187], v[8:11], 0\n"
      "v_add_f32_dpp v37, v29, v37 quad_perm:[1,1,1,1] row_mask:0xf bank_mask:0xf\n"
      "v_add_f32_dpp v43, v31, v43 quad_perm:[3,3,3,3] row_mask:0xf bank_mask:0xf\n"
      "v_add_f32 v37, v37, v43\n"
      "v_mov_b32_dpp v38, v32 quad_perm:[0,0,0,0] row_mask:0xf bank_mask:0xf\n"
      "v_mov_b32_dpp v43, v34 quad_perm:[2,2,2,2] row_mask:0xf bank_mask:0xf\n"
      "v_mfma_f32_16x16x32_bf16 v[28:31], v[192:195], v[8:11], 0\n"
      "v_add_f32_dpp v38, v33, v38 quad_perm:[1,1,1,1] row_mask:0xf bank_mask:0xf\n"
      "v_add_f32_dpp v43, v35, v43 quad_perm:[3,3,3,3] row_mask:0xf bank_mask:0xf\n"
      "v_add_f32 v38, v38, v43\n"
      "v_mov_b32_dpp v39, v24 quad_perm:[0,0,0,0] row_mask:0xf bank_mask:0xf\n"
      "v_mov_b32_dpp v43, v26 quad_perm:[2,2,2,2] row_mask:0xf bank_mask:0xf\n"
      "v_mfma_f32_16x16x32_bf16 v[32:35], v[200:203], v[8:11], 0\n"
      "v_add_f32_dpp v39, v25, v39 quad_perm:[1,1,1,1] row_mask:0xf bank_mask:0xf\n"
      "v_add_f32_dpp v43, v27, v43 quad_perm:[3,3,3,3] row_mask:0xf bank_mask:0xf\n"
      "v_add_f32 v39, v39, v43\n"
      "v_mov_b32_dpp v40, v28 quad_perm:[0,0,0,0] row_mask:0xf bank_mask:0xf\n"
      "v_mov_b32_dpp v43, v30 quad_perm:[2,2,2,2] row_mask:0xf bank_mask:0xf\n"
      "v_mfma_f32_16x16x32_bf16 v[24:27], v[208:211], v[8:11], 0\n"
      "v_add_f32_dpp v40, v29, v40 quad_perm:[1,1,1,1] row_mask:0xf bank_mask:0xf\n"
      "v_add_f32_dpp v43, v31, v43 quad_perm:[3,3,3,3] row_mask:0xf bank_mask:0xf\n"
      "v_add_f32 v40, v40, v43\n"
      "v_mov_b32_dpp v41, v32 quad_perm:[0,0,0,0] row_mask:0xf bank_mask:0xf\n"
      "v_mov_b32_dpp v43, v34 quad_perm:[2,2,2,2] row_mask:0xf bank_mask:0xf\n"
      "s_nop 0\n"
      "v_add_f32_dpp v41, v33, v41 quad_perm:[1,1,1,1] row_mask:0xf bank_mask:0xf\n"
      "v_add_f32_dpp v43, v35, v43 quad_perm:[3,3,3,3] row_mask:0xf bank_mask:0xf\n"
      "v_add_f32 v41, v41, v43\n"
      "v_mov_b32_dpp v42, v24 quad_perm:[0,0,0,0] row_mask:0xf bank_mask:0xf\n"
      "v_mov_b32_dpp v43, v26 quad_perm:[2,2,2,2] row_mask:0xf bank_mask:0xf\n"
      "s_nop 0\n"
      "v_add_f32_dpp v42, v25, v42 quad_perm:[1,1,1,1] row_mask:0xf bank_mask:0xf\n"
      "v_add_f32_dpp v43, v27, v43 quad_perm:[3,3,3,3] row_mask:0xf bank_mask:0xf\n"
      "v_add_f32 v42, v42, v43\n"
      "v_max3_f32 v49, v36, v37, v38\n"
      "v_max3_f32 v49, v49, v39, v40\n"
      "v_max3_f32 v49, v49, v41, v42\n"
      "v_max_f32 v49, v49, v6\n"
      "v_mov_b32 v50, v49\n"
      "s_nop 1\n"
      "v_permlane32_swap_b32 v50, v49\n"
      "v_max_f32 v49, v50, v49\n"
      "v_mov_b32 v50, v49\n"
      "s_nop 1\n"
      "v_permlane16_swap_b32 v50, v49\n"
      "v_max_f32 v49, v50, v49\n"
      "v_sub_f32 v50, v6, v49\n"
      "v_exp_f32 v50, v50\n"
      "v_mov_b32 v6, v49\n"
      "v_mul_f32 v7, v50, v7\n"
      "v_mul_f32 v16, v50, v16\n"
      "v_mul_f32 v20, v50, v20\n"
      "v_mul_f32 v17, v50, v17\n"
      "v_mul_f32 v21, v50, v21\n"
      "v_mul_f32 v18, v50, v18\n"
      "v_mul_f32 v22, v50, v22\n"
      "v_mul_f32 v19, v50, v19\n"
      "v_mul_f32 v23, v50, v23\n"
      "v_sub_f32 v36, v36, v6\n"
      "v_exp_f32 v36, v36\n"
      "v_sub_f32 v37, v37, v6\n"
      "v_exp_f32 v37, v37\n"
      "v_cvt_pk_bf16_f32 v48, v36, v36\n"
      "v_add_f32 v7, v7, v36\n"
      "v_and_b32 v44, v4, v48\n"
      "v_and_b32 v45, v5, v48\n"
      "v_sub_f32 v38, v38, v6\n"
      "v_exp_f32 v38, v38\n"
      "v_cvt_pk_bf16_f32 v48, v37, v37\n"
      "v_add_f32 v7, v7, v37\n"
      "v_and_b32 v46, v4, v48\n"
      "v_and_b32 v47, v5, v48\n"
      "v_mfma_f32_16x16x16_bf16 v[16:19], v[164:165], v[44:45], v[16:19]\n"
      "v_mfma_f32_16x16x16_bf16 v[20:23], v[166:167], v[44:45], v[20:23]\n"
      "v_sub_f32 v39, v39, v6\n"
      "v_exp_f32 v39, v39\n"
      "v_cvt_pk_bf16_f32 v48, v38, v38\n"
      "v_add_f32 v7, v7, v38\n"
      "v_and_b32 v44, v4, v48\n"
      "v_and_b32 v45, v5, v48\n"
      "v_mfma_f32_16x16x16_bf16 v[16:19], v[172:173], v[46:47], v[16:19]\n"
      "v_mfma_f32_16x16x16_bf16 v[20:23], v[174:175], v[46:47], v[20:23]\n"
      "v_sub_f32 v40, v40, v6\n"
      "v_exp_f32 v40, v40\n"
      "v_cvt_pk_bf16_f32 v48, v39, v39\n"
      "v_add_f32 v7, v7, v39\n"
      "v_and_b32 v46, v4, v48\n"
      "v_and_b32 v47, v5, v48\n"
      "v_mfma_f32_16x16x16_bf16 v[16:19], v[180:181], v[44:45], v[16:19]\n"
      "v_mfma_f32_16x16x16_bf16 v[20:23], v[182:183], v[44:45], v[20:23]\n"
      "v_sub_f32 v41, v41, v6\n"
      "v_exp_f32 v41, v41\n"
      "v_cvt_pk_bf16_f32 v48, v40, v40\n"
      "v_add_f32 v7, v7, v40\n"
      "v_and_b32 v44, v4, v48\n"
      "v_and_b32 v45, v5, v48\n"
      "v_mfma_f32_16x16x16_bf16 v[16:19], v[188:189], v[46:47], v[16:19]\n"
      "v_mfma_f32_16x16x16_bf16 v[20:23], v[190:191], v[46:47], v[20:23]\n"
      "v_sub_f32 v42, v42, v6\n"
      "v_exp_f32 v42, v42\n"
      "v_cvt_pk_bf16_f32 v48, v41, v41\n"
      "v_add_f32 v7, v7, v41\n"
      "v_and_b32 v46, v4, v48\n"
      "v_and_b32 v47, v5, v48\n"
      "v_mfma_f32_16x16x16_bf16 v[16:19], v[196:197], v[44:45], v[16:19]\n"
      "v_mfma_f32_16x16x16_bf16 v[20:23], v[198:199], v[44:45], v[20:23]\n"
      "v_cvt_pk_bf16_f32 v48, v42, v42\n"
      "v_add_f32 v7, v7, v42\n"
      "v_and_b32 v44, v4, v48\n"
      "v_and_b32 v45, v5, v48\n"
      "v_mfma_f32_16x16x16_bf16 v[16:19], v[204:205], v[46:47], v[16:19]\n"
      "v_mfma_f32_16x16x16_bf16 v[20:23], v[206:207], v[46:47], v[20:23]\n"
      "v_mfma_f32_16x16x16_bf16 v[16:19], v[212:213], v[44:45], v[16:19]\n"
      "v_mfma_f32_16x16x16_bf16 v[20:23], v[214:215], v[44:45], v[20:23]\n"
      "v_mfma_f32_16x16x32_bf16 v[24:27], v[216:219], v[8:11], 0\n"
      "v_mfma_f32_16x16x32_bf16 v[28:31], v[224:227], v[8:11], 0\n"
      "s_nop 6\n"
      "v_mov_b32_dpp v36, v24 quad_perm:[0,0,0,0] row_mask:0xf bank_mask:0xf\n"
      "v_mov_b32_dpp v43, v26 quad_perm:[2,2,2,2] row_mask:0xf bank_mask:0xf\n"
      "v_mfma_f32_16x16x32_bf16 v[32:35], v[232:235], v[8:11], 0\n"
      "v_add_f32_dpp v36, v25, v36 quad_perm:[1,1,1,1] row_mask:0xf bank_mask:0xf\n"
      "v_add_f32_dpp v43, v27, v43 quad_perm:[3,3,3,3] row_mask:0xf bank_mask:0xf\n"
      "v_add_f32 v36, v36, v43\n"
      "v_mov_b32_dpp v37, v28 quad_perm:[0,0,0,0] row_mask:0xf bank_mask:0xf\n"
      "v_mov_b32_dpp v43, v30 quad_perm:[2,2,2,2] row_mask:0xf bank_mask:0xf\n"
      "v_mfma_f32_16x16x32_bf16 v[24:27], v[240:243], v[8:11], 0\n"
      "v_add_f32_dpp v37, v29, v37 quad_perm:[1,1,1,1] row_mask:0xf bank_mask:0xf\n"
      "v_add_f32_dpp v43, v31, v43 quad_perm:[3,3,3,3] row_mask:0xf bank_mask:0xf\n"
      "v_add_f32 v37, v37, v43\n"
      "v_mov_b32_dpp v38, v32 quad_perm:[0,0,0,0] row_mask:0xf bank_mask:0xf\n"
      "v_mov_b32_dpp v43, v34 quad_perm:[2,2,2,2] row_mask:0xf bank_mask:0xf\n"
      "v_mfma_f32_16x16x32_bf16 v[28:31], v[248:251], v[8:11], 0\n"
      "v_add_f32_dpp v38, v33, v38 quad_perm:[1,1,1,1] row_mask:0xf bank_mask:0xf\n"
      "v_add_f32_dpp v43, v35, v43 quad_perm:[3,3,3,3] row_mask:0xf bank_mask:0xf\n"
      "v_add_f32 v38, v38, v43\n"
      "v_mov_b32_dpp v39, v24 quad_perm:[0,0,0,0] row_mask:0xf bank_mask:0xf\n"
      "v_mov_b32_dpp v43, v26 quad_perm:[2,2,2,2] row_mask:0xf bank_mask:0xf\n"
      "s_nop 0\n"
      "v_add_f32_dpp v39, v25, v39 quad_perm:[1,1,1,1] row_mask:0xf bank_mask:0xf\n"
      "v_add_f32_dpp v43, v27, v43 quad_perm:[3,3,3,3] row_mask:0xf bank_mask:0xf\n"
      "v_add_f32 v39, v39, v43\n"
      "v_mov_b32_dpp v40, v28 quad_perm:[0,0,0,0] row_mask:0xf bank_mask:0xf\n"
      "v_mov_b32_dpp v43, v30 quad_perm:[2,2,2,2] row_mask:0xf bank_mask:0xf\n"
      "s_nop 0\n"
      "v_add_f32_dpp v40, v29, v40 quad_perm:[1,1,1,1] row_mask:0xf bank_mask:0xf\n"
      "v_add_f32_dpp v43, v31, v43 quad_perm:[3,3,3,3] row_mask:0xf bank_mask:0xf\n"
      "v_add_f32 v40, v40, v43\n"
      "v_add_f32 v40, s69, v40\n"
      "v_max3_f32 v49, v36, v37, v38\n"
      "v_max3_f32 v49, v49, v39, v40\n"
      "v_max_f32 v49, v49, v6\n"
      "v_mov_b32 v50, v49\n"
      "s_nop 1\n"
      "v_permlane32_swap_b32 v50, v49\n"
      "v_max_f32 v49, v50, v49\n"
      "v_mov_b32 v50, v49\n"
      "s_nop 1\n"
      "v_permlane16_swap_b32 v50, v49\n"
      "v_max_f32 v49, v50, v49\n"
      "v_sub_f32 v50, v6, v49\n"
      "v_exp_f32 v50, v50\n"
      "v_mov_b32 v6, v49\n"
      "v_mul_f32 v7, v50, v7\n"
      "v_mul_f32 v16, v50, v16\n"
      "v_mul_f32 v20, v50, v20\n"
      "v_mul_f32 v17, v50, v17\n"
      "v_mul_f32 v21, v50, v21\n"
      "v_mul_f32 v18, v50, v18\n"
      "v_mul_f32 v22, v50, v22\n"
      "v_mul_f32 v19, v50, v19\n"
      "v_mul_f32 v23, v50, v23\n"
      "v_sub_f32 v36, v36, v6\n"
      "v_exp_f32 v36, v36\n"
      "v_sub_f32 v37, v37, v6\n"
      "v_exp_f32 v37, v37\n"
      "v_cvt_pk_bf16_f32 v48, v36, v36\n"
      "v_add_f32 v7, v7, v36\n"
      "v_and_b32 v44, v4, v48\n"
      "v_and_b32 v45, v5, v48\n"
      "v_sub_f32 v38, v38, v6\n"
      "v_exp_f32 v38, v38\n"
      "v_cvt_pk_bf16_f32 v48, v37, v37\n"
      "v_add_f32 v7, v7, v37\n"
      "v_and_b32 v46, v4, v48\n"
      "v_and_b32 v47, v5, v48\n"
      "v_mfma_f32_16x16x16_bf16 v[16:19], v[220:221], v[44:45], v[16:19]\n"
      "v_mfma_f32_16x16x16_bf16 v[20:23], v[222:223], v[44:45], v[20:23]\n"
      "v_sub_f32 v39, v39, v6\n"
      "v_exp_f32 v39, v39\n"
      "v_cvt_pk_bf16_f32 v48, v38, v38\n"
      "v_add_f32 v7, v7, v38\n"
      "v_and_b32 v44, v4, v48\n"
      "v_and_b32 v45, v5, v48\n"
      "v_mfma_f32_16x16x16_bf16 v[16:19], v[228:229], v[46:47], v[16:19]\n"
      "v_mfma_f32_16x16x16_bf16 v[20:23], v[230:231], v[46:47], v[20:23]\n"
      "v_sub_f32 v40, v40, v6\n"
      "v_exp_f32 v40, v40\n"
      "v_cvt_pk_bf16_f32 v48, v39, v39\n"
      "v_add_f32 v7, v7, v39\n"
      "v_and_b32 v46, v4, v48\n"
      "v_and_b32 v47, v5, v48\n"
      "v_mfma_f32_16x16x16_bf16 v[16:19], v[236:237], v[44:45], v[16:19]\n"
      "v_mfma_f32_16x16x16_bf16 v[20:23], v[238:239], v[44:45], v[20:23]\n"
      "v_cvt_pk_bf16_f32 v48, v40, v40\n"
      "v_add_f32 v7, v7, v40\n"
      "v_and_b32 v44, v4, v48\n"
      "v_and_b32 v45, v5, v48\n"
      "v_mfma_f32_16x16x16_bf16 v[16:19], v[244:245], v[46:47], v[16:19]\n"
      "v_mfma_f32_16x16x16_bf16 v[20:23], v[246:247], v[46:47], v[20:23]\n"
      "v_mfma_f32_16x16x16_bf16 v[16:19], v[252:253], v[44:45], v[16:19]\n"
      "v_mfma_f32_16x16x16_bf16 v[20:23], v[254:255], v[44:45], v[20:23]\n"
      "s_nop 7\n"
      "s_setpc_b64 s[80:81]\n"
      "L_safeL_%=:\n"
      "s_nop 7\n"
      "v_mov_b32 v16, 0\n"
      "v_mov_b32 v20, 0\n"
      "v_mov_b32 v17, 0\n"
      "v_mov_b32 v21, 0\n"
      "v_mov_b32 v18, 0\n"
      "v_mov_b32 v22, 0\n"
      "v_mov_b32 v19, 0\n"
      "v_mov_b32 v23, 0\n"
      "v_mov_b32 v7, 0\n"
      "v_mov_b32 v6, 0xff61b1e6\n"
      "ds_read_b128 v[44:47], v3 offset:0\n"
      "ds_read_b128 v[48:51], v3 offset:2048\n"
      "ds_read_b128 v[52:55], v3 offset:4096\n"
      "s_waitcnt lgkmcnt(2)\n"
      "v_mfma_f32_16x16x32_bf16 v[24:27], v[44:47], v[8:11], 0\n"
      "ds_read_b128 v[44:47], v3 offset:6144\n"
      "s_waitcnt lgkmcnt(2)\n"
      "v_mfma_f32_16x16x32_bf16 v[28:31], v[48:51], v[8:11], 0\n"
      "ds_read_b128 v[48:51], v3 offset:8192\n"
      "s_nop 3\n"
      "v_mov_b32_dpp v36, v24 quad_perm:[0,0,0,0] row_mask:0xf bank_mask:0xf\n"
      "v_mov_b32_dpp v43, v26 quad_perm:[2,2,2,2] row_mask:0xf bank_mask:0xf\n"
      "s_waitcnt lgkmcnt(2)\n"
      "v_mfma_f32_16x16x32_bf16 v[32:35], v[52:55], v[8:11], 0\n"
      "ds_read_b128 v[52:55], v3 offset:10240\n"
      "v_add_f32_dpp v36, v25, v36 quad_perm:[1,1,1,1] row_mask:0xf bank_mask:0xf\n"
      "v_add_f32_dpp v43, v27, v43 quad_perm:[3,3,3,3] row_mask:0xf bank_mask:0xf\n"
      "v_add_f32 v36, v36, v43\n"
      "v_mov_b32_dpp v37, v28 quad_perm:[0,0,0,0] row_mask:0xf bank_mask:0xf\n"
      "v_mov_b32_dpp v43, v30 quad_perm:[2,2,2,2] row_mask:0xf bank_mask:0xf\n"
      "s_waitcnt lgkmcnt(2)\n"
      "v_mfma_f32_16x16x32_bf16 v[24:27], v[44:47], v[8:11], 0\n"
      "ds_read_b128 v[44:47], v3 offset:12288\n"
      "v_add_f32_dpp v37, v29, v37 quad_perm:[1,1,1,1] row_mask:0xf bank_mask:0xf\n"
      "v_add_f32_dpp v43, v31, v43 quad_perm:[3,3,3,3] row_mask:0xf bank_mask:0xf\n"
      "v_add_f32 v37, v37, v43\n"
      "v_mov_b32_dpp v38, v32 quad_perm:[0,0,0,0] row_mask:0xf bank_mask:0xf\n"
      "v_mov_b32_dpp v43, v34 quad_perm:[2,2,2,2] row_mask:0xf bank_mask:0xf\n"
      "s_waitcnt lgkmcnt(2)\n"
      "v_mfma_f32_16x16x32_bf16 v[28:31], v[48:51], v[8:11], 0\n"
      "v_add_f32_dpp v38, v33, v38 quad_perm:[1,1,1,1] row_mask:0xf bank_mask:0xf\n"
      "v_add_f32_dpp v43, v35, v43 quad_perm:[3,3,3,3] row_mask:0xf bank_mask:0xf\n"
      "v_add_f32 v38, v38, v43\n"
      "v_mov_b32_dpp v39, v24 quad_perm:[0,0,0,0] row_mask:0xf bank_mask:0xf\n"
      "v_mov_b32_dpp v43, v26 quad_perm:[2,2,2,2] row_mask:0xf bank_mask:0xf\n"
      "s_waitcnt lgkmcnt(1)\n"
      "v_mfma_f32_16x16x32_bf16 v[32:35], v[52:55], v[8:11], 0\n"
      "v_add_f32_dpp v39, v25, v39 quad_perm:[1,1,1,1] row_mask:0xf bank_mask:0xf\n"
      "v_add_f32_dpp v43, v27, v43 quad_perm:[3,3,3,3] row_mask:0xf bank_mask:0xf\n"
      "v_add_f32 v39, v39, v43\n"
      "v_mov_b32_dpp v40, v28 quad_perm:[0,0,0,0] row_mask:0xf bank_mask:0xf\n"
      "v_mov_b32_dpp v43, v30 quad_perm:[2,2,2,2] row_mask:0xf bank_mask:0xf\n"
      "s_waitcnt lgkmcnt(0)\n"
      "v_mfma_f32_16x16x32_bf16 v[24:27], v[44:47], v[8:11], 0\n"
      "v_add_f32_dpp v40, v29, v40 quad_perm:[1,1,1,1] row_mask:0xf bank_mask:0xf\n"
      "v_add_f32_dpp v43, v31, v43 quad_perm:[3,3,3,3] row_mask:0xf bank_mask:0xf\n"
      "v_add_f32 v40, v40, v43\n"
      "v_mov_b32_dpp v41, v32 quad_perm:[0,0,0,0] row_mask:0xf bank_mask:0xf\n"
      "v_mov_b32_dpp v43, v34 quad_perm:[2,2,2,2] row_mask:0xf bank_mask:0xf\n"
      "s_nop 0\n"
      "v_add_f32_dpp v41, v33, v41 quad_perm:[1,1,1,1] row_mask:0xf bank_mask:0xf\n"
      "v_add_f32_dpp v43, v35, v43 quad_perm:[3,3,3,3] row_mask:0xf bank_mask:0xf\n"
      "v_add_f32 v41, v41, v43\n"
      "v_mov_b32_dpp v42, v24 quad_perm:[0,0,0,0] row_mask:0xf bank_mask:0xf\n"
      "v_mov_b32_dpp v43, v26 quad_perm:[2,2,2,2] row_mask:0xf bank_mask:0xf\n"
      "s_nop 0\n"
      "v_add_f32_dpp v42, v25, v42 quad_perm:[1,1,1,1] row_mask:0xf bank_mask:0xf\n"
      "v_add_f32_dpp v43, v27, v43 quad_perm:[3,3,3,3] row_mask:0xf bank_mask:0xf\n"
      "v_add_f32 v42, v42, v43\n"
      "v_max3_f32 v49, v36, v37, v38\n"
      "v_max3_f32 v49, v49, v39, v40\n"
      "v_max3_f32 v49, v49, v41, v42\n"
      "v_max_f32 v49, v49, v6\n"
      "ds_read_b128 v[24:27], v3 offset:1024\n"
      "ds_read_b128 v[28:31], v3 offset:3072\n"
      "ds_read_b128 v[32:35], v3 offset:5120\n"
      "v_mov_b32 v50, v49\n"
      "s_nop 1\n"
      "v_permlane32_swap_b32 v50, v49\n"
      "v_max_f32 v49, v50, v49\n"
      "v_mov_b32 v50, v49\n"
      "s_nop 1\n"
      "v_permlane16_swap_b32 v50, v49\n"
      "v_max_f32 v49, v50, v49\n"
      "v_sub_f32 v50, v6, v49\n"
      "v_exp_f32 v50, v50\n"
      "v_mov_b32 v6, v49\n"
      "v_mul_f32 v7, v50, v7\n"
      "v_mul_f32 v16, v50, v16\n"
      "v_mul_f32 v20, v50, v20\n"
      "v_mul_f32 v17, v50, v17\n"
      "v_mul_f32 v21, v50, v21\n"
      "v_mul_f32 v18, v50, v18\n"
      "v_mul_f32 v22, v50, v22\n"
      "v_mul_f32 v19, v50, v19\n"
      "v_mul_f32 v23, v50, v23\n"
      "v_sub_f32 v36, v36, v6\n"
      "v_exp_f32 v36, v36\n"
      "v_sub_f32 v37, v37, v6\n"
      "v_exp_f32 v37, v37\n"
      "v_cvt_pk_bf16_f32 v48, v36, v36\n"
      "v_add_f32 v7, v7, v36\n"
      "v_and_b32 v44, v4, v48\n"
      "v_and_b32 v45, v5, v48\n"
      "v_sub_f32 v38, v38, v6\n"
      "v_exp_f32 v38, v38\n"
      "v_cvt_pk_bf16_f32 v48, v37, v37\n"
      "v_add_f32 v7, v7, v37\n"
      "v_and_b32 v46, v4, v48\n"
      "v_and_b32 v47, v5, v48\n"
      "s_waitcnt lgkmcnt(2)\n"
      "v_mfma_f32_16x16x16_bf16 v[16:19], v[24:25], v[44:45], v[16:19]\n"
      "v_mfma_f32_16x16x16_bf16 v[20:23], v[26:27], v[44:45], v[20:23]\n"
      "ds_read_b128 v[24:27], v3 offset:7168\n"
      "v_sub_f32 v39, v39, v6\n"
      "v_exp_f32 v39, v39\n"
      "v_cvt_pk_bf16_f32 v48, v38, v38\n"
      "v_add_f32 v7, v7, v38\n"
      "v_and_b32 v44, v4, v48\n"
      "v_and_b32 v45, v5, v48\n"
      "s_waitcnt lgkmcnt(2)\n"
      "v_mfma_f32_16x16x16_bf16 v[16:19], v[28:29], v[46:47], v[16:19]\n"
      "v_mfma_f32_16x16x16_bf16 v[20:23], v[30:31], v[46:47], v[20:23]\n"
      "ds_read_b128 v[28:31], v3 offset:9216\n"
      "v_sub_f32 v40, v40, v6\n"
      "v_exp_f32 v40, v40\n"
      "v_cvt_pk_bf16_f32 v48, v39, v39\n"
      "v_add_f32 v7, v7, v39\n"
      "v_and_b32 v46, v4, v48\n"
      "v_and_b32 v47, v5, v48\n"
      "s_waitcnt lgkmcnt(2)\n"
      "v_mfma_f32_16x16x16_bf16 v[16:19], v[32:33], v[44:45], v[16:19]\n"
      "v_mfma_f32_16x16x16_bf16 v[20:23], v[34:35], v[44:45], v[20:23]\n"
      "ds_read_b128 v[32:35], v3 offset:11264\n"
      "v_sub_f32 v41, v41, v6\n"
      "v_exp_f32 v41, v41\n"
      "v_cvt_pk_bf16_f32 v48, v40, v40\n"
      "v_add_f32 v7, v7, v40\n"
      "v_and_b32 v44, v4, v48\n"
      "v_and_b32 v45, v5, v48\n"
      "s_waitcnt lgkmcnt(2)\n"
      "v_mfma_f32_16x16x16_bf16 v[16:19], v[24:25], v[46:47], v[16:19]\n"
      "v_mfma_f32_16x16x16_bf16 v[20:23], v[26:27], v[46:47], v[20:23]\n"
      "ds_read_b128 v[24:27], v3 offset:13312\n"
      "v_sub_f32 v42, v42, v6\n"
      "v_exp_f32 v42, v42\n"
      "v_cvt_pk_bf16_f32 v48, v41, v41\n"
      "v_add_f32 v7, v7, v41\n"
      "v_and_b32 v46, v4, v48\n"
      "v_and_b32 v47, v5, v48\n"
      "s_waitcnt lgkmcnt(2)\n"
      "v_mfma_f32_16x16x16_bf16 v[16:19], v[28:29], v[44:45], v[16:19]\n"
      "v_mfma_f32_16x16x16_bf16 v[20:23], v[30:31], v[44:45], v[20:23]\n"
      "v_cvt_pk_bf16_f32 v48, v42, v42\n"
      "v_add_f32 v7, v7, v42\n"
      "v_and_b32 v44, v4, v48\n"
      "v_and_b32 v45, v5, v48\n"
      "s_waitcnt lgkmcnt(1)\n"
      "v_mfma_f32_16x16x16_bf16 v[16:19], v[32:33], v[46:47], v[16:19]\n"
      "v_mfma_f32_16x16x16_bf16 v[20:23], v[34:35], v[46:47], v[20:23]\n"
      "s_waitcnt lgkmcnt(0)\n"
      "v_mfma_f32_16x16x16_bf16 v[16:19], v[24:25], v[44:45], v[16:19]\n"
      "v_mfma_f32_16x16x16_bf16 v[20:23], v[26:27], v[44:45], v[20:23]\n"
      "ds_read_b128 v[44:47], v3 offset:14336\n"
      "ds_read_b128 v[48:51], v3 offset:16384\n"
      "ds_read_b128 v[52:55], v3 offset:18432\n"
      "s_waitcnt lgkmcnt(2)\n"
      "v_mfma_f32_16x16x32_bf16 v[24:27], v[44:47], v[8:11], 0\n"
      "ds_read_b128 v[44:47], v3 offset:20480\n"
      "s_waitcnt lgkmcnt(2)\n"
      "v_mfma_f32_16x16x32_bf16 v[28:31], v[48:51], v[8:11], 0\n"
      "ds_read_b128 v[48:51], v3 offset:22528\n"
      "s_nop 3\n"
      "v_mov_b32_dpp v36, v24 quad_perm:[0,0,0,0] row_mask:0xf bank_mask:0xf\n"
      "v_mov_b32_dpp v43, v26 quad_perm:[2,2,2,2] row_mask:0xf bank_mask:0xf\n"
      "s_waitcnt lgkmcnt(2)\n"
      "v_mfma_f32_16x16x32_bf16 v[32:35], v[52:55], v[8:11], 0\n"
      "ds_read_b128 v[52:55], v3 offset:24576\n"
      "v_add_f32_dpp v36, v25, v36 quad_perm:[1,1,1,1] row_mask:0xf bank_mask:0xf\n"
      "v_add_f32_dpp v43, v27, v43 quad_perm:[3,3,3,3] row_mask:0xf bank_mask:0xf\n"
      "v_add_f32 v36, v36, v43\n"
      "v_mov_b32_dpp v37, v28 quad_perm:[0,0,0,0] row_mask:0xf bank_mask:0xf\n"
      "v_mov_b32_dpp v43, v30 quad_perm:[2,2,2,2] row_mask:0xf bank_mask:0xf\n"
      "s_waitcnt lgkmcnt(2)\n"
      "v_mfma_f32_16x16x32_bf16 v[24:27], v[44:47], v[8:11], 0\n"
      "ds_read_b128 v[44:47], v3 offset:26624\n"
      "v_add_f32_dpp v37, v29, v37 quad_perm:[1,1,1,1] row_mask:0xf bank_mask:0xf\n"
      "v_add_f32_dpp v43, v31, v43 quad_perm:[3,3,3,3] row_mask:0xf bank_mask:0xf\n"
      "v_add_f32 v37, v37, v43\n"
      "v_mov_b32_dpp v38, v32 quad_perm:[0,0,0,0] row_mask:0xf bank_mask:0xf\n"
      "v_mov_b32_dpp v43, v34 quad_perm:[2,2,2,2] row_mask:0xf bank_mask:0xf\n"
      "s_waitcnt lgkmcnt(2)\n"
      "v_mfma_f32_16x16x32_bf16 v[28:31], v[48:51], v[8:11], 0\n"
      "v_add_f32_dpp v38, v33, v38 quad_perm:[1,1,1,1] row_mask:0xf bank_mask:0xf\n"
      "v_add_f32_dpp v43, v35, v43 quad_perm:[3,3,3,3] row_mask:0xf bank_mask:0xf\n"
      "v_add_f32 v38, v38, v43\n"
      "v_mov_b32_dpp v39, v24 quad_perm:[0,0,0,0] row_mask:0xf bank_mask:0xf\n"
      "v_mov_b32_dpp v43, v26 quad_perm:[2,2,2,2] row_mask:0xf bank_mask:0xf\n"
      "s_waitcnt lgkmcnt(1)\n"
      "v_mfma_f32_16x16x32_bf16 v[32:35], v[52:55], v[8:11], 0\n"
      "v_add_f32_dpp v39, v25, v39 quad_perm:[1,1,1,1] row_mask:0xf bank_mask:0xf\n"
      "v_add_f32_dpp v43, v27, v43 quad_perm:[3,3,3,3] row_mask:0xf bank_mask:0xf\n"
      "v_add_f32 v39, v39, v43\n"
      "v_mov_b32_dpp v40, v28 quad_perm:[0,0,0,0] row_mask:0xf bank_mask:0xf\n"
      "v_mov_b32_dpp v43, v30 quad_perm:[2,2,2,2] row_mask:0xf bank_mask:0xf\n"
      "s_waitcnt lgkmcnt(0)\n"
      "v_mfma_f32_16x16x32_bf16 v[24:27], v[44:47], v[8:11], 0\n"
      "v_add_f32_dpp v40, v29, v40 quad_perm:[1,1,1,1] row_mask:0xf bank_mask:0xf\n"
      "v_add_f32_dpp v43, v31, v43 quad_perm:[3,3,3,3] row_mask:0xf bank_mask:0xf\n"
      "v_add_f32 v40, v40, v43\n"
      "v_mov_b32_dpp v41, v32 quad_perm:[0,0,0,0] row_mask:0xf bank_mask:0xf\n"
      "v_mov_b32_dpp v43, v34 quad_perm:[2,2,2,2] row_mask:0xf bank_mask:0xf\n"
      "s_nop 0\n"
      "v_add_f32_dpp v41, v33, v41 quad_perm:[1,1,1,1] row_mask:0xf bank_mask:0xf\n"
      "v_add_f32_dpp v43, v35, v43 quad_perm:[3,3,3,3] row_mask:0xf bank_mask:0xf\n"
      "v_add_f32 v41, v41, v43\n"
      "v_mov_b32_dpp v42, v24 quad_perm:[0,0,0,0] row_mask:0xf bank_mask:0xf\n"
      "v_mov_b32_dpp v43, v26 quad_perm:[2,2,2,2] row_mask:0xf bank_mask:0xf\n"
      "s_nop 0\n"
      "v_add_f32_dpp v42, v25, v42 quad_perm:[1,1,1,1] row_mask:0xf bank_mask:0xf\n"
      "v_add_f32_dpp v43, v27, v43 quad_perm:[3,3,3,3] row_mask:0xf bank_mask:0xf\n"
      "v_add_f32 v42, v42, v43\n"
      "v_max3_f32 v49, v36, v37, v38\n"
      "v_max3_f32 v49, v49, v39, v40\n"
      "v_max3_f32 v49, v49, v41, v42\n"
      "v_max_f32 v49, v49, v6\n"
      "ds_read_b128 v[24:27], v3 offset:15360\n"
      "ds_read_b128 v[28:31], v3 offset:17408\n"
      "ds_read_b128 v[32:35], v3 offset:19456\n"
      "v_mov_b32 v50, v49\n"
      "s_nop 1\n"
      "v_permlane32_swap_b32 v50, v49\n"
      "v_max_f32 v49, v50, v49\n"
      "v_mov_b32 v50, v49\n"
      "s_nop 1\n"
      "v_permlane16_swap_b32 v50, v49\n"
      "v_max_f32 v49, v50, v49\n"
      "v_sub_f32 v50, v6, v49\n"
      "v_exp_f32 v50, v50\n"
      "v_mov_b32 v6, v49\n"
      "v_mul_f32 v7, v50, v7\n"
      "v_mul_f32 v16, v50, v16\n"
      "v_mul_f32 v20, v50, v20\n"
      "v_mul_f32 v17, v50, v17\n"
      "v_mul_f32 v21, v50, v21\n"
      "v_mul_f32 v18, v50, v18\n"
      "v_mul_f32 v22, v50, v22\n"
      "v_mul_f32 v19, v50, v19\n"
      "v_mul_f32 v23, v50, v23\n"
      "v_sub_f32 v36, v36, v6\n"
      "v_exp_f32 v36, v36\n"
      "v_sub_f32 v37, v37, v6\n"
      "v_exp_f32 v37, v37\n"
      "v_cvt_pk_bf16_f32 v48, v36, v36\n"
      "v_add_f32 v7, v7, v36\n"
      "v_and_b32 v44, v4, v48\n"
      "v_and_b32 v45, v5, v48\n"
      "v_sub_f32 v38, v38, v6\n"
      "v_exp_f32 v38, v38\n"
      "v_cvt_pk_bf16_f32 v48, v37, v37\n"
      "v_add_f32 v7, v7, v37\n"
      "v_and_b32 v46, v4, v48\n"
      "v_and_b32 v47, v5, v48\n"
      "s_waitcnt lgkmcnt(2)\n"
      "v_mfma_f32_16x16x16_bf16 v[16:19], v[24:25], v[44:45], v[16:19]\n"
      "v_mfma_f32_16x16x16_bf16 v[20:23], v[26:27], v[44:45], v[20:23]\n"
      "ds_read_b128 v[24:27], v3 offset:21504\n"
      "v_sub_f32 v39, v39, v6\n"
      "v_exp_f32 v39, v39\n"
      "v_cvt_pk_bf16_f32 v48, v38, v38\n"
      "v_add_f32 v7, v7, v38\n"
      "v_and_b32 v44, v4, v48\n"
      "v_and_b32 v45, v5, v48\n"
      "s_waitcnt lgkmcnt(2)\n"
      "v_mfma_f32_16x16x16_bf16 v[16:19], v[28:29], v[46:47], v[16:19]\n"
      "v_mfma_f32_16x16x16_bf16 v[20:23], v[30:31], v[46:47], v[20:23]\n"
      "ds_read_b128 v[28:31], v3 offset:23552\n"
      "v_sub_f32 v40, v40, v6\n"
      "v_exp_f32 v40, v40\n"
      "v_cvt_pk_bf16_f32 v48, v39, v39\n"
      "v_add_f32 v7, v7, v39\n"
      "v_and_b32 v46, v4, v48\n"
      "v_and_b32 v47, v5, v48\n"
      "s_waitcnt lgkmcnt(2)\n"
      "v_mfma_f32_16x16x16_bf16 v[16:19], v[32:33], v[44:45], v[16:19]\n"
      "v_mfma_f32_16x16x16_bf16 v[20:23], v[34:35], v[44:45], v[20:23]\n"
      "ds_read_b128 v[32:35], v3 offset:25600\n"
      "v_sub_f32 v41, v41, v6\n"
      "v_exp_f32 v41, v41\n"
      "v_cvt_pk_bf16_f32 v48, v40, v40\n"
      "v_add_f32 v7, v7, v40\n"
      "v_and_b32 v44, v4, v48\n"
      "v_and_b32 v45, v5, v48\n"
      "s_waitcnt lgkmcnt(2)\n"
      "v_mfma_f32_16x16x16_bf16 v[16:19], v[24:25], v[46:47], v[16:19]\n"
      "v_mfma_f32_16x16x16_bf16 v[20:23], v[26:27], v[46:47], v[20:23]\n"
      "ds_read_b128 v[24:27], v3 offset:27648\n"
      "v_sub_f32 v42, v42, v6\n"
      "v_exp_f32 v42, v42\n"
      "v_cvt_pk_bf16_f32 v48, v41, v41\n"
      "v_add_f32 v7, v7, v41\n"
      "v_and_b32 v46, v4, v48\n"
      "v_and_b32 v47, v5, v48\n"
      "s_waitcnt lgkmcnt(2)\n"
      "v_mfma_f32_16x16x16_bf16 v[16:19], v[28:29], v[44:45], v[16:19]\n"
      "v_mfma_f32_16x16x16_bf16 v[20:23], v[30:31], v[44:45], v[20:23]\n"
      "v_cvt_pk_bf16_f32 v48, v42, v42\n"
      "v_add_f32 v7, v7, v42\n"
      "v_and_b32 v44, v4, v48\n"
      "v_and_b32 v45, v5, v48\n"
      "s_waitcnt lgkmcnt(1)\n"
      "v_mfma_f32_16x16x16_bf16 v[16:19], v[32:33], v[46:47], v[16:19]\n"
      "v_mfma_f32_16x16x16_bf16 v[20:23], v[34:35], v[46:47], v[20:23]\n"
      "s_waitcnt lgkmcnt(0)\n"
      "v_mfma_f32_16x16x16_bf16 v[16:19], v[24:25], v[44:45], v[16:19]\n"
      "v_mfma_f32_16x16x16_bf16 v[20:23], v[26:27], v[44:45], v[20:23]\n"
      "ds_read_b128 v[44:47], v3 offset:28672\n"
      "ds_read_b128 v[48:51], v3 offset:30720\n"
      "ds_read_b128 v[52:55], v3 offset:32768\n"
      "s_waitcnt lgkmcnt(2)\n"
      "v_mfma_f32_16x16x32_bf16 v[24:27], v[44:47], v[8:11], 0\n"
      "ds_read_b128 v[44:47], v3 offset:34816\n"
      "s_waitcnt lgkmcnt(2)\n"
      "v_mfma_f32_16x16x32_bf16 v[28:31], v[48:51], v[8:11], 0\n"
      "ds_read_b128 v[48:51], v3 offset:36864\n"
      "s_nop 3\n"
      "v_mov_b32_dpp v36, v24 quad_perm:[0,0,0,0] row_mask:0xf bank_mask:0xf\n"
      "v_mov_b32_dpp v43, v26 quad_perm:[2,2,2,2] row_mask:0xf bank_mask:0xf\n"
      "s_waitcnt lgkmcnt(2)\n"
      "v_mfma_f32_16x16x32_bf16 v[32:35], v[52:55], v[8:11], 0\n"
      "v_add_f32_dpp v36, v25, v36 quad_perm:[1,1,1,1] row_mask:0xf bank_mask:0xf\n"
      "v_add_f32_dpp v43, v27, v43 quad_perm:[3,3,3,3] row_mask:0xf bank_mask:0xf\n"
      "v_add_f32 v36, v36, v43\n"
      "v_mov_b32_dpp v37, v28 quad_perm:[0,0,0,0] row_mask:0xf bank_mask:0xf\n"
      "v_mov_b32_dpp v43, v30 quad_perm:[2,2,2,2] row_mask:0xf bank_mask:0xf\n"
      "s_waitcnt lgkmcnt(1)\n"
      "v_mfma_f32_16x16x32_bf16 v[24:27], v[44:47], v[8:11], 0\n"
      "v_add_f32_dpp v37, v29, v37 quad_perm:[1,1,1,1] row_mask:0xf bank_mask:0xf\n"
      "v_add_f32_dpp v43, v31, v43 quad_perm:[3,3,3,3] row_mask:0xf bank_mask:0xf\n"
      "v_add_f32 v37, v37, v43\n"
      "v_mov_b32_dpp v38, v32 quad_perm:[0,0,0,0] row_mask:0xf bank_mask:0xf\n"
      "v_mov_b32_dpp v43, v34 quad_perm:[2,2,2,2] row_mask:0xf bank_mask:0xf\n"
      "s_waitcnt lgkmcnt(0)\n"
      "v_mfma_f32_16x16x32_bf16 v[28:31], v[48:51], v[8:11], 0\n"
      "v_add_f32_dpp v38, v33, v38 quad_perm:[1,1,1,1] row_mask:0xf bank_mask:0xf\n"
      "v_add_f32_dpp v43, v35, v43 quad_perm:[3,3,3,3] row_mask:0xf bank_mask:0xf\n"
      "v_add_f32 v38, v38, v43\n"
      "v_mov_b32_dpp v39, v24 quad_perm:[0,0,0,0] row_mask:0xf bank_mask:0xf\n"
      "v_mov_b32_dpp v43, v26 quad_perm:[2,2,2,2] row_mask:0xf bank_mask:0xf\n"
      "s_nop 0\n"
      "v_add_f32_dpp v39, v25, v39 quad_perm:[1,1,1,1] row_mask:0xf bank_mask:0xf\n"
      "v_add_f32_dpp v43, v27, v43 quad_perm:[3,3,3,3] row_mask:0xf bank_mask:0xf\n"
      "v_add_f32 v39, v39, v43\n"
      "v_mov_b32_dpp v40, v28 quad_perm:[0,0,0,0] row_mask:0xf bank_mask:0xf\n"
      "v_mov_b32_dpp v43, v30 quad_perm:[2,2,2,2] row_mask:0xf bank_mask:0xf\n"
      "s_nop 0\n"
      "v_add_f32_dpp v40, v29, v40 quad_perm:[1,1,1,1] row_mask:0xf bank_mask:0xf\n"
      "v_add_f32_dpp v43, v31, v43 quad_perm:[3,3,3,3] row_mask:0xf bank_mask:0xf\n"
      "v_add_f32 v40, v40, v43\n"
      "v_add_f32 v40, s69, v40\n"
      "v_max3_f32 v49, v36, v37, v38\n"
      "v_max3_f32 v49, v49, v39, v40\n"
      "v_max_f32 v49, v49, v6\n"
      "ds_read_b128 v[24:27], v3 offset:29696\n"
      "ds_read_b128 v[28:31], v3 offset:31744\n"
      "ds_read_b128 v[32:35], v3 offset:33792\n"
      "v_mov_b32 v50, v49\n"
      "s_nop 1\n"
      "v_permlane32_swap_b32 v50, v49\n"
      "v_max_f32 v49, v50, v49\n"
      "v_mov_b32 v50, v49\n"
      "s_nop 1\n"
      "v_permlane16_swap_b32 v50, v49\n"
      "v_max_f32 v49, v50, v49\n"
      "v_sub_f32 v50, v6, v49\n"
      "v_exp_f32 v50, v50\n"
      "v_mov_b32 v6, v49\n"
      "v_mul_f32 v7, v50, v7\n"
      "v_mul_f32 v16, v50, v16\n"
      "v_mul_f32 v20, v50, v20\n"
      "v_mul_f32 v17, v50, v17\n"
      "v_mul_f32 v21, v50, v21\n"
      "v_mul_f32 v18, v50, v18\n"
      "v_mul_f32 v22, v50, v22\n"
      "v_mul_f32 v19, v50, v19\n"
      "v_mul_f32 v23, v50, v23\n"
      "v_sub_f32 v36, v36, v6\n"
      "v_exp_f32 v36, v36\n"
      "v_sub_f32 v37, v37, v6\n"
      "v_exp_f32 v37, v37\n"
      "v_cvt_pk_bf16_f32 v48, v36, v36\n"
      "v_add_f32 v7, v7, v36\n"
      "v_and_b32 v44, v4, v48\n"
      "v_and_b32 v45, v5, v48\n"
      "v_sub_f32 v38, v38, v6\n"
      "v_exp_f32 v38, v38\n"
      "v_cvt_pk_bf16_f32 v48, v37, v37\n"
      "v_add_f32 v7, v7, v37\n"
      "v_and_b32 v46, v4, v48\n"
      "v_and_b32 v47, v5, v48\n"
      "s_waitcnt lgkmcnt(2)\n"
      "v_mfma_f32_16x16x16_bf16 v[16:19], v[24:25], v[44:45], v[16:19]\n"
      "v_mfma_f32_16x16x16_bf16 v[20:23], v[26:27], v[44:45], v[20:23]\n"
      "ds_read_b128 v[24:27], v3 offset:35840\n"
      "v_sub_f32 v39, v39, v6\n"
      "v_exp_f32 v39, v39\n"
      "v_cvt_pk_bf16_f32 v48, v38, v38\n"
      "v_add_f32 v7, v7, v38\n"
      "v_and_b32 v44, v4, v48\n"
      "v_and_b32 v45, v5, v48\n"
      "s_waitcnt lgkmcnt(2)\n"
      "v_mfma_f32_16x16x16_bf16 v[16:19], v[28:29], v[46:47], v[16:19]\n"
      "v_mfma_f32_16x16x16_bf16 v[20:23], v[30:31], v[46:47], v[20:23]\n"
      "ds_read_b128 v[28:31], v3 offset:37888\n"
      "v_sub_f32 v40, v40, v6\n"
      "v_exp_f32 v40, v40\n"
      "v_cvt_pk_bf16_f32 v48, v39, v39\n"
      "v_add_f32 v7, v7, v39\n"
      "v_and_b32 v46, v4, v48\n"
      "v_and_b32 v47, v5, v48\n"
      "s_waitcnt lgkmcnt(2)\n"
      "v_mfma_f32_16x16x16_bf16 v[16:19], v[32:33], v[44:45], v[16:19]\n"
      "v_mfma_f32_16x16x16_bf16 v[20:23], v[34:35], v[44:45], v[20:23]\n"
      "v_cvt_pk_bf16_f32 v48, v40, v40\n"
      "v_add_f32 v7, v7, v40\n"
      "v_and_b32 v44, v4, v48\n"
      "v_and_b32 v45, v5, v48\n"
      "s_waitcnt lgkmcnt(1)\n"
      "v_mfma_f32_16x16x16_bf16 v[16:19], v[24:25], v[46:47], v[16:19]\n"
      "v_mfma_f32_16x16x16_bf16 v[20:23], v[26:27], v[46:47], v[20:23]\n"
      "s_waitcnt lgkmcnt(0)\n"
      "v_mfma_f32_16x16x16_bf16 v[16:19], v[28:29], v[44:45], v[16:19]\n"
      "v_mfma_f32_16x16x16_bf16 v[20:23], v[30:31], v[44:45], v[20:23]\n"
      "s_nop 7\n"
      "s_setpc_b64 s[80:81]\n"
      "L_endfb_%=:\n"
      "L_end_%=:\n"
      :
      : [K] "s"(K), [V] "s"(V), [ws] "s"(ws), [bid] "s"(bid), [wave] "s"(wave), [wq] "s"(wq), [wo] "s"(wo), [w1] "s"(w1), [w2] "s"(w2)
      : "v0", "v1", "v2", "v3", "v4", "v5", "v6", "v7", "v8", "v9", "v10", "v11", "v12", "v13", "v14", "v15", "v16", "v17", "v18", "v19", "v20", "v21", "v22", "v23", "v24", "v25", "v26", "v27", "v28", "v29", "v30", "v31", "v32", "v33", "v34", "v35", "v36", "v37", "v38", "v39", "v40", "v41", "v42", "v43", "v44", "v45", "v46", "v47", "v48", "v49", "v50", "v51", "v52", "v53", "v54", "v55", "v56", "v57", "v58", "v59", "v60", "v61", "v62", "v63", "v64", "v65", "v66", "v67", "v68", "v69", "v70", "v71", "v72", "v73", "v74", "v75", "v76", "v77", "v78", "v79", "v80", "v81", "v82", "v83", "v84", "v85", "v86", "v87", "v88", "v89", "v90", "v91", "v92", "v93", "v94", "v95", "v96", "v97", "v98", "v99", "v100", "v101", "v102", "v103", "v104", "v105", "v106", "v107", "v108", "v109", "v110", "v111", "v112", "v113", "v114", "v115", "v116", "v117", "v118", "v119", "v120", "v121", "v122", "v123", "v124", "v125", "v126", "v127", "v128", "v129", "v130", "v131", "v132", "v133", "v134", "v135", "v136", "v137", "v138", "v139", "v140", "v141", "v142", "v143", "v144", "v145", "v146", "v147", "v148", "v149", "v150", "v151", "v152", "v153", "v154", "v155", "v156", "v157", "v158", "v159", "v160", "v161", "v162", "v163", "v164", "v165", "v166", "v167", "v168", "v169", "v170", "v171", "v172", "v173", "v174", "v175", "v176", "v177", "v178", "v179", "v180", "v181", "v182", "v183", "v184", "v185", "v186", "v187", "v188", "v189", "v190", "v191", "v192", "v193", "v194", "v195", "v196", "v197", "v198", "v199", "v200", "v201", "v202", "v203", "v204", "v205", "v206", "v207", "v208", "v209", "v210", "v211", "v212", "v213", "v214", "v215", "v216", "v217", "v218", "v219", "v220", "v221", "v222", "v223", "v224", "v225", "v226", "v227", "v228", "v229", "v230", "v231", "v232", "v233", "v234", "v235", "v236", "v237", "v238", "v239", "v240", "v241", "v242", "v243", "v244", "v245", "v246", "v247", "v248", "v249", "v250", "v251", "v252", "v253", "v254", "v255", "a0", "a1", "a2", "a3", "a4", "a5", "a6", "a7", "a8", "a9", "a10", "a11", "a12", "a13", "a14", "a15", "a16", "a17", "a18", "a19", "a20", "a21", "a22", "a23", "a24", "a25", "a26", "a27", "a28", "a29", "a30", "a31", "a32", "a33", "a34", "a35", "a36", "a37", "a38", "a39", "a40", "a41", "a42", "a43", "a44", "a45", "a46", "a47", "a48", "a49", "a50", "a51", "a52", "a53", "a54", "a55", "a56", "a57", "a58", "a59", "a60", "a61", "a62", "a63", "a64", "a65", "a66", "a67", "a68", "a69", "a70", "a71", "a72", "a73", "a74", "a75", "a76", "a77", "a78", "a79", "a80", "a81", "a82", "a83", "a84", "a85", "a86", "a87", "a88", "a89", "a90", "a91", "a92", "a93", "a94", "a95", "a96", "a97", "a98", "a99", "a100", "a101", "a102", "a103", "a104", "a105", "a106", "a107", "a108", "a109", "a110", "a111", "a112", "a113", "a114", "a115", "a116", "a117", "a118", "a119", "a120", "a121", "a122", "a123", "a124", "a125", "a126", "a127", "a128", "a129", "a130", "a131", "a132", "a133", "a134", "a135", "a136", "a137", "a138", "a139", "a140", "a141", "a142", "a143", "a144", "a145", "a146", "a147", "a148", "a149", "a150", "a151", "a152", "a153", "a154", "a155", "a156", "a157", "a158", "a159", "a160", "a161", "a162", "a163", "a164", "a165", "a166", "a167", "a168", "a169", "a170", "a171", "a172", "a173", "a174", "a175", "a176", "a177", "a178", "a179", "a180", "a181", "a182", "a183", "a184", "a185", "a186", "a187", "a188", "a189", "a190", "a191", "a192", "a193", "a194", "a195", "a196", "a197", "a198", "a199", "a200", "a201", "a202", "a203", "a204", "a205", "a206", "a207", "a208", "a209", "a210", "a211", "a212", "a213", "a214", "a215", "a216", "a217", "a218", "a219", "a220", "a221", "a222", "a223", "a224", "a225", "a226", "a227", "a228", "a229", "a230", "a231", "a232", "a233", "a234", "a235", "a236", "a237", "a238", "a239", "a240", "a241", "a242", "a243", "a244", "a245", "a246", "a247", "a248", "a249", "a250", "a251", "a252", "a253", "a254", "a255", "s40", "s41", "s42", "s43", "s44", "s45", "s46", "s47", "s48", "s49", "s50", "s51", "s52", "s53", "s54", "s55", "s56", "s57", "s58", "s59", "s60", "s61", "s62", "s63", "s64", "s65", "s66", "s67", "s68", "s69", "s70", "s71", "s72", "s73", "s74", "s75", "s76", "s77", "s78", "s79", "s80", "s81", "s82", "s83", "s84", "s85", "s86", "s87", "s88", "s89", "vcc", "scc", "memory");
  } else {
    asm volatile(
      "s_mov_b64 s[40:41], %[ws]\n"
      "s_mov_b64 s[42:43], %[x]\n"
      "s_mov_b64 s[44:45], %[out]\n"
      "s_mov_b64 s[80:81], %[wq]\n"
      "s_mov_b64 s[82:83], %[wo]\n"
      "s_mov_b64 s[84:85], %[w1]\n"
      "s_mov_b64 s[86:87], %[w2]\n"
      "s_sub_u32 s52, %[bid], 216\n"
      "s_mov_b32 s53, %[wave]\n"
      "s_mov_b32 s64, 0\n"
      "fdk_chain_begin:\n"
      "v_mbcnt_lo_u32_b32 v0, -1, 0\n"
      "v_mbcnt_hi_u32_b32 v0, -1, v0\n"
      "v_lshlrev_b32 v1, 4, v0\n"
      "s_lshl_b32 s65, s53, 6\n"
      "v_add_u32 v15, s65, v0\n"
      "s_load_dword s58, s[40:41], 0\n"
      "s_lshl_b32 s65, s53, 11\n"
      "s_add_u32 s65, s65, 0\n"
      "s_add_u32 s46, s42, s65\n"
      "s_addc_u32 s47, s43, 0\n"
      "global_load_dwordx4 v[16:19], v1, s[46:47]\n"
      "s_lshl_b32 s65, s53, 11\n"
      "s_add_u32 s65, s65, 1024\n"
      "s_add_u32 s46, s42, s65\n"
      "s_addc_u32 s47, s43, 0\n"
      "global_load_dwordx4 v[20:23], v1, s[46:47]\n"
      "s_and_b32 s65, s52, 7\n"
      "s_lshr_b32 s66, s52, 3\n"
      "s_lshl_b32 s67, s65, 2\n"
      "s_add_u32 s55, s67, s66\n"
      "s_mov_b32 s56, s55\n"
      "s_mov_b32 s54, 0\n"
      "s_mov_b32 s57, s65\n"
      "s_cmp_lt_u32 s66, 2\n"
      "s_cbranch_scc1 C_rd_%=\n"
      "s_mov_b32 s54, 1\n"
      "s_cmp_eq_u32 s66, 2\n"
      "s_cbranch_scc1 C_rd_%=\n"
      "s_mov_b32 s54, 2\n"
      "s_sub_u32 s56, s55, 1\n"
      "s_mov_b32 s55, 31\n"
      "s_cmp_eq_u32 s66, 3\n"
      "s_cbranch_scc1 C_rd_%=\n"
      "s_add_u32 s57, s65, 8\n"
      "s_cmp_lt_u32 s65, 6\n"
      "s_cselect_b32 s54, 2, 3\n"
      "C_rd_%=:\n"
      "s_cmp_eq_u32 s54, 0\n"
      "s_cbranch_scc0 C_tw1_%=\n"
      "s_mov_b32 s70, 0x2200100\n"
      "s_mov_b32 s71, 0x8220321\n"
      "s_mov_b32 s72, 0x5092304\n"
      "s_mov_b32 s73, 0x25060a24\n"
      "s_mov_b32 s74, 0xc26070b\n"
      "s_mov_b32 s75, 0xf0e0d27\n"
      "s_mov_b32 s76, 0xffffffff\n"
      "s_mov_b32 s77, 0xffffffff\n"
      "s_mov_b32 s78, 24\n"
      "C_tw1_%=:\n"
      "s_cmp_eq_u32 s54, 1\n"
      "s_cbranch_scc0 C_tw2_%=\n"
      "s_mov_b32 s70, 0x3020100\n"
      "s_mov_b32 s71, 0x5090408\n"
      "s_mov_b32 s72, 0x70b060a\n"
      "s_mov_b32 s73, 0xf0e0d0c\n"
      "s_mov_b32 s74, 0xffffffff\n"
      "s_mov_b32 s75, 0xffffffff\n"
      "s_mov_b32 s76, 0xffffffff\n"
      "s_mov_b32 s77, 0xffffffff\n"
      "s_mov_b32 s78, 16\n"
      "C_tw2_%=:\n"
      "s_cmp_eq_u32 s54, 2\n"
      "s_cbranch_scc0 C_tw3_%=\n"
      "s_mov_b32 s70, 0x21112010\n"
      "s_mov_b32 s71, 0x18132212\n"
      "s_mov_b32 s72, 0x24191423\n"
      "s_mov_b32 s73, 0x16251a15\n"
      "s_mov_b32 s74, 0x2717261b\n"
      "s_mov_b32 s75, 0x1f1e1d1c\n"
      "s_mov_b32 s76, 0xffffffff\n"
      "s_mov_b32 s77, 0xffffffff\n"
      "s_mov_b32 s78, 24\n"
      "C_tw3_%=:\n"
      "s_cmp_eq_u32 s54, 3\n"
      "s_cbranch_scc0 C_tw4_%=\n"
      "s_mov_b32 s70, 0x23222120\n"
      "s_mov_b32 s71, 0x25392438\n"
      "s_mov_b32 s72, 0x273b263a\n"
      "s_mov_b32 s73, 0x3f3e3d3c\n"
      "s_mov_b32 s74, 0xffffffff\n"
      "s_mov_b32 s75, 0xffffffff\n"
      "s_mov_b32 s76, 0xffffffff\n"
      "s_mov_b32 s77, 0xffffffff\n"
      "s_mov_b32 s78, 16\n"
      "C_tw4_%=:\n"
      "s_mov_b32 s24, 0x3f80\n"
      "s_mov_b32 s25, 0x3f800000\n"
      "s_mov_b32 s92, 0x7ffffff\n"
      "s_mov_b32 s93, 0\n"
      "s_mov_b32 s94, 0xffff0000\n"
      "s_mov_b32 s95, 0\n"
      "s_mov_b32 s96, 0\n"
      "s_mov_b32 s97, 0xffff0000\n"
      "s_mov_b32 s98, 0\n"
      "s_mov_b32 s99, -1\n"
      "s_cmp_eq_u32 s53, 3\n"
      "s_cselect_b32 s38, 0, -1\n"
      "v_mov_b32 v24, -1\n"
      "v_mov_b32 v25, -1\n"
      "v_mov_b32 v26, -1\n"
      "v_mov_b32 v27, -1\n"
      "s_cmp_eq_u32 s54, 1\n"
      "s_cbranch_scc1 C_n45_%=\n"
      "v_lshrrev_b32 v32, 4, v15\n"
      "v_lshlrev_b32 v32, 13, v32\n"
      "v_and_b32 v33, 15, v15\n"
      "v_lshlrev_b32 v33, 4, v33\n"
      "v_add_u32 v32, v32, v33\n"
      "s_lshl_b32 s65, s56, 8\n"
      "s_add_u32 s65, s65, 528384\n"
      "s_add_u32 s46, s40, s65\n"
      "s_addc_u32 s47, s41, 0\n"
      "global_store_dwordx4 v32, v[24:27], s[46:47] sc1\n"
      "C_n45_%=:\n"
      "s_cmp_eq_u32 s54, 0\n"
      "s_cbranch_scc0 C_n16_%=\n"
      "v_lshrrev_b32 v32, 5, v15\n"
      "v_lshlrev_b32 v32, 14, v32\n"
      "v_and_b32 v33, 31, v15\n"
      "v_lshlrev_b32 v33, 4, v33\n"
      "v_add_u32 v32, v32, v33\n"
      "s_lshl_b32 s65, s55, 9\n"
      "s_add_u32 s65, s65, 5967872\n"
      "s_add_u32 s46, s40, s65\n"
      "s_addc_u32 s47, s41, 0\n"
      "global_store_dwordx4 v32, v[24:27], s[46:47] sc1\n"
      "s_add_u32 s46, s46, 131072\n"
      "s_addc_u32 s47, s47, 0\n"
      "global_store_dwordx4 v32, v[24:27], s[46:47] sc1\n"
      "C_n16_%=:\n"
      "s_cmp_eq_u32 s54, 1\n"
      "s_cbranch_scc0 C_n1b8_%=\n"
      "s_mul_i32 s65, s53, 16384\n"
      "s_lshl_b32 s66, s55, 9\n"
      "s_add_u32 s65, s65, s66\n"
      "s_add_u32 s65, s65, 5967872\n"
      "s_add_u32 s46, s40, s65\n"
      "s_addc_u32 s47, s41, 0\n"
      "global_store_dwordx4 v1, v[24:27], s[46:47] sc1\n"
      "s_add_u32 s46, s46, 65536\n"
      "s_addc_u32 s47, s47, 0\n"
      "global_store_dwordx4 v1, v[24:27], s[46:47] sc1\n"
      "s_add_u32 s46, s46, 65536\n"
      "s_addc_u32 s47, s47, 0\n"
      "global_store_dwordx4 v1, v[24:27], s[46:47] sc1\n"
      "s_add_u32 s46, s46, 65536\n"
      "s_addc_u32 s47, s47, 0\n"
      "global_store_dwordx4 v1, v[24:27], s[46:47] sc1\n"
      "C_n1b8_%=:\n"
      "s_cmp_eq_u32 s54, 2\n"
      "s_cbranch_scc0 C_n237_%=\n"
      "s_mul_i32 s65, s53, 14336\n"
      "s_lshl_b32 s66, s57, 10\n"
      "s_add_u32 s65, s65, s66\n"
      "s_add_u32 s65, s65, 6230016\n"
      "s_add_u32 s46, s40, s65\n"
      "s_addc_u32 s47, s41, 0\n"
      "global_store_dwordx4 v1, v[24:27], s[46:47] sc1\n"
      "s_add_u32 s46, s46, 57344\n"
      "s_addc_u32 s47, s47, 0\n"
      "global_store_dwordx4 v1, v[24:27], s[46:47] sc1\n"
      "s_add_u32 s46, s46, 57344\n"
      "s_addc_u32 s47, s47, 0\n"
      "global_store_dwordx4 v1, v[24:27], s[46:47] sc1\n"
      "s_add_u32 s46, s46, 57344\n"
      "s_addc_u32 s47, s47, 0\n"
      "global_store_dwordx4 v1, v[24:27], s[46:47] sc1\n"
      "C_n237_%=:\n"
      "s_lshl_b32 s65, s56, 8\n"
      "s_add_u32 s80, s80, s65\n"
      "s_addc_u32 s81, s81, 0\n"
      "s_lshr_b32 s65, s55, 1\n"
      "s_lshl_b32 s65, s65, 18\n"
      "s_and_b32 s66, s55, 1\n"
      "s_lshl_b32 s66, s66, 9\n"
      "s_add_u32 s65, s65, s66\n"
      "s_add_u32 s82, s82, s65\n"
      "s_addc_u32 s83, s83, 0\n"
      "s_min_u32 s67, s57, 13\n"
      "s_lshl_b32 s65, s67, 8\n"
      "s_add_u32 s84, s84, s65\n"
      "s_addc_u32 s85, s85, 0\n"
      "s_lshl_b32 s65, s67, 17\n"
      "s_add_u32 s86, s86, s65\n"
      "s_addc_u32 s87, s87, 0\n"
      "s_add_u32 s50, s82, 512\n"
      "s_addc_u32 s51, s83, 0\n"
      "s_cmp_eq_u32 s54, 0\n"
      "s_cbranch_scc0 C_wr10_%=\n"
      "v_lshrrev_b32 v2, 4, v15\n"
      "v_mul_u32_u24 v5, 272, v2\n"
      "v_mul_u32_u24 v2, 8192, v2\n"
      "v_and_b32 v14, 15, v15\n"
      "v_lshlrev_b32 v14, 4, v14\n"
      "v_add_u32 v2, v2, v14\n"
      "v_add_u32 v5, v5, v14\n"
      "v_lshrrev_b32 v5, 5, v15\n"
      "v_and_b32 v3, 1, v5\n"
      "v_lshlrev_b32 v3, 3, v3\n"
      "v_lshrrev_b32 v14, 1, v5\n"
      "v_add_u32 v3, v3, v14\n"
      "v_lshlrev_b32 v3, 10, v3\n"
      "v_mul_u32_u24 v5, 528, v5\n"
      "v_and_b32 v14, 31, v15\n"
      "v_lshlrev_b32 v14, 4, v14\n"
      "v_add_u32 v3, v3, v14\n"
      "v_add_u32 v5, v5, v14\n"
      "s_add_u32 s48, s80, 0\n"
      "s_addc_u32 s49, s81, 0\n"
      "global_load_dwordx4 v[64:67], v2, s[48:49] \n"
      "s_add_u32 s48, s80, 131072\n"
      "s_addc_u32 s49, s81, 0\n"
      "global_load_dwordx4 v[68:71], v2, s[48:49] \n"
      "s_add_u32 s48, s80, 262144\n"
      "s_addc_u32 s49, s81, 0\n"
      "global_load_dwordx4 v[72:75], v2, s[48:49] \n"
      "s_add_u32 s48, s80, 393216\n"
      "s_addc_u32 s49, s81, 0\n"
      "global_load_dwordx4 v[76:79], v2, s[48:49] \n"
      "s_add_u32 s48, s80, 524288\n"
      "s_addc_u32 s49, s81, 0\n"
      "global_load_dwordx4 v[80:83], v2, s[48:49] \n"
      "s_add_u32 s48, s80, 655360\n"
      "s_addc_u32 s49, s81, 0\n"
      "global_load_dwordx4 v[84:87], v2, s[48:49] \n"
      "s_add_u32 s48, s80, 786432\n"
      "s_addc_u32 s49, s81, 0\n"
      "global_load_dwordx4 v[88:91], v2, s[48:49] \n"
      "s_add_u32 s48, s80, 917504\n"
      "s_addc_u32 s49, s81, 0\n"
      "global_load_dwordx4 v[92:95], v2, s[48:49] \n"
      "s_add_u32 s48, s80, 1048576\n"
      "s_addc_u32 s49, s81, 0\n"
      "global_load_dwordx4 v[96:99], v2, s[48:49] \n"
      "s_add_u32 s48, s80, 1179648\n"
      "s_addc_u32 s49, s81, 0\n"
      "global_load_dwordx4 v[100:103], v2, s[48:49] \n"
      "s_add_u32 s48, s80, 1310720\n"
      "s_addc_u32 s49, s81, 0\n"
      "global_load_dwordx4 v[104:107], v2, s[48:49] \n"
      "s_add_u32 s48, s80, 1441792\n"
      "s_addc_u32 s49, s81, 0\n"
      "global_load_dwordx4 v[108:111], v2, s[48:49] \n"
      "s_add_u32 s48, s80, 1572864\n"
      "s_addc_u32 s49, s81, 0\n"
      "global_load_dwordx4 v[112:115], v2, s[48:49] \n"
      "s_add_u32 s48, s80, 1703936\n"
      "s_addc_u32 s49, s81, 0\n"
      "global_load_dwordx4 v[116:119], v2, s[48:49] \n"
      "s_add_u32 s48, s80, 1835008\n"
      "s_addc_u32 s49, s81, 0\n"
      "global_load_dwordx4 v[120:123], v2, s[48:49] \n"
      "s_add_u32 s48, s80, 1966080\n"
      "s_addc_u32 s49, s81, 0\n"
      "global_load_dwordx4 v[124:127], v2, s[48:49] \n"
      "s_waitcnt vmcnt(16)\n"
      "s_waitcnt lgkmcnt(0)\n"
      "s_barrier\n"
      "s_add_u32 s58, s58, 1\n"
      "s_and_b32 s67, s52, 7\n"
      "s_lshl_b32 s67, s67, 6\n"
      "s_lshr_b32 s68, s52, 3\n"
      "s_add_u32 s67, s67, s68\n"
      "s_lshl_b32 s67, s67, 2\n"
      "s_add_u32 s67, s67, 1152\n"
      "v_mov_b32 v8, s67\n"
      "v_mov_b32 v9, s58\n"
      "s_mov_b64 exec, 1\n"
      "global_store_dword v8, v9, s[40:41] sc1\n"
      "s_mov_b64 exec, -1\n"
      "v_lshlrev_b32 v32, 16, v16\n"
      "v_and_b32 v33, 0xffff0000, v16\n"
      "v_lshlrev_b32 v34, 16, v17\n"
      "v_and_b32 v35, 0xffff0000, v17\n"
      "v_lshlrev_b32 v36, 16, v18\n"
      "v_and_b32 v37, 0xffff0000, v18\n"
      "v_lshlrev_b32 v38, 16, v19\n"
      "v_and_b32 v39, 0xffff0000, v19\n"
      "v_readfirstlane_b32 s66, v32\n"
      "s_nop 0\n"
      "s_mov_b32 s67, s66\n"
      "v_pk_add_f32 v[40:41], v[32:33], s[66:67] neg_lo:[0,1] neg_hi:[0,1]\n"
      "v_pk_add_f32 v[42:43], v[34:35], s[66:67] neg_lo:[0,1] neg_hi:[0,1]\n"
      "v_pk_add_f32 v[44:45], v[36:37], s[66:67] neg_lo:[0,1] neg_hi:[0,1]\n"
      "v_pk_add_f32 v[46:47], v[38:39], s[66:67] neg_lo:[0,1] neg_hi:[0,1]\n"
      "v_pk_mul_f32 v[56:57], v[40:41], v[40:41]\n"
      "v_pk_add_f32 v[48:49], v[40:41], v[42:43]\n"
      "v_pk_fma_f32 v[56:57], v[42:43], v[42:43], v[56:57]\n"
      "v_pk_add_f32 v[50:51], v[44:45], v[46:47]\n"
      "v_pk_fma_f32 v[56:57], v[44:45], v[44:45], v[56:57]\n"
      "v_pk_add_f32 v[48:49], v[48:49], v[50:51]\n"
      "v_pk_fma_f32 v[56:57], v[46:47], v[46:47], v[56:57]\n"
      "v_add_f32 v48, v48, v49\n"
      "v_add_f32 v49, v56, v57\n"
      "s_nop 0\n"
      "v_add_f32_dpp v48, v48, v48 quad_perm:[1,0,3,2] row_mask:0xf bank_mask:0xf\n"
      "v_add_f32_dpp v49, v49, v49 quad_perm:[1,0,3,2] row_mask:0xf bank_mask:0xf\n"
      "s_nop 0\n"
      "v_add_f32_dpp v48, v48, v48 quad_perm:[2,3,0,1] row_mask:0xf bank_mask:0xf\n"
      "v_add_f32_dpp v49, v49, v49 quad_perm:[2,3,0,1] row_mask:0xf bank_mask:0xf\n"
      "s_nop 0\n"
      "v_add_f32_dpp v48, v48, v48 row_half_mirror row_mask:0xf bank_mask:0xf\n"
      "v_add_f32_dpp v49, v49, v49 row_half_mirror row_mask:0xf bank_mask:0xf\n"
      "s_nop 0\n"
      "v_add_f32_dpp v48, v48, v48 row_ror:8 row_mask:0xf bank_mask:0xf\n"
      "v_add_f32_dpp v49, v49, v49 row_ror:8 row_mask:0xf bank_mask:0xf\n"
      "v_mov_b32 v12, v48\n"
      "v_mov_b32 v13, v49\n"
      "s_nop 0\n"
      "v_permlane16_swap_b32 v12, v48\n"
      "v_permlane16_swap_b32 v13, v49\n"
      "v_add_f32 v48, v12, v48\n"
      "v_add_f32 v49, v13, v49\n"
      "v_mov_b32 v12, v48\n"
      "v_mov_b32 v13, v49\n"
      "s_nop 0\n"
      "v_permlane32_swap_b32 v12, v48\n"
      "v_permlane32_swap_b32 v13, v49\n"
      "v_add_f32 v48, v12, v48\n"
      "v_add_f32 v49, v13, v49\n"
      "v_mul_f32 v48, 0x3b000000, v48\n"
      "v_mul_f32 v49, 0x3b000000, v49\n"
      "v_fma_f32 v49, -v48, v48, v49\n"
      "v_add_f32 v49, 0x3727c5ac, v49\n"
      "v_rsq_f32 v49, v49\n"
      "s_nop 0\n"
      "v_mul_f32 v48, v48, v49\n"
      "v_pk_fma_f32 v[40:41], v[40:41], v[48:49], v[48:49] op_sel:[0,1,0] op_sel_hi:[1,1,0] neg_lo:[0,0,1] neg_hi:[0,0,1]\n"
      "v_pk_fma_f32 v[42:43], v[42:43], v[48:49], v[48:49] op_sel:[0,1,0] op_sel_hi:[1,1,0] neg_lo:[0,0,1] neg_hi:[0,0,1]\n"
      "v_pk_fma_f32 v[44:45], v[44:45], v[48:49], v[48:49] op_sel:[0,1,0] op_sel_hi:[1,1,0] neg_lo:[0,0,1] neg_hi:[0,0,1]\n"
      "v_pk_fma_f32 v[46:47], v[46:47], v[48:49], v[48:49] op_sel:[0,1,0] op_sel_hi:[1,1,0] neg_lo:[0,0,1] neg_hi:[0,0,1]\n"
      "v_cvt_pk_bf16_f32 v52, v40, v41\n"
      "v_cvt_pk_bf16_f32 v53, v42, v43\n"
      "v_cvt_pk_bf16_f32 v54, v44, v45\n"
      "v_cvt_pk_bf16_f32 v55, v46, v47\n"
      "s_mul_i32 s65, s53, 2080\n"
      "s_add_u32 s65, s65, 143360\n"
      "v_add_u32 v12, s65, v1\n"
      "ds_write_b128 v12, v[52:55]\n"
      "v_lshlrev_b32 v32, 16, v20\n"
      "v_and_b32 v33, 0xffff0000, v20\n"
      "v_lshlrev_b32 v34, 16, v21\n"
      "v_and_b32 v35, 0xffff0000, v21\n"
      "v_lshlrev_b32 v36, 16, v22\n"
      "v_and_b32 v37, 0xffff0000, v22\n"
      "v_lshlrev_b32 v38, 16, v23\n"
      "v_and_b32 v39, 0xffff0000, v23\n"
      "v_readfirstlane_b32 s66, v32\n"
      "s_nop 0\n"
      "s_mov_b32 s67, s66\n"
      "v_pk_add_f32 v[40:41], v[32:33], s[66:67] neg_lo:[0,1] neg_hi:[0,1]\n"
      "v_pk_add_f32 v[42:43], v[34:35], s[66:67] neg_lo:[0,1] neg_hi:[0,1]\n"
      "v_pk_add_f32 v[44:45], v[36:37], s[66:67] neg_lo:[0,1] neg_hi:[0,1]\n"
      "v_pk_add_f32 v[46:47], v[38:39], s[66:67] neg_lo:[0,1] neg_hi:[0,1]\n"
      "v_pk_mul_f32 v[56:57], v[40:41], v[40:41]\n"
      "v_pk_add_f32 v[48:49], v[40:41], v[42:43]\n"
      "v_pk_fma_f32 v[56:57], v[42:43], v[42:43], v[56:57]\n"
      "v_pk_add_f32 v[50:51], v[44:45], v[46:47]\n"
      "v_pk_fma_f32 v[56:57], v[44:45], v[44:45], v[56:57]\n"
      "v_pk_add_f32 v[48:49], v[48:49], v[50:51]\n"
      "v_pk_fma_f32 v[56:57], v[46:47], v[46:47], v[56:57]\n"
      "v_add_f32 v48, v48, v49\n"
      "v_add_f32 v49, v56, v57\n"
      "s_nop 0\n"
      "v_add_f32_dpp v48, v48, v48 quad_perm:[1,0,3,2] row_mask:0xf bank_mask:0xf\n"
      "v_add_f32_dpp v49, v49, v49 quad_perm:[1,0,3,2] row_mask:0xf bank_mask:0xf\n"
      "s_nop 0\n"
      "v_add_f32_dpp v48, v48, v48 quad_perm:[2,3,0,1] row_mask:0xf bank_mask:0xf\n"
      "v_add_f32_dpp v49, v49, v49 quad_perm:[2,3,0,1] row_mask:0xf bank_mask:0xf\n"
      "s_nop 0\n"
      "v_add_f32_dpp v48, v48, v48 row_half_mirror row_mask:0xf bank_mask:0xf\n"
      "v_add_f32_dpp v49, v49, v49 row_half_mirror row_mask:0xf bank_mask:0xf\n"
      "s_nop 0\n"
      "v_add_f32_dpp v48, v48, v48 row_ror:8 row_mask:0xf bank_mask:0xf\n"
      "v_add_f32_dpp v49, v49, v49 row_ror:8 row_mask:0xf bank_mask:0xf\n"
      "v_mov_b32 v12, v48\n"
      "v_mov_b32 v13, v49\n"
      "s_nop 0\n"
      "v_permlane16_swap_b32 v12, v48\n"
      "v_permlane16_swap_b32 v13, v49\n"
      "v_add_f32 v48, v12, v48\n"
      "v_add_f32 v49, v13, v49\n"
      "v_mov_b32 v12, v48\n"
      "v_mov_b32 v13, v49\n"
      "s_nop 0\n"
      "v_permlane32_swap_b32 v12, v48\n"
      "v_permlane32_swap_b32 v13, v49\n"
      "v_add_f32 v48, v12, v48\n"
      "v_add_f32 v49, v13, v49\n"
      "v_mul_f32 v48, 0x3b000000, v48\n"
      "v_mul_f32 v49, 0x3b000000, v49\n"
      "v_fma_f32 v49, -v48, v48, v49\n"
      "v_add_f32 v49, 0x3727c5ac, v49\n"
      "v_rsq_f32 v49, v49\n"
      "s_nop 0\n"
      "v_mul_f32 v48, v48, v49\n"
      "v_pk_fma_f32 v[40:41], v[40:41], v[48:49], v[48:49] op_sel:[0,1,0] op_sel_hi:[1,1,0] neg_lo:[0,0,1] neg_hi:[0,0,1]\n"
      "v_pk_fma_f32 v[42:43], v[42:43], v[48:49], v[48:49] op_sel:[0,1,0] op_sel_hi:[1,1,0] neg_lo:[0,0,1] neg_hi:[0,0,1]\n"
      "v_pk_fma_f32 v[44:45], v[44:45], v[48:49], v[48:49] op_sel:[0,1,0] op_sel_hi:[1,1,0] neg_lo:[0,0,1] neg_hi:[0,0,1]\n"
      "v_pk_fma_f32 v[46:47], v[46:47], v[48:49], v[48:49] op_sel:[0,1,0] op_sel_hi:[1,1,0] neg_lo:[0,0,1] neg_hi:[0,0,1]\n"
      "v_cvt_pk_bf16_f32 v52, v40, v41\n"
      "v_cvt_pk_bf16_f32 v53, v42, v43\n"
      "v_cvt_pk_bf16_f32 v54, v44, v45\n"
      "v_cvt_pk_bf16_f32 v55, v46, v47\n"
      "s_mul_i32 s65, s53, 2080\n"
      "s_add_u32 s65, s65, 144400\n"
      "v_add_u32 v12, s65, v1\n"
      "ds_write_b128 v12, v[52:55]\n"
      "v_lshrrev_b32 v8, 4, v15\n"
      "v_mul_u32_u24 v5, 272, v8\n"
      "v_mul_u32_u24 v8, 8192, v8\n"
      "v_and_b32 v14, 15, v15\n"
      "v_lshlrev_b32 v14, 4, v14\n"
      "v_add_u32 v8, v8, v14\n"
      "v_add_u32 v5, v5, v14\n"
      "v_add_u32 v6, 69632, v5\n"
      "s_waitcnt vmcnt(15)\n"
      "ds_write_b128 v5, v[64:67] offset:0\n"
      "s_waitcnt lgkmcnt(0)\n"
      "s_add_u32 s48, s80, 2097152\n"
      "s_addc_u32 s49, s81, 0\n"
      "global_load_dwordx4 v[64:67], v2, s[48:49] \n"
      "s_waitcnt vmcnt(15)\n"
      "ds_write_b128 v5, v[68:71] offset:4352\n"
      "s_waitcnt lgkmcnt(0)\n"
      "s_add_u32 s48, s80, 2228224\n"
      "s_addc_u32 s49, s81, 0\n"
      "global_load_dwordx4 v[68:71], v2, s[48:49] \n"
      "s_waitcnt vmcnt(15)\n"
      "ds_write_b128 v5, v[72:75] offset:8704\n"
      "s_waitcnt lgkmcnt(0)\n"
      "s_add_u32 s48, s80, 2359296\n"
      "s_addc_u32 s49, s81, 0\n"
      "global_load_dwordx4 v[72:75], v2, s[48:49] \n"
      "s_waitcnt vmcnt(15)\n"
      "ds_write_b128 v5, v[76:79] offset:13056\n"
      "s_waitcnt lgkmcnt(0)\n"
      "s_add_u32 s48, s80, 2490368\n"
      "s_addc_u32 s49, s81, 0\n"
      "global_load_dwordx4 v[76:79], v2, s[48:49] \n"
      "s_waitcnt vmcnt(15)\n"
      "ds_write_b128 v5, v[80:83] offset:17408\n"
      "s_waitcnt lgkmcnt(0)\n"
      "s_add_u32 s48, s80, 2621440\n"
      "s_addc_u32 s49, s81, 0\n"
      "global_load_dwordx4 v[80:83], v2, s[48:49] \n"
      "s_waitcnt vmcnt(15)\n"
      "ds_write_b128 v5, v[84:87] offset:21760\n"
      "s_waitcnt lgkmcnt(0)\n"
      "s_add_u32 s48, s80, 2752512\n"
      "s_addc_u32 s49, s81, 0\n"
      "global_load_dwordx4 v[84:87], v2, s[48:49] \n"
      "s_waitcnt vmcnt(15)\n"
      "ds_write_b128 v5, v[88:91] offset:26112\n"
      "s_waitcnt lgkmcnt(0)\n"
      "s_add_u32 s48, s80, 2883584\n"
      "s_addc_u32 s49, s81, 0\n"
      "global_load_dwordx4 v[88:91], v2, s[48:49] \n"
      "s_waitcnt vmcnt(15)\n"
      "ds_write_b128 v5, v[92:95] offset:30464\n"
      "s_waitcnt lgkmcnt(0)\n"
      "s_add_u32 s48, s80, 3014656\n"
      "s_addc_u32 s49, s81, 0\n"
      "global_load_dwordx4 v[92:95], v2, s[48:49] \n"
      "s_waitcnt vmcnt(15)\n"
      "ds_write_b128 v5, v[96:99] offset:34816\n"
      "s_waitcnt lgkmcnt(0)\n"
      "s_add_u32 s48, s80, 3145728\n"
      "s_addc_u32 s49, s81, 0\n"
      "global_load_dwordx4 v[96:99], v2, s[48:49] \n"
      "s_waitcnt vmcnt(15)\n"
      "ds_write_b128 v5, v[100:103] offset:39168\n"
      "s_waitcnt lgkmcnt(0)\n"
      "s_add_u32 s48, s80, 3276800\n"
      "s_addc_u32 s49, s81, 0\n"
      "global_load_dwordx4 v[100:103], v2, s[48:49] \n"
      "s_waitcnt vmcnt(15)\n"
      "ds_write_b128 v5, v[104:107] offset:43520\n"
      "s_waitcnt lgkmcnt(0)\n"
      "s_add_u32 s48, s80, 3407872\n"
      "s_addc_u32 s49, s81, 0\n"
      "global_load_dwordx4 v[104:107], v2, s[48:49] \n"
      "s_waitcnt vmcnt(15)\n"
      "ds_write_b128 v5, v[108:111] offset:47872\n"
      "s_waitcnt lgkmcnt(0)\n"
      "s_add_u32 s48, s80, 3538944\n"
      "s_addc_u32 s49, s81, 0\n"
      "global_load_dwordx4 v[108:111], v2, s[48:49] \n"
      "s_waitcnt vmcnt(15)\n"
      "ds_write_b128 v5, v[112:115] offset:52224\n"
      "s_waitcnt lgkmcnt(0)\n"
      "s_add_u32 s48, s80, 3670016\n"
      "s_addc_u32 s49, s81, 0\n"
      "global_load_dwordx4 v[112:115], v2, s[48:49] \n"
      "s_waitcnt vmcnt(15)\n"
      "ds_write_b128 v5, v[116:119] offset:56576\n"
      "s_waitcnt lgkmcnt(0)\n"
      "s_add_u32 s48, s80, 3801088\n"
      "s_addc_u32 s49, s81, 0\n"
      "global_load_dwordx4 v[116:119], v2, s[48:49] \n"
      "s_waitcnt vmcnt(15)\n"
      "ds_write_b128 v5, v[120:123] offset:60928\n"
      "s_waitcnt lgkmcnt(0)\n"
      "s_add_u32 s48, s80, 3932160\n"
      "s_addc_u32 s49, s81, 0\n"
      "global_load_dwordx4 v[120:123], v2, s[48:49] \n"
      "s_waitcnt vmcnt(15)\n"
      "ds_write_b128 v5, v[124:127] offset:65280\n"
      "s_waitcnt lgkmcnt(0)\n"
      "s_add_u32 s48, s80, 4063232\n"
      "s_addc_u32 s49, s81, 0\n"
      "global_load_dwordx4 v[124:127], v2, s[48:49] \n"
      "s_waitcnt vmcnt(15)\n"
      "ds_write_b128 v6, v[64:67] offset:0\n"
      "s_waitcnt lgkmcnt(0)\n"
      "s_add_u32 s48, s82, 0\n"
      "s_addc_u32 s49, s83, 0\n"
      "global_load_dwordx4 v[64:67], v3, s[48:49] \n"
      "s_waitcnt vmcnt(15)\n"
      "ds_write_b128 v6, v[68:71] offset:4352\n"
      "s_waitcnt lgkmcnt(0)\n"
      "s_add_u32 s48, s82, 32768\n"
      "s_addc_u32 s49, s83, 0\n"
      "global_load_dwordx4 v[68:71], v3, s[48:49] \n"
      "s_waitcnt vmcnt(15)\n"
      "ds_write_b128 v6, v[72:75] offset:8704\n"
      "s_waitcnt lgkmcnt(0)\n"
      "s_add_u32 s48, s82, 65536\n"
      "s_addc_u32 s49, s83, 0\n"
      "global_load_dwordx4 v[72:75], v3, s[48:49] \n"
      "s_waitcnt vmcnt(15)\n"
      "ds_write_b128 v6, v[76:79] offset:13056\n"
      "s_waitcnt lgkmcnt(0)\n"
      "s_add_u32 s48, s82, 98304\n"
      "s_addc_u32 s49, s83, 0\n"
      "global_load_dwordx4 v[76:79], v3, s[48:49] \n"
      "s_waitcnt vmcnt(15)\n"
      "ds_write_b128 v6, v[80:83] offset:17408\n"
      "s_waitcnt lgkmcnt(0)\n"
      "s_add_u32 s48, s82, 16384\n"
      "s_addc_u32 s49, s83, 0\n"
      "global_load_dwordx4 v[80:83], v3, s[48:49] \n"
      "s_waitcnt vmcnt(15)\n"
      "ds_write_b128 v6, v[84:87] offset:21760\n"
      "s_waitcnt lgkmcnt(0)\n"
      "s_add_u32 s48, s82, 49152\n"
      "s_addc_u32 s49, s83, 0\n"
      "global_load_dwordx4 v[84:87], v3, s[48:49] \n"
      "s_waitcnt vmcnt(15)\n"
      "ds_write_b128 v6, v[88:91] offset:26112\n"
      "s_waitcnt lgkmcnt(0)\n"
      "s_add_u32 s48, s82, 81920\n"
      "s_addc_u32 s49, s83, 0\n"
      "global_load_dwordx4 v[88:91], v3, s[48:49] \n"
      "s_waitcnt vmcnt(15)\n"
      "ds_write_b128 v6, v[92:95] offset:30464\n"
      "s_waitcnt lgkmcnt(0)\n"
      "s_add_u32 s48, s82, 114688\n"
      "s_addc_u32 s49, s83, 0\n"
      "global_load_dwordx4 v[92:95], v3, s[48:49] \n"
      "s_waitcnt vmcnt(15)\n"
      "ds_write_b128 v6, v[96:99] offset:34816\n"
      "s_waitcnt lgkmcnt(0)\n"
      "s_add_u32 s48, s82, 4096\n"
      "s_addc_u32 s49, s83, 0\n"
      "global_load_dwordx4 v[96:99], v3, s[48:49] \n"
      "s_waitcnt vmcnt(15)\n"
      "ds_write_b128 v6, v[100:103] offset:39168\n"
      "s_waitcnt lgkmcnt(0)\n"
      "s_add_u32 s48, s82, 36864\n"
      "s_addc_u32 s49, s83, 0\n"
      "global_load_dwordx4 v[100:103], v3, s[48:49] \n"
      "s_waitcnt vmcnt(15)\n"
      "ds_write_b128 v6, v[104:107] offset:43520\n"
      "s_waitcnt lgkmcnt(0)\n"
      "s_add_u32 s48, s82, 69632\n"
      "s_addc_u32 s49, s83, 0\n"
      "global_load_dwordx4 v[104:107], v3, s[48:49] \n"
      "s_waitcnt vmcnt(15)\n"
      "ds_write_b128 v6, v[108:111] offset:47872\n"
      "s_waitcnt lgkmcnt(0)\n"
      "s_add_u32 s48, s82, 102400\n"
      "s_addc_u32 s49, s83, 0\n"
      "global_load_dwordx4 v[108:111], v3, s[48:49] \n"
      "s_waitcnt vmcnt(15)\n"
      "ds_write_b128 v6, v[112:115] offset:52224\n"
      "s_waitcnt lgkmcnt(0)\n"
      "s_add_u32 s48, s82, 20480\n"
      "s_addc_u32 s49, s83, 0\n"
      "global_load_dwordx4 v[112:115], v3, s[48:49] \n"
      "s_waitcnt vmcnt(15)\n"
      "ds_write_b128 v6, v[116:119] offset:56576\n"
      "s_waitcnt lgkmcnt(0)\n"
      "s_add_u32 s48, s82, 53248\n"
      "s_addc_u32 s49, s83, 0\n"
      "global_load_dwordx4 v[116:119], v3, s[48:49] \n"
      "s_waitcnt vmcnt(15)\n"
      "ds_write_b128 v6, v[120:123] offset:60928\n"
      "s_waitcnt lgkmcnt(0)\n"
      "s_add_u32 s48, s82, 86016\n"
      "s_addc_u32 s49, s83, 0\n"
      "global_load_dwordx4 v[120:123], v3, s[48:49] \n"
      "s_waitcnt vmcnt(15)\n"
      "ds_write_b128 v6, v[124:127] offset:65280\n"
      "s_waitcnt lgkmcnt(0)\n"
      "s_add_u32 s48, s82, 118784\n"
      "s_addc_u32 s49, s83, 0\n"
      "global_load_dwordx4 v[124:127], v3, s[48:49] \n"
      "s_waitcnt lgkmcnt(0)\n"
      "s_barrier\n"
      "v_lshrrev_b32 v12, 4, v0\n"
      "v_lshlrev_b32 v12, 3, v12\n"
      "v_bfe_u32 v13, v0, 2, 2\n"
      "v_add_u32 v12, v12, v13\n"
      "v_mul_u32_u24 v12, 272, v12\n"
      "v_and_b32 v13, 3, v0\n"
      "v_lshlrev_b32 v13, 3, v13\n"
      "v_add_u32 v12, v12, v13\n"
      "s_mul_i32 s66, s53, 64\n"
      "v_add_u32 v12, s66, v12\n"
      "v_add_u32 v13, 52224, v12\n"
      "v_add_u32 v14, 104448, v12\n"
      "ds_read_b64_tr_b16 a[128:129], v12 offset:0\n"
      "ds_read_b64_tr_b16 a[130:131], v12 offset:1088\n"
      "ds_read_b64_tr_b16 a[132:133], v12 offset:8704\n"
      "ds_read_b64_tr_b16 a[134:135], v12 offset:9792\n"
      "ds_read_b64_tr_b16 a[136:137], v12 offset:17408\n"
      "ds_read_b64_tr_b16 a[138:139], v12 offset:18496\n"
      "ds_read_b64_tr_b16 a[140:141], v12 offset:26112\n"
      "ds_read_b64_tr_b16 a[142:143], v12 offset:27200\n"
      "ds_read_b64_tr_b16 a[144:145], v12 offset:34816\n"
      "ds_read_b64_tr_b16 a[146:147], v12 offset:35904\n"
      "s_waitcnt lgkmcnt(4)\n"
      "ds_read_b64_tr_b16 a[148:149], v12 offset:43520\n"
      "ds_read_b64_tr_b16 a[150:151], v12 offset:44608\n"
      "ds_read_b64_tr_b16 a[152:153], v13 offset:0\n"
      "ds_read_b64_tr_b16 a[154:155], v13 offset:1088\n"
      "ds_read_b64_tr_b16 a[156:157], v13 offset:8704\n"
      "ds_read_b64_tr_b16 a[158:159], v13 offset:9792\n"
      "ds_read_b64_tr_b16 a[160:161], v13 offset:17408\n"
      "ds_read_b64_tr_b16 a[162:163], v13 offset:18496\n"
      "ds_read_b64_tr_b16 a[164:165], v13 offset:26112\n"
      "ds_read_b64_tr_b16 a[166:167], v13 offset:27200\n"
      "s_waitcnt lgkmcnt(4)\n"
      "ds_read_b64_tr_b16 a[168:169], v13 offset:34816\n"
      "ds_read_b64_tr_b16 a[170:171], v13 offset:35904\n"
      "ds_read_b64_tr_b16 a[172:173], v13 offset:43520\n"
      "ds_read_b64_tr_b16 a[174:175], v13 offset:44608\n"
      "ds_read_b64_tr_b16 a[176:177], v14 offset:0\n"
      "ds_read_b64_tr_b16 a[178:179], v14 offset:1088\n"
      "ds_read_b64_tr_b16 a[180:181], v14 offset:8704\n"
      "ds_read_b64_tr_b16 a[182:183], v14 offset:9792\n"
      "ds_read_b64_tr_b16 a[184:185], v14 offset:17408\n"
      "ds_read_b64_tr_b16 a[186:187], v14 offset:18496\n"
      "s_waitcnt lgkmcnt(4)\n"
      "ds_read_b64_tr_b16 a[188:189], v14 offset:26112\n"
      "ds_read_b64_tr_b16 a[190:191], v14 offset:27200\n"
      "ds_read_b64_tr_b16 a[192:193], v12 offset:32\n"
      "ds_read_b64_tr_b16 a[194:195], v12 offset:1120\n"
      "ds_read_b64_tr_b16 a[196:197], v12 offset:8736\n"
      "ds_read_b64_tr_b16 a[198:199], v12 offset:9824\n"
      "ds_read_b64_tr_b16 a[200:201], v12 offset:17440\n"
      "ds_read_b64_tr_b16 a[202:203], v12 offset:18528\n"
      "ds_read_b64_tr_b16 a[204:205], v12 offset:26144\n"
      "ds_read_b64_tr_b16 a[206:207], v12 offset:27232\n"
      "s_waitcnt lgkmcnt(4)\n"
      "ds_read_b64_tr_b16 a[208:209], v12 offset:34848\n"
      "ds_read_b64_tr_b16 a[210:211], v12 offset:35936\n"
      "ds_read_b64_tr_b16 a[212:213], v12 offset:43552\n"
      "ds_read_b64_tr_b16 a[214:215], v12 offset:44640\n"
      "ds_read_b64_tr_b16 a[216:217], v13 offset:32\n"
      "ds_read_b64_tr_b16 a[218:219], v13 offset:1120\n"
      "ds_read_b64_tr_b16 a[220:221], v13 offset:8736\n"
      "ds_read_b64_tr_b16 a[222:223], v13 offset:9824\n"
      "ds_read_b64_tr_b16 a[224:225], v13 offset:17440\n"
      "ds_read_b64_tr_b16 a[226:227], v13 offset:18528\n"
      "s_waitcnt lgkmcnt(4)\n"
      "ds_read_b64_tr_b16 a[228:229], v13 offset:26144\n"
      "ds_read_b64_tr_b16 a[230:231], v13 offset:27232\n"
      "ds_read_b64_tr_b16 a[232:233], v13 offset:34848\n"
      "ds_read_b64_tr_b16 a[234:235], v13 offset:35936\n"
      "ds_read_b64_tr_b16 a[236:237], v13 offset:43552\n"
      "ds_read_b64_tr_b16 a[238:239], v13 offset:44640\n"
      "ds_read_b64_tr_b16 a[240:241], v14 offset:32\n"
      "ds_read_b64_tr_b16 a[242:243], v14 offset:1120\n"
      "ds_read_b64_tr_b16 a[244:245], v14 offset:8736\n"
      "ds_read_b64_tr_b16 a[246:247], v14 offset:9824\n"
      "s_waitcnt lgkmcnt(4)\n"
      "ds_read_b64_tr_b16 a[248:249], v14 offset:17440\n"
      "ds_read_b64_tr_b16 a[250:251], v14 offset:18528\n"
      "ds_read_b64_tr_b16 a[252:253], v14 offset:26144\n"
      "ds_read_b64_tr_b16 a[254:255], v14 offset:27232\n"
      "s_waitcnt lgkmcnt(0)\n"
      "s_barrier\n"
      "v_and_b32 v12, 7, v0\n"
      "v_mul_u32_u24 v12, 1040, v12\n"
      "v_lshrrev_b32 v13, 4, v0\n"
      "v_lshlrev_b32 v13, 4, v13\n"
      "v_add_u32 v12, v12, v13\n"
      "v_add_u32 v12, 143360, v12\n"
      "ds_read_b128 v[24:27], v12 offset:0\n"
      "ds_read_b128 v[28:31], v12 offset:64\n"
      "ds_read_b128 v[32:35], v12 offset:128\n"
      "ds_read_b128 v[36:39], v12 offset:192\n"
      "ds_read_b128 v[40:43], v12 offset:256\n"
      "ds_read_b128 v[44:47], v12 offset:320\n"
      "ds_read_b128 v[48:51], v12 offset:384\n"
      "ds_read_b128 v[52:55], v12 offset:448\n"
      "s_waitcnt lgkmcnt(7)\n"
      "v_mfma_f32_16x16x32_bf16 v[56:59], v[24:27], a[128:131], 0\n"
      "v_mfma_f32_16x16x32_bf16 v[60:63], v[24:27], a[192:195], 0\n"
      "ds_read_b128 v[24:27], v12 offset:512\n"
      "s_waitcnt lgkmcnt(7)\n"
      "v_mfma_f32_16x16x32_bf16 v[56:59], v[28:31], a[132:135], v[56:59]\n"
      "v_mfma_f32_16x16x32_bf16 v[60:63], v[28:31], a[196:199], v[60:63]\n"
      "ds_read_b128 v[28:31], v12 offset:576\n"
      "s_waitcnt lgkmcnt(7)\n"
      "v_mfma_f32_16x16x32_bf16 v[56:59], v[32:35], a[136:139], v[56:59]\n"
      "v_mfma_f32_16x16x32_bf16 v[60:63], v[32:35], a[200:203], v[60:63]\n"
      "ds_read_b128 v[32:35], v12 offset:640\n"
      "s_waitcnt lgkmcnt(7)\n"
      "v_mfma_f32_16x16x32_bf16 v[56:59], v[36:39], a[140:143], v[56:59]\n"
      "v_mfma_f32_16x16x32_bf16 v[60:63], v[36:39], a[204:207], v[60:63]\n"
      "ds_read_b128 v[36:39], v12 offset:704\n"
      "s_waitcnt lgkmcnt(7)\n"
      "v_mfma_f32_16x16x32_bf16 v[56:59], v[40:43], a[144:147], v[56:59]\n"
      "v_mfma_f32_16x16x32_bf16 v[60:63], v[40:43], a[208:211], v[60:63]\n"
      "ds_read_b128 v[40:43], v12 offset:768\n"
      "s_waitcnt lgkmcnt(7)\n"
      "v_mfma_f32_16x16x32_bf16 v[56:59], v[44:47], a[148:151], v[56:59]\n"
      "v_mfma_f32_16x16x32_bf16 v[60:63], v[44:47], a[212:215], v[60:63]\n"
      "ds_read_b128 v[44:47], v12 offset:832\n"
      "s_waitcnt lgkmcnt(7)\n"
      "v_mfma_f32_16x16x32_bf16 v[56:59], v[48:51], a[152:155], v[56:59]\n"
      "v_mfma_f32_16x16x32_bf16 v[60:63], v[48:51], a[216:219], v[60:63]\n"
      "ds_read_b128 v[48:51], v12 offset:896\n"
      "s_waitcnt lgkmcnt(7)\n"
      "v_mfma_f32_16x16x32_bf16 v[56:59], v[52:55], a[156:159], v[56:59]\n"
      "v_mfma_f32_16x16x32_bf16 v[60:63], v[52:55], a[220:223], v[60:63]\n"
      "ds_read_b128 v[52:55], v12 offset:960\n"
      "s_waitcnt lgkmcnt(7)\n"
      "v_mfma_f32_16x16x32_bf16 v[56:59], v[24:27], a[160:163], v[56:59]\n"
      "v_mfma_f32_16x16x32_bf16 v[60:63], v[24:27], a[224:227], v[60:63]\n"
      "s_waitcnt lgkmcnt(6)\n"
      "v_mfma_f32_16x16x32_bf16 v[56:59], v[28:31], a[164:167], v[56:59]\n"
      "v_mfma_f32_16x16x32_bf16 v[60:63], v[28:31], a[228:231], v[60:63]\n"
      "s_waitcnt lgkmcnt(5)\n"
      "v_mfma_f32_16x16x32_bf16 v[56:59], v[32:35], a[168:171], v[56:59]\n"
      "v_mfma_f32_16x16x32_bf16 v[60:63], v[32:35], a[232:235], v[60:63]\n"
      "s_waitcnt lgkmcnt(4)\n"
      "v_mfma_f32_16x16x32_bf16 v[56:59], v[36:39], a[172:175], v[56:59]\n"
      "v_mfma_f32_16x16x32_bf16 v[60:63], v[36:39], a[236:239], v[60:63]\n"
      "s_waitcnt lgkmcnt(3)\n"
      "v_mfma_f32_16x16x32_bf16 v[56:59], v[40:43], a[176:179], v[56:59]\n"
      "v_mfma_f32_16x16x32_bf16 v[60:63], v[40:43], a[240:243], v[60:63]\n"
      "s_waitcnt lgkmcnt(2)\n"
      "v_mfma_f32_16x16x32_bf16 v[56:59], v[44:47], a[180:183], v[56:59]\n"
      "v_mfma_f32_16x16x32_bf16 v[60:63], v[44:47], a[244:247], v[60:63]\n"
      "s_waitcnt lgkmcnt(1)\n"
      "v_mfma_f32_16x16x32_bf16 v[56:59], v[48:51], a[184:187], v[56:59]\n"
      "v_mfma_f32_16x16x32_bf16 v[60:63], v[48:51], a[248:251], v[60:63]\n"
      "s_waitcnt lgkmcnt(0)\n"
      "v_mfma_f32_16x16x32_bf16 v[56:59], v[52:55], a[188:191], v[56:59]\n"
      "v_mfma_f32_16x16x32_bf16 v[60:63], v[52:55], a[252:255], v[60:63]\n"
      "v_lshrrev_b32 v40, 4, v0\n"
      "v_lshlrev_b32 v40, 15, v40\n"
      "v_and_b32 v41, 15, v0\n"
      "v_lshrrev_b32 v41, 1, v41\n"
      "v_lshlrev_b32 v41, 2, v41\n"
      "v_add_u32 v40, v40, v41\n"
      "s_lshl_b32 s65, s56, 8\n"
      "s_lshl_b32 s66, s53, 6\n"
      "s_add_u32 s65, s65, s66\n"
      "s_add_u32 s65, s65, 528384\n"
      "v_mul_f32 v32, 0x3e0293ee, v56\n"
      "s_nop 1\n"
      "v_mov_b32_dpp v13, v32 quad_perm:[1,0,3,2] row_mask:0xf bank_mask:0xf\n"
      "v_cvt_pk_bf16_f32 v32, v32, v13\n"
      "v_min_u32 v32, -2, v32\n"
      "s_add_u32 s66, s65, 0\n"
      "s_add_u32 s50, s40, s66\n"
      "s_addc_u32 s51, s41, 0\n"
      "s_mov_b32 exec_lo, 0x55555555\n"
      "s_mov_b32 exec_hi, 0x0\n"
      "global_store_dword v40, v32, s[50:51] sc1\n"
      "s_mov_b64 exec, -1\n"
      "v_mul_f32 v32, 0x3e0293ee, v57\n"
      "s_nop 1\n"
      "v_mov_b32_dpp v13, v32 quad_perm:[1,0,3,2] row_mask:0xf bank_mask:0xf\n"
      "v_cvt_pk_bf16_f32 v32, v32, v13\n"
      "v_min_u32 v32, -2, v32\n"
      "s_add_u32 s66, s65, 8192\n"
      "s_add_u32 s50, s40, s66\n"
      "s_addc_u32 s51, s41, 0\n"
      "s_mov_b32 exec_lo, 0x55555555\n"
      "s_mov_b32 exec_hi, 0x0\n"
      "global_store_dword v40, v32, s[50:51] sc1\n"
      "s_mov_b64 exec, -1\n"
      "v_mul_f32 v32, 0x3e0293ee, v58\n"
      "s_nop 1\n"
      "v_mov_b32_dpp v13, v32 quad_perm:[1,0,3,2] row_mask:0xf bank_mask:0xf\n"
      "v_cvt_pk_bf16_f32 v32, v32, v13\n"
      "v_min_u32 v32, -2, v32\n"
      "s_add_u32 s66, s65, 16384\n"
      "s_add_u32 s50, s40, s66\n"
      "s_addc_u32 s51, s41, 0\n"
      "s_mov_b32 exec_lo, 0x55555555\n"
      "s_mov_b32 exec_hi, 0x0\n"
      "global_store_dword v40, v32, s[50:51] sc1\n"
      "s_mov_b64 exec, -1\n"
      "v_mul_f32 v32, 0x3e0293ee, v59\n"
      "s_nop 1\n"
      "v_mov_b32_dpp v13, v32 quad_perm:[1,0,3,2] row_mask:0xf bank_mask:0xf\n"
      "v_cvt_pk_bf16_f32 v32, v32, v13\n"
      "v_min_u32 v32, -2, v32\n"
      "s_add_u32 s66, s65, 24576\n"
      "s_add_u32 s50, s40, s66\n"
      "s_addc_u32 s51, s41, 0\n"
      "s_mov_b32 exec_lo, 0x55555555\n"
      "s_mov_b32 exec_hi, 0x0\n"
      "global_store_dword v40, v32, s[50:51] sc1\n"
      "s_mov_b64 exec, -1\n"
      "v_mul_f32 v32, 0x3e0293ee, v60\n"
      "s_nop 1\n"
      "v_mov_b32_dpp v13, v32 quad_perm:[1,0,3,2] row_mask:0xf bank_mask:0xf\n"
      "v_cvt_pk_bf16_f32 v32, v32, v13\n"
      "v_min_u32 v32, -2, v32\n"
      "s_add_u32 s66, s65, 32\n"
      "s_add_u32 s50, s40, s66\n"
      "s_addc_u32 s51, s41, 0\n"
      "s_mov_b32 exec_lo, 0x55555555\n"
      "s_mov_b32 exec_hi, 0x0\n"
      "global_store_dword v40, v32, s[50:51] sc1\n"
      "s_mov_b64 exec, -1\n"
      "v_mul_f32 v32, 0x3e0293ee, v61\n"
      "s_nop 1\n"
      "v_mov_b32_dpp v13, v32 quad_perm:[1,0,3,2] row_mask:0xf bank_mask:0xf\n"
      "v_cvt_pk_bf16_f32 v32, v32, v13\n"
      "v_min_u32 v32, -2, v32\n"
      "s_add_u32 s66, s65, 8224\n"
      "s_add_u32 s50, s40, s66\n"
      "s_addc_u32 s51, s41, 0\n"
      "s_mov_b32 exec_lo, 0x55555555\n"
      "s_mov_b32 exec_hi, 0x0\n"
      "global_store_dword v40, v32, s[50:51] sc1\n"
      "s_mov_b64 exec, -1\n"
      "v_mul_f32 v32, 0x3e0293ee, v62\n"
      "s_nop 1\n"
      "v_mov_b32_dpp v13, v32 quad_perm:[1,0,3,2] row_mask:0xf bank_mask:0xf\n"
      "v_cvt_pk_bf16_f32 v32, v32, v13\n"
      "v_min_u32 v32, -2, v32\n"
      "s_add_u32 s66, s65, 16416\n"
      "s_add_u32 s50, s40, s66\n"
      "s_addc_u32 s51, s41, 0\n"
      "s_mov_b32 exec_lo, 0x55555555\n"
      "s_mov_b32 exec_hi, 0x0\n"
      "global_store_dword v40, v32, s[50:51] sc1\n"
      "s_mov_b64 exec, -1\n"
      "v_mul_f32 v32, 0x3e0293ee, v63\n"
      "s_nop 1\n"
      "v_mov_b32_dpp v13, v32 quad_perm:[1,0,3,2] row_mask:0xf bank_mask:0xf\n"
      "v_cvt_pk_bf16_f32 v32, v32, v13\n"
      "v_min_u32 v32, -2, v32\n"
      "s_add_u32 s66, s65, 24608\n"
      "s_add_u32 s50, s40, s66\n"
      "s_addc_u32 s51, s41, 0\n"
      "s_mov_b32 exec_lo, 0x55555555\n"
      "s_mov_b32 exec_hi, 0x0\n"
      "global_store_dword v40, v32, s[50:51] sc1\n"
      "s_mov_b64 exec, -1\n"
      "s_waitcnt lgkmcnt(0)\n"
      "s_barrier\n"
      "v_lshrrev_b32 v5, 5, v15\n"
      "v_and_b32 v8, 1, v5\n"
      "v_lshlrev_b32 v8, 3, v8\n"
      "v_lshrrev_b32 v14, 1, v5\n"
      "v_add_u32 v8, v8, v14\n"
      "v_lshlrev_b32 v8, 10, v8\n"
      "v_mul_u32_u24 v5, 528, v5\n"
      "v_and_b32 v14, 31, v15\n"
      "v_lshlrev_b32 v14, 4, v14\n"
      "v_add_u32 v8, v8, v14\n"
      "v_add_u32 v5, v5, v14\n"
      "v_add_u32 v6, 67584, v5\n"
      "s_waitcnt vmcnt(15)\n"
      "ds_write_b128 v5, v[64:67] offset:0\n"
      "s_waitcnt lgkmcnt(0)\n"
      "s_add_u32 s48, s82, 131072\n"
      "s_addc_u32 s49, s83, 0\n"
      "global_load_dwordx4 v[64:67], v3, s[48:49] \n"
      "s_waitcnt vmcnt(15)\n"
      "ds_write_b128 v5, v[68:71] offset:4224\n"
      "s_waitcnt lgkmcnt(0)\n"
      "s_add_u32 s48, s82, 163840\n"
      "s_addc_u32 s49, s83, 0\n"
      "global_load_dwordx4 v[68:71], v3, s[48:49] \n"
      "s_waitcnt vmcnt(15)\n"
      "ds_write_b128 v5, v[72:75] offset:8448\n"
      "s_waitcnt lgkmcnt(0)\n"
      "s_add_u32 s48, s82, 196608\n"
      "s_addc_u32 s49, s83, 0\n"
      "global_load_dwordx4 v[72:75], v3, s[48:49] \n"
      "s_waitcnt vmcnt(15)\n"
      "ds_write_b128 v5, v[76:79] offset:12672\n"
      "s_waitcnt lgkmcnt(0)\n"
      "s_add_u32 s48, s82, 229376\n"
      "s_addc_u32 s49, s83, 0\n"
      "global_load_dwordx4 v[76:79], v3, s[48:49] \n"
      "s_waitcnt vmcnt(15)\n"
      "ds_write_b128 v5, v[80:83] offset:16896\n"
      "s_waitcnt lgkmcnt(0)\n"
      "s_add_u32 s48, s82, 147456\n"
      "s_addc_u32 s49, s83, 0\n"
      "global_load_dwordx4 v[80:83], v3, s[48:49] \n"
      "s_waitcnt vmcnt(15)\n"
      "ds_write_b128 v5, v[84:87] offset:21120\n"
      "s_waitcnt lgkmcnt(0)\n"
      "s_add_u32 s48, s82, 180224\n"
      "s_addc_u32 s49, s83, 0\n"
      "global_load_dwordx4 v[84:87], v3, s[48:49] \n"
      "s_waitcnt vmcnt(15)\n"
      "ds_write_b128 v5, v[88:91] offset:25344\n"
      "s_waitcnt lgkmcnt(0)\n"
      "s_add_u32 s48, s82, 212992\n"
      "s_addc_u32 s49, s83, 0\n"
      "global_load_dwordx4 v[88:91], v3, s[48:49] \n"
      "s_waitcnt vmcnt(15)\n"
      "ds_write_b128 v5, v[92:95] offset:29568\n"
      "s_waitcnt lgkmcnt(0)\n"
      "s_add_u32 s48, s82, 245760\n"
      "s_addc_u32 s49, s83, 0\n"
      "global_load_dwordx4 v[92:95], v3, s[48:49] \n"
      "s_waitcnt vmcnt(15)\n"
      "ds_write_b128 v5, v[96:99] offset:33792\n"
      "s_waitcnt lgkmcnt(0)\n"
      "s_add_u32 s48, s82, 135168\n"
      "s_addc_u32 s49, s83, 0\n"
      "global_load_dwordx4 v[96:99], v3, s[48:49] \n"
      "s_waitcnt vmcnt(15)\n"
      "ds_write_b128 v5, v[100:103] offset:38016\n"
      "s_waitcnt lgkmcnt(0)\n"
      "s_add_u32 s48, s82, 167936\n"
      "s_addc_u32 s49, s83, 0\n"
      "global_load_dwordx4 v[100:103], v3, s[48:49] \n"
      "s_waitcnt vmcnt(15)\n"
      "ds_write_b128 v5, v[104:107] offset:42240\n"
      "s_waitcnt lgkmcnt(0)\n"
      "s_add_u32 s48, s82, 200704\n"
      "s_addc_u32 s49, s83, 0\n"
      "global_load_dwordx4 v[104:107], v3, s[48:49] \n"
      "s_waitcnt vmcnt(15)\n"
      "ds_write_b128 v5, v[108:111] offset:46464\n"
      "s_waitcnt lgkmcnt(0)\n"
      "s_add_u32 s48, s82, 233472\n"
      "s_addc_u32 s49, s83, 0\n"
      "global_load_dwordx4 v[108:111], v3, s[48:49] \n"
      "s_waitcnt vmcnt(15)\n"
      "ds_write_b128 v5, v[112:115] offset:50688\n"
      "s_waitcnt lgkmcnt(0)\n"
      "s_add_u32 s48, s82, 151552\n"
      "s_addc_u32 s49, s83, 0\n"
      "global_load_dwordx4 v[112:115], v3, s[48:49] \n"
      "s_waitcnt vmcnt(15)\n"
      "ds_write_b128 v5, v[116:119] offset:54912\n"
      "s_waitcnt lgkmcnt(0)\n"
      "s_add_u32 s48, s82, 184320\n"
      "s_addc_u32 s49, s83, 0\n"
      "global_load_dwordx4 v[116:119], v3, s[48:49] \n"
      "s_waitcnt vmcnt(15)\n"
      "ds_write_b128 v5, v[120:123] offset:59136\n"
      "s_waitcnt lgkmcnt(0)\n"
      "s_add_u32 s48, s82, 217088\n"
      "s_addc_u32 s49, s83, 0\n"
      "global_load_dwordx4 v[120:123], v3, s[48:49] \n"
      "s_waitcnt vmcnt(15)\n"
      "ds_write_b128 v5, v[124:127] offset:63360\n"
      "s_waitcnt lgkmcnt(0)\n"
      "s_add_u32 s48, s82, 249856\n"
      "s_addc_u32 s49, s83, 0\n"
      "global_load_dwordx4 v[124:127], v3, s[48:49] \n"
      "s_waitcnt vmcnt(15)\n"
      "ds_write_b128 v6, v[64:67] offset:0\n"
      "s_waitcnt vmcnt(14)\n"
      "ds_write_b128 v6, v[68:71] offset:4224\n"
      "s_waitcnt vmcnt(13)\n"
      "ds_write_b128 v6, v[72:75] offset:8448\n"
      "s_waitcnt vmcnt(12)\n"
      "ds_write_b128 v6, v[76:79] offset:12672\n"
      "s_waitcnt vmcnt(11)\n"
      "ds_write_b128 v6, v[80:83] offset:16896\n"
      "s_waitcnt vmcnt(10)\n"
      "ds_write_b128 v6, v[84:87] offset:21120\n"
      "s_waitcnt vmcnt(9)\n"
      "ds_write_b128 v6, v[88:91] offset:25344\n"
      "s_waitcnt vmcnt(8)\n"
      "ds_write_b128 v6, v[92:95] offset:29568\n"
      "s_waitcnt vmcnt(7)\n"
      "ds_write_b128 v6, v[96:99] offset:33792\n"
      "s_waitcnt vmcnt(6)\n"
      "ds_write_b128 v6, v[100:103] offset:38016\n"
      "s_waitcnt vmcnt(5)\n"
      "ds_write_b128 v6, v[104:107] offset:42240\n"
      "s_waitcnt vmcnt(4)\n"
      "ds_write_b128 v6, v[108:111] offset:46464\n"
      "s_waitcnt vmcnt(3)\n"
      "ds_write_b128 v6, v[112:115] offset:50688\n"
      "s_waitcnt vmcnt(2)\n"
      "ds_write_b128 v6, v[116:119] offset:54912\n"
      "s_waitcnt vmcnt(1)\n"
      "ds_write_b128 v6, v[120:123] offset:59136\n"
      "s_waitcnt vmcnt(0)\n"
      "ds_write_b128 v6, v[124:127] offset:63360\n"
      "s_waitcnt lgkmcnt(0)\n"
      "s_barrier\n"
      "v_lshrrev_b32 v12, 4, v0\n"
      "v_lshlrev_b32 v12, 3, v12\n"
      "v_bfe_u32 v13, v0, 2, 2\n"
      "v_add_u32 v12, v12, v13\n"
      "v_mul_u32_u24 v12, 528, v12\n"
      "v_and_b32 v13, 3, v0\n"
      "v_lshlrev_b32 v13, 3, v13\n"
      "v_add_u32 v12, v12, v13\n"
      "s_mul_i32 s66, s53, 128\n"
      "v_add_u32 v12, s66, v12\n"
      "v_add_u32 v13, 67584, v12\n"
      "v_add_u32 v14, 135168, v12\n"
      "ds_read_b64_tr_b16 a[0:1], v12 offset:0\n"
      "ds_read_b64_tr_b16 a[2:3], v12 offset:2112\n"
      "ds_read_b64_tr_b16 a[4:5], v12 offset:16896\n"
      "ds_read_b64_tr_b16 a[6:7], v12 offset:19008\n"
      "ds_read_b64_tr_b16 a[8:9], v12 offset:33792\n"
      "ds_read_b64_tr_b16 a[10:11], v12 offset:35904\n"
      "ds_read_b64_tr_b16 a[12:13], v12 offset:50688\n"
      "ds_read_b64_tr_b16 a[14:15], v12 offset:52800\n"
      "ds_read_b64_tr_b16 a[16:17], v13 offset:0\n"
      "ds_read_b64_tr_b16 a[18:19], v13 offset:2112\n"
      "s_waitcnt lgkmcnt(4)\n"
      "ds_read_b64_tr_b16 a[20:21], v13 offset:16896\n"
      "ds_read_b64_tr_b16 a[22:23], v13 offset:19008\n"
      "ds_read_b64_tr_b16 a[24:25], v13 offset:33792\n"
      "ds_read_b64_tr_b16 a[26:27], v13 offset:35904\n"
      "ds_read_b64_tr_b16 a[28:29], v13 offset:50688\n"
      "ds_read_b64_tr_b16 a[30:31], v13 offset:52800\n"
      "ds_read_b64_tr_b16 a[32:33], v12 offset:32\n"
      "ds_read_b64_tr_b16 a[34:35], v12 offset:2144\n"
      "ds_read_b64_tr_b16 a[36:37], v12 offset:16928\n"
      "ds_read_b64_tr_b16 a[38:39], v12 offset:19040\n"
      "s_waitcnt lgkmcnt(4)\n"
      "ds_read_b64_tr_b16 a[40:41], v12 offset:33824\n"
      "ds_read_b64_tr_b16 a[42:43], v12 offset:35936\n"
      "ds_read_b64_tr_b16 a[44:45], v12 offset:50720\n"
      "ds_read_b64_tr_b16 a[46:47], v12 offset:52832\n"
      "ds_read_b64_tr_b16 a[48:49], v13 offset:32\n"
      "ds_read_b64_tr_b16 a[50:51], v13 offset:2144\n"
      "ds_read_b64_tr_b16 a[52:53], v13 offset:16928\n"
      "ds_read_b64_tr_b16 a[54:55], v13 offset:19040\n"
      "ds_read_b64_tr_b16 a[56:57], v13 offset:33824\n"
      "ds_read_b64_tr_b16 a[58:59], v13 offset:35936\n"
      "s_waitcnt lgkmcnt(4)\n"
      "ds_read_b64_tr_b16 a[60:61], v13 offset:50720\n"
      "ds_read_b64_tr_b16 a[62:63], v13 offset:52832\n"
      "ds_read_b64_tr_b16 a[64:65], v12 offset:64\n"
      "ds_read_b64_tr_b16 a[66:67], v12 offset:2176\n"
      "ds_read_b64_tr_b16 a[68:69], v12 offset:16960\n"
      "ds_read_b64_tr_b16 a[70:71], v12 offset:19072\n"
      "ds_read_b64_tr_b16 a[72:73], v12 offset:33856\n"
      "ds_read_b64_tr_b16 a[74:75], v12 offset:35968\n"
      "ds_read_b64_tr_b16 a[76:77], v12 offset:50752\n"
      "ds_read_b64_tr_b16 a[78:79], v12 offset:52864\n"
      "s_waitcnt lgkmcnt(4)\n"
      "ds_read_b64_tr_b16 a[80:81], v13 offset:64\n"
      "ds_read_b64_tr_b16 a[82:83], v13 offset:2176\n"
      "ds_read_b64_tr_b16 a[84:85], v13 offset:16960\n"
      "ds_read_b64_tr_b16 a[86:87], v13 offset:19072\n"
      "ds_read_b64_tr_b16 a[88:89], v13 offset:33856\n"
      "ds_read_b64_tr_b16 a[90:91], v13 offset:35968\n"
      "ds_read_b64_tr_b16 a[92:93], v13 offset:50752\n"
      "ds_read_b64_tr_b16 a[94:95], v13 offset:52864\n"
      "ds_read_b64_tr_b16 a[96:97], v12 offset:96\n"
      "ds_read_b64_tr_b16 a[98:99], v12 offset:2208\n"
      "s_waitcnt lgkmcnt(4)\n"
      "ds_read_b64_tr_b16 a[100:101], v12 offset:16992\n"
      "ds_read_b64_tr_b16 a[102:103], v12 offset:19104\n"
      "ds_read_b64_tr_b16 a[104:105], v12 offset:33888\n"
      "ds_read_b64_tr_b16 a[106:107], v12 offset:36000\n"
      "ds_read_b64_tr_b16 a[108:109], v12 offset:50784\n"
      "ds_read_b64_tr_b16 a[110:111], v12 offset:52896\n"
      "ds_read_b64_tr_b16 a[112:113], v13 offset:96\n"
      "ds_read_b64_tr_b16 a[114:115], v13 offset:2208\n"
      "ds_read_b64_tr_b16 a[116:117], v13 offset:16992\n"
      "ds_read_b64_tr_b16 a[118:119], v13 offset:19104\n"
      "s_waitcnt lgkmcnt(4)\n"
      "ds_read_b64_tr_b16 a[120:121], v13 offset:33888\n"
      "ds_read_b64_tr_b16 a[122:123], v13 offset:36000\n"
      "ds_read_b64_tr_b16 a[124:125], v13 offset:50784\n"
      "ds_read_b64_tr_b16 a[126:127], v13 offset:52896\n"
      "s_waitcnt lgkmcnt(0)\n"
      "s_barrier\n"
      "s_branch C_wdone9_%=\n"
      "C_wr10_%=:\n"
      "s_cmp_eq_u32 s54, 1\n"
      "s_cbranch_scc0 C_wr11_%=\n"
      "v_lshrrev_b32 v5, 5, v15\n"
      "v_and_b32 v2, 1, v5\n"
      "v_lshlrev_b32 v2, 3, v2\n"
      "v_lshrrev_b32 v14, 1, v5\n"
      "v_add_u32 v2, v2, v14\n"
      "v_lshlrev_b32 v2, 10, v2\n"
      "v_mul_u32_u24 v5, 528, v5\n"
      "v_and_b32 v14, 31, v15\n"
      "v_lshlrev_b32 v14, 4, v14\n"
      "v_add_u32 v2, v2, v14\n"
      "v_add_u32 v5, v5, v14\n"
      "v_lshrrev_b32 v5, 5, v15\n"
      "v_and_b32 v3, 1, v5\n"
      "v_lshlrev_b32 v3, 3, v3\n"
      "v_lshrrev_b32 v14, 1, v5\n"
      "v_add_u32 v3, v3, v14\n"
      "v_lshlrev_b32 v3, 10, v3\n"
      "v_mul_u32_u24 v5, 528, v5\n"
      "v_and_b32 v14, 31, v15\n"
      "v_lshlrev_b32 v14, 4, v14\n"
      "v_add_u32 v3, v3, v14\n"
      "v_add_u32 v5, v5, v14\n"
      "s_add_u32 s48, s82, 0\n"
      "s_addc_u32 s49, s83, 0\n"
      "global_load_dwordx4 v[64:67], v2, s[48:49] \n"
      "s_add_u32 s48, s82, 32768\n"
      "s_addc_u32 s49, s83, 0\n"
      "global_load_dwordx4 v[68:71], v2, s[48:49] \n"
      "s_add_u32 s48, s82, 65536\n"
      "s_addc_u32 s49, s83, 0\n"
      "global_load_dwordx4 v[72:75], v2, s[48:49] \n"
      "s_add_u32 s48, s82, 98304\n"
      "s_addc_u32 s49, s83, 0\n"
      "global_load_dwordx4 v[76:79], v2, s[48:49] \n"
      "s_add_u32 s48, s82, 16384\n"
      "s_addc_u32 s49, s83, 0\n"
      "global_load_dwordx4 v[80:83], v2, s[48:49] \n"
      "s_add_u32 s48, s82, 49152\n"
      "s_addc_u32 s49, s83, 0\n"
      "global_load_dwordx4 v[84:87], v2, s[48:49] \n"
      "s_add_u32 s48, s82, 81920\n"
      "s_addc_u32 s49, s83, 0\n"
      "global_load_dwordx4 v[88:91], v2, s[48:49] \n"
      "s_add_u32 s48, s82, 114688\n"
      "s_addc_u32 s49, s83, 0\n"
      "global_load_dwordx4 v[92:95], v2, s[48:49] \n"
      "s_add_u32 s48, s82, 4096\n"
      "s_addc_u32 s49, s83, 0\n"
      "global_load_dwordx4 v[96:99], v2, s[48:49] \n"
      "s_add_u32 s48, s82, 36864\n"
      "s_addc_u32 s49, s83, 0\n"
      "global_load_dwordx4 v[100:103], v2, s[48:49] \n"
      "s_add_u32 s48, s82, 69632\n"
      "s_addc_u32 s49, s83, 0\n"
      "global_load_dwordx4 v[104:107], v2, s[48:49] \n"
      "s_add_u32 s48, s82, 102400\n"
      "s_addc_u32 s49, s83, 0\n"
      "global_load_dwordx4 v[108:111], v2, s[48:49] \n"
      "s_add_u32 s48, s82, 20480\n"
      "s_addc_u32 s49, s83, 0\n"
      "global_load_dwordx4 v[112:115], v2, s[48:49] \n"
      "s_add_u32 s48, s82, 53248\n"
      "s_addc_u32 s49, s83, 0\n"
      "global_load_dwordx4 v[116:119], v2, s[48:49] \n"
      "s_add_u32 s48, s82, 86016\n"
      "s_addc_u32 s49, s83, 0\n"
      "global_load_dwordx4 v[120:123], v2, s[48:49] \n"
      "s_add_u32 s48, s82, 118784\n"
      "s_addc_u32 s49, s83, 0\n"
      "global_load_dwordx4 v[124:127], v2, s[48:49] \n"
      "s_waitcnt vmcnt(16)\n"
      "s_waitcnt lgkmcnt(0)\n"
      "s_barrier\n"
      "s_add_u32 s58, s58, 1\n"
      "s_and_b32 s67, s52, 7\n"
      "s_lshl_b32 s67, s67, 6\n"
      "s_lshr_b32 s68, s52, 3\n"
      "s_add_u32 s67, s67, s68\n"
      "s_lshl_b32 s67, s67, 2\n"
      "s_add_u32 s67, s67, 1152\n"
      "v_mov_b32 v8, s67\n"
      "v_mov_b32 v9, s58\n"
      "s_mov_b64 exec, 1\n"
      "global_store_dword v8, v9, s[40:41] sc1\n"
      "s_mov_b64 exec, -1\n"
      "v_lshrrev_b32 v5, 5, v15\n"
      "v_and_b32 v8, 1, v5\n"
      "v_lshlrev_b32 v8, 3, v8\n"
      "v_lshrrev_b32 v14, 1, v5\n"
      "v_add_u32 v8, v8, v14\n"
      "v_lshlrev_b32 v8, 10, v8\n"
      "v_mul_u32_u24 v5, 528, v5\n"
      "v_and_b32 v14, 31, v15\n"
      "v_lshlrev_b32 v14, 4, v14\n"
      "v_add_u32 v8, v8, v14\n"
      "v_add_u32 v5, v5, v14\n"
      "v_add_u32 v6, 67584, v5\n"
      "s_waitcnt vmcnt(15)\n"
      "ds_write_b128 v5, v[64:67] offset:0\n"
      "s_waitcnt lgkmcnt(0)\n"
      "s_add_u32 s48, s82, 131072\n"
      "s_addc_u32 s49, s83, 0\n"
      "global_load_dwordx4 v[64:67], v2, s[48:49] \n"
      "s_waitcnt vmcnt(15)\n"
      "ds_write_b128 v5, v[68:71] offset:4224\n"
      "s_waitcnt lgkmcnt(0)\n"
      "s_add_u32 s48, s82, 163840\n"
      "s_addc_u32 s49, s83, 0\n"
      "global_load_dwordx4 v[68:71], v2, s[48:49] \n"
      "s_waitcnt vmcnt(15)\n"
      "ds_write_b128 v5, v[72:75] offset:8448\n"
      "s_waitcnt lgkmcnt(0)\n"
      "s_add_u32 s48, s82, 196608\n"
      "s_addc_u32 s49, s83, 0\n"
      "global_load_dwordx4 v[72:75], v2, s[48:49] \n"
      "s_waitcnt vmcnt(15)\n"
      "ds_write_b128 v5, v[76:79] offset:12672\n"
      "s_waitcnt lgkmcnt(0)\n"
      "s_add_u32 s48, s82, 229376\n"
      "s_addc_u32 s49, s83, 0\n"
      "global_load_dwordx4 v[76:79], v2, s[48:49] \n"
      "s_waitcnt vmcnt(15)\n"
      "ds_write_b128 v5, v[80:83] offset:16896\n"
      "s_waitcnt lgkmcnt(0)\n"
      "s_add_u32 s48, s82, 147456\n"
      "s_addc_u32 s49, s83, 0\n"
      "global_load_dwordx4 v[80:83], v2, s[48:49] \n"
      "s_waitcnt vmcnt(15)\n"
      "ds_write_b128 v5, v[84:87] offset:21120\n"
      "s_waitcnt lgkmcnt(0)\n"
      "s_add_u32 s48, s82, 180224\n"
      "s_addc_u32 s49, s83, 0\n"
      "global_load_dwordx4 v[84:87], v2, s[48:49] \n"
      "s_waitcnt vmcnt(15)\n"
      "ds_write_b128 v5, v[88:91] offset:25344\n"
      "s_waitcnt lgkmcnt(0)\n"
      "s_add_u32 s48, s82, 212992\n"
      "s_addc_u32 s49, s83, 0\n"
      "global_load_dwordx4 v[88:91], v2, s[48:49] \n"
      "s_waitcnt vmcnt(15)\n"
      "ds_write_b128 v5, v[92:95] offset:29568\n"
      "s_waitcnt lgkmcnt(0)\n"
      "s_add_u32 s48, s82, 245760\n"
      "s_addc_u32 s49, s83, 0\n"
      "global_load_dwordx4 v[92:95], v2, s[48:49] \n"
      "s_waitcnt vmcnt(15)\n"
      "ds_write_b128 v5, v[96:99] offset:33792\n"
      "s_waitcnt lgkmcnt(0)\n"
      "s_add_u32 s48, s82, 135168\n"
      "s_addc_u32 s49, s83, 0\n"
      "global_load_dwordx4 v[96:99], v2, s[48:49] \n"
      "s_waitcnt vmcnt(15)\n"
      "ds_write_b128 v5, v[100:103] offset:38016\n"
      "s_waitcnt lgkmcnt(0)\n"
      "s_add_u32 s48, s82, 167936\n"
      "s_addc_u32 s49, s83, 0\n"
      "global_load_dwordx4 v[100:103], v2, s[48:49] \n"
      "s_waitcnt vmcnt(15)\n"
      "ds_write_b128 v5, v[104:107] offset:42240\n"
      "s_waitcnt lgkmcnt(0)\n"
      "s_add_u32 s48, s82, 200704\n"
      "s_addc_u32 s49, s83, 0\n"
      "global_load_dwordx4 v[104:107], v2, s[48:49] \n"
      "s_waitcnt vmcnt(15)\n"
      "ds_write_b128 v5, v[108:111] offset:46464\n"
      "s_waitcnt lgkmcnt(0)\n"
      "s_add_u32 s48, s82, 233472\n"
      "s_addc_u32 s49, s83, 0\n"
      "global_load_dwordx4 v[108:111], v2, s[48:49] \n"
      "s_waitcnt vmcnt(15)\n"
      "ds_write_b128 v5, v[112:115] offset:50688\n"
      "s_waitcnt lgkmcnt(0)\n"
      "s_add_u32 s48, s82, 151552\n"
      "s_addc_u32 s49, s83, 0\n"
      "global_load_dwordx4 v[112:115], v2, s[48:49] \n"
      "s_waitcnt vmcnt(15)\n"
      "ds_write_b128 v5, v[116:119] offset:54912\n"
      "s_waitcnt lgkmcnt(0)\n"
      "s_add_u32 s48, s82, 184320\n"
      "s_addc_u32 s49, s83, 0\n"
      "global_load_dwordx4 v[116:119], v2, s[48:49] \n"
      "s_waitcnt vmcnt(15)\n"
      "ds_write_b128 v5, v[120:123] offset:59136\n"
      "s_waitcnt lgkmcnt(0)\n"
      "s_add_u32 s48, s82, 217088\n"
      "s_addc_u32 s49, s83, 0\n"
      "global_load_dwordx4 v[120:123], v2, s[48:49] \n"
      "s_waitcnt vmcnt(15)\n"
      "ds_write_b128 v5, v[124:127] offset:63360\n"
      "s_waitcnt lgkmcnt(0)\n"
      "s_add_u32 s48, s82, 249856\n"
      "s_addc_u32 s49, s83, 0\n"
      "global_load_dwordx4 v[124:127], v2, s[48:49] \n"
      "s_waitcnt vmcnt(15)\n"
      "ds_write_b128 v6, v[64:67] offset:0\n"
      "s_waitcnt lgkmcnt(0)\n"
      "s_add_u32 s48, s50, 0\n"
      "s_addc_u32 s49, s51, 0\n"
      "global_load_dwordx4 v[64:67], v3, s[48:49] \n"
      "s_waitcnt vmcnt(15)\n"
      "ds_write_b128 v6, v[68:71] offset:4224\n"
      "s_waitcnt lgkmcnt(0)\n"
      "s_add_u32 s48, s50, 32768\n"
      "s_addc_u32 s49, s51, 0\n"
      "global_load_dwordx4 v[68:71], v3, s[48:49] \n"
      "s_waitcnt vmcnt(15)\n"
      "ds_write_b128 v6, v[72:75] offset:8448\n"
      "s_waitcnt lgkmcnt(0)\n"
      "s_add_u32 s48, s50, 65536\n"
      "s_addc_u32 s49, s51, 0\n"
      "global_load_dwordx4 v[72:75], v3, s[48:49] \n"
      "s_waitcnt vmcnt(15)\n"
      "ds_write_b128 v6, v[76:79] offset:12672\n"
      "s_waitcnt lgkmcnt(0)\n"
      "s_add_u32 s48, s50, 98304\n"
      "s_addc_u32 s49, s51, 0\n"
      "global_load_dwordx4 v[76:79], v3, s[48:49] \n"
      "s_waitcnt vmcnt(15)\n"
      "ds_write_b128 v6, v[80:83] offset:16896\n"
      "s_waitcnt lgkmcnt(0)\n"
      "s_add_u32 s48, s50, 16384\n"
      "s_addc_u32 s49, s51, 0\n"
      "global_load_dwordx4 v[80:83], v3, s[48:49] \n"
      "s_waitcnt vmcnt(15)\n"
      "ds_write_b128 v6, v[84:87] offset:21120\n"
      "s_waitcnt lgkmcnt(0)\n"
      "s_add_u32 s48, s50, 49152\n"
      "s_addc_u32 s49, s51, 0\n"
      "global_load_dwordx4 v[84:87], v3, s[48:49] \n"
      "s_waitcnt vmcnt(15)\n"
      "ds_write_b128 v6, v[88:91] offset:25344\n"
      "s_waitcnt lgkmcnt(0)\n"
      "s_add_u32 s48, s50, 81920\n"
      "s_addc_u32 s49, s51, 0\n"
      "global_load_dwordx4 v[88:91], v3, s[48:49] \n"
      "s_waitcnt vmcnt(15)\n"
      "ds_write_b128 v6, v[92:95] offset:29568\n"
      "s_waitcnt lgkmcnt(0)\n"
      "s_add_u32 s48, s50, 114688\n"
      "s_addc_u32 s49, s51, 0\n"
      "global_load_dwordx4 v[92:95], v3, s[48:49] \n"
      "s_waitcnt vmcnt(15)\n"
      "ds_write_b128 v6, v[96:99] offset:33792\n"
      "s_waitcnt lgkmcnt(0)\n"
      "s_add_u32 s48, s50, 4096\n"
      "s_addc_u32 s49, s51, 0\n"
      "global_load_dwordx4 v[96:99], v3, s[48:49] \n"
      "s_waitcnt vmcnt(15)\n"
      "ds_write_b128 v6, v[100:103] offset:38016\n"
      "s_waitcnt lgkmcnt(0)\n"
      "s_add_u32 s48, s50, 36864\n"
      "s_addc_u32 s49, s51, 0\n"
      "global_load_dwordx4 v[100:103], v3, s[48:49] \n"
      "s_waitcnt vmcnt(15)\n"
      "ds_write_b128 v6, v[104:107] offset:42240\n"
      "s_waitcnt lgkmcnt(0)\n"
      "s_add_u32 s48, s50, 69632\n"
      "s_addc_u32 s49, s51, 0\n"
      "global_load_dwordx4 v[104:107], v3, s[48:49] \n"
      "s_waitcnt vmcnt(15)\n"
      "ds_write_b128 v6, v[108:111] offset:46464\n"
      "s_waitcnt lgkmcnt(0)\n"
      "s_add_u32 s48, s50, 102400\n"
      "s_addc_u32 s49, s51, 0\n"
      "global_load_dwordx4 v[108:111], v3, s[48:49] \n"
      "s_waitcnt vmcnt(15)\n"
      "ds_write_b128 v6, v[112:115] offset:50688\n"
      "s_waitcnt lgkmcnt(0)\n"
      "s_add_u32 s48, s50, 20480\n"
      "s_addc_u32 s49, s51, 0\n"
      "global_load_dwordx4 v[112:115], v3, s[48:49] \n"
      "s_waitcnt vmcnt(15)\n"
      "ds_write_b128 v6, v[116:119] offset:54912\n"
      "s_waitcnt lgkmcnt(0)\n"
      "s_add_u32 s48, s50, 53248\n"
      "s_addc_u32 s49, s51, 0\n"
      "global_load_dwordx4 v[116:119], v3, s[48:49] \n"
      "s_waitcnt vmcnt(15)\n"
      "ds_write_b128 v6, v[120:123] offset:59136\n"
      "s_waitcnt lgkmcnt(0)\n"
      "s_add_u32 s48, s50, 86016\n"
      "s_addc_u32 s49, s51, 0\n"
      "global_load_dwordx4 v[120:123], v3, s[48:49] \n"
      "s_waitcnt vmcnt(15)\n"
      "ds_write_b128 v6, v[124:127] offset:63360\n"
      "s_waitcnt lgkmcnt(0)\n"
      "s_add_u32 s48, s50, 118784\n"
      "s_addc_u32 s49, s51, 0\n"
      "global_load_dwordx4 v[124:127], v3, s[48:49] \n"
      "s_waitcnt lgkmcnt(0)\n"
      "s_barrier\n"
      "v_lshrrev_b32 v12, 4, v0\n"
      "v_lshlrev_b32 v12, 3, v12\n"
      "v_bfe_u32 v13, v0, 2, 2\n"
      "v_add_u32 v12, v12, v13\n"
      "v_mul_u32_u24 v12, 528, v12\n"
      "v_and_b32 v13, 3, v0\n"
      "v_lshlrev_b32 v13, 3, v13\n"
      "v_add_u32 v12, v12, v13\n"
      "s_mul_i32 s66, s53, 128\n"
      "v_add_u32 v12, s66, v12\n"
      "v_add_u32 v13, 67584, v12\n"
      "v_add_u32 v14, 135168, v12\n"
      "ds_read_b64_tr_b16 a[0:1], v12 offset:0\n"
      "ds_read_b64_tr_b16 a[2:3], v12 offset:2112\n"
      "ds_read_b64_tr_b16 a[4:5], v12 offset:16896\n"
      "ds_read_b64_tr_b16 a[6:7], v12 offset:19008\n"
      "ds_read_b64_tr_b16 a[8:9], v12 offset:33792\n"
      "ds_read_b64_tr_b16 a[10:11], v12 offset:35904\n"
      "ds_read_b64_tr_b16 a[12:13], v12 offset:50688\n"
      "ds_read_b64_tr_b16 a[14:15], v12 offset:52800\n"
      "ds_read_b64_tr_b16 a[16:17], v13 offset:0\n"
      "ds_read_b64_tr_b16 a[18:19], v13 offset:2112\n"
      "s_waitcnt lgkmcnt(4)\n"
      "ds_read_b64_tr_b16 a[20:21], v13 offset:16896\n"
      "ds_read_b64_tr_b16 a[22:23], v13 offset:19008\n"
      "ds_read_b64_tr_b16 a[24:25], v13 offset:33792\n"
      "ds_read_b64_tr_b16 a[26:27], v13 offset:35904\n"
      "ds_read_b64_tr_b16 a[28:29], v13 offset:50688\n"
      "ds_read_b64_tr_b16 a[30:31], v13 offset:52800\n"
      "ds_read_b64_tr_b16 a[32:33], v12 offset:32\n"
      "ds_read_b64_tr_b16 a[34:35], v12 offset:2144\n"
      "ds_read_b64_tr_b16 a[36:37], v12 offset:16928\n"
      "ds_read_b64_tr_b16 a[38:39], v12 offset:19040\n"
      "s_waitcnt lgkmcnt(4)\n"
      "ds_read_b64_tr_b16 a[40:41], v12 offset:33824\n"
      "ds_read_b64_tr_b16 a[42:43], v12 offset:35936\n"
      "ds_read_b64_tr_b16 a[44:45], v12 offset:50720\n"
      "ds_read_b64_tr_b16 a[46:47], v12 offset:52832\n"
      "ds_read_b64_tr_b16 a[48:49], v13 offset:32\n"
      "ds_read_b64_tr_b16 a[50:51], v13 offset:2144\n"
      "ds_read_b64_tr_b16 a[52:53], v13 offset:16928\n"
      "ds_read_b64_tr_b16 a[54:55], v13 offset:19040\n"
      "ds_read_b64_tr_b16 a[56:57], v13 offset:33824\n"
      "ds_read_b64_tr_b16 a[58:59], v13 offset:35936\n"
      "s_waitcnt lgkmcnt(4)\n"
      "ds_read_b64_tr_b16 a[60:61], v13 offset:50720\n"
      "ds_read_b64_tr_b16 a[62:63], v13 offset:52832\n"
      "ds_read_b64_tr_b16 a[64:65], v12 offset:64\n"
      "ds_read_b64_tr_b16 a[66:67], v12 offset:2176\n"
      "ds_read_b64_tr_b16 a[68:69], v12 offset:16960\n"
      "ds_read_b64_tr_b16 a[70:71], v12 offset:19072\n"
      "ds_read_b64_tr_b16 a[72:73], v12 offset:33856\n"
      "ds_read_b64_tr_b16 a[74:75], v12 offset:35968\n"
      "ds_read_b64_tr_b16 a[76:77], v12 offset:50752\n"
      "ds_read_b64_tr_b16 a[78:79], v12 offset:52864\n"
      "s_waitcnt lgkmcnt(4)\n"
      "ds_read_b64_tr_b16 a[80:81], v13 offset:64\n"
      "ds_read_b64_tr_b16 a[82:83], v13 offset:2176\n"
      "ds_read_b64_tr_b16 a[84:85], v13 offset:16960\n"
      "ds_read_b64_tr_b16 a[86:87], v13 offset:19072\n"
      "ds_read_b64_tr_b16 a[88:89], v13 offset:33856\n"
      "ds_read_b64_tr_b16 a[90:91], v13 offset:35968\n"
      "ds_read_b64_tr_b16 a[92:93], v13 offset:50752\n"
      "ds_read_b64_tr_b16 a[94:95], v13 offset:52864\n"
      "ds_read_b64_tr_b16 a[96:97], v12 offset:96\n"
      "ds_read_b64_tr_b16 a[98:99], v12 offset:2208\n"
      "s_waitcnt lgkmcnt(4)\n"
      "ds_read_b64_tr_b16 a[100:101], v12 offset:16992\n"
      "ds_read_b64_tr_b16 a[102:103], v12 offset:19104\n"
      "ds_read_b64_tr_b16 a[104:105], v12 offset:33888\n"
      "ds_read_b64_tr_b16 a[106:107], v12 offset:36000\n"
      "ds_read_b64_tr_b16 a[108:109], v12 offset:50784\n"
      "ds_read_b64_tr_b16 a[110:111], v12 offset:52896\n"
      "ds_read_b64_tr_b16 a[112:113], v13 offset:96\n"
      "ds_read_b64_tr_b16 a[114:115], v13 offset:2208\n"
      "ds_read_b64_tr_b16 a[116:117], v13 offset:16992\n"
      "ds_read_b64_tr_b16 a[118:119], v13 offset:19104\n"
      "s_waitcnt lgkmcnt(4)\n"
      "ds_read_b64_tr_b16 a[120:121], v13 offset:33888\n"
      "ds_read_b64_tr_b16 a[122:123], v13 offset:36000\n"
      "ds_read_b64_tr_b16 a[124:125], v13 offset:50784\n"
      "ds_read_b64_tr_b16 a[126:127], v13 offset:52896\n"
      "s_waitcnt lgkmcnt(0)\n"
      "s_barrier\n"
      "v_lshrrev_b32 v5, 5, v15\n"
      "v_and_b32 v8, 1, v5\n"
      "v_lshlrev_b32 v8, 3, v8\n"
      "v_lshrrev_b32 v14, 1, v5\n"
      "v_add_u32 v8, v8, v14\n"
      "v_lshlrev_b32 v8, 10, v8\n"
      "v_mul_u32_u24 v5, 528, v5\n"
      "v_and_b32 v14, 31, v15\n"
      "v_lshlrev_b32 v14, 4, v14\n"
      "v_add_u32 v8, v8, v14\n"
      "v_add_u32 v5, v5, v14\n"
      "v_add_u32 v6, 67584, v5\n"
      "s_waitcnt vmcnt(15)\n"
      "ds_write_b128 v5, v[64:67] offset:0\n"
      "s_waitcnt lgkmcnt(0)\n"
      "s_add_u32 s48, s50, 131072\n"
      "s_addc_u32 s49, s51, 0\n"
      "global_load_dwordx4 v[64:67], v3, s[48:49] \n"
      "s_waitcnt vmcnt(15)\n"
      "ds_write_b128 v5, v[68:71] offset:4224\n"
      "s_waitcnt lgkmcnt(0)\n"
      "s_add_u32 s48, s50, 163840\n"
      "s_addc_u32 s49, s51, 0\n"
      "global_load_dwordx4 v[68:71], v3, s[48:49] \n"
      "s_waitcnt vmcnt(15)\n"
      "ds_write_b128 v5, v[72:75] offset:8448\n"
      "s_waitcnt lgkmcnt(0)\n"
      "s_add_u32 s48, s50, 196608\n"
      "s_addc_u32 s49, s51, 0\n"
      "global_load_dwordx4 v[72:75], v3, s[48:49] \n"
      "s_waitcnt vmcnt(15)\n"
      "ds_write_b128 v5, v[76:79] offset:12672\n"
      "s_waitcnt lgkmcnt(0)\n"
      "s_add_u32 s48, s50, 229376\n"
      "s_addc_u32 s49, s51, 0\n"
      "global_load_dwordx4 v[76:79], v3, s[48:49] \n"
      "s_waitcnt vmcnt(15)\n"
      "ds_write_b128 v5, v[80:83] offset:16896\n"
      "s_waitcnt lgkmcnt(0)\n"
      "s_add_u32 s48, s50, 147456\n"
      "s_addc_u32 s49, s51, 0\n"
      "global_load_dwordx4 v[80:83], v3, s[48:49] \n"
      "s_waitcnt vmcnt(15)\n"
      "ds_write_b128 v5, v[84:87] offset:21120\n"
      "s_waitcnt lgkmcnt(0)\n"
      "s_add_u32 s48, s50, 180224\n"
      "s_addc_u32 s49, s51, 0\n"
      "global_load_dwordx4 v[84:87], v3, s[48:49] \n"
      "s_waitcnt vmcnt(15)\n"
      "ds_write_b128 v5, v[88:91] offset:25344\n"
      "s_waitcnt lgkmcnt(0)\n"
      "s_add_u32 s48, s50, 212992\n"
      "s_addc_u32 s49, s51, 0\n"
      "global_load_dwordx4 v[88:91], v3, s[48:49] \n"
      "s_waitcnt vmcnt(15)\n"
      "ds_write_b128 v5, v[92:95] offset:29568\n"
      "s_waitcnt lgkmcnt(0)\n"
      "s_add_u32 s48, s50, 245760\n"
      "s_addc_u32 s49, s51, 0\n"
      "global_load_dwordx4 v[92:95], v3, s[48:49] \n"
      "s_waitcnt vmcnt(15)\n"
      "ds_write_b128 v5, v[96:99] offset:33792\n"
      "s_waitcnt lgkmcnt(0)\n"
      "s_add_u32 s48, s50, 135168\n"
      "s_addc_u32 s49, s51, 0\n"
      "global_load_dwordx4 v[96:99], v3, s[48:49] \n"
      "s_waitcnt vmcnt(15)\n"
      "ds_write_b128 v5, v[100:103] offset:38016\n"
      "s_waitcnt lgkmcnt(0)\n"
      "s_add_u32 s48, s50, 167936\n"
      "s_addc_u32 s49, s51, 0\n"
      "global_load_dwordx4 v[100:103], v3, s[48:49] \n"
      "s_waitcnt vmcnt(15)\n"
      "ds_write_b128 v5, v[104:107] offset:42240\n"
      "s_waitcnt lgkmcnt(0)\n"
      "s_add_u32 s48, s50, 200704\n"
      "s_addc_u32 s49, s51, 0\n"
      "global_load_dwordx4 v[104:107], v3, s[48:49] \n"
      "s_waitcnt vmcnt(15)\n"
      "ds_write_b128 v5, v[108:111] offset:46464\n"
      "s_waitcnt lgkmcnt(0)\n"
      "s_add_u32 s48, s50, 233472\n"
      "s_addc_u32 s49, s51, 0\n"
      "global_load_dwordx4 v[108:111], v3, s[48:49] \n"
      "s_waitcnt vmcnt(15)\n"
      "ds_write_b128 v5, v[112:115] offset:50688\n"
      "s_waitcnt lgkmcnt(0)\n"
      "s_add_u32 s48, s50, 151552\n"
      "s_addc_u32 s49, s51, 0\n"
      "global_load_dwordx4 v[112:115], v3, s[48:49] \n"
      "s_waitcnt vmcnt(15)\n"
      "ds_write_b128 v5, v[116:119] offset:54912\n"
      "s_waitcnt lgkmcnt(0)\n"
      "s_add_u32 s48, s50, 184320\n"
      "s_addc_u32 s49, s51, 0\n"
      "global_load_dwordx4 v[116:119], v3, s[48:49] \n"
      "s_waitcnt vmcnt(15)\n"
      "ds_write_b128 v5, v[120:123] offset:59136\n"
      "s_waitcnt lgkmcnt(0)\n"
      "s_add_u32 s48, s50, 217088\n"
      "s_addc_u32 s49, s51, 0\n"
      "global_load_dwordx4 v[120:123], v3, s[48:49] \n"
      "s_waitcnt vmcnt(15)\n"
      "ds_write_b128 v5, v[124:127] offset:63360\n"
      "s_waitcnt lgkmcnt(0)\n"
      "s_add_u32 s48, s50, 249856\n"
      "s_addc_u32 s49, s51, 0\n"
      "global_load_dwordx4 v[124:127], v3, s[48:49] \n"
      "s_waitcnt vmcnt(15)\n"
      "ds_write_b128 v6, v[64:67] offset:0\n"
      "s_waitcnt vmcnt(14)\n"
      "ds_write_b128 v6, v[68:71] offset:4224\n"
      "s_waitcnt vmcnt(13)\n"
      "ds_write_b128 v6, v[72:75] offset:8448\n"
      "s_waitcnt vmcnt(12)\n"
      "ds_write_b128 v6, v[76:79] offset:12672\n"
      "s_waitcnt vmcnt(11)\n"
      "ds_write_b128 v6, v[80:83] offset:16896\n"
      "s_waitcnt vmcnt(10)\n"
      "ds_write_b128 v6, v[84:87] offset:21120\n"
      "s_waitcnt vmcnt(9)\n"
      "ds_write_b128 v6, v[88:91] offset:25344\n"
      "s_waitcnt vmcnt(8)\n"
      "ds_write_b128 v6, v[92:95] offset:29568\n"
      "s_waitcnt vmcnt(7)\n"
      "ds_write_b128 v6, v[96:99] offset:33792\n"
      "s_waitcnt vmcnt(6)\n"
      "ds_write_b128 v6, v[100:103] offset:38016\n"
      "s_waitcnt vmcnt(5)\n"
      "ds_write_b128 v6, v[104:107] offset:42240\n"
      "s_waitcnt vmcnt(4)\n"
      "ds_write_b128 v6, v[108:111] offset:46464\n"
      "s_waitcnt vmcnt(3)\n"
      "ds_write_b128 v6, v[112:115] offset:50688\n"
      "s_waitcnt vmcnt(2)\n"
      "ds_write_b128 v6, v[116:119] offset:54912\n"
      "s_waitcnt vmcnt(1)\n"
      "ds_write_b128 v6, v[120:123] offset:59136\n"
      "s_waitcnt vmcnt(0)\n"
      "ds_write_b128 v6, v[124:127] offset:63360\n"
      "s_waitcnt lgkmcnt(0)\n"
      "s_barrier\n"
      "v_lshrrev_b32 v12, 4, v0\n"
      "v_lshlrev_b32 v12, 3, v12\n"
      "v_bfe_u32 v13, v0, 2, 2\n"
      "v_add_u32 v12, v12, v13\n"
      "v_mul_u32_u24 v12, 528, v12\n"
      "v_and_b32 v13, 3, v0\n"
      "v_lshlrev_b32 v13, 3, v13\n"
      "v_add_u32 v12, v12, v13\n"
      "s_mul_i32 s66, s53, 128\n"
      "v_add_u32 v12, s66, v12\n"
      "v_add_u32 v13, 67584, v12\n"
      "v_add_u32 v14, 135168, v12\n"
      "ds_read_b64_tr_b16 a[128:129], v12 offset:0\n"
      "ds_read_b64_tr_b16 a[130:131], v12 offset:2112\n"
      "ds_read_b64_tr_b16 a[132:133], v12 offset:16896\n"
      "ds_read_b64_tr_b16 a[134:135], v12 offset:19008\n"
      "ds_read_b64_tr_b16 a[136:137], v12 offset:33792\n"
      "ds_read_b64_tr_b16 a[138:139], v12 offset:35904\n"
      "ds_read_b64_tr_b16 a[140:141], v12 offset:50688\n"
      "ds_read_b64_tr_b16 a[142:143], v12 offset:52800\n"
      "ds_read_b64_tr_b16 a[144:145], v13 offset:0\n"
      "ds_read_b64_tr_b16 a[146:147], v13 offset:2112\n"
      "s_waitcnt lgkmcnt(4)\n"
      "ds_read_b64_tr_b16 a[148:149], v13 offset:16896\n"
      "ds_read_b64_tr_b16 a[150:151], v13 offset:19008\n"
      "ds_read_b64_tr_b16 a[152:153], v13 offset:33792\n"
      "ds_read_b64_tr_b16 a[154:155], v13 offset:35904\n"
      "ds_read_b64_tr_b16 a[156:157], v13 offset:50688\n"
      "ds_read_b64_tr_b16 a[158:159], v13 offset:52800\n"
      "ds_read_b64_tr_b16 a[160:161], v12 offset:32\n"
      "ds_read_b64_tr_b16 a[162:163], v12 offset:2144\n"
      "ds_read_b64_tr_b16 a[164:165], v12 offset:16928\n"
      "ds_read_b64_tr_b16 a[166:167], v12 offset:19040\n"
      "s_waitcnt lgkmcnt(4)\n"
      "ds_read_b64_tr_b16 a[168:169], v12 offset:33824\n"
      "ds_read_b64_tr_b16 a[170:171], v12 offset:35936\n"
      "ds_read_b64_tr_b16 a[172:173], v12 offset:50720\n"
      "ds_read_b64_tr_b16 a[174:175], v12 offset:52832\n"
      "ds_read_b64_tr_b16 a[176:177], v13 offset:32\n"
      "ds_read_b64_tr_b16 a[178:179], v13 offset:2144\n"
      "ds_read_b64_tr_b16 a[180:181], v13 offset:16928\n"
      "ds_read_b64_tr_b16 a[182:183], v13 offset:19040\n"
      "ds_read_b64_tr_b16 a[184:185], v13 offset:33824\n"
      "ds_read_b64_tr_b16 a[186:187], v13 offset:35936\n"
      "s_waitcnt lgkmcnt(4)\n"
      "ds_read_b64_tr_b16 a[188:189], v13 offset:50720\n"
      "ds_read_b64_tr_b16 a[190:191], v13 offset:52832\n"
      "ds_read_b64_tr_b16 a[192:193], v12 offset:64\n"
      "ds_read_b64_tr_b16 a[194:195], v12 offset:2176\n"
      "ds_read_b64_tr_b16 a[196:197], v12 offset:16960\n"
      "ds_read_b64_tr_b16 a[198:199], v12 offset:19072\n"
      "ds_read_b64_tr_b16 a[200:201], v12 offset:33856\n"
      "ds_read_b64_tr_b16 a[202:203], v12 offset:35968\n"
      "ds_read_b64_tr_b16 a[204:205], v12 offset:50752\n"
      "ds_read_b64_tr_b16 a[206:207], v12 offset:52864\n"
      "s_waitcnt lgkmcnt(4)\n"
      "ds_read_b64_tr_b16 a[208:209], v13 offset:64\n"
      "ds_read_b64_tr_b16 a[210:211], v13 offset:2176\n"
      "ds_read_b64_tr_b16 a[212:213], v13 offset:16960\n"
      "ds_read_b64_tr_b16 a[214:215], v13 offset:19072\n"
      "ds_read_b64_tr_b16 a[216:217], v13 offset:33856\n"
      "ds_read_b64_tr_b16 a[218:219], v13 offset:35968\n"
      "ds_read_b64_tr_b16 a[220:221], v13 offset:50752\n"
      "ds_read_b64_tr_b16 a[222:223], v13 offset:52864\n"
      "ds_read_b64_tr_b16 a[224:225], v12 offset:96\n"
      "ds_read_b64_tr_b16 a[226:227], v12 offset:2208\n"
      "s_waitcnt lgkmcnt(4)\n"
      "ds_read_b64_tr_b16 a[228:229], v12 offset:16992\n"
      "ds_read_b64_tr_b16 a[230:231], v12 offset:19104\n"
      "ds_read_b64_tr_b16 a[232:233], v12 offset:33888\n"
      "ds_read_b64_tr_b16 a[234:235], v12 offset:36000\n"
      "ds_read_b64_tr_b16 a[236:237], v12 offset:50784\n"
      "ds_read_b64_tr_b16 a[238:239], v12 offset:52896\n"
      "ds_read_b64_tr_b16 a[240:241], v13 offset:96\n"
      "ds_read_b64_tr_b16 a[242:243], v13 offset:2208\n"
      "ds_read_b64_tr_b16 a[244:245], v13 offset:16992\n"
      "ds_read_b64_tr_b16 a[246:247], v13 offset:19104\n"
      "s_waitcnt lgkmcnt(4)\n"
      "ds_read_b64_tr_b16 a[248:249], v13 offset:33888\n"
      "ds_read_b64_tr_b16 a[250:251], v13 offset:36000\n"
      "ds_read_b64_tr_b16 a[252:253], v13 offset:50784\n"
      "ds_read_b64_tr_b16 a[254:255], v13 offset:52896\n"
      "s_waitcnt lgkmcnt(0)\n"
      "s_barrier\n"
      "s_branch C_wdone9_%=\n"
      "C_wr11_%=:\n"
      "s_cmp_eq_u32 s54, 2\n"
      "s_cbranch_scc0 C_wr12_%=\n"
      "v_lshrrev_b32 v2, 4, v15\n"
      "v_mul_u32_u24 v5, 272, v2\n"
      "v_mul_u32_u24 v2, 8192, v2\n"
      "v_and_b32 v14, 15, v15\n"
      "v_lshlrev_b32 v14, 4, v14\n"
      "v_add_u32 v2, v2, v14\n"
      "v_add_u32 v5, v5, v14\n"
      "v_lshrrev_b32 v3, 4, v15\n"
      "v_mul_u32_u24 v5, 272, v3\n"
      "v_mul_u32_u24 v3, 3584, v3\n"
      "v_and_b32 v14, 15, v15\n"
      "v_lshlrev_b32 v14, 4, v14\n"
      "v_add_u32 v3, v3, v14\n"
      "v_add_u32 v5, v5, v14\n"
      "v_lshrrev_b32 v5, 6, v15\n"
      "v_lshlrev_b32 v4, 10, v5\n"
      "v_add_u32 v4, v4, v1\n"
      "v_mul_u32_u24 v5, 1040, v5\n"
      "v_add_u32 v5, v5, v1\n"
      "s_add_u32 s48, s80, 0\n"
      "s_addc_u32 s49, s81, 0\n"
      "global_load_dwordx4 v[64:67], v2, s[48:49] \n"
      "s_add_u32 s48, s80, 131072\n"
      "s_addc_u32 s49, s81, 0\n"
      "global_load_dwordx4 v[68:71], v2, s[48:49] \n"
      "s_add_u32 s48, s80, 262144\n"
      "s_addc_u32 s49, s81, 0\n"
      "global_load_dwordx4 v[72:75], v2, s[48:49] \n"
      "s_add_u32 s48, s80, 393216\n"
      "s_addc_u32 s49, s81, 0\n"
      "global_load_dwordx4 v[76:79], v2, s[48:49] \n"
      "s_add_u32 s48, s80, 524288\n"
      "s_addc_u32 s49, s81, 0\n"
      "global_load_dwordx4 v[80:83], v2, s[48:49] \n"
      "s_add_u32 s48, s80, 655360\n"
      "s_addc_u32 s49, s81, 0\n"
      "global_load_dwordx4 v[84:87], v2, s[48:49] \n"
      "s_add_u32 s48, s80, 786432\n"
      "s_addc_u32 s49, s81, 0\n"
      "global_load_dwordx4 v[88:91], v2, s[48:49] \n"
      "s_add_u32 s48, s80, 917504\n"
      "s_addc_u32 s49, s81, 0\n"
      "global_load_dwordx4 v[92:95], v2, s[48:49] \n"
      "s_add_u32 s48, s80, 1048576\n"
      "s_addc_u32 s49, s81, 0\n"
      "global_load_dwordx4 v[96:99], v2, s[48:49] \n"
      "s_add_u32 s48, s80, 1179648\n"
      "s_addc_u32 s49, s81, 0\n"
      "global_load_dwordx4 v[100:103], v2, s[48:49] \n"
      "s_add_u32 s48, s80, 1310720\n"
      "s_addc_u32 s49, s81, 0\n"
      "global_load_dwordx4 v[104:107], v2, s[48:49] \n"
      "s_add_u32 s48, s80, 1441792\n"
      "s_addc_u32 s49, s81, 0\n"
      "global_load_dwordx4 v[108:111], v2, s[48:49] \n"
      "s_add_u32 s48, s80, 1572864\n"
      "s_addc_u32 s49, s81, 0\n"
      "global_load_dwordx4 v[112:115], v2, s[48:49] \n"
      "s_add_u32 s48, s80, 1703936\n"
      "s_addc_u32 s49, s81, 0\n"
      "global_load_dwordx4 v[116:119], v2, s[48:49] \n"
      "s_add_u32 s48, s80, 1835008\n"
      "s_addc_u32 s49, s81, 0\n"
      "global_load_dwordx4 v[120:123], v2, s[48:49] \n"
      "s_add_u32 s48, s80, 1966080\n"
      "s_addc_u32 s49, s81, 0\n"
      "global_load_dwordx4 v[124:127], v2, s[48:49] \n"
      "s_waitcnt vmcnt(16)\n"
      "s_waitcnt lgkmcnt(0)\n"
      "s_barrier\n"
      "s_add_u32 s58, s58, 1\n"
      "s_and_b32 s67, s52, 7\n"
      "s_lshl_b32 s67, s67, 6\n"
      "s_lshr_b32 s68, s52, 3\n"
      "s_add_u32 s67, s67, s68\n"
      "s_lshl_b32 s67, s67, 2\n"
      "s_add_u32 s67, s67, 1152\n"
      "v_mov_b32 v8, s67\n"
      "v_mov_b32 v9, s58\n"
      "s_mov_b64 exec, 1\n"
      "global_store_dword v8, v9, s[40:41] sc1\n"
      "s_mov_b64 exec, -1\n"
      "v_lshlrev_b32 v32, 16, v16\n"
      "v_and_b32 v33, 0xffff0000, v16\n"
      "v_lshlrev_b32 v34, 16, v17\n"
      "v_and_b32 v35, 0xffff0000, v17\n"
      "v_lshlrev_b32 v36, 16, v18\n"
      "v_and_b32 v37, 0xffff0000, v18\n"
      "v_lshlrev_b32 v38, 16, v19\n"
      "v_and_b32 v39, 0xffff0000, v19\n"
      "v_readfirstlane_b32 s66, v32\n"
      "s_nop 0\n"
      "s_mov_b32 s67, s66\n"
      "v_pk_add_f32 v[40:41], v[32:33], s[66:67] neg_lo:[0,1] neg_hi:[0,1]\n"
      "v_pk_add_f32 v[42:43], v[34:35], s[66:67] neg_lo:[0,1] neg_hi:[0,1]\n"
      "v_pk_add_f32 v[44:45], v[36:37], s[66:67] neg_lo:[0,1] neg_hi:[0,1]\n"
      "v_pk_add_f32 v[46:47], v[38:39], s[66:67] neg_lo:[0,1] neg_hi:[0,1]\n"
      "v_pk_mul_f32 v[56:57], v[40:41], v[40:41]\n"
      "v_pk_add_f32 v[48:49], v[40:41], v[42:43]\n"
      "v_pk_fma_f32 v[56:57], v[42:43], v[42:43], v[56:57]\n"
      "v_pk_add_f32 v[50:51], v[44:45], v[46:47]\n"
      "v_pk_fma_f32 v[56:57], v[44:45], v[44:45], v[56:57]\n"
      "v_pk_add_f32 v[48:49], v[48:49], v[50:51]\n"
      "v_pk_fma_f32 v[56:57], v[46:47], v[46:47], v[56:57]\n"
      "v_add_f32 v48, v48, v49\n"
      "v_add_f32 v49, v56, v57\n"
      "s_nop 0\n"
      "v_add_f32_dpp v48, v48, v48 quad_perm:[1,0,3,2] row_mask:0xf bank_mask:0xf\n"
      "v_add_f32_dpp v49, v49, v49 quad_perm:[1,0,3,2] row_mask:0xf bank_mask:0xf\n"
      "s_nop 0\n"
      "v_add_f32_dpp v48, v48, v48 quad_perm:[2,3,0,1] row_mask:0xf bank_mask:0xf\n"
      "v_add_f32_dpp v49, v49, v49 quad_perm:[2,3,0,1] row_mask:0xf bank_mask:0xf\n"
      "s_nop 0\n"
      "v_add_f32_dpp v48, v48, v48 row_half_mirror row_mask:0xf bank_mask:0xf\n"
      "v_add_f32_dpp v49, v49, v49 row_half_mirror row_mask:0xf bank_mask:0xf\n"
      "s_nop 0\n"
      "v_add_f32_dpp v48, v48, v48 row_ror:8 row_mask:0xf bank_mask:0xf\n"
      "v_add_f32_dpp v49, v49, v49 row_ror:8 row_mask:0xf bank_mask:0xf\n"
      "v_mov_b32 v12, v48\n"
      "v_mov_b32 v13, v49\n"
      "s_nop 0\n"
      "v_permlane16_swap_b32 v12, v48\n"
      "v_permlane16_swap_b32 v13, v49\n"
      "v_add_f32 v48, v12, v48\n"
      "v_add_f32 v49, v13, v49\n"
      "v_mov_b32 v12, v48\n"
      "v_mov_b32 v13, v49\n"
      "s_nop 0\n"
      "v_permlane32_swap_b32 v12, v48\n"
      "v_permlane32_swap_b32 v13, v49\n"
      "v_add_f32 v48, v12, v48\n"
      "v_add_f32 v49, v13, v49\n"
      "v_mul_f32 v48, 0x3b000000, v48\n"
      "v_mul_f32 v49, 0x3b000000, v49\n"
      "v_fma_f32 v49, -v48, v48, v49\n"
      "v_add_f32 v49, 0x3727c5ac, v49\n"
      "v_rsq_f32 v49, v49\n"
      "s_nop 0\n"
      "v_mul_f32 v48, v48, v49\n"
      "v_pk_fma_f32 v[40:41], v[40:41], v[48:49], v[48:49] op_sel:[0,1,0] op_sel_hi:[1,1,0] neg_lo:[0,0,1] neg_hi:[0,0,1]\n"
      "v_pk_fma_f32 v[42:43], v[42:43], v[48:49], v[48:49] op_sel:[0,1,0] op_sel_hi:[1,1,0] neg_lo:[0,0,1] neg_hi:[0,0,1]\n"
      "v_pk_fma_f32 v[44:45], v[44:45], v[48:49], v[48:49] op_sel:[0,1,0] op_sel_hi:[1,1,0] neg_lo:[0,0,1] neg_hi:[0,0,1]\n"
      "v_pk_fma_f32 v[46:47], v[46:47], v[48:49], v[48:49] op_sel:[0,1,0] op_sel_hi:[1,1,0] neg_lo:[0,0,1] neg_hi:[0,0,1]\n"
      "v_cvt_pk_bf16_f32 v52, v40, v41\n"
      "v_cvt_pk_bf16_f32 v53, v42, v43\n"
      "v_cvt_pk_bf16_f32 v54, v44, v45\n"
      "v_cvt_pk_bf16_f32 v55, v46, v47\n"
      "s_mul_i32 s65, s53, 2080\n"
      "s_add_u32 s65, s65, 143360\n"
      "v_add_u32 v12, s65, v1\n"
      "ds_write_b128 v12, v[52:55]\n"
      "v_lshlrev_b32 v32, 16, v20\n"
      "v_and_b32 v33, 0xffff0000, v20\n"
      "v_lshlrev_b32 v34, 16, v21\n"
      "v_and_b32 v35, 0xffff0000, v21\n"
      "v_lshlrev_b32 v36, 16, v22\n"
      "v_and_b32 v37, 0xffff0000, v22\n"
      "v_lshlrev_b32 v38, 16, v23\n"
      "v_and_b32 v39, 0xffff0000, v23\n"
      "v_readfirstlane_b32 s66, v32\n"
      "s_nop 0\n"
      "s_mov_b32 s67, s66\n"
      "v_pk_add_f32 v[40:41], v[32:33], s[66:67] neg_lo:[0,1] neg_hi:[0,1]\n"
      "v_pk_add_f32 v[42:43], v[34:35], s[66:67] neg_lo:[0,1] neg_hi:[0,1]\n"
      "v_pk_add_f32 v[44:45], v[36:37], s[66:67] neg_lo:[0,1] neg_hi:[0,1]\n"
      "v_pk_add_f32 v[46:47], v[38:39], s[66:67] neg_lo:[0,1] neg_hi:[0,1]\n"
      "v_pk_mul_f32 v[56:57], v[40:41], v[40:41]\n"
      "v_pk_add_f32 v[48:49], v[40:41], v[42:43]\n"
      "v_pk_fma_f32 v[56:57], v[42:43], v[42:43], v[56:57]\n"
      "v_pk_add_f32 v[50:51], v[44:45], v[46:47]\n"
      "v_pk_fma_f32 v[56:57], v[44:45], v[44:45], v[56:57]\n"
      "v_pk_add_f32 v[48:49], v[48:49], v[50:51]\n"
      "v_pk_fma_f32 v[56:57], v[46:47], v[46:47], v[56:57]\n"
      "v_add_f32 v48, v48, v49\n"
      "v_add_f32 v49, v56, v57\n"
      "s_nop 0\n"
      "v_add_f32_dpp v48, v48, v48 quad_perm:[1,0,3,2] row_mask:0xf bank_mask:0xf\n"
      "v_add_f32_dpp v49, v49, v49 quad_perm:[1,0,3,2] row_mask:0xf bank_mask:0xf\n"
      "s_nop 0\n"
      "v_add_f32_dpp v48, v48, v48 quad_perm:[2,3,0,1] row_mask:0xf bank_mask:0xf\n"
      "v_add_f32_dpp v49, v49, v49 quad_perm:[2,3,0,1] row_mask:0xf bank_mask:0xf\n"
      "s_nop 0\n"
      "v_add_f32_dpp v48, v48, v48 row_half_mirror row_mask:0xf bank_mask:0xf\n"
      "v_add_f32_dpp v49, v49, v49 row_half_mirror row_mask:0xf bank_mask:0xf\n"
      "s_nop 0\n"
      "v_add_f32_dpp v48, v48, v48 row_ror:8 row_mask:0xf bank_mask:0xf\n"
      "v_add_f32_dpp v49, v49, v49 row_ror:8 row_mask:0xf bank_mask:0xf\n"
      "v_mov_b32 v12, v48\n"
      "v_mov_b32 v13, v49\n"
      "s_nop 0\n"
      "v_permlane16_swap_b32 v12, v48\n"
      "v_permlane16_swap_b32 v13, v49\n"
      "v_add_f32 v48, v12, v48\n"
      "v_add_f32 v49, v13, v49\n"
      "v_mov_b32 v12, v48\n"
      "v_mov_b32 v13, v49\n"
      "s_nop 0\n"
      "v_permlane32_swap_b32 v12, v48\n"
      "v_permlane32_swap_b32 v13, v49\n"
      "v_add_f32 v48, v12, v48\n"
      "v_add_f32 v49, v13, v49\n"
      "v_mul_f32 v48, 0x3b000000, v48\n"
      "v_mul_f32 v49, 0x3b000000, v49\n"
      "v_fma_f32 v49, -v48, v48, v49\n"
      "v_add_f32 v49, 0x3727c5ac, v49\n"
      "v_rsq_f32 v49, v49\n"
      "s_nop 0\n"
      "v_mul_f32 v48, v48, v49\n"
      "v_pk_fma_f32 v[40:41], v[40:41], v[48:49], v[48:49] op_sel:[0,1,0] op_sel_hi:[1,1,0] neg_lo:[0,0,1] neg_hi:[0,0,1]\n"
      "v_pk_fma_f32 v[42:43], v[42:43], v[48:49], v[48:49] op_sel:[0,1,0] op_sel_hi:[1,1,0] neg_lo:[0,0,1] neg_hi:[0,0,1]\n"
      "v_pk_fma_f32 v[44:45], v[44:45], v[48:49], v[48:49] op_sel:[0,1,0] op_sel_hi:[1,1,0] neg_lo:[0,0,1] neg_hi:[0,0,1]\n"
      "v_pk_fma_f32 v[46:47], v[46:47], v[48:49], v[48:49] op_sel:[0,1,0] op_sel_hi:[1,1,0] neg_lo:[0,0,1] neg_hi:[0,0,1]\n"
      "v_cvt_pk_bf16_f32 v52, v40, v41\n"
      "v_cvt_pk_bf16_f32 v53, v42, v43\n"
      "v_cvt_pk_bf16_f32 v54, v44, v45\n"
      "v_cvt_pk_bf16_f32 v55, v46, v47\n"
      "s_mul_i32 s65, s53, 2080\n"
      "s_add_u32 s65, s65, 144400\n"
      "v_add_u32 v12, s65, v1\n"
      "ds_write_b128 v12, v[52:55]\n"
      "v_lshrrev_b32 v8, 4, v15\n"
      "v_mul_u32_u24 v5, 272, v8\n"
      "v_mul_u32_u24 v8, 8192, v8\n"
      "v_and_b32 v14, 15, v15\n"
      "v_lshlrev_b32 v14, 4, v14\n"
      "v_add_u32 v8, v8, v14\n"
      "v_add_u32 v5, v5, v14\n"
      "v_add_u32 v6, 69632, v5\n"
      "s_waitcnt vmcnt(15)\n"
      "ds_write_b128 v5, v[64:67] offset:0\n"
      "s_waitcnt lgkmcnt(0)\n"
      "s_add_u32 s48, s80, 2097152\n"
      "s_addc_u32 s49, s81, 0\n"
      "global_load_dwordx4 v[64:67], v2, s[48:49] \n"
      "s_waitcnt vmcnt(15)\n"
      "ds_write_b128 v5, v[68:71] offset:4352\n"
      "s_waitcnt lgkmcnt(0)\n"
      "s_add_u32 s48, s80, 2228224\n"
      "s_addc_u32 s49, s81, 0\n"
      "global_load_dwordx4 v[68:71], v2, s[48:49] \n"
      "s_waitcnt vmcnt(15)\n"
      "ds_write_b128 v5, v[72:75] offset:8704\n"
      "s_waitcnt lgkmcnt(0)\n"
      "s_add_u32 s48, s80, 2359296\n"
      "s_addc_u32 s49, s81, 0\n"
      "global_load_dwordx4 v[72:75], v2, s[48:49] \n"
      "s_waitcnt vmcnt(15)\n"
      "ds_write_b128 v5, v[76:79] offset:13056\n"
      "s_waitcnt lgkmcnt(0)\n"
      "s_add_u32 s48, s80, 2490368\n"
      "s_addc_u32 s49, s81, 0\n"
      "global_load_dwordx4 v[76:79], v2, s[48:49] \n"
      "s_waitcnt vmcnt(15)\n"
      "ds_write_b128 v5, v[80:83] offset:17408\n"
      "s_waitcnt lgkmcnt(0)\n"
      "s_add_u32 s48, s80, 2621440\n"
      "s_addc_u32 s49, s81, 0\n"
      "global_load_dwordx4 v[80:83], v2, s[48:49] \n"
      "s_waitcnt vmcnt(15)\n"
      "ds_write_b128 v5, v[84:87] offset:21760\n"
      "s_waitcnt lgkmcnt(0)\n"
      "s_add_u32 s48, s80, 2752512\n"
      "s_addc_u32 s49, s81, 0\n"
      "global_load_dwordx4 v[84:87], v2, s[48:49] \n"
      "s_waitcnt vmcnt(15)\n"
      "ds_write_b128 v5, v[88:91] offset:26112\n"
      "s_waitcnt lgkmcnt(0)\n"
      "s_add_u32 s48, s80, 2883584\n"
      "s_addc_u32 s49, s81, 0\n"
      "global_load_dwordx4 v[88:91], v2, s[48:49] \n"
      "s_waitcnt vmcnt(15)\n"
      "ds_write_b128 v5, v[92:95] offset:30464\n"
      "s_waitcnt lgkmcnt(0)\n"
      "s_add_u32 s48, s80, 3014656\n"
      "s_addc_u32 s49, s81, 0\n"
      "global_load_dwordx4 v[92:95], v2, s[48:49] \n"
      "s_waitcnt vmcnt(15)\n"
      "ds_write_b128 v5, v[96:99] offset:34816\n"
      "s_waitcnt lgkmcnt(0)\n"
      "s_add_u32 s48, s80, 3145728\n"
      "s_addc_u32 s49, s81, 0\n"
      "global_load_dwordx4 v[96:99], v2, s[48:49] \n"
      "s_waitcnt vmcnt(15)\n"
      "ds_write_b128 v5, v[100:103] offset:39168\n"
      "s_waitcnt lgkmcnt(0)\n"
      "s_add_u32 s48, s80, 3276800\n"
      "s_addc_u32 s49, s81, 0\n"
      "global_load_dwordx4 v[100:103], v2, s[48:49] \n"
      "s_waitcnt vmcnt(15)\n"
      "ds_write_b128 v5, v[104:107] offset:43520\n"
      "s_waitcnt lgkmcnt(0)\n"
      "s_add_u32 s48, s80, 3407872\n"
      "s_addc_u32 s49, s81, 0\n"
      "global_load_dwordx4 v[104:107], v2, s[48:49] \n"
      "s_waitcnt vmcnt(15)\n"
      "ds_write_b128 v5, v[108:111] offset:47872\n"
      "s_waitcnt lgkmcnt(0)\n"
      "s_add_u32 s48, s80, 3538944\n"
      "s_addc_u32 s49, s81, 0\n"
      "global_load_dwordx4 v[108:111], v2, s[48:49] \n"
      "s_waitcnt vmcnt(15)\n"
      "ds_write_b128 v5, v[112:115] offset:52224\n"
      "s_waitcnt lgkmcnt(0)\n"
      "s_add_u32 s48, s80, 3670016\n"
      "s_addc_u32 s49, s81, 0\n"
      "global_load_dwordx4 v[112:115], v2, s[48:49] \n"
      "s_waitcnt vmcnt(15)\n"
      "ds_write_b128 v5, v[116:119] offset:56576\n"
      "s_waitcnt lgkmcnt(0)\n"
      "s_add_u32 s48, s80, 3801088\n"
      "s_addc_u32 s49, s81, 0\n"
      "global_load_dwordx4 v[116:119], v2, s[48:49] \n"
      "s_waitcnt vmcnt(15)\n"
      "ds_write_b128 v5, v[120:123] offset:60928\n"
      "s_waitcnt lgkmcnt(0)\n"
      "s_add_u32 s48, s80, 3932160\n"
      "s_addc_u32 s49, s81, 0\n"
      "global_load_dwordx4 v[120:123], v2, s[48:49] \n"
      "s_waitcnt vmcnt(15)\n"
      "ds_write_b128 v5, v[124:127] offset:65280\n"
      "s_waitcnt lgkmcnt(0)\n"
      "s_add_u32 s48, s80, 4063232\n"
      "s_addc_u32 s49, s81, 0\n"
      "global_load_dwordx4 v[124:127], v2, s[48:49] \n"
      "s_waitcnt vmcnt(15)\n"
      "ds_write_b128 v6, v[64:67] offset:0\n"
      "s_waitcnt lgkmcnt(0)\n"
      "s_add_u32 s48, s84, 0\n"
      "s_addc_u32 s49, s85, 0\n"
      "global_load_dwordx4 v[64:67], v3, s[48:49] \n"
      "s_waitcnt vmcnt(15)\n"
      "ds_write_b128 v6, v[68:71] offset:4352\n"
      "s_waitcnt lgkmcnt(0)\n"
      "s_add_u32 s48, s84, 57344\n"
      "s_addc_u32 s49, s85, 0\n"
      "global_load_dwordx4 v[68:71], v3, s[48:49] \n"
      "s_waitcnt vmcnt(15)\n"
      "ds_write_b128 v6, v[72:75] offset:8704\n"
      "s_waitcnt lgkmcnt(0)\n"
      "s_add_u32 s48, s84, 114688\n"
      "s_addc_u32 s49, s85, 0\n"
      "global_load_dwordx4 v[72:75], v3, s[48:49] \n"
      "s_waitcnt vmcnt(15)\n"
      "ds_write_b128 v6, v[76:79] offset:13056\n"
      "s_waitcnt lgkmcnt(0)\n"
      "s_add_u32 s48, s84, 172032\n"
      "s_addc_u32 s49, s85, 0\n"
      "global_load_dwordx4 v[76:79], v3, s[48:49] \n"
      "s_waitcnt vmcnt(15)\n"
      "ds_write_b128 v6, v[80:83] offset:17408\n"
      "s_waitcnt lgkmcnt(0)\n"
      "s_add_u32 s48, s84, 229376\n"
      "s_addc_u32 s49, s85, 0\n"
      "global_load_dwordx4 v[80:83], v3, s[48:49] \n"
      "s_waitcnt vmcnt(15)\n"
      "ds_write_b128 v6, v[84:87] offset:21760\n"
      "s_waitcnt lgkmcnt(0)\n"
      "s_add_u32 s48, s84, 286720\n"
      "s_addc_u32 s49, s85, 0\n"
      "global_load_dwordx4 v[84:87], v3, s[48:49] \n"
      "s_waitcnt vmcnt(15)\n"
      "ds_write_b128 v6, v[88:91] offset:26112\n"
      "s_waitcnt lgkmcnt(0)\n"
      "s_add_u32 s48, s84, 344064\n"
      "s_addc_u32 s49, s85, 0\n"
      "global_load_dwordx4 v[88:91], v3, s[48:49] \n"
      "s_waitcnt vmcnt(15)\n"
      "ds_write_b128 v6, v[92:95] offset:30464\n"
      "s_waitcnt lgkmcnt(0)\n"
      "s_add_u32 s48, s84, 401408\n"
      "s_addc_u32 s49, s85, 0\n"
      "global_load_dwordx4 v[92:95], v3, s[48:49] \n"
      "s_waitcnt vmcnt(15)\n"
      "ds_write_b128 v6, v[96:99] offset:34816\n"
      "s_waitcnt lgkmcnt(0)\n"
      "s_add_u32 s48, s84, 458752\n"
      "s_addc_u32 s49, s85, 0\n"
      "global_load_dwordx4 v[96:99], v3, s[48:49] \n"
      "s_waitcnt vmcnt(15)\n"
      "ds_write_b128 v6, v[100:103] offset:39168\n"
      "s_waitcnt lgkmcnt(0)\n"
      "s_add_u32 s48, s84, 516096\n"
      "s_addc_u32 s49, s85, 0\n"
      "global_load_dwordx4 v[100:103], v3, s[48:49] \n"
      "s_waitcnt vmcnt(15)\n"
      "ds_write_b128 v6, v[104:107] offset:43520\n"
      "s_waitcnt lgkmcnt(0)\n"
      "s_add_u32 s48, s84, 573440\n"
      "s_addc_u32 s49, s85, 0\n"
      "global_load_dwordx4 v[104:107], v3, s[48:49] \n"
      "s_waitcnt vmcnt(15)\n"
      "ds_write_b128 v6, v[108:111] offset:47872\n"
      "s_waitcnt lgkmcnt(0)\n"
      "s_add_u32 s48, s84, 630784\n"
      "s_addc_u32 s49, s85, 0\n"
      "global_load_dwordx4 v[108:111], v3, s[48:49] \n"
      "s_waitcnt vmcnt(15)\n"
      "ds_write_b128 v6, v[112:115] offset:52224\n"
      "s_waitcnt lgkmcnt(0)\n"
      "s_add_u32 s48, s84, 688128\n"
      "s_addc_u32 s49, s85, 0\n"
      "global_load_dwordx4 v[112:115], v3, s[48:49] \n"
      "s_waitcnt vmcnt(15)\n"
      "ds_write_b128 v6, v[116:119] offset:56576\n"
      "s_waitcnt lgkmcnt(0)\n"
      "s_add_u32 s48, s84, 745472\n"
      "s_addc_u32 s49, s85, 0\n"
      "global_load_dwordx4 v[116:119], v3, s[48:49] \n"
      "s_waitcnt vmcnt(15)\n"
      "ds_write_b128 v6, v[120:123] offset:60928\n"
      "s_waitcnt lgkmcnt(0)\n"
      "s_add_u32 s48, s84, 802816\n"
      "s_addc_u32 s49, s85, 0\n"
      "global_load_dwordx4 v[120:123], v3, s[48:49] \n"
      "s_waitcnt vmcnt(15)\n"
      "ds_write_b128 v6, v[124:127] offset:65280\n"
      "s_waitcnt lgkmcnt(0)\n"
      "s_add_u32 s48, s84, 860160\n"
      "s_addc_u32 s49, s85, 0\n"
      "global_load_dwordx4 v[124:127], v3, s[48:49] \n"
      "s_waitcnt lgkmcnt(0)\n"
      "s_barrier\n"
      "v_lshrrev_b32 v12, 4, v0\n"
      "v_lshlrev_b32 v12, 3, v12\n"
      "v_bfe_u32 v13, v0, 2, 2\n"
      "v_add_u32 v12, v12, v13\n"
      "v_mul_u32_u24 v12, 272, v12\n"
      "v_and_b32 v13, 3, v0\n"
      "v_lshlrev_b32 v13, 3, v13\n"
      "v_add_u32 v12, v12, v13\n"
      "s_mul_i32 s66, s53, 64\n"
      "v_add_u32 v12, s66, v12\n"
      "v_add_u32 v13, 52224, v12\n"
      "v_add_u32 v14, 104448, v12\n"
      "ds_read_b64_tr_b16 a[128:129], v12 offset:0\n"
      "ds_read_b64_tr_b16 a[130:131], v12 offset:1088\n"
      "ds_read_b64_tr_b16 a[132:133], v12 offset:8704\n"
      "ds_read_b64_tr_b16 a[134:135], v12 offset:9792\n"
      "ds_read_b64_tr_b16 a[136:137], v12 offset:17408\n"
      "ds_read_b64_tr_b16 a[138:139], v12 offset:18496\n"
      "ds_read_b64_tr_b16 a[140:141], v12 offset:26112\n"
      "ds_read_b64_tr_b16 a[142:143], v12 offset:27200\n"
      "ds_read_b64_tr_b16 a[144:145], v12 offset:34816\n"
      "ds_read_b64_tr_b16 a[146:147], v12 offset:35904\n"
      "s_waitcnt lgkmcnt(4)\n"
      "ds_read_b64_tr_b16 a[148:149], v12 offset:43520\n"
      "ds_read_b64_tr_b16 a[150:151], v12 offset:44608\n"
      "ds_read_b64_tr_b16 a[152:153], v13 offset:0\n"
      "ds_read_b64_tr_b16 a[154:155], v13 offset:1088\n"
      "ds_read_b64_tr_b16 a[156:157], v13 offset:8704\n"
      "ds_read_b64_tr_b16 a[158:159], v13 offset:9792\n"
      "ds_read_b64_tr_b16 a[160:161], v13 offset:17408\n"
      "ds_read_b64_tr_b16 a[162:163], v13 offset:18496\n"
      "ds_read_b64_tr_b16 a[164:165], v13 offset:26112\n"
      "ds_read_b64_tr_b16 a[166:167], v13 offset:27200\n"
      "s_waitcnt lgkmcnt(4)\n"
      "ds_read_b64_tr_b16 a[168:169], v13 offset:34816\n"
      "ds_read_b64_tr_b16 a[170:171], v13 offset:35904\n"
      "ds_read_b64_tr_b16 a[172:173], v13 offset:43520\n"
      "ds_read_b64_tr_b16 a[174:175], v13 offset:44608\n"
      "ds_read_b64_tr_b16 a[176:177], v14 offset:0\n"
      "ds_read_b64_tr_b16 a[178:179], v14 offset:1088\n"
      "ds_read_b64_tr_b16 a[180:181], v14 offset:8704\n"
      "ds_read_b64_tr_b16 a[182:183], v14 offset:9792\n"
      "ds_read_b64_tr_b16 a[184:185], v14 offset:17408\n"
      "ds_read_b64_tr_b16 a[186:187], v14 offset:18496\n"
      "s_waitcnt lgkmcnt(4)\n"
      "ds_read_b64_tr_b16 a[188:189], v14 offset:26112\n"
      "ds_read_b64_tr_b16 a[190:191], v14 offset:27200\n"
      "ds_read_b64_tr_b16 a[192:193], v12 offset:32\n"
      "ds_read_b64_tr_b16 a[194:195], v12 offset:1120\n"
      "ds_read_b64_tr_b16 a[196:197], v12 offset:8736\n"
      "ds_read_b64_tr_b16 a[198:199], v12 offset:9824\n"
      "ds_read_b64_tr_b16 a[200:201], v12 offset:17440\n"
      "ds_read_b64_tr_b16 a[202:203], v12 offset:18528\n"
      "ds_read_b64_tr_b16 a[204:205], v12 offset:26144\n"
      "ds_read_b64_tr_b16 a[206:207], v12 offset:27232\n"
      "s_waitcnt lgkmcnt(4)\n"
      "ds_read_b64_tr_b16 a[208:209], v12 offset:34848\n"
      "ds_read_b64_tr_b16 a[210:211], v12 offset:35936\n"
      "ds_read_b64_tr_b16 a[212:213], v12 offset:43552\n"
      "ds_read_b64_tr_b16 a[214:215], v12 offset:44640\n"
      "ds_read_b64_tr_b16 a[216:217], v13 offset:32\n"
      "ds_read_b64_tr_b16 a[218:219], v13 offset:1120\n"
      "ds_read_b64_tr_b16 a[220:221], v13 offset:8736\n"
      "ds_read_b64_tr_b16 a[222:223], v13 offset:9824\n"
      "ds_read_b64_tr_b16 a[224:225], v13 offset:17440\n"
      "ds_read_b64_tr_b16 a[226:227], v13 offset:18528\n"
      "s_waitcnt lgkmcnt(4)\n"
      "ds_read_b64_tr_b16 a[228:229], v13 offset:26144\n"
      "ds_read_b64_tr_b16 a[230:231], v13 offset:27232\n"
      "ds_read_b64_tr_b16 a[232:233], v13 offset:34848\n"
      "ds_read_b64_tr_b16 a[234:235], v13 offset:35936\n"
      "ds_read_b64_tr_b16 a[236:237], v13 offset:43552\n"
      "ds_read_b64_tr_b16 a[238:239], v13 offset:44640\n"
      "ds_read_b64_tr_b16 a[240:241], v14 offset:32\n"
      "ds_read_b64_tr_b16 a[242:243], v14 offset:1120\n"
      "ds_read_b64_tr_b16 a[244:245], v14 offset:8736\n"
      "ds_read_b64_tr_b16 a[246:247], v14 offset:9824\n"
      "s_waitcnt lgkmcnt(4)\n"
      "ds_read_b64_tr_b16 a[248:249], v14 offset:17440\n"
      "ds_read_b64_tr_b16 a[250:251], v14 offset:18528\n"
      "ds_read_b64_tr_b16 a[252:253], v14 offset:26144\n"
      "ds_read_b64_tr_b16 a[254:255], v14 offset:27232\n"
      "s_waitcnt lgkmcnt(0)\n"
      "s_barrier\n"
      "v_and_b32 v12, 7, v0\n"
      "v_mul_u32_u24 v12, 1040, v12\n"
      "v_lshrrev_b32 v13, 4, v0\n"
      "v_lshlrev_b32 v13, 4, v13\n"
      "v_add_u32 v12, v12, v13\n"
      "v_add_u32 v12, 143360, v12\n"
      "ds_read_b128 v[24:27], v12 offset:0\n"
      "ds_read_b128 v[28:31], v12 offset:64\n"
      "ds_read_b128 v[32:35], v12 offset:128\n"
      "ds_read_b128 v[36:39], v12 offset:192\n"
      "ds_read_b128 v[40:43], v12 offset:256\n"
      "ds_read_b128 v[44:47], v12 offset:320\n"
      "ds_read_b128 v[48:51], v12 offset:384\n"
      "ds_read_b128 v[52:55], v12 offset:448\n"
      "s_waitcnt lgkmcnt(7)\n"
      "v_mfma_f32_16x16x32_bf16 v[56:59], v[24:27], a[128:131], 0\n"
      "v_mfma_f32_16x16x32_bf16 v[60:63], v[24:27], a[192:195], 0\n"
      "ds_read_b128 v[24:27], v12 offset:512\n"
      "s_waitcnt lgkmcnt(7)\n"
      "v_mfma_f32_16x16x32_bf16 v[56:59], v[28:31], a[132:135], v[56:59]\n"
      "v_mfma_f32_16x16x32_bf16 v[60:63], v[28:31], a[196:199], v[60:63]\n"
      "ds_read_b128 v[28:31], v12 offset:576\n"
      "s_waitcnt lgkmcnt(7)\n"
      "v_mfma_f32_16x16x32_bf16 v[56:59], v[32:35], a[136:139], v[56:59]\n"
      "v_mfma_f32_16x16x32_bf16 v[60:63], v[32:35], a[200:203], v[60:63]\n"
      "ds_read_b128 v[32:35], v12 offset:640\n"
      "s_waitcnt lgkmcnt(7)\n"
      "v_mfma_f32_16x16x32_bf16 v[56:59], v[36:39], a[140:143], v[56:59]\n"
      "v_mfma_f32_16x16x32_bf16 v[60:63], v[36:39], a[204:207], v[60:63]\n"
      "ds_read_b128 v[36:39], v12 offset:704\n"
      "s_waitcnt lgkmcnt(7)\n"
      "v_mfma_f32_16x16x32_bf16 v[56:59], v[40:43], a[144:147], v[56:59]\n"
      "v_mfma_f32_16x16x32_bf16 v[60:63], v[40:43], a[208:211], v[60:63]\n"
      "ds_read_b128 v[40:43], v12 offset:768\n"
      "s_waitcnt lgkmcnt(7)\n"
      "v_mfma_f32_16x16x32_bf16 v[56:59], v[44:47], a[148:151], v[56:59]\n"
      "v_mfma_f32_16x16x32_bf16 v[60:63], v[44:47], a[212:215], v[60:63]\n"
      "ds_read_b128 v[44:47], v12 offset:832\n"
      "s_waitcnt lgkmcnt(7)\n"
      "v_mfma_f32_16x16x32_bf16 v[56:59], v[48:51], a[152:155], v[56:59]\n"
      "v_mfma_f32_16x16x32_bf16 v[60:63], v[48:51], a[216:219], v[60:63]\n"
      "ds_read_b128 v[48:51], v12 offset:896\n"
      "s_waitcnt lgkmcnt(7)\n"
      "v_mfma_f32_16x16x32_bf16 v[56:59], v[52:55], a[156:159], v[56:59]\n"
      "v_mfma_f32_16x16x32_bf16 v[60:63], v[52:55], a[220:223], v[60:63]\n"
      "ds_read_b128 v[52:55], v12 offset:960\n"
      "s_waitcnt lgkmcnt(7)\n"
      "v_mfma_f32_16x16x32_bf16 v[56:59], v[24:27], a[160:163], v[56:59]\n"
      "v_mfma_f32_16x16x32_bf16 v[60:63], v[24:27], a[224:227], v[60:63]\n"
      "s_waitcnt lgkmcnt(6)\n"
      "v_mfma_f32_16x16x32_bf16 v[56:59], v[28:31], a[164:167], v[56:59]\n"
      "v_mfma_f32_16x16x32_bf16 v[60:63], v[28:31], a[228:231], v[60:63]\n"
      "s_waitcnt lgkmcnt(5)\n"
      "v_mfma_f32_16x16x32_bf16 v[56:59], v[32:35], a[168:171], v[56:59]\n"
      "v_mfma_f32_16x16x32_bf16 v[60:63], v[32:35], a[232:235], v[60:63]\n"
      "s_waitcnt lgkmcnt(4)\n"
      "v_mfma_f32_16x16x32_bf16 v[56:59], v[36:39], a[172:175], v[56:59]\n"
      "v_mfma_f32_16x16x32_bf16 v[60:63], v[36:39], a[236:239], v[60:63]\n"
      "s_waitcnt lgkmcnt(3)\n"
      "v_mfma_f32_16x16x32_bf16 v[56:59], v[40:43], a[176:179], v[56:59]\n"
      "v_mfma_f32_16x16x32_bf16 v[60:63], v[40:43], a[240:243], v[60:63]\n"
      "s_waitcnt lgkmcnt(2)\n"
      "v_mfma_f32_16x16x32_bf16 v[56:59], v[44:47], a[180:183], v[56:59]\n"
      "v_mfma_f32_16x16x32_bf16 v[60:63], v[44:47], a[244:247], v[60:63]\n"
      "s_waitcnt lgkmcnt(1)\n"
      "v_mfma_f32_16x16x32_bf16 v[56:59], v[48:51], a[184:187], v[56:59]\n"
      "v_mfma_f32_16x16x32_bf16 v[60:63], v[48:51], a[248:251], v[60:63]\n"
      "s_waitcnt lgkmcnt(0)\n"
      "v_mfma_f32_16x16x32_bf16 v[56:59], v[52:55], a[188:191], v[56:59]\n"
      "v_mfma_f32_16x16x32_bf16 v[60:63], v[52:55], a[252:255], v[60:63]\n"
      "v_lshrrev_b32 v40, 4, v0\n"
      "v_lshlrev_b32 v40, 15, v40\n"
      "v_and_b32 v41, 15, v0\n"
      "v_lshrrev_b32 v41, 1, v41\n"
      "v_lshlrev_b32 v41, 2, v41\n"
      "v_add_u32 v40, v40, v41\n"
      "s_lshl_b32 s65, s56, 8\n"
      "s_lshl_b32 s66, s53, 6\n"
      "s_add_u32 s65, s65, s66\n"
      "s_add_u32 s65, s65, 528384\n"
      "v_mul_f32 v32, 0x3e0293ee, v56\n"
      "s_nop 1\n"
      "v_mov_b32_dpp v13, v32 quad_perm:[1,0,3,2] row_mask:0xf bank_mask:0xf\n"
      "v_cvt_pk_bf16_f32 v32, v32, v13\n"
      "v_min_u32 v32, -2, v32\n"
      "s_add_u32 s66, s65, 0\n"
      "s_add_u32 s50, s40, s66\n"
      "s_addc_u32 s51, s41, 0\n"
      "s_mov_b32 exec_lo, 0x55555555\n"
      "s_mov_b32 exec_hi, 0x0\n"
      "global_store_dword v40, v32, s[50:51] sc1\n"
      "s_mov_b64 exec, -1\n"
      "v_mul_f32 v32, 0x3e0293ee, v57\n"
      "s_nop 1\n"
      "v_mov_b32_dpp v13, v32 quad_perm:[1,0,3,2] row_mask:0xf bank_mask:0xf\n"
      "v_cvt_pk_bf16_f32 v32, v32, v13\n"
      "v_min_u32 v32, -2, v32\n"
      "s_add_u32 s66, s65, 8192\n"
      "s_add_u32 s50, s40, s66\n"
      "s_addc_u32 s51, s41, 0\n"
      "s_mov_b32 exec_lo, 0x55555555\n"
      "s_mov_b32 exec_hi, 0x0\n"
      "global_store_dword v40, v32, s[50:51] sc1\n"
      "s_mov_b64 exec, -1\n"
      "v_mul_f32 v32, 0x3e0293ee, v58\n"
      "s_nop 1\n"
      "v_mov_b32_dpp v13, v32 quad_perm:[1,0,3,2] row_mask:0xf bank_mask:0xf\n"
      "v_cvt_pk_bf16_f32 v32, v32, v13\n"
      "v_min_u32 v32, -2, v32\n"
      "s_add_u32 s66, s65, 16384\n"
      "s_add_u32 s50, s40, s66\n"
      "s_addc_u32 s51, s41, 0\n"
      "s_mov_b32 exec_lo, 0x55555555\n"
      "s_mov_b32 exec_hi, 0x0\n"
      "global_store_dword v40, v32, s[50:51] sc1\n"
      "s_mov_b64 exec, -1\n"
      "v_mul_f32 v32, 0x3e0293ee, v59\n"
      "s_nop 1\n"
      "v_mov_b32_dpp v13, v32 quad_perm:[1,0,3,2] row_mask:0xf bank_mask:0xf\n"
      "v_cvt_pk_bf16_f32 v32, v32, v13\n"
      "v_min_u32 v32, -2, v32\n"
      "s_add_u32 s66, s65, 24576\n"
      "s_add_u32 s50, s40, s66\n"
      "s_addc_u32 s51, s41, 0\n"
      "s_mov_b32 exec_lo, 0x55555555\n"
      "s_mov_b32 exec_hi, 0x0\n"
      "global_store_dword v40, v32, s[50:51] sc1\n"
      "s_mov_b64 exec, -1\n"
      "v_mul_f32 v32, 0x3e0293ee, v60\n"
      "s_nop 1\n"
      "v_mov_b32_dpp v13, v32 quad_perm:[1,0,3,2] row_mask:0xf bank_mask:0xf\n"
      "v_cvt_pk_bf16_f32 v32, v32, v13\n"
      "v_min_u32 v32, -2, v32\n"
      "s_add_u32 s66, s65, 32\n"
      "s_add_u32 s50, s40, s66\n"
      "s_addc_u32 s51, s41, 0\n"
      "s_mov_b32 exec_lo, 0x55555555\n"
      "s_mov_b32 exec_hi, 0x0\n"
      "global_store_dword v40, v32, s[50:51] sc1\n"
      "s_mov_b64 exec, -1\n"
      "v_mul_f32 v32, 0x3e0293ee, v61\n"
      "s_nop 1\n"
      "v_mov_b32_dpp v13, v32 quad_perm:[1,0,3,2] row_mask:0xf bank_mask:0xf\n"
      "v_cvt_pk_bf16_f32 v32, v32, v13\n"
      "v_min_u32 v32, -2, v32\n"
      "s_add_u32 s66, s65, 8224\n"
      "s_add_u32 s50, s40, s66\n"
      "s_addc_u32 s51, s41, 0\n"
      "s_mov_b32 exec_lo, 0x55555555\n"
      "s_mov_b32 exec_hi, 0x0\n"
      "global_store_dword v40, v32, s[50:51] sc1\n"
      "s_mov_b64 exec, -1\n"
      "v_mul_f32 v32, 0x3e0293ee, v62\n"
      "s_nop 1\n"
      "v_mov_b32_dpp v13, v32 quad_perm:[1,0,3,2] row_mask:0xf bank_mask:0xf\n"
      "v_cvt_pk_bf16_f32 v32, v32, v13\n"
      "v_min_u32 v32, -2, v32\n"
      "s_add_u32 s66, s65, 16416\n"
      "s_add_u32 s50, s40, s66\n"
      "s_addc_u32 s51, s41, 0\n"
      "s_mov_b32 exec_lo, 0x55555555\n"
      "s_mov_b32 exec_hi, 0x0\n"
      "global_store_dword v40, v32, s[50:51] sc1\n"
      "s_mov_b64 exec, -1\n"
      "v_mul_f32 v32, 0x3e0293ee, v63\n"
      "s_nop 1\n"
      "v_mov_b32_dpp v13, v32 quad_perm:[1,0,3,2] row_mask:0xf bank_mask:0xf\n"
      "v_cvt_pk_bf16_f32 v32, v32, v13\n"
      "v_min_u32 v32, -2, v32\n"
      "s_add_u32 s66, s65, 24608\n"
      "s_add_u32 s50, s40, s66\n"
      "s_addc_u32 s51, s41, 0\n"
      "s_mov_b32 exec_lo, 0x55555555\n"
      "s_mov_b32 exec_hi, 0x0\n"
      "global_store_dword v40, v32, s[50:51] sc1\n"
      "s_mov_b64 exec, -1\n"
      "s_waitcnt lgkmcnt(0)\n"
      "s_barrier\n"
      "v_lshrrev_b32 v8, 4, v15\n"
      "v_mul_u32_u24 v5, 272, v8\n"
      "v_mul_u32_u24 v8, 3584, v8\n"
      "v_and_b32 v14, 15, v15\n"
      "v_lshlrev_b32 v14, 4, v14\n"
      "v_add_u32 v8, v8, v14\n"
      "v_add_u32 v5, v5, v14\n"
      "v_add_u32 v6, 69632, v5\n"
      "s_waitcnt vmcnt(15)\n"
      "ds_write_b128 v5, v[64:67] offset:0\n"
      "s_waitcnt lgkmcnt(0)\n"
      "s_add_u32 s48, s84, 917504\n"
      "s_addc_u32 s49, s85, 0\n"
      "global_load_dwordx4 v[64:67], v3, s[48:49] \n"
      "s_waitcnt vmcnt(15)\n"
      "ds_write_b128 v5, v[68:71] offset:4352\n"
      "s_waitcnt lgkmcnt(0)\n"
      "s_add_u32 s48, s84, 974848\n"
      "s_addc_u32 s49, s85, 0\n"
      "global_load_dwordx4 v[68:71], v3, s[48:49] \n"
      "s_waitcnt vmcnt(15)\n"
      "ds_write_b128 v5, v[72:75] offset:8704\n"
      "s_waitcnt lgkmcnt(0)\n"
      "s_add_u32 s48, s84, 1032192\n"
      "s_addc_u32 s49, s85, 0\n"
      "global_load_dwordx4 v[72:75], v3, s[48:49] \n"
      "s_waitcnt vmcnt(15)\n"
      "ds_write_b128 v5, v[76:79] offset:13056\n"
      "s_waitcnt lgkmcnt(0)\n"
      "s_add_u32 s48, s84, 1089536\n"
      "s_addc_u32 s49, s85, 0\n"
      "global_load_dwordx4 v[76:79], v3, s[48:49] \n"
      "s_waitcnt vmcnt(15)\n"
      "ds_write_b128 v5, v[80:83] offset:17408\n"
      "s_waitcnt lgkmcnt(0)\n"
      "s_add_u32 s48, s84, 1146880\n"
      "s_addc_u32 s49, s85, 0\n"
      "global_load_dwordx4 v[80:83], v3, s[48:49] \n"
      "s_waitcnt vmcnt(15)\n"
      "ds_write_b128 v5, v[84:87] offset:21760\n"
      "s_waitcnt lgkmcnt(0)\n"
      "s_add_u32 s48, s84, 1204224\n"
      "s_addc_u32 s49, s85, 0\n"
      "global_load_dwordx4 v[84:87], v3, s[48:49] \n"
      "s_waitcnt vmcnt(15)\n"
      "ds_write_b128 v5, v[88:91] offset:26112\n"
      "s_waitcnt lgkmcnt(0)\n"
      "s_add_u32 s48, s84, 1261568\n"
      "s_addc_u32 s49, s85, 0\n"
      "global_load_dwordx4 v[88:91], v3, s[48:49] \n"
      "s_waitcnt vmcnt(15)\n"
      "ds_write_b128 v5, v[92:95] offset:30464\n"
      "s_waitcnt lgkmcnt(0)\n"
      "s_add_u32 s48, s84, 1318912\n"
      "s_addc_u32 s49, s85, 0\n"
      "global_load_dwordx4 v[92:95], v3, s[48:49] \n"
      "s_waitcnt vmcnt(15)\n"
      "ds_write_b128 v5, v[96:99] offset:34816\n"
      "s_waitcnt lgkmcnt(0)\n"
      "s_add_u32 s48, s84, 1376256\n"
      "s_addc_u32 s49, s85, 0\n"
      "global_load_dwordx4 v[96:99], v3, s[48:49] \n"
      "s_waitcnt vmcnt(15)\n"
      "ds_write_b128 v5, v[100:103] offset:39168\n"
      "s_waitcnt lgkmcnt(0)\n"
      "s_add_u32 s48, s84, 1433600\n"
      "s_addc_u32 s49, s85, 0\n"
      "global_load_dwordx4 v[100:103], v3, s[48:49] \n"
      "s_waitcnt vmcnt(15)\n"
      "ds_write_b128 v5, v[104:107] offset:43520\n"
      "s_waitcnt lgkmcnt(0)\n"
      "s_add_u32 s48, s84, 1490944\n"
      "s_addc_u32 s49, s85, 0\n"
      "global_load_dwordx4 v[104:107], v3, s[48:49] \n"
      "s_waitcnt vmcnt(15)\n"
      "ds_write_b128 v5, v[108:111] offset:47872\n"
      "s_waitcnt lgkmcnt(0)\n"
      "s_add_u32 s48, s84, 1548288\n"
      "s_addc_u32 s49, s85, 0\n"
      "global_load_dwordx4 v[108:111], v3, s[48:49] \n"
      "s_waitcnt vmcnt(15)\n"
      "ds_write_b128 v5, v[112:115] offset:52224\n"
      "s_waitcnt lgkmcnt(0)\n"
      "s_add_u32 s48, s84, 1605632\n"
      "s_addc_u32 s49, s85, 0\n"
      "global_load_dwordx4 v[112:115], v3, s[48:49] \n"
      "s_waitcnt vmcnt(15)\n"
      "ds_write_b128 v5, v[116:119] offset:56576\n"
      "s_waitcnt lgkmcnt(0)\n"
      "s_add_u32 s48, s84, 1662976\n"
      "s_addc_u32 s49, s85, 0\n"
      "global_load_dwordx4 v[116:119], v3, s[48:49] \n"
      "s_waitcnt vmcnt(15)\n"
      "ds_write_b128 v5, v[120:123] offset:60928\n"
      "s_waitcnt lgkmcnt(0)\n"
      "s_add_u32 s48, s84, 1720320\n"
      "s_addc_u32 s49, s85, 0\n"
      "global_load_dwordx4 v[120:123], v3, s[48:49] \n"
      "s_waitcnt vmcnt(15)\n"
      "ds_write_b128 v5, v[124:127] offset:65280\n"
      "s_waitcnt lgkmcnt(0)\n"
      "s_add_u32 s48, s84, 1777664\n"
      "s_addc_u32 s49, s85, 0\n"
      "global_load_dwordx4 v[124:127], v3, s[48:49] \n"
      "s_waitcnt vmcnt(15)\n"
      "ds_write_b128 v6, v[64:67] offset:0\n"
      "s_waitcnt lgkmcnt(0)\n"
      "s_add_u32 s48, s86, 0\n"
      "s_addc_u32 s49, s87, 0\n"
      "global_load_dwordx4 v[64:67], v4, s[48:49] \n"
      "s_waitcnt vmcnt(15)\n"
      "ds_write_b128 v6, v[68:71] offset:4352\n"
      "s_waitcnt lgkmcnt(0)\n"
      "s_add_u32 s48, s86, 4096\n"
      "s_addc_u32 s49, s87, 0\n"
      "global_load_dwordx4 v[68:71], v4, s[48:49] \n"
      "s_waitcnt vmcnt(15)\n"
      "ds_write_b128 v6, v[72:75] offset:8704\n"
      "s_waitcnt lgkmcnt(0)\n"
      "s_add_u32 s48, s86, 8192\n"
      "s_addc_u32 s49, s87, 0\n"
      "global_load_dwordx4 v[72:75], v4, s[48:49] \n"
      "s_waitcnt vmcnt(15)\n"
      "ds_write_b128 v6, v[76:79] offset:13056\n"
      "s_waitcnt lgkmcnt(0)\n"
      "s_add_u32 s48, s86, 12288\n"
      "s_addc_u32 s49, s87, 0\n"
      "global_load_dwordx4 v[76:79], v4, s[48:49] \n"
      "s_waitcnt vmcnt(15)\n"
      "ds_write_b128 v6, v[80:83] offset:17408\n"
      "s_waitcnt lgkmcnt(0)\n"
      "s_add_u32 s48, s86, 16384\n"
      "s_addc_u32 s49, s87, 0\n"
      "global_load_dwordx4 v[80:83], v4, s[48:49] \n"
      "s_waitcnt vmcnt(15)\n"
      "ds_write_b128 v6, v[84:87] offset:21760\n"
      "s_waitcnt lgkmcnt(0)\n"
      "s_add_u32 s48, s86, 20480\n"
      "s_addc_u32 s49, s87, 0\n"
      "global_load_dwordx4 v[84:87], v4, s[48:49] \n"
      "s_waitcnt vmcnt(15)\n"
      "ds_write_b128 v6, v[88:91] offset:26112\n"
      "s_waitcnt lgkmcnt(0)\n"
      "s_add_u32 s48, s86, 24576\n"
      "s_addc_u32 s49, s87, 0\n"
      "global_load_dwordx4 v[88:91], v4, s[48:49] \n"
      "s_waitcnt vmcnt(15)\n"
      "ds_write_b128 v6, v[92:95] offset:30464\n"
      "s_waitcnt lgkmcnt(0)\n"
      "s_add_u32 s48, s86, 28672\n"
      "s_addc_u32 s49, s87, 0\n"
      "global_load_dwordx4 v[92:95], v4, s[48:49] \n"
      "s_waitcnt vmcnt(15)\n"
      "ds_write_b128 v6, v[96:99] offset:34816\n"
      "s_waitcnt lgkmcnt(0)\n"
      "s_add_u32 s48, s86, 32768\n"
      "s_addc_u32 s49, s87, 0\n"
      "global_load_dwordx4 v[96:99], v4, s[48:49] \n"
      "s_waitcnt vmcnt(15)\n"
      "ds_write_b128 v6, v[100:103] offset:39168\n"
      "s_waitcnt lgkmcnt(0)\n"
      "s_add_u32 s48, s86, 36864\n"
      "s_addc_u32 s49, s87, 0\n"
      "global_load_dwordx4 v[100:103], v4, s[48:49] \n"
      "s_waitcnt vmcnt(15)\n"
      "ds_write_b128 v6, v[104:107] offset:43520\n"
      "s_waitcnt lgkmcnt(0)\n"
      "s_add_u32 s48, s86, 40960\n"
      "s_addc_u32 s49, s87, 0\n"
      "global_load_dwordx4 v[104:107], v4, s[48:49] \n"
      "s_waitcnt vmcnt(15)\n"
      "ds_write_b128 v6, v[108:111] offset:47872\n"
      "s_waitcnt lgkmcnt(0)\n"
      "s_add_u32 s48, s86, 45056\n"
      "s_addc_u32 s49, s87, 0\n"
      "global_load_dwordx4 v[108:111], v4, s[48:49] \n"
      "s_waitcnt vmcnt(15)\n"
      "ds_write_b128 v6, v[112:115] offset:52224\n"
      "s_waitcnt lgkmcnt(0)\n"
      "s_add_u32 s48, s86, 49152\n"
      "s_addc_u32 s49, s87, 0\n"
      "global_load_dwordx4 v[112:115], v4, s[48:49] \n"
      "s_waitcnt vmcnt(15)\n"
      "ds_write_b128 v6, v[116:119] offset:56576\n"
      "s_waitcnt lgkmcnt(0)\n"
      "s_add_u32 s48, s86, 53248\n"
      "s_addc_u32 s49, s87, 0\n"
      "global_load_dwordx4 v[116:119], v4, s[48:49] \n"
      "s_waitcnt vmcnt(15)\n"
      "ds_write_b128 v6, v[120:123] offset:60928\n"
      "s_waitcnt lgkmcnt(0)\n"
      "s_add_u32 s48, s86, 57344\n"
      "s_addc_u32 s49, s87, 0\n"
      "global_load_dwordx4 v[120:123], v4, s[48:49] \n"
      "s_waitcnt vmcnt(15)\n"
      "ds_write_b128 v6, v[124:127] offset:65280\n"
      "s_waitcnt lgkmcnt(0)\n"
      "s_add_u32 s48, s86, 61440\n"
      "s_addc_u32 s49, s87, 0\n"
      "global_load_dwordx4 v[124:127], v4, s[48:49] \n"
      "s_waitcnt lgkmcnt(0)\n"
      "s_barrier\n"
      "v_lshrrev_b32 v12, 4, v0\n"
      "v_lshlrev_b32 v12, 3, v12\n"
      "v_bfe_u32 v13, v0, 2, 2\n"
      "v_add_u32 v12, v12, v13\n"
      "v_mul_u32_u24 v12, 272, v12\n"
      "v_and_b32 v13, 3, v0\n"
      "v_lshlrev_b32 v13, 3, v13\n"
      "v_add_u32 v12, v12, v13\n"
      "s_mul_i32 s66, s53, 64\n"
      "v_add_u32 v12, s66, v12\n"
      "v_add_u32 v13, 52224, v12\n"
      "v_add_u32 v14, 104448, v12\n"
      "ds_read_b64_tr_b16 a[0:1], v12 offset:0\n"
      "ds_read_b64_tr_b16 a[2:3], v12 offset:1088\n"
      "ds_read_b64_tr_b16 a[4:5], v12 offset:8704\n"
      "ds_read_b64_tr_b16 a[6:7], v12 offset:9792\n"
      "ds_read_b64_tr_b16 a[8:9], v12 offset:17408\n"
      "ds_read_b64_tr_b16 a[10:11], v12 offset:18496\n"
      "ds_read_b64_tr_b16 a[12:13], v12 offset:26112\n"
      "ds_read_b64_tr_b16 a[14:15], v12 offset:27200\n"
      "ds_read_b64_tr_b16 a[16:17], v12 offset:34816\n"
      "ds_read_b64_tr_b16 a[18:19], v12 offset:35904\n"
      "s_waitcnt lgkmcnt(4)\n"
      "ds_read_b64_tr_b16 a[20:21], v12 offset:43520\n"
      "ds_read_b64_tr_b16 a[22:23], v12 offset:44608\n"
      "ds_read_b64_tr_b16 a[24:25], v13 offset:0\n"
      "ds_read_b64_tr_b16 a[26:27], v13 offset:1088\n"
      "ds_read_b64_tr_b16 a[28:29], v13 offset:8704\n"
      "ds_read_b64_tr_b16 a[30:31], v13 offset:9792\n"
      "ds_read_b64_tr_b16 a[32:33], v13 offset:17408\n"
      "ds_read_b64_tr_b16 a[34:35], v13 offset:18496\n"
      "ds_read_b64_tr_b16 a[36:37], v13 offset:26112\n"
      "ds_read_b64_tr_b16 a[38:39], v13 offset:27200\n"
      "s_waitcnt lgkmcnt(4)\n"
      "ds_read_b64_tr_b16 a[40:41], v13 offset:34816\n"
      "ds_read_b64_tr_b16 a[42:43], v13 offset:35904\n"
      "ds_read_b64_tr_b16 a[44:45], v13 offset:43520\n"
      "ds_read_b64_tr_b16 a[46:47], v13 offset:44608\n"
      "ds_read_b64_tr_b16 a[48:49], v14 offset:0\n"
      "ds_read_b64_tr_b16 a[50:51], v14 offset:1088\n"
      "ds_read_b64_tr_b16 a[52:53], v14 offset:8704\n"
      "ds_read_b64_tr_b16 a[54:55], v14 offset:9792\n"
      "ds_read_b64_tr_b16 a[56:57], v14 offset:17408\n"
      "ds_read_b64_tr_b16 a[58:59], v14 offset:18496\n"
      "s_waitcnt lgkmcnt(4)\n"
      "ds_read_b64_tr_b16 a[60:61], v14 offset:26112\n"
      "ds_read_b64_tr_b16 a[62:63], v14 offset:27200\n"
      "ds_read_b64_tr_b16 a[64:65], v12 offset:32\n"
      "ds_read_b64_tr_b16 a[66:67], v12 offset:1120\n"
      "ds_read_b64_tr_b16 a[68:69], v12 offset:8736\n"
      "ds_read_b64_tr_b16 a[70:71], v12 offset:9824\n"
      "ds_read_b64_tr_b16 a[72:73], v12 offset:17440\n"
      "ds_read_b64_tr_b16 a[74:75], v12 offset:18528\n"
      "ds_read_b64_tr_b16 a[76:77], v12 offset:26144\n"
      "ds_read_b64_tr_b16 a[78:79], v12 offset:27232\n"
      "s_waitcnt lgkmcnt(4)\n"
      "ds_read_b64_tr_b16 a[80:81], v12 offset:34848\n"
      "ds_read_b64_tr_b16 a[82:83], v12 offset:35936\n"
      "ds_read_b64_tr_b16 a[84:85], v12 offset:43552\n"
      "ds_read_b64_tr_b16 a[86:87], v12 offset:44640\n"
      "ds_read_b64_tr_b16 a[88:89], v13 offset:32\n"
      "ds_read_b64_tr_b16 a[90:91], v13 offset:1120\n"
      "ds_read_b64_tr_b16 a[92:93], v13 offset:8736\n"
      "ds_read_b64_tr_b16 a[94:95], v13 offset:9824\n"
      "ds_read_b64_tr_b16 a[96:97], v13 offset:17440\n"
      "ds_read_b64_tr_b16 a[98:99], v13 offset:18528\n"
      "s_waitcnt lgkmcnt(4)\n"
      "ds_read_b64_tr_b16 a[100:101], v13 offset:26144\n"
      "ds_read_b64_tr_b16 a[102:103], v13 offset:27232\n"
      "ds_read_b64_tr_b16 a[104:105], v13 offset:34848\n"
      "ds_read_b64_tr_b16 a[106:107], v13 offset:35936\n"
      "ds_read_b64_tr_b16 a[108:109], v13 offset:43552\n"
      "ds_read_b64_tr_b16 a[110:111], v13 offset:44640\n"
      "ds_read_b64_tr_b16 a[112:113], v14 offset:32\n"
      "ds_read_b64_tr_b16 a[114:115], v14 offset:1120\n"
      "ds_read_b64_tr_b16 a[116:117], v14 offset:8736\n"
      "ds_read_b64_tr_b16 a[118:119], v14 offset:9824\n"
      "s_waitcnt lgkmcnt(4)\n"
      "ds_read_b64_tr_b16 a[120:121], v14 offset:17440\n"
      "ds_read_b64_tr_b16 a[122:123], v14 offset:18528\n"
      "ds_read_b64_tr_b16 a[124:125], v14 offset:26144\n"
      "ds_read_b64_tr_b16 a[126:127], v14 offset:27232\n"
      "s_waitcnt lgkmcnt(0)\n"
      "s_barrier\n"
      "v_lshrrev_b32 v5, 6, v15\n"
      "v_lshlrev_b32 v8, 10, v5\n"
      "v_add_u32 v8, v8, v1\n"
      "v_mul_u32_u24 v5, 1040, v5\n"
      "v_add_u32 v5, v5, v1\n"
      "v_add_u32 v6, 66560, v5\n"
      "s_waitcnt vmcnt(15)\n"
      "ds_write_b128 v5, v[64:67] offset:0\n"
      "s_waitcnt lgkmcnt(0)\n"
      "s_add_u32 s48, s86, 65536\n"
      "s_addc_u32 s49, s87, 0\n"
      "global_load_dwordx4 v[64:67], v4, s[48:49] \n"
      "s_waitcnt vmcnt(15)\n"
      "ds_write_b128 v5, v[68:71] offset:4160\n"
      "s_waitcnt lgkmcnt(0)\n"
      "s_add_u32 s48, s86, 69632\n"
      "s_addc_u32 s49, s87, 0\n"
      "global_load_dwordx4 v[68:71], v4, s[48:49] \n"
      "s_waitcnt vmcnt(15)\n"
      "ds_write_b128 v5, v[72:75] offset:8320\n"
      "s_waitcnt lgkmcnt(0)\n"
      "s_add_u32 s48, s86, 73728\n"
      "s_addc_u32 s49, s87, 0\n"
      "global_load_dwordx4 v[72:75], v4, s[48:49] \n"
      "s_waitcnt vmcnt(15)\n"
      "ds_write_b128 v5, v[76:79] offset:12480\n"
      "s_waitcnt lgkmcnt(0)\n"
      "s_add_u32 s48, s86, 77824\n"
      "s_addc_u32 s49, s87, 0\n"
      "global_load_dwordx4 v[76:79], v4, s[48:49] \n"
      "s_waitcnt vmcnt(15)\n"
      "ds_write_b128 v5, v[80:83] offset:16640\n"
      "s_waitcnt lgkmcnt(0)\n"
      "s_add_u32 s48, s86, 81920\n"
      "s_addc_u32 s49, s87, 0\n"
      "global_load_dwordx4 v[80:83], v4, s[48:49] \n"
      "s_waitcnt vmcnt(15)\n"
      "ds_write_b128 v5, v[84:87] offset:20800\n"
      "s_waitcnt lgkmcnt(0)\n"
      "s_add_u32 s48, s86, 86016\n"
      "s_addc_u32 s49, s87, 0\n"
      "global_load_dwordx4 v[84:87], v4, s[48:49] \n"
      "s_waitcnt vmcnt(15)\n"
      "ds_write_b128 v5, v[88:91] offset:24960\n"
      "s_waitcnt lgkmcnt(0)\n"
      "s_add_u32 s48, s86, 90112\n"
      "s_addc_u32 s49, s87, 0\n"
      "global_load_dwordx4 v[88:91], v4, s[48:49] \n"
      "s_waitcnt vmcnt(15)\n"
      "ds_write_b128 v5, v[92:95] offset:29120\n"
      "s_waitcnt lgkmcnt(0)\n"
      "s_add_u32 s48, s86, 94208\n"
      "s_addc_u32 s49, s87, 0\n"
      "global_load_dwordx4 v[92:95], v4, s[48:49] \n"
      "s_waitcnt vmcnt(15)\n"
      "ds_write_b128 v5, v[96:99] offset:33280\n"
      "s_waitcnt lgkmcnt(0)\n"
      "s_add_u32 s48, s86, 98304\n"
      "s_addc_u32 s49, s87, 0\n"
      "global_load_dwordx4 v[96:99], v4, s[48:49] \n"
      "s_waitcnt vmcnt(15)\n"
      "ds_write_b128 v5, v[100:103] offset:37440\n"
      "s_waitcnt lgkmcnt(0)\n"
      "s_add_u32 s48, s86, 102400\n"
      "s_addc_u32 s49, s87, 0\n"
      "global_load_dwordx4 v[100:103], v4, s[48:49] \n"
      "s_waitcnt vmcnt(15)\n"
      "ds_write_b128 v5, v[104:107] offset:41600\n"
      "s_waitcnt lgkmcnt(0)\n"
      "s_add_u32 s48, s86, 106496\n"
      "s_addc_u32 s49, s87, 0\n"
      "global_load_dwordx4 v[104:107], v4, s[48:49] \n"
      "s_waitcnt vmcnt(15)\n"
      "ds_write_b128 v5, v[108:111] offset:45760\n"
      "s_waitcnt lgkmcnt(0)\n"
      "s_add_u32 s48, s86, 110592\n"
      "s_addc_u32 s49, s87, 0\n"
      "global_load_dwordx4 v[108:111], v4, s[48:49] \n"
      "s_waitcnt vmcnt(15)\n"
      "ds_write_b128 v5, v[112:115] offset:49920\n"
      "s_waitcnt lgkmcnt(0)\n"
      "s_add_u32 s48, s86, 114688\n"
      "s_addc_u32 s49, s87, 0\n"
      "global_load_dwordx4 v[112:115], v4, s[48:49] \n"
      "s_waitcnt vmcnt(15)\n"
      "ds_write_b128 v5, v[116:119] offset:54080\n"
      "s_waitcnt lgkmcnt(0)\n"
      "s_add_u32 s48, s86, 118784\n"
      "s_addc_u32 s49, s87, 0\n"
      "global_load_dwordx4 v[116:119], v4, s[48:49] \n"
      "s_waitcnt vmcnt(15)\n"
      "ds_write_b128 v5, v[120:123] offset:58240\n"
      "s_waitcnt lgkmcnt(0)\n"
      "s_add_u32 s48, s86, 122880\n"
      "s_addc_u32 s49, s87, 0\n"
      "global_load_dwordx4 v[120:123], v4, s[48:49] \n"
      "s_waitcnt vmcnt(15)\n"
      "ds_write_b128 v5, v[124:127] offset:62400\n"
      "s_waitcnt lgkmcnt(0)\n"
      "s_add_u32 s48, s86, 126976\n"
      "s_addc_u32 s49, s87, 0\n"
      "global_load_dwordx4 v[124:127], v4, s[48:49] \n"
      "s_waitcnt vmcnt(15)\n"
      "ds_write_b128 v6, v[64:67] offset:0\n"
      "s_waitcnt vmcnt(14)\n"
      "ds_write_b128 v6, v[68:71] offset:4160\n"
      "s_waitcnt vmcnt(13)\n"
      "ds_write_b128 v6, v[72:75] offset:8320\n"
      "s_waitcnt vmcnt(12)\n"
      "ds_write_b128 v6, v[76:79] offset:12480\n"
      "s_waitcnt vmcnt(11)\n"
      "ds_write_b128 v6, v[80:83] offset:16640\n"
      "s_waitcnt vmcnt(10)\n"
      "ds_write_b128 v6, v[84:87] offset:20800\n"
      "s_waitcnt vmcnt(9)\n"
      "ds_write_b128 v6, v[88:91] offset:24960\n"
      "s_waitcnt vmcnt(8)\n"
      "ds_write_b128 v6, v[92:95] offset:29120\n"
      "s_waitcnt vmcnt(7)\n"
      "ds_write_b128 v6, v[96:99] offset:33280\n"
      "s_waitcnt vmcnt(6)\n"
      "ds_write_b128 v6, v[100:103] offset:37440\n"
      "s_waitcnt vmcnt(5)\n"
      "ds_write_b128 v6, v[104:107] offset:41600\n"
      "s_waitcnt vmcnt(4)\n"
      "ds_write_b128 v6, v[108:111] offset:45760\n"
      "s_waitcnt vmcnt(3)\n"
      "ds_write_b128 v6, v[112:115] offset:49920\n"
      "s_waitcnt vmcnt(2)\n"
      "ds_write_b128 v6, v[116:119] offset:54080\n"
      "s_waitcnt vmcnt(1)\n"
      "ds_write_b128 v6, v[120:123] offset:58240\n"
      "s_waitcnt vmcnt(0)\n"
      "ds_write_b128 v6, v[124:127] offset:62400\n"
      "s_waitcnt lgkmcnt(0)\n"
      "s_barrier\n"
      "v_lshrrev_b32 v12, 4, v0\n"
      "v_lshlrev_b32 v12, 3, v12\n"
      "v_bfe_u32 v13, v0, 2, 2\n"
      "v_add_u32 v12, v12, v13\n"
      "v_mul_u32_u24 v12, 1040, v12\n"
      "v_and_b32 v13, 3, v0\n"
      "v_lshlrev_b32 v13, 3, v13\n"
      "v_add_u32 v12, v12, v13\n"
      "s_mul_i32 s66, s53, 256\n"
      "v_add_u32 v12, s66, v12\n"
      "v_add_u32 v13, 66560, v12\n"
      "v_add_u32 v14, 133120, v12\n"
      "ds_read_b64_tr_b16 v[128:129], v12 offset:0\n"
      "ds_read_b64_tr_b16 v[130:131], v12 offset:4160\n"
      "ds_read_b64_tr_b16 v[132:133], v12 offset:33280\n"
      "ds_read_b64_tr_b16 v[134:135], v12 offset:37440\n"
      "ds_read_b64_tr_b16 v[136:137], v13 offset:0\n"
      "ds_read_b64_tr_b16 v[138:139], v13 offset:4160\n"
      "ds_read_b64_tr_b16 v[140:141], v13 offset:33280\n"
      "ds_read_b64_tr_b16 v[142:143], v13 offset:37440\n"
      "ds_read_b64_tr_b16 v[144:145], v12 offset:32\n"
      "ds_read_b64_tr_b16 v[146:147], v12 offset:4192\n"
      "s_waitcnt lgkmcnt(4)\n"
      "ds_read_b64_tr_b16 v[148:149], v12 offset:33312\n"
      "ds_read_b64_tr_b16 v[150:151], v12 offset:37472\n"
      "ds_read_b64_tr_b16 v[152:153], v13 offset:32\n"
      "ds_read_b64_tr_b16 v[154:155], v13 offset:4192\n"
      "ds_read_b64_tr_b16 v[156:157], v13 offset:33312\n"
      "ds_read_b64_tr_b16 v[158:159], v13 offset:37472\n"
      "ds_read_b64_tr_b16 v[160:161], v12 offset:64\n"
      "ds_read_b64_tr_b16 v[162:163], v12 offset:4224\n"
      "ds_read_b64_tr_b16 v[164:165], v12 offset:33344\n"
      "ds_read_b64_tr_b16 v[166:167], v12 offset:37504\n"
      "s_waitcnt lgkmcnt(4)\n"
      "ds_read_b64_tr_b16 v[168:169], v13 offset:64\n"
      "ds_read_b64_tr_b16 v[170:171], v13 offset:4224\n"
      "ds_read_b64_tr_b16 v[172:173], v13 offset:33344\n"
      "ds_read_b64_tr_b16 v[174:175], v13 offset:37504\n"
      "ds_read_b64_tr_b16 v[176:177], v12 offset:96\n"
      "ds_read_b64_tr_b16 v[178:179], v12 offset:4256\n"
      "ds_read_b64_tr_b16 v[180:181], v12 offset:33376\n"
      "ds_read_b64_tr_b16 v[182:183], v12 offset:37536\n"
      "ds_read_b64_tr_b16 v[184:185], v13 offset:96\n"
      "ds_read_b64_tr_b16 v[186:187], v13 offset:4256\n"
      "s_waitcnt lgkmcnt(4)\n"
      "ds_read_b64_tr_b16 v[188:189], v13 offset:33376\n"
      "ds_read_b64_tr_b16 v[190:191], v13 offset:37536\n"
      "ds_read_b64_tr_b16 v[192:193], v12 offset:128\n"
      "ds_read_b64_tr_b16 v[194:195], v12 offset:4288\n"
      "ds_read_b64_tr_b16 v[196:197], v12 offset:33408\n"
      "ds_read_b64_tr_b16 v[198:199], v12 offset:37568\n"
      "ds_read_b64_tr_b16 v[200:201], v13 offset:128\n"
      "ds_read_b64_tr_b16 v[202:203], v13 offset:4288\n"
      "ds_read_b64_tr_b16 v[204:205], v13 offset:33408\n"
      "ds_read_b64_tr_b16 v[206:207], v13 offset:37568\n"
      "s_waitcnt lgkmcnt(4)\n"
      "ds_read_b64_tr_b16 v[208:209], v12 offset:160\n"
      "ds_read_b64_tr_b16 v[210:211], v12 offset:4320\n"
      "ds_read_b64_tr_b16 v[212:213], v12 offset:33440\n"
      "ds_read_b64_tr_b16 v[214:215], v12 offset:37600\n"
      "ds_read_b64_tr_b16 v[216:217], v13 offset:160\n"
      "ds_read_b64_tr_b16 v[218:219], v13 offset:4320\n"
      "ds_read_b64_tr_b16 v[220:221], v13 offset:33440\n"
      "ds_read_b64_tr_b16 v[222:223], v13 offset:37600\n"
      "ds_read_b64_tr_b16 v[224:225], v12 offset:192\n"
      "ds_read_b64_tr_b16 v[226:227], v12 offset:4352\n"
      "s_waitcnt lgkmcnt(4)\n"
      "ds_read_b64_tr_b16 v[228:229], v12 offset:33472\n"
      "ds_read_b64_tr_b16 v[230:231], v12 offset:37632\n"
      "ds_read_b64_tr_b16 v[232:233], v13 offset:192\n"
      "ds_read_b64_tr_b16 v[234:235], v13 offset:4352\n"
      "ds_read_b64_tr_b16 v[236:237], v13 offset:33472\n"
      "ds_read_b64_tr_b16 v[238:239], v13 offset:37632\n"
      "ds_read_b64_tr_b16 v[240:241], v12 offset:224\n"
      "ds_read_b64_tr_b16 v[242:243], v12 offset:4384\n"
      "ds_read_b64_tr_b16 v[244:245], v12 offset:33504\n"
      "ds_read_b64_tr_b16 v[246:247], v12 offset:37664\n"
      "s_waitcnt lgkmcnt(4)\n"
      "ds_read_b64_tr_b16 v[248:249], v13 offset:224\n"
      "ds_read_b64_tr_b16 v[250:251], v13 offset:4384\n"
      "ds_read_b64_tr_b16 v[252:253], v13 offset:33504\n"
      "ds_read_b64_tr_b16 v[254:255], v13 offset:37664\n"
      "s_waitcnt lgkmcnt(0)\n"
      "s_barrier\n"
      "s_branch C_wdone9_%=\n"
      "C_wr12_%=:\n"
      "s_cmp_eq_u32 s54, 3\n"
      "s_cbranch_scc0 C_wr13_%=\n"
      "v_lshrrev_b32 v2, 4, v15\n"
      "v_mul_u32_u24 v5, 272, v2\n"
      "v_mul_u32_u24 v2, 8192, v2\n"
      "v_and_b32 v14, 15, v15\n"
      "v_lshlrev_b32 v14, 4, v14\n"
      "v_add_u32 v2, v2, v14\n"
      "v_add_u32 v5, v5, v14\n"
      "s_add_u32 s48, s80, 0\n"
      "s_addc_u32 s49, s81, 0\n"
      "global_load_dwordx4 v[64:67], v2, s[48:49] \n"
      "s_add_u32 s48, s80, 131072\n"
      "s_addc_u32 s49, s81, 0\n"
      "global_load_dwordx4 v[68:71], v2, s[48:49] \n"
      "s_add_u32 s48, s80, 262144\n"
      "s_addc_u32 s49, s81, 0\n"
      "global_load_dwordx4 v[72:75], v2, s[48:49] \n"
      "s_add_u32 s48, s80, 393216\n"
      "s_addc_u32 s49, s81, 0\n"
      "global_load_dwordx4 v[76:79], v2, s[48:49] \n"
      "s_add_u32 s48, s80, 524288\n"
      "s_addc_u32 s49, s81, 0\n"
      "global_load_dwordx4 v[80:83], v2, s[48:49] \n"
      "s_add_u32 s48, s80, 655360\n"
      "s_addc_u32 s49, s81, 0\n"
      "global_load_dwordx4 v[84:87], v2, s[48:49] \n"
      "s_add_u32 s48, s80, 786432\n"
      "s_addc_u32 s49, s81, 0\n"
      "global_load_dwordx4 v[88:91], v2, s[48:49] \n"
      "s_add_u32 s48, s80, 917504\n"
      "s_addc_u32 s49, s81, 0\n"
      "global_load_dwordx4 v[92:95], v2, s[48:49] \n"
      "s_add_u32 s48, s80, 1048576\n"
      "s_addc_u32 s49, s81, 0\n"
      "global_load_dwordx4 v[96:99], v2, s[48:49] \n"
      "s_add_u32 s48, s80, 1179648\n"
      "s_addc_u32 s49, s81, 0\n"
      "global_load_dwordx4 v[100:103], v2, s[48:49] \n"
      "s_add_u32 s48, s80, 1310720\n"
      "s_addc_u32 s49, s81, 0\n"
      "global_load_dwordx4 v[104:107], v2, s[48:49] \n"
      "s_add_u32 s48, s80, 1441792\n"
      "s_addc_u32 s49, s81, 0\n"
      "global_load_dwordx4 v[108:111], v2, s[48:49] \n"
      "s_add_u32 s48, s80, 1572864\n"
      "s_addc_u32 s49, s81, 0\n"
      "global_load_dwordx4 v[112:115], v2, s[48:49] \n"
      "s_add_u32 s48, s80, 1703936\n"
      "s_addc_u32 s49, s81, 0\n"
      "global_load_dwordx4 v[116:119], v2, s[48:49] \n"
      "s_add_u32 s48, s80, 1835008\n"
      "s_addc_u32 s49, s81, 0\n"
      "global_load_dwordx4 v[120:123], v2, s[48:49] \n"
      "s_add_u32 s48, s80, 1966080\n"
      "s_addc_u32 s49, s81, 0\n"
      "global_load_dwordx4 v[124:127], v2, s[48:49] \n"
      "s_waitcnt vmcnt(16)\n"
      "s_waitcnt lgkmcnt(0)\n"
      "s_barrier\n"
      "s_add_u32 s58, s58, 1\n"
      "s_and_b32 s67, s52, 7\n"
      "s_lshl_b32 s67, s67, 6\n"
      "s_lshr_b32 s68, s52, 3\n"
      "s_add_u32 s67, s67, s68\n"
      "s_lshl_b32 s67, s67, 2\n"
      "s_add_u32 s67, s67, 1152\n"
      "v_mov_b32 v8, s67\n"
      "v_mov_b32 v9, s58\n"
      "s_mov_b64 exec, 1\n"
      "global_store_dword v8, v9, s[40:41] sc1\n"
      "s_mov_b64 exec, -1\n"
      "v_lshlrev_b32 v32, 16, v16\n"
      "v_and_b32 v33, 0xffff0000, v16\n"
      "v_lshlrev_b32 v34, 16, v17\n"
      "v_and_b32 v35, 0xffff0000, v17\n"
      "v_lshlrev_b32 v36, 16, v18\n"
      "v_and_b32 v37, 0xffff0000, v18\n"
      "v_lshlrev_b32 v38, 16, v19\n"
      "v_and_b32 v39, 0xffff0000, v19\n"
      "v_readfirstlane_b32 s66, v32\n"
      "s_nop 0\n"
      "s_mov_b32 s67, s66\n"
      "v_pk_add_f32 v[40:41], v[32:33], s[66:67] neg_lo:[0,1] neg_hi:[0,1]\n"
      "v_pk_add_f32 v[42:43], v[34:35], s[66:67] neg_lo:[0,1] neg_hi:[0,1]\n"
      "v_pk_add_f32 v[44:45], v[36:37], s[66:67] neg_lo:[0,1] neg_hi:[0,1]\n"
      "v_pk_add_f32 v[46:47], v[38:39], s[66:67] neg_lo:[0,1] neg_hi:[0,1]\n"
      "v_pk_mul_f32 v[56:57], v[40:41], v[40:41]\n"
      "v_pk_add_f32 v[48:49], v[40:41], v[42:43]\n"
      "v_pk_fma_f32 v[56:57], v[42:43], v[42:43], v[56:57]\n"
      "v_pk_add_f32 v[50:51], v[44:45], v[46:47]\n"
      "v_pk_fma_f32 v[56:57], v[44:45], v[44:45], v[56:57]\n"
      "v_pk_add_f32 v[48:49], v[48:49], v[50:51]\n"
      "v_pk_fma_f32 v[56:57], v[46:47], v[46:47], v[56:57]\n"
      "v_add_f32 v48, v48, v49\n"
      "v_add_f32 v49, v56, v57\n"
      "s_nop 0\n"
      "v_add_f32_dpp v48, v48, v48 quad_perm:[1,0,3,2] row_mask:0xf bank_mask:0xf\n"
      "v_add_f32_dpp v49, v49, v49 quad_perm:[1,0,3,2] row_mask:0xf bank_mask:0xf\n"
      "s_nop 0\n"
      "v_add_f32_dpp v48, v48, v48 quad_perm:[2,3,0,1] row_mask:0xf bank_mask:0xf\n"
      "v_add_f32_dpp v49, v49, v49 quad_perm:[2,3,0,1] row_mask:0xf bank_mask:0xf\n"
      "s_nop 0\n"
      "v_add_f32_dpp v48, v48, v48 row_half_mirror row_mask:0xf bank_mask:0xf\n"
      "v_add_f32_dpp v49, v49, v49 row_half_mirror row_mask:0xf bank_mask:0xf\n"
      "s_nop 0\n"
      "v_add_f32_dpp v48, v48, v48 row_ror:8 row_mask:0xf bank_mask:0xf\n"
      "v_add_f32_dpp v49, v49, v49 row_ror:8 row_mask:0xf bank_mask:0xf\n"
      "v_mov_b32 v12, v48\n"
      "v_mov_b32 v13, v49\n"
      "s_nop 0\n"
      "v_permlane16_swap_b32 v12, v48\n"
      "v_permlane16_swap_b32 v13, v49\n"
      "v_add_f32 v48, v12, v48\n"
      "v_add_f32 v49, v13, v49\n"
      "v_mov_b32 v12, v48\n"
      "v_mov_b32 v13, v49\n"
      "s_nop 0\n"
      "v_permlane32_swap_b32 v12, v48\n"
      "v_permlane32_swap_b32 v13, v49\n"
      "v_add_f32 v48, v12, v48\n"
      "v_add_f32 v49, v13, v49\n"
      "v_mul_f32 v48, 0x3b000000, v48\n"
      "v_mul_f32 v49, 0x3b000000, v49\n"
      "v_fma_f32 v49, -v48, v48, v49\n"
      "v_add_f32 v49, 0x3727c5ac, v49\n"
      "v_rsq_f32 v49, v49\n"
      "s_nop 0\n"
      "v_mul_f32 v48, v48, v49\n"
      "v_pk_fma_f32 v[40:41], v[40:41], v[48:49], v[48:49] op_sel:[0,1,0] op_sel_hi:[1,1,0] neg_lo:[0,0,1] neg_hi:[0,0,1]\n"
      "v_pk_fma_f32 v[42:43], v[42:43], v[48:49], v[48:49] op_sel:[0,1,0] op_sel_hi:[1,1,0] neg_lo:[0,0,1] neg_hi:[0,0,1]\n"
      "v_pk_fma_f32 v[44:45], v[44:45], v[48:49], v[48:49] op_sel:[0,1,0] op_sel_hi:[1,1,0] neg_lo:[0,0,1] neg_hi:[0,0,1]\n"
      "v_pk_fma_f32 v[46:47], v[46:47], v[48:49], v[48:49] op_sel:[0,1,0] op_sel_hi:[1,1,0] neg_lo:[0,0,1] neg_hi:[0,0,1]\n"
      "v_cvt_pk_bf16_f32 v52, v40, v41\n"
      "v_cvt_pk_bf16_f32 v53, v42, v43\n"
      "v_cvt_pk_bf16_f32 v54, v44, v45\n"
      "v_cvt_pk_bf16_f32 v55, v46, v47\n"
      "s_mul_i32 s65, s53, 2080\n"
      "s_add_u32 s65, s65, 143360\n"
      "v_add_u32 v12, s65, v1\n"
      "ds_write_b128 v12, v[52:55]\n"
      "v_lshlrev_b32 v32, 16, v20\n"
      "v_and_b32 v33, 0xffff0000, v20\n"
      "v_lshlrev_b32 v34, 16, v21\n"
      "v_and_b32 v35, 0xffff0000, v21\n"
      "v_lshlrev_b32 v36, 16, v22\n"
      "v_and_b32 v37, 0xffff0000, v22\n"
      "v_lshlrev_b32 v38, 16, v23\n"
      "v_and_b32 v39, 0xffff0000, v23\n"
      "v_readfirstlane_b32 s66, v32\n"
      "s_nop 0\n"
      "s_mov_b32 s67, s66\n"
      "v_pk_add_f32 v[40:41], v[32:33], s[66:67] neg_lo:[0,1] neg_hi:[0,1]\n"
      "v_pk_add_f32 v[42:43], v[34:35], s[66:67] neg_lo:[0,1] neg_hi:[0,1]\n"
      "v_pk_add_f32 v[44:45], v[36:37], s[66:67] neg_lo:[0,1] neg_hi:[0,1]\n"
      "v_pk_add_f32 v[46:47], v[38:39], s[66:67] neg_lo:[0,1] neg_hi:[0,1]\n"
      "v_pk_mul_f32 v[56:57], v[40:41], v[40:41]\n"
      "v_pk_add_f32 v[48:49], v[40:41], v[42:43]\n"
      "v_pk_fma_f32 v[56:57], v[42:43], v[42:43], v[56:57]\n"
      "v_pk_add_f32 v[50:51], v[44:45], v[46:47]\n"
      "v_pk_fma_f32 v[56:57], v[44:45], v[44:45], v[56:57]\n"
      "v_pk_add_f32 v[48:49], v[48:49], v[50:51]\n"
      "v_pk_fma_f32 v[56:57], v[46:47], v[46:47], v[56:57]\n"
      "v_add_f32 v48, v48, v49\n"
      "v_add_f32 v49, v56, v57\n"
      "s_nop 0\n"
      "v_add_f32_dpp v48, v48, v48 quad_perm:[1,0,3,2] row_mask:0xf bank_mask:0xf\n"
      "v_add_f32_dpp v49, v49, v49 quad_perm:[1,0,3,2] row_mask:0xf bank_mask:0xf\n"
      "s_nop 0\n"
      "v_add_f32_dpp v48, v48, v48 quad_perm:[2,3,0,1] row_mask:0xf bank_mask:0xf\n"
      "v_add_f32_dpp v49, v49, v49 quad_perm:[2,3,0,1] row_mask:0xf bank_mask:0xf\n"
      "s_nop 0\n"
      "v_add_f32_dpp v48, v48, v48 row_half_mirror row_mask:0xf bank_mask:0xf\n"
      "v_add_f32_dpp v49, v49, v49 row_half_mirror row_mask:0xf bank_mask:0xf\n"
      "s_nop 0\n"
      "v_add_f32_dpp v48, v48, v48 row_ror:8 row_mask:0xf bank_mask:0xf\n"
      "v_add_f32_dpp v49, v49, v49 row_ror:8 row_mask:0xf bank_mask:0xf\n"
      "v_mov_b32 v12, v48\n"
      "v_mov_b32 v13, v49\n"
      "s_nop 0\n"
      "v_permlane16_swap_b32 v12, v48\n"
      "v_permlane16_swap_b32 v13, v49\n"
      "v_add_f32 v48, v12, v48\n"
      "v_add_f32 v49, v13, v49\n"
      "v_mov_b32 v12, v48\n"
      "v_mov_b32 v13, v49\n"
      "s_nop 0\n"
      "v_permlane32_swap_b32 v12, v48\n"
      "v_permlane32_swap_b32 v13, v49\n"
      "v_add_f32 v48, v12, v48\n"
      "v_add_f32 v49, v13, v49\n"
      "v_mul_f32 v48, 0x3b000000, v48\n"
      "v_mul_f32 v49, 0x3b000000, v49\n"
      "v_fma_f32 v49, -v48, v48, v49\n"
      "v_add_f32 v49, 0x3727c5ac, v49\n"
      "v_rsq_f32 v49, v49\n"
      "s_nop 0\n"
      "v_mul_f32 v48, v48, v49\n"
      "v_pk_fma_f32 v[40:41], v[40:41], v[48:49], v[48:49] op_sel:[0,1,0] op_sel_hi:[1,1,0] neg_lo:[0,0,1] neg_hi:[0,0,1]\n"
      "v_pk_fma_f32 v[42:43], v[42:43], v[48:49], v[48:49] op_sel:[0,1,0] op_sel_hi:[1,1,0] neg_lo:[0,0,1] neg_hi:[0,0,1]\n"
      "v_pk_fma_f32 v[44:45], v[44:45], v[48:49], v[48:49] op_sel:[0,1,0] op_sel_hi:[1,1,0] neg_lo:[0,0,1] neg_hi:[0,0,1]\n"
      "v_pk_fma_f32 v[46:47], v[46:47], v[48:49], v[48:49] op_sel:[0,1,0] op_sel_hi:[1,1,0] neg_lo:[0,0,1] neg_hi:[0,0,1]\n"
      "v_cvt_pk_bf16_f32 v52, v40, v41\n"
      "v_cvt_pk_bf16_f32 v53, v42, v43\n"
      "v_cvt_pk_bf16_f32 v54, v44, v45\n"
      "v_cvt_pk_bf16_f32 v55, v46, v47\n"
      "s_mul_i32 s65, s53, 2080\n"
      "s_add_u32 s65, s65, 144400\n"
      "v_add_u32 v12, s65, v1\n"
      "ds_write_b128 v12, v[52:55]\n"
      "v_lshrrev_b32 v8, 4, v15\n"
      "v_mul_u32_u24 v5, 272, v8\n"
      "v_mul_u32_u24 v8, 8192, v8\n"
      "v_and_b32 v14, 15, v15\n"
      "v_lshlrev_b32 v14, 4, v14\n"
      "v_add_u32 v8, v8, v14\n"
      "v_add_u32 v5, v5, v14\n"
      "v_add_u32 v6, 69632, v5\n"
      "s_waitcnt vmcnt(15)\n"
      "ds_write_b128 v5, v[64:67] offset:0\n"
      "s_waitcnt lgkmcnt(0)\n"
      "s_add_u32 s48, s80, 2097152\n"
      "s_addc_u32 s49, s81, 0\n"
      "global_load_dwordx4 v[64:67], v2, s[48:49] \n"
      "s_waitcnt vmcnt(15)\n"
      "ds_write_b128 v5, v[68:71] offset:4352\n"
      "s_waitcnt lgkmcnt(0)\n"
      "s_add_u32 s48, s80, 2228224\n"
      "s_addc_u32 s49, s81, 0\n"
      "global_load_dwordx4 v[68:71], v2, s[48:49] \n"
      "s_waitcnt vmcnt(15)\n"
      "ds_write_b128 v5, v[72:75] offset:8704\n"
      "s_waitcnt lgkmcnt(0)\n"
      "s_add_u32 s48, s80, 2359296\n"
      "s_addc_u32 s49, s81, 0\n"
      "global_load_dwordx4 v[72:75], v2, s[48:49] \n"
      "s_waitcnt vmcnt(15)\n"
      "ds_write_b128 v5, v[76:79] offset:13056\n"
      "s_waitcnt lgkmcnt(0)\n"
      "s_add_u32 s48, s80, 2490368\n"
      "s_addc_u32 s49, s81, 0\n"
      "global_load_dwordx4 v[76:79], v2, s[48:49] \n"
      "s_waitcnt vmcnt(15)\n"
      "ds_write_b128 v5, v[80:83] offset:17408\n"
      "s_waitcnt lgkmcnt(0)\n"
      "s_add_u32 s48, s80, 2621440\n"
      "s_addc_u32 s49, s81, 0\n"
      "global_load_dwordx4 v[80:83], v2, s[48:49] \n"
      "s_waitcnt vmcnt(15)\n"
      "ds_write_b128 v5, v[84:87] offset:21760\n"
      "s_waitcnt lgkmcnt(0)\n"
      "s_add_u32 s48, s80, 2752512\n"
      "s_addc_u32 s49, s81, 0\n"
      "global_load_dwordx4 v[84:87], v2, s[48:49] \n"
      "s_waitcnt vmcnt(15)\n"
      "ds_write_b128 v5, v[88:91] offset:26112\n"
      "s_waitcnt lgkmcnt(0)\n"
      "s_add_u32 s48, s80, 2883584\n"
      "s_addc_u32 s49, s81, 0\n"
      "global_load_dwordx4 v[88:91], v2, s[48:49] \n"
      "s_waitcnt vmcnt(15)\n"
      "ds_write_b128 v5, v[92:95] offset:30464\n"
      "s_waitcnt lgkmcnt(0)\n"
      "s_add_u32 s48, s80, 3014656\n"
      "s_addc_u32 s49, s81, 0\n"
      "global_load_dwordx4 v[92:95], v2, s[48:49] \n"
      "s_waitcnt vmcnt(15)\n"
      "ds_write_b128 v5, v[96:99] offset:34816\n"
      "s_waitcnt lgkmcnt(0)\n"
      "s_add_u32 s48, s80, 3145728\n"
      "s_addc_u32 s49, s81, 0\n"
      "global_load_dwordx4 v[96:99], v2, s[48:49] \n"
      "s_waitcnt vmcnt(15)\n"
      "ds_write_b128 v5, v[100:103] offset:39168\n"
      "s_waitcnt lgkmcnt(0)\n"
      "s_add_u32 s48, s80, 3276800\n"
      "s_addc_u32 s49, s81, 0\n"
      "global_load_dwordx4 v[100:103], v2, s[48:49] \n"
      "s_waitcnt vmcnt(15)\n"
      "ds_write_b128 v5, v[104:107] offset:43520\n"
      "s_waitcnt lgkmcnt(0)\n"
      "s_add_u32 s48, s80, 3407872\n"
      "s_addc_u32 s49, s81, 0\n"
      "global_load_dwordx4 v[104:107], v2, s[48:49] \n"
      "s_waitcnt vmcnt(15)\n"
      "ds_write_b128 v5, v[108:111] offset:47872\n"
      "s_waitcnt lgkmcnt(0)\n"
      "s_add_u32 s48, s80, 3538944\n"
      "s_addc_u32 s49, s81, 0\n"
      "global_load_dwordx4 v[108:111], v2, s[48:49] \n"
      "s_waitcnt vmcnt(15)\n"
      "ds_write_b128 v5, v[112:115] offset:52224\n"
      "s_waitcnt lgkmcnt(0)\n"
      "s_add_u32 s48, s80, 3670016\n"
      "s_addc_u32 s49, s81, 0\n"
      "global_load_dwordx4 v[112:115], v2, s[48:49] \n"
      "s_waitcnt vmcnt(15)\n"
      "ds_write_b128 v5, v[116:119] offset:56576\n"
      "s_waitcnt lgkmcnt(0)\n"
      "s_add_u32 s48, s80, 3801088\n"
      "s_addc_u32 s49, s81, 0\n"
      "global_load_dwordx4 v[116:119], v2, s[48:49] \n"
      "s_waitcnt vmcnt(15)\n"
      "ds_write_b128 v5, v[120:123] offset:60928\n"
      "s_waitcnt lgkmcnt(0)\n"
      "s_add_u32 s48, s80, 3932160\n"
      "s_addc_u32 s49, s81, 0\n"
      "global_load_dwordx4 v[120:123], v2, s[48:49] \n"
      "s_waitcnt vmcnt(15)\n"
      "ds_write_b128 v5, v[124:127] offset:65280\n"
      "s_waitcnt lgkmcnt(0)\n"
      "s_add_u32 s48, s80, 4063232\n"
      "s_addc_u32 s49, s81, 0\n"
      "global_load_dwordx4 v[124:127], v2, s[48:49] \n"
      "s_waitcnt vmcnt(15)\n"
      "ds_write_b128 v6, v[64:67] offset:0\n"
      "s_waitcnt vmcnt(14)\n"
      "ds_write_b128 v6, v[68:71] offset:4352\n"
      "s_waitcnt vmcnt(13)\n"
      "ds_write_b128 v6, v[72:75] offset:8704\n"
      "s_waitcnt vmcnt(12)\n"
      "ds_write_b128 v6, v[76:79] offset:13056\n"
      "s_waitcnt vmcnt(11)\n"
      "ds_write_b128 v6, v[80:83] offset:17408\n"
      "s_waitcnt vmcnt(10)\n"
      "ds_write_b128 v6, v[84:87] offset:21760\n"
      "s_waitcnt vmcnt(9)\n"
      "ds_write_b128 v6, v[88:91] offset:26112\n"
      "s_waitcnt vmcnt(8)\n"
      "ds_write_b128 v6, v[92:95] offset:30464\n"
      "s_waitcnt vmcnt(7)\n"
      "ds_write_b128 v6, v[96:99] offset:34816\n"
      "s_waitcnt vmcnt(6)\n"
      "ds_write_b128 v6, v[100:103] offset:39168\n"
      "s_waitcnt vmcnt(5)\n"
      "ds_write_b128 v6, v[104:107] offset:43520\n"
      "s_waitcnt vmcnt(4)\n"
      "ds_write_b128 v6, v[108:111] offset:47872\n"
      "s_waitcnt vmcnt(3)\n"
      "ds_write_b128 v6, v[112:115] offset:52224\n"
      "s_waitcnt vmcnt(2)\n"
      "ds_write_b128 v6, v[116:119] offset:56576\n"
      "s_waitcnt vmcnt(1)\n"
      "ds_write_b128 v6, v[120:123] offset:60928\n"
      "s_waitcnt vmcnt(0)\n"
      "ds_write_b128 v6, v[124:127] offset:65280\n"
      "s_waitcnt lgkmcnt(0)\n"
      "s_barrier\n"
      "v_lshrrev_b32 v12, 4, v0\n"
      "v_lshlrev_b32 v12, 3, v12\n"
      "v_bfe_u32 v13, v0, 2, 2\n"
      "v_add_u32 v12, v12, v13\n"
      "v_mul_u32_u24 v12, 272, v12\n"
      "v_and_b32 v13, 3, v0\n"
      "v_lshlrev_b32 v13, 3, v13\n"
      "v_add_u32 v12, v12, v13\n"
      "s_mul_i32 s66, s53, 64\n"
      "v_add_u32 v12, s66, v12\n"
      "v_add_u32 v13, 52224, v12\n"
      "v_add_u32 v14, 104448, v12\n"
      "ds_read_b64_tr_b16 a[128:129], v12 offset:0\n"
      "ds_read_b64_tr_b16 a[130:131], v12 offset:1088\n"
      "ds_read_b64_tr_b16 a[132:133], v12 offset:8704\n"
      "ds_read_b64_tr_b16 a[134:135], v12 offset:9792\n"
      "ds_read_b64_tr_b16 a[136:137], v12 offset:17408\n"
      "ds_read_b64_tr_b16 a[138:139], v12 offset:18496\n"
      "ds_read_b64_tr_b16 a[140:141], v12 offset:26112\n"
      "ds_read_b64_tr_b16 a[142:143], v12 offset:27200\n"
      "ds_read_b64_tr_b16 a[144:145], v12 offset:34816\n"
      "ds_read_b64_tr_b16 a[146:147], v12 offset:35904\n"
      "s_waitcnt lgkmcnt(4)\n"
      "ds_read_b64_tr_b16 a[148:149], v12 offset:43520\n"
      "ds_read_b64_tr_b16 a[150:151], v12 offset:44608\n"
      "ds_read_b64_tr_b16 a[152:153], v13 offset:0\n"
      "ds_read_b64_tr_b16 a[154:155], v13 offset:1088\n"
      "ds_read_b64_tr_b16 a[156:157], v13 offset:8704\n"
      "ds_read_b64_tr_b16 a[158:159], v13 offset:9792\n"
      "ds_read_b64_tr_b16 a[160:161], v13 offset:17408\n"
      "ds_read_b64_tr_b16 a[162:163], v13 offset:18496\n"
      "ds_read_b64_tr_b16 a[164:165], v13 offset:26112\n"
      "ds_read_b64_tr_b16 a[166:167], v13 offset:27200\n"
      "s_waitcnt lgkmcnt(4)\n"
      "ds_read_b64_tr_b16 a[168:169], v13 offset:34816\n"
      "ds_read_b64_tr_b16 a[170:171], v13 offset:35904\n"
      "ds_read_b64_tr_b16 a[172:173], v13 offset:43520\n"
      "ds_read_b64_tr_b16 a[174:175], v13 offset:44608\n"
      "ds_read_b64_tr_b16 a[176:177], v14 offset:0\n"
      "ds_read_b64_tr_b16 a[178:179], v14 offset:1088\n"
      "ds_read_b64_tr_b16 a[180:181], v14 offset:8704\n"
      "ds_read_b64_tr_b16 a[182:183], v14 offset:9792\n"
      "ds_read_b64_tr_b16 a[184:185], v14 offset:17408\n"
      "ds_read_b64_tr_b16 a[186:187], v14 offset:18496\n"
      "s_waitcnt lgkmcnt(4)\n"
      "ds_read_b64_tr_b16 a[188:189], v14 offset:26112\n"
      "ds_read_b64_tr_b16 a[190:191], v14 offset:27200\n"
      "ds_read_b64_tr_b16 a[192:193], v12 offset:32\n"
      "ds_read_b64_tr_b16 a[194:195], v12 offset:1120\n"
      "ds_read_b64_tr_b16 a[196:197], v12 offset:8736\n"
      "ds_read_b64_tr_b16 a[198:199], v12 offset:9824\n"
      "ds_read_b64_tr_b16 a[200:201], v12 offset:17440\n"
      "ds_read_b64_tr_b16 a[202:203], v12 offset:18528\n"
      "ds_read_b64_tr_b16 a[204:205], v12 offset:26144\n"
      "ds_read_b64_tr_b16 a[206:207], v12 offset:27232\n"
      "s_waitcnt lgkmcnt(4)\n"
      "ds_read_b64_tr_b16 a[208:209], v12 offset:34848\n"
      "ds_read_b64_tr_b16 a[210:211], v12 offset:35936\n"
      "ds_read_b64_tr_b16 a[212:213], v12 offset:43552\n"
      "ds_read_b64_tr_b16 a[214:215], v12 offset:44640\n"
      "ds_read_b64_tr_b16 a[216:217], v13 offset:32\n"
      "ds_read_b64_tr_b16 a[218:219], v13 offset:1120\n"
      "ds_read_b64_tr_b16 a[220:221], v13 offset:8736\n"
      "ds_read_b64_tr_b16 a[222:223], v13 offset:9824\n"
      "ds_read_b64_tr_b16 a[224:225], v13 offset:17440\n"
      "ds_read_b64_tr_b16 a[226:227], v13 offset:18528\n"
      "s_waitcnt lgkmcnt(4)\n"
      "ds_read_b64_tr_b16 a[228:229], v13 offset:26144\n"
      "ds_read_b64_tr_b16 a[230:231], v13 offset:27232\n"
      "ds_read_b64_tr_b16 a[232:233], v13 offset:34848\n"
      "ds_read_b64_tr_b16 a[234:235], v13 offset:35936\n"
      "ds_read_b64_tr_b16 a[236:237], v13 offset:43552\n"
      "ds_read_b64_tr_b16 a[238:239], v13 offset:44640\n"
      "ds_read_b64_tr_b16 a[240:241], v14 offset:32\n"
      "ds_read_b64_tr_b16 a[242:243], v14 offset:1120\n"
      "ds_read_b64_tr_b16 a[244:245], v14 offset:8736\n"
      "ds_read_b64_tr_b16 a[246:247], v14 offset:9824\n"
      "s_waitcnt lgkmcnt(4)\n"
      "ds_read_b64_tr_b16 a[248:249], v14 offset:17440\n"
      "ds_read_b64_tr_b16 a[250:251], v14 offset:18528\n"
      "ds_read_b64_tr_b16 a[252:253], v14 offset:26144\n"
      "ds_read_b64_tr_b16 a[254:255], v14 offset:27232\n"
      "s_waitcnt lgkmcnt(0)\n"
      "s_barrier\n"
      "v_and_b32 v12, 7, v0\n"
      "v_mul_u32_u24 v12, 1040, v12\n"
      "v_lshrrev_b32 v13, 4, v0\n"
      "v_lshlrev_b32 v13, 4, v13\n"
      "v_add_u32 v12, v12, v13\n"
      "v_add_u32 v12, 143360, v12\n"
      "ds_read_b128 v[24:27], v12 offset:0\n"
      "ds_read_b128 v[28:31], v12 offset:64\n"
      "ds_read_b128 v[32:35], v12 offset:128\n"
      "ds_read_b128 v[36:39], v12 offset:192\n"
      "ds_read_b128 v[40:43], v12 offset:256\n"
      "ds_read_b128 v[44:47], v12 offset:320\n"
      "ds_read_b128 v[48:51], v12 offset:384\n"
      "ds_read_b128 v[52:55], v12 offset:448\n"
      "s_waitcnt lgkmcnt(7)\n"
      "v_mfma_f32_16x16x32_bf16 v[56:59], v[24:27], a[128:131], 0\n"
      "v_mfma_f32_16x16x32_bf16 v[60:63], v[24:27], a[192:195], 0\n"
      "ds_read_b128 v[24:27], v12 offset:512\n"
      "s_waitcnt lgkmcnt(7)\n"
      "v_mfma_f32_16x16x32_bf16 v[56:59], v[28:31], a[132:135], v[56:59]\n"
      "v_mfma_f32_16x16x32_bf16 v[60:63], v[28:31], a[196:199], v[60:63]\n"
      "ds_read_b128 v[28:31], v12 offset:576\n"
      "s_waitcnt lgkmcnt(7)\n"
      "v_mfma_f32_16x16x32_bf16 v[56:59], v[32:35], a[136:139], v[56:59]\n"
      "v_mfma_f32_16x16x32_bf16 v[60:63], v[32:35], a[200:203], v[60:63]\n"
      "ds_read_b128 v[32:35], v12 offset:640\n"
      "s_waitcnt lgkmcnt(7)\n"
      "v_mfma_f32_16x16x32_bf16 v[56:59], v[36:39], a[140:143], v[56:59]\n"
      "v_mfma_f32_16x16x32_bf16 v[60:63], v[36:39], a[204:207], v[60:63]\n"
      "ds_read_b128 v[36:39], v12 offset:704\n"
      "s_waitcnt lgkmcnt(7)\n"
      "v_mfma_f32_16x16x32_bf16 v[56:59], v[40:43], a[144:147], v[56:59]\n"
      "v_mfma_f32_16x16x32_bf16 v[60:63], v[40:43], a[208:211], v[60:63]\n"
      "ds_read_b128 v[40:43], v12 offset:768\n"
      "s_waitcnt lgkmcnt(7)\n"
      "v_mfma_f32_16x16x32_bf16 v[56:59], v[44:47], a[148:151], v[56:59]\n"
      "v_mfma_f32_16x16x32_bf16 v[60:63], v[44:47], a[212:215], v[60:63]\n"
      "ds_read_b128 v[44:47], v12 offset:832\n"
      "s_waitcnt lgkmcnt(7)\n"
      "v_mfma_f32_16x16x32_bf16 v[56:59], v[48:51], a[152:155], v[56:59]\n"
      "v_mfma_f32_16x16x32_bf16 v[60:63], v[48:51], a[216:219], v[60:63]\n"
      "ds_read_b128 v[48:51], v12 offset:896\n"
      "s_waitcnt lgkmcnt(7)\n"
      "v_mfma_f32_16x16x32_bf16 v[56:59], v[52:55], a[156:159], v[56:59]\n"
      "v_mfma_f32_16x16x32_bf16 v[60:63], v[52:55], a[220:223], v[60:63]\n"
      "ds_read_b128 v[52:55], v12 offset:960\n"
      "s_waitcnt lgkmcnt(7)\n"
      "v_mfma_f32_16x16x32_bf16 v[56:59], v[24:27], a[160:163], v[56:59]\n"
      "v_mfma_f32_16x16x32_bf16 v[60:63], v[24:27], a[224:227], v[60:63]\n"
      "s_waitcnt lgkmcnt(6)\n"
      "v_mfma_f32_16x16x32_bf16 v[56:59], v[28:31], a[164:167], v[56:59]\n"
      "v_mfma_f32_16x16x32_bf16 v[60:63], v[28:31], a[228:231], v[60:63]\n"
      "s_waitcnt lgkmcnt(5)\n"
      "v_mfma_f32_16x16x32_bf16 v[56:59], v[32:35], a[168:171], v[56:59]\n"
      "v_mfma_f32_16x16x32_bf16 v[60:63], v[32:35], a[232:235], v[60:63]\n"
      "s_waitcnt lgkmcnt(4)\n"
      "v_mfma_f32_16x16x32_bf16 v[56:59], v[36:39], a[172:175], v[56:59]\n"
      "v_mfma_f32_16x16x32_bf16 v[60:63], v[36:39], a[236:239], v[60:63]\n"
      "s_waitcnt lgkmcnt(3)\n"
      "v_mfma_f32_16x16x32_bf16 v[56:59], v[40:43], a[176:179], v[56:59]\n"
      "v_mfma_f32_16x16x32_bf16 v[60:63], v[40:43], a[240:243], v[60:63]\n"
      "s_waitcnt lgkmcnt(2)\n"
      "v_mfma_f32_16x16x32_bf16 v[56:59], v[44:47], a[180:183], v[56:59]\n"
      "v_mfma_f32_16x16x32_bf16 v[60:63], v[44:47], a[244:247], v[60:63]\n"
      "s_waitcnt lgkmcnt(1)\n"
      "v_mfma_f32_16x16x32_bf16 v[56:59], v[48:51], a[184:187], v[56:59]\n"
      "v_mfma_f32_16x16x32_bf16 v[60:63], v[48:51], a[248:251], v[60:63]\n"
      "s_waitcnt lgkmcnt(0)\n"
      "v_mfma_f32_16x16x32_bf16 v[56:59], v[52:55], a[188:191], v[56:59]\n"
      "v_mfma_f32_16x16x32_bf16 v[60:63], v[52:55], a[252:255], v[60:63]\n"
      "v_lshrrev_b32 v40, 4, v0\n"
      "v_lshlrev_b32 v40, 15, v40\n"
      "v_and_b32 v41, 15, v0\n"
      "v_lshrrev_b32 v41, 1, v41\n"
      "v_lshlrev_b32 v41, 2, v41\n"
      "v_add_u32 v40, v40, v41\n"
      "s_lshl_b32 s65, s56, 8\n"
      "s_lshl_b32 s66, s53, 6\n"
      "s_add_u32 s65, s65, s66\n"
      "s_add_u32 s65, s65, 528384\n"
      "v_mul_f32 v32, 0x3e0293ee, v56\n"
      "s_nop 1\n"
      "v_mov_b32_dpp v13, v32 quad_perm:[1,0,3,2] row_mask:0xf bank_mask:0xf\n"
      "v_cvt_pk_bf16_f32 v32, v32, v13\n"
      "v_min_u32 v32, -2, v32\n"
      "s_add_u32 s66, s65, 0\n"
      "s_add_u32 s50, s40, s66\n"
      "s_addc_u32 s51, s41, 0\n"
      "s_mov_b32 exec_lo, 0x55555555\n"
      "s_mov_b32 exec_hi, 0x0\n"
      "global_store_dword v40, v32, s[50:51] sc1\n"
      "s_mov_b64 exec, -1\n"
      "v_mul_f32 v32, 0x3e0293ee, v57\n"
      "s_nop 1\n"
      "v_mov_b32_dpp v13, v32 quad_perm:[1,0,3,2] row_mask:0xf bank_mask:0xf\n"
      "v_cvt_pk_bf16_f32 v32, v32, v13\n"
      "v_min_u32 v32, -2, v32\n"
      "s_add_u32 s66, s65, 8192\n"
      "s_add_u32 s50, s40, s66\n"
      "s_addc_u32 s51, s41, 0\n"
      "s_mov_b32 exec_lo, 0x55555555\n"
      "s_mov_b32 exec_hi, 0x0\n"
      "global_store_dword v40, v32, s[50:51] sc1\n"
      "s_mov_b64 exec, -1\n"
      "v_mul_f32 v32, 0x3e0293ee, v58\n"
      "s_nop 1\n"
      "v_mov_b32_dpp v13, v32 quad_perm:[1,0,3,2] row_mask:0xf bank_mask:0xf\n"
      "v_cvt_pk_bf16_f32 v32, v32, v13\n"
      "v_min_u32 v32, -2, v32\n"
      "s_add_u32 s66, s65, 16384\n"
      "s_add_u32 s50, s40, s66\n"
      "s_addc_u32 s51, s41, 0\n"
      "s_mov_b32 exec_lo, 0x55555555\n"
      "s_mov_b32 exec_hi, 0x0\n"
      "global_store_dword v40, v32, s[50:51] sc1\n"
      "s_mov_b64 exec, -1\n"
      "v_mul_f32 v32, 0x3e0293ee, v59\n"
      "s_nop 1\n"
      "v_mov_b32_dpp v13, v32 quad_perm:[1,0,3,2] row_mask:0xf bank_mask:0xf\n"
      "v_cvt_pk_bf16_f32 v32, v32, v13\n"
      "v_min_u32 v32, -2, v32\n"
      "s_add_u32 s66, s65, 24576\n"
      "s_add_u32 s50, s40, s66\n"
      "s_addc_u32 s51, s41, 0\n"
      "s_mov_b32 exec_lo, 0x55555555\n"
      "s_mov_b32 exec_hi, 0x0\n"
      "global_store_dword v40, v32, s[50:51] sc1\n"
      "s_mov_b64 exec, -1\n"
      "v_mul_f32 v32, 0x3e0293ee, v60\n"
      "s_nop 1\n"
      "v_mov_b32_dpp v13, v32 quad_perm:[1,0,3,2] row_mask:0xf bank_mask:0xf\n"
      "v_cvt_pk_bf16_f32 v32, v32, v13\n"
      "v_min_u32 v32, -2, v32\n"
      "s_add_u32 s66, s65, 32\n"
      "s_add_u32 s50, s40, s66\n"
      "s_addc_u32 s51, s41, 0\n"
      "s_mov_b32 exec_lo, 0x55555555\n"
      "s_mov_b32 exec_hi, 0x0\n"
      "global_store_dword v40, v32, s[50:51] sc1\n"
      "s_mov_b64 exec, -1\n"
      "v_mul_f32 v32, 0x3e0293ee, v61\n"
      "s_nop 1\n"
      "v_mov_b32_dpp v13, v32 quad_perm:[1,0,3,2] row_mask:0xf bank_mask:0xf\n"
      "v_cvt_pk_bf16_f32 v32, v32, v13\n"
      "v_min_u32 v32, -2, v32\n"
      "s_add_u32 s66, s65, 8224\n"
      "s_add_u32 s50, s40, s66\n"
      "s_addc_u32 s51, s41, 0\n"
      "s_mov_b32 exec_lo, 0x55555555\n"
      "s_mov_b32 exec_hi, 0x0\n"
      "global_store_dword v40, v32, s[50:51] sc1\n"
      "s_mov_b64 exec, -1\n"
      "v_mul_f32 v32, 0x3e0293ee, v62\n"
      "s_nop 1\n"
      "v_mov_b32_dpp v13, v32 quad_perm:[1,0,3,2] row_mask:0xf bank_mask:0xf\n"
      "v_cvt_pk_bf16_f32 v32, v32, v13\n"
      "v_min_u32 v32, -2, v32\n"
      "s_add_u32 s66, s65, 16416\n"
      "s_add_u32 s50, s40, s66\n"
      "s_addc_u32 s51, s41, 0\n"
      "s_mov_b32 exec_lo, 0x55555555\n"
      "s_mov_b32 exec_hi, 0x0\n"
      "global_store_dword v40, v32, s[50:51] sc1\n"
      "s_mov_b64 exec, -1\n"
      "v_mul_f32 v32, 0x3e0293ee, v63\n"
      "s_nop 1\n"
      "v_mov_b32_dpp v13, v32 quad_perm:[1,0,3,2] row_mask:0xf bank_mask:0xf\n"
      "v_cvt_pk_bf16_f32 v32, v32, v13\n"
      "v_min_u32 v32, -2, v32\n"
      "s_add_u32 s66, s65, 24608\n"
      "s_add_u32 s50, s40, s66\n"
      "s_addc_u32 s51, s41, 0\n"
      "s_mov_b32 exec_lo, 0x55555555\n"
      "s_mov_b32 exec_hi, 0x0\n"
      "global_store_dword v40, v32, s[50:51] sc1\n"
      "s_mov_b64 exec, -1\n"
      "s_waitcnt lgkmcnt(0)\n"
      "s_barrier\n"
      "s_branch C_wdone9_%=\n"
      "C_wr13_%=:\n"
      "C_wdone9_%=:\n"
      "v_mov_b32 v11, 0xff61b1e6\n"
      "v_lshlrev_b32 v9, 2, v0\n"
      "v_lshrrev_b32 v10, 1, v0\n"
      "v_lshlrev_b32 v10, 2, v10\n"
      "v_lshrrev_b32 v6, 4, v0\n"
      "v_lshlrev_b32 v6, 4, v6\n"
      "s_and_b32 s65, s53, 1\n"
      "s_lshl_b32 s65, s65, 7\n"
      "v_and_b32 v3, 31, v0\n"
      "v_lshlrev_b32 v3, 2, v3\n"
      "v_add_u32 v3, s65, v3\n"
      "v_lshrrev_b32 v5, 5, v0\n"
      "v_mul_u32_u24 v2, 384, v5\n"
      "v_add_u32 v2, v2, v3\n"
      "v_lshlrev_b32 v5, 2, v5\n"
      "v_min_u32 v4, 26, v0\n"
      "v_mul_u32_u24 v4, 384, v4\n"
      "v_add_u32 v4, 256, v4\n"
      "v_lshrrev_b32 v7, 3, v0\n"
      "v_lshlrev_b32 v7, 10, v7\n"
      "v_and_b32 v13, 7, v0\n"
      "v_lshlrev_b32 v13, 4, v13\n"
      "v_add_u32 v7, v7, v13\n"
      "v_add_u32 v8, 8192, v7\n"
      "v_cndmask_b32 v8, v8, v7, s[96:97]\n"
      "s_cmp_lt_u32 s55, 2\n"
      "s_cbranch_scc0 C_nres14_%=\n"
      "s_lshl_b32 s65, s53, 11\n"
      "s_add_u32 s65, s65, 0\n"
      "s_add_u32 s46, s42, s65\n"
      "s_addc_u32 s47, s43, 0\n"
      "global_load_dwordx4 v[16:19], v1, s[46:47]\n"
      "s_waitcnt vmcnt(0)\n"
      "v_lshlrev_b32 v32, 16, v16\n"
      "v_and_b32 v33, 0xffff0000, v16\n"
      "v_lshlrev_b32 v34, 16, v17\n"
      "v_and_b32 v35, 0xffff0000, v17\n"
      "v_lshlrev_b32 v36, 16, v18\n"
      "v_and_b32 v37, 0xffff0000, v18\n"
      "v_lshlrev_b32 v38, 16, v19\n"
      "v_and_b32 v39, 0xffff0000, v19\n"
      "s_lshl_b32 s65, s53, 12\n"
      "s_add_u32 s65, s65, 32768\n"
      "v_lshlrev_b32 v40, 1, v1\n"
      "v_add_u32 v40, s65, v40\n"
      "ds_write_b128 v40, v[32:35]\n"
      "ds_write_b128 v40, v[36:39] offset:16\n"
      "s_lshl_b32 s65, s53, 11\n"
      "s_add_u32 s65, s65, 1024\n"
      "s_add_u32 s46, s42, s65\n"
      "s_addc_u32 s47, s43, 0\n"
      "global_load_dwordx4 v[16:19], v1, s[46:47]\n"
      "s_waitcnt vmcnt(0)\n"
      "v_lshlrev_b32 v32, 16, v16\n"
      "v_and_b32 v33, 0xffff0000, v16\n"
      "v_lshlrev_b32 v34, 16, v17\n"
      "v_and_b32 v35, 0xffff0000, v17\n"
      "v_lshlrev_b32 v36, 16, v18\n"
      "v_and_b32 v37, 0xffff0000, v18\n"
      "v_lshlrev_b32 v38, 16, v19\n"
      "v_and_b32 v39, 0xffff0000, v19\n"
      "s_lshl_b32 s65, s53, 12\n"
      "s_add_u32 s65, s65, 34816\n"
      "v_lshlrev_b32 v40, 1, v1\n"
      "v_add_u32 v40, s65, v40\n"
      "ds_write_b128 v40, v[32:35]\n"
      "ds_write_b128 v40, v[36:39] offset:16\n"
      "C_nres14_%=:\n"
      "s_waitcnt lgkmcnt(0)\n"
      "s_barrier\n"
      "s_mov_b32 s63, 0\n"
      "v_min_u32 v32, 26, v0\n"
      "v_lshlrev_b32 v32, 2, v32\n"
      "s_and_b32 s65, s52, 7\n"
      "s_lshl_b32 s65, s65, 8\n"
      "v_add_u32 v32, s65, v32\n"
      "v_lshrrev_b32 v33, 3, v0\n"
      "v_lshlrev_b32 v33, 8, v33\n"
      "v_and_b32 v34, 7, v0\n"
      "v_min_u32 v34, 4, v34\n"
      "v_lshlrev_b32 v34, 2, v34\n"
      "v_add_u32 v33, v33, v34\n"
      "v_add_u32 v33, 128, v33\n"
      "s_add_u32 s46, s40, 1024\n"
      "s_addc_u32 s47, s41, 0\n"
      "C_fl15_%=:\n"
      "global_load_dword v16, v32, s[46:47] sc1\n"
      "global_load_dword v17, v33, s[46:47] sc1\n"
      "s_waitcnt vmcnt(0)\n"
      "v_xor_b32 v16, s58, v16\n"
      "v_xor_b32 v17, s58, v17\n"
      "v_or_b32 v16, v16, v17\n"
      "v_cmp_ne_u32 vcc, 0, v16\n"
      "s_nop 0\n"
      "s_cbranch_vccz C_fo16_%=\n"
      "s_sleep 4\n"
      "s_add_u32 s63, s63, 1\n"
      "s_cmp_lt_u32 s63, 30000\n"
      "s_cbranch_scc1 C_fl15_%=\n"
      "s_mov_b32 s64, 1\n"
      "v_mov_b32 v32, 256\n"
      "v_mov_b32 v33, 800\n"
      "global_store_dword v32, v33, s[40:41] sc1\n"
      "C_fo16_%=:\n"
      "s_getpc_b64 s[42:43]\n"
      "C_pc_%=:\n"
      "s_add_u32 s36, s42, C_issue_%=-C_pc_%=\n"
      "s_addc_u32 s37, s43, 0\n"
      "s_add_u32 s80, s42, C_s1_%=-C_pc_%=\n"
      "s_addc_u32 s81, s43, 0\n"
      "s_add_u32 s82, s42, C_s4_%=-C_pc_%=\n"
      "s_addc_u32 s83, s43, 0\n"
      "s_add_u32 s84, s42, C_fin_%=-C_pc_%=\n"
      "s_addc_u32 s85, s43, 0\n"
      "s_add_u32 s86, s42, C_s23A_%=-C_pc_%=\n"
      "s_addc_u32 s87, s43, 0\n"
      "s_add_u32 s46, s42, C_s1b_%=-C_pc_%=\n"
      "s_addc_u32 s47, s43, 0\n"
      "s_add_u32 s26, s42, C_issueB_%=-C_pc_%=\n"
      "s_addc_u32 s27, s43, 0\n"
      "s_cmp_eq_u32 s54, 1\n"
      "s_cselect_b64 s[80:81], s[46:47], s[80:81]\n"
      "s_mov_b32 s23, 0\n"
      "s_mov_b32 s59, -1\n"
      "s_and_b32 s61, s70, 0xff\n"
      "s_mov_b32 s68, s61\n"
      "s_swappc_b64 s[34:35], s[36:37]\n"
      "s_branch C_next0_%=\n"
      "C_next0_%=:\n"
      "s_add_u32 s59, s59, 1\n"
      "s_mov_b32 s60, s61\n"
      "s_cmp_lt_u32 s59, s78\n"
      "s_cbranch_scc0 C_done_%=\n"
      "s_add_u32 s65, s59, 1\n"
      "s_lshr_b32 s66, s65, 2\n"
      "s_and_b32 s65, s65, 3\n"
      "s_lshl_b32 s65, s65, 3\n"
      "s_mov_b32 s61, -1\n"
      "s_cmp_eq_u32 s66, 0\n"
      "s_cselect_b32 s61, s70, s61\n"
      "s_cmp_eq_u32 s66, 1\n"
      "s_cselect_b32 s61, s71, s61\n"
      "s_cmp_eq_u32 s66, 2\n"
      "s_cselect_b32 s61, s72, s61\n"
      "s_cmp_eq_u32 s66, 3\n"
      "s_cselect_b32 s61, s73, s61\n"
      "s_cmp_eq_u32 s66, 4\n"
      "s_cselect_b32 s61, s74, s61\n"
      "s_cmp_eq_u32 s66, 5\n"
      "s_cselect_b32 s61, s75, s61\n"
      "s_cmp_eq_u32 s66, 6\n"
      "s_cselect_b32 s61, s76, s61\n"
      "s_cmp_eq_u32 s66, 7\n"
      "s_cselect_b32 s61, s77, s61\n"
      "s_lshr_b32 s61, s61, s65\n"
      "s_and_b32 s61, s61, 0xff\n"
      "s_and_b32 s62, s60, 15\n"
      "s_and_b32 s65, s59, 1\n"
      "s_lshl_b32 s66, s65, 9\n"
      "s_add_u32 s79, s66, 12288\n"
      "s_lshl_b32 s39, s65, 16\n"
      "s_lshr_b32 s65, s60, 4\n"
      "s_cmp_eq_u32 s65, 0\n"
      "s_cselect_b64 s[42:43], s[80:81], s[86:87]\n"
      "s_cmp_eq_u32 s65, 2\n"
      "s_cselect_b64 s[42:43], s[82:83], s[42:43]\n"
      "s_cmp_eq_u32 s65, 3\n"
      "s_cselect_b64 s[42:43], s[84:85], s[42:43]\n"
      "s_mov_b32 s63, 0\n"
      "s_waitcnt vmcnt(0)\n"
      "s_branch C_chk_%=\n"
      "C_next1_%=:\n"
      "s_add_u32 s59, s59, 1\n"
      "s_mov_b32 s60, s61\n"
      "s_cmp_lt_u32 s59, s78\n"
      "s_cbranch_scc0 C_done_%=\n"
      "s_add_u32 s65, s59, 1\n"
      "s_lshr_b32 s66, s65, 2\n"
      "s_and_b32 s65, s65, 3\n"
      "s_lshl_b32 s65, s65, 3\n"
      "s_mov_b32 s61, -1\n"
      "s_cmp_eq_u32 s66, 0\n"
      "s_cselect_b32 s61, s70, s61\n"
      "s_cmp_eq_u32 s66, 1\n"
      "s_cselect_b32 s61, s71, s61\n"
      "s_cmp_eq_u32 s66, 2\n"
      "s_cselect_b32 s61, s72, s61\n"
      "s_cmp_eq_u32 s66, 3\n"
      "s_cselect_b32 s61, s73, s61\n"
      "s_cmp_eq_u32 s66, 4\n"
      "s_cselect_b32 s61, s74, s61\n"
      "s_cmp_eq_u32 s66, 5\n"
      "s_cselect_b32 s61, s75, s61\n"
      "s_cmp_eq_u32 s66, 6\n"
      "s_cselect_b32 s61, s76, s61\n"
      "s_cmp_eq_u32 s66, 7\n"
      "s_cselect_b32 s61, s77, s61\n"
      "s_lshr_b32 s61, s61, s65\n"
      "s_and_b32 s61, s61, 0xff\n"
      "s_and_b32 s62, s60, 15\n"
      "s_and_b32 s65, s59, 1\n"
      "s_lshl_b32 s66, s65, 9\n"
      "s_add_u32 s79, s66, 12288\n"
      "s_lshl_b32 s39, s65, 16\n"
      "s_lshr_b32 s65, s60, 4\n"
      "s_cmp_eq_u32 s65, 0\n"
      "s_cselect_b64 s[42:43], s[80:81], s[86:87]\n"
      "s_cmp_eq_u32 s65, 2\n"
      "s_cselect_b64 s[42:43], s[82:83], s[42:43]\n"
      "s_cmp_eq_u32 s65, 3\n"
      "s_cselect_b64 s[42:43], s[84:85], s[42:43]\n"
      "s_mov_b32 s63, 0\n"
      "s_waitcnt vmcnt(1)\n"
      "s_branch C_chk_%=\n"
      "C_next2_%=:\n"
      "s_add_u32 s59, s59, 1\n"
      "s_mov_b32 s60, s61\n"
      "s_cmp_lt_u32 s59, s78\n"
      "s_cbranch_scc0 C_done_%=\n"
      "s_add_u32 s65, s59, 1\n"
      "s_lshr_b32 s66, s65, 2\n"
      "s_and_b32 s65, s65, 3\n"
      "s_lshl_b32 s65, s65, 3\n"
      "s_mov_b32 s61, -1\n"
      "s_cmp_eq_u32 s66, 0\n"
      "s_cselect_b32 s61, s70, s61\n"
      "s_cmp_eq_u32 s66, 1\n"
      "s_cselect_b32 s61, s71, s61\n"
      "s_cmp_eq_u32 s66, 2\n"
      "s_cselect_b32 s61, s72, s61\n"
      "s_cmp_eq_u32 s66, 3\n"
      "s_cselect_b32 s61, s73, s61\n"
      "s_cmp_eq_u32 s66, 4\n"
      "s_cselect_b32 s61, s74, s61\n"
      "s_cmp_eq_u32 s66, 5\n"
      "s_cselect_b32 s61, s75, s61\n"
      "s_cmp_eq_u32 s66, 6\n"
      "s_cselect_b32 s61, s76, s61\n"
      "s_cmp_eq_u32 s66, 7\n"
      "s_cselect_b32 s61, s77, s61\n"
      "s_lshr_b32 s61, s61, s65\n"
      "s_and_b32 s61, s61, 0xff\n"
      "s_and_b32 s62, s60, 15\n"
      "s_and_b32 s65, s59, 1\n"
      "s_lshl_b32 s66, s65, 9\n"
      "s_add_u32 s79, s66, 12288\n"
      "s_lshl_b32 s39, s65, 16\n"
      "s_lshr_b32 s65, s60, 4\n"
      "s_cmp_eq_u32 s65, 0\n"
      "s_cselect_b64 s[42:43], s[80:81], s[86:87]\n"
      "s_cmp_eq_u32 s65, 2\n"
      "s_cselect_b64 s[42:43], s[82:83], s[42:43]\n"
      "s_cmp_eq_u32 s65, 3\n"
      "s_cselect_b64 s[42:43], s[84:85], s[42:43]\n"
      "s_mov_b32 s63, 0\n"
      "s_waitcnt vmcnt(2)\n"
      "C_chk_%=:\n"
      "v_max3_u32 v32, v16, v17, v18\n"
      "v_max3_u32 v33, v19, v20, v21\n"
      "v_max3_u32 v34, v22, v23, v24\n"
      "v_max3_u32 v35, v25, v26, v27\n"
      "v_max3_u32 v32, v32, v28, v29\n"
      "v_max3_u32 v33, v33, v30, v31\n"
      "v_max3_u32 v32, v32, v33, v34\n"
      "v_max_u32 v32, v32, v35\n"
      "v_cmp_eq_u32 vcc, -1, v32\n"
      "s_nop 0\n"
      "s_cbranch_vccnz C_re_%=\n"
      "s_setpc_b64 s[42:43]\n"
      "C_re_%=:\n"
      "s_cmp_lg_u32 s64, 0\n"
      "s_cbranch_scc1 C_go_%=\n"
      "s_cmp_eq_u32 s62, 7\n"
      "s_cbranch_scc1 C_dual_%=\n"
      "s_cmp_ge_u32 s62, 12\n"
      "s_cbranch_scc1 C_dual_%=\n"
      "s_lshr_b32 s65, s60, 4\n"
      "s_cmp_eq_u32 s65, 3\n"
      "s_cbranch_scc1 C_pf19_%=\n"
      "s_cmp_eq_u32 s62, 7\n"
      "s_cbranch_scc1 C_pf19_%=\n"
      "s_cmp_ge_u32 s62, 12\n"
      "s_cbranch_scc1 C_pf19_%=\n"
      "C_pl18_%=:\n"
      "s_lshr_b32 s65, s60, 4\n"
      "s_cmp_eq_u32 s65, 0\n"
      "s_cbranch_scc0 C_pg20_%=\n"
      "v_cmp_ne_u32 vcc, -1, v30\n"
      "s_nop 0\n"
      "s_and_b64 vcc, vcc, s[92:93]\n"
      "s_branch C_ps21_%=\n"
      "C_pg20_%=:\n"
      "v_min_u32 v32, v16, v20\n"
      "v_cmp_ne_u32 vcc, -1, v32\n"
      "s_nop 0\n"
      "s_and_b64 vcc, vcc, exec\n"
      "C_ps21_%=:\n"
      "s_cbranch_scc1 C_pf19_%=\n"
      "s_add_u32 s63, s63, 1\n"
      "s_cmp_lt_u32 s63, 30000\n"
      "s_cbranch_scc0 C_pf19_%=\n"
      "s_sleep 1\n"
      "s_mov_b32 s23, 1\n"
      "s_mov_b32 s68, s60\n"
      "s_swappc_b64 s[34:35], s[36:37]\n"
      "s_waitcnt vmcnt(0)\n"
      "s_branch C_pl18_%=\n"
      "C_pf19_%=:\n"
      "s_mov_b32 s23, 0\n"
      "s_mov_b32 s68, s60\n"
      "s_swappc_b64 s[34:35], s[36:37]\n"
      "s_mov_b32 s23, 0\n"
      "s_add_u32 s63, s63, 1\n"
      "s_waitcnt vmcnt(0)\n"
      "s_cmp_lt_u32 s63, 30000\n"
      "s_cbranch_scc1 C_chk_%=\n"
      "s_mov_b32 s64, 1\n"
      "s_add_u32 s65, s60, 1000\n"
      "v_mov_b32 v32, 256\n"
      "v_mov_b32 v33, s65\n"
      "global_store_dword v32, v33, s[40:41] sc1\n"
      "C_go_%=:\n"
      "s_setpc_b64 s[42:43]\n"
      "C_dual_%=:\n"
      "s_lshr_b32 s65, s60, 4\n"
      "s_cmp_eq_u32 s65, 0\n"
      "s_cbranch_scc1 C_du15_%=\n"
      "s_cmp_eq_u32 s65, 3\n"
      "s_cbranch_scc1 C_du2_%=\n"
      "C_du4_%=:\n"
      "s_mov_b32 s68, s60\n"
      "s_swappc_b64 s[34:35], s[26:27]\n"
      "s_sleep 4\n"
      "s_mov_b32 s68, s60\n"
      "s_swappc_b64 s[34:35], s[36:37]\n"
      "C_dl4_%=:\n"
      "s_waitcnt vmcnt(4)\n"
      "v_max3_u32 v32, v112, v113, v114\n"
      "v_max3_u32 v33, v115, v116, v117\n"
      "v_max3_u32 v34, v118, v119, v120\n"
      "v_max3_u32 v35, v121, v122, v123\n"
      "v_max3_u32 v32, v32, v124, v125\n"
      "v_max3_u32 v33, v33, v126, v127\n"
      "v_max3_u32 v32, v32, v33, v34\n"
      "v_max_u32 v32, v32, v35\n"
      "v_cmp_eq_u32 vcc, -1, v32\n"
      "s_nop 0\n"
      "s_cbranch_vccz C_dokB_%=\n"
      "s_mov_b32 s68, s60\n"
      "s_swappc_b64 s[34:35], s[26:27]\n"
      "s_waitcnt vmcnt(4)\n"
      "v_max3_u32 v32, v16, v17, v18\n"
      "v_max3_u32 v33, v19, v20, v21\n"
      "v_max3_u32 v34, v22, v23, v24\n"
      "v_max3_u32 v35, v25, v26, v27\n"
      "v_max3_u32 v32, v32, v28, v29\n"
      "v_max3_u32 v33, v33, v30, v31\n"
      "v_max3_u32 v32, v32, v33, v34\n"
      "v_max_u32 v32, v32, v35\n"
      "v_cmp_eq_u32 vcc, -1, v32\n"
      "s_nop 0\n"
      "s_cbranch_vccz C_go_%=\n"
      "s_mov_b32 s68, s60\n"
      "s_swappc_b64 s[34:35], s[36:37]\n"
      "s_add_u32 s63, s63, 1\n"
      "s_cmp_lt_u32 s63, 30000\n"
      "s_cbranch_scc1 C_dl4_%=\n"
      "s_waitcnt vmcnt(0)\n"
      "s_mov_b32 s64, 1\n"
      "s_add_u32 s65, s60, 1000\n"
      "v_mov_b32 v32, 256\n"
      "v_mov_b32 v33, s65\n"
      "global_store_dword v32, v33, s[40:41] sc1\n"
      "s_branch C_go_%=\n"
      "C_du15_%=:\n"
      "s_mov_b32 s68, s60\n"
      "s_swappc_b64 s[34:35], s[26:27]\n"
      "s_sleep 4\n"
      "s_mov_b32 s68, s60\n"
      "s_swappc_b64 s[34:35], s[36:37]\n"
      "C_dl15_%=:\n"
      "s_waitcnt vmcnt(15)\n"
      "v_max3_u32 v32, v112, v113, v114\n"
      "v_max3_u32 v33, v115, v116, v117\n"
      "v_max3_u32 v34, v118, v119, v120\n"
      "v_max3_u32 v35, v121, v122, v123\n"
      "v_max3_u32 v32, v32, v124, v125\n"
      "v_max3_u32 v33, v33, v126, v127\n"
      "v_max3_u32 v32, v32, v33, v34\n"
      "v_max_u32 v32, v32, v35\n"
      "v_cmp_eq_u32 vcc, -1, v32\n"
      "s_nop 0\n"
      "s_cbranch_vccz C_dokB_%=\n"
      "s_mov_b32 s68, s60\n"
      "s_swappc_b64 s[34:35], s[26:27]\n"
      "s_waitcnt vmcnt(15)\n"
      "v_max3_u32 v32, v16, v17, v18\n"
      "v_max3_u32 v33, v19, v20, v21\n"
      "v_max3_u32 v34, v22, v23, v24\n"
      "v_max3_u32 v35, v25, v26, v27\n"
      "v_max3_u32 v32, v32, v28, v29\n"
      "v_max3_u32 v33, v33, v30, v31\n"
      "v_max3_u32 v32, v32, v33, v34\n"
      "v_max_u32 v32, v32, v35\n"
      "v_cmp_eq_u32 vcc, -1, v32\n"
      "s_nop 0\n"
      "s_cbranch_vccz C_go_%=\n"
      "s_mov_b32 s68, s60\n"
      "s_swappc_b64 s[34:35], s[36:37]\n"
      "s_add_u32 s63, s63, 1\n"
      "s_cmp_lt_u32 s63, 30000\n"
      "s_cbranch_scc1 C_dl15_%=\n"
      "s_waitcnt vmcnt(0)\n"
      "s_mov_b32 s64, 1\n"
      "s_add_u32 s65, s60, 1000\n"
      "v_mov_b32 v32, 256\n"
      "v_mov_b32 v33, s65\n"
      "global_store_dword v32, v33, s[40:41] sc1\n"
      "s_branch C_go_%=\n"
      "C_du2_%=:\n"
      "s_mov_b32 s68, s60\n"
      "s_swappc_b64 s[34:35], s[26:27]\n"
      "s_sleep 4\n"
      "s_mov_b32 s68, s60\n"
      "s_swappc_b64 s[34:35], s[36:37]\n"
      "C_dl2_%=:\n"
      "s_waitcnt vmcnt(2)\n"
      "v_max3_u32 v32, v112, v113, v114\n"
      "v_max3_u32 v33, v115, v116, v117\n"
      "v_max3_u32 v34, v118, v119, v120\n"
      "v_max3_u32 v35, v121, v122, v123\n"
      "v_max3_u32 v32, v32, v124, v125\n"
      "v_max3_u32 v33, v33, v126, v127\n"
      "v_max3_u32 v32, v32, v33, v34\n"
      "v_max_u32 v32, v32, v35\n"
      "v_cmp_eq_u32 vcc, -1, v32\n"
      "s_nop 0\n"
      "s_cbranch_vccz C_dokB_%=\n"
      "s_mov_b32 s68, s60\n"
      "s_swappc_b64 s[34:35], s[26:27]\n"
      "s_waitcnt vmcnt(2)\n"
      "v_max3_u32 v32, v16, v17, v18\n"
      "v_max3_u32 v33, v19, v20, v21\n"
      "v_max3_u32 v34, v22, v23, v24\n"
      "v_max3_u32 v35, v25, v26, v27\n"
      "v_max3_u32 v32, v32, v28, v29\n"
      "v_max3_u32 v33, v33, v30, v31\n"
      "v_max3_u32 v32, v32, v33, v34\n"
      "v_max_u32 v32, v32, v35\n"
      "v_cmp_eq_u32 vcc, -1, v32\n"
      "s_nop 0\n"
      "s_cbranch_vccz C_go_%=\n"
      "s_mov_b32 s68, s60\n"
      "s_swappc_b64 s[34:35], s[36:37]\n"
      "s_add_u32 s63, s63, 1\n"
      "s_cmp_lt_u32 s63, 30000\n"
      "s_cbranch_scc1 C_dl2_%=\n"
      "s_waitcnt vmcnt(0)\n"
      "s_mov_b32 s64, 1\n"
      "s_add_u32 s65, s60, 1000\n"
      "v_mov_b32 v32, 256\n"
      "v_mov_b32 v33, s65\n"
      "global_store_dword v32, v33, s[40:41] sc1\n"
      "s_branch C_go_%=\n"
      "C_dokB_%=:\n"
      "v_mov_b32 v16, v112\n"
      "v_mov_b32 v17, v113\n"
      "v_mov_b32 v18, v114\n"
      "v_mov_b32 v19, v115\n"
      "v_mov_b32 v20, v116\n"
      "v_mov_b32 v21, v117\n"
      "v_mov_b32 v22, v118\n"
      "v_mov_b32 v23, v119\n"
      "v_mov_b32 v24, v120\n"
      "v_mov_b32 v25, v121\n"
      "v_mov_b32 v26, v122\n"
      "v_mov_b32 v27, v123\n"
      "v_mov_b32 v28, v124\n"
      "v_mov_b32 v29, v125\n"
      "v_mov_b32 v30, v126\n"
      "v_mov_b32 v31, v127\n"
      "s_setpc_b64 s[42:43]\n"
      "C_done_%=:\n"
      "s_cmp_eq_u32 s52, 38\n"
      "s_cbranch_scc0 C_end22_%=\n"
      "s_cmp_eq_u32 s53, 0\n"
      "s_cbranch_scc0 C_end22_%=\n"
      "v_mov_b32 v32, 0\n"
      "v_mov_b32 v33, s58\n"
      "s_mov_b64 exec, 1\n"
      "global_store_dword v32, v33, s[40:41] sc1\n"
      "s_mov_b64 exec, -1\n"
      "C_end22_%=:\n"
      "s_waitcnt vmcnt(0)\n"
      "s_branch C_exit_%=\n"
      "C_s1_%=:\n"
      "v_cndmask_b32 v32, v11, v30, s[92:93]\n"
      "v_mov_b32 v33, v32\n"
      "v_lshlrev_b32 v64, 16, v16\n"
      "v_and_b32 v65, 0xffff0000, v16\n"
      "v_max_f32_dpp v33, v33, v33 quad_perm:[1,0,3,2] row_mask:0xf bank_mask:0xf\n"
      "v_lshlrev_b32 v66, 16, v17\n"
      "v_and_b32 v67, 0xffff0000, v17\n"
      "v_max_f32_dpp v33, v33, v33 quad_perm:[2,3,0,1] row_mask:0xf bank_mask:0xf\n"
      "v_lshlrev_b32 v68, 16, v18\n"
      "v_and_b32 v69, 0xffff0000, v18\n"
      "v_max_f32_dpp v33, v33, v33 row_half_mirror row_mask:0xf bank_mask:0xf\n"
      "v_lshlrev_b32 v70, 16, v19\n"
      "v_and_b32 v71, 0xffff0000, v19\n"
      "v_max_f32_dpp v33, v33, v33 row_ror:8 row_mask:0xf bank_mask:0xf\n"
      "v_mov_b32 v12, v33\n"
      "v_lshlrev_b32 v72, 16, v20\n"
      "v_and_b32 v73, 0xffff0000, v20\n"
      "v_permlane16_swap_b32 v12, v33\n"
      "v_max_f32 v33, v12, v33\n"
      "v_mov_b32 v12, v33\n"
      "v_lshlrev_b32 v74, 16, v21\n"
      "v_and_b32 v75, 0xffff0000, v21\n"
      "v_permlane32_swap_b32 v12, v33\n"
      "v_max_f32 v33, v12, v33\n"
      "v_sub_f32 v34, v32, v33\n"
      "v_exp_f32 v34, v34\n"
      "v_lshlrev_b32 v76, 16, v22\n"
      "ds_bpermute_b32 v40, v5, v34 offset:0\n"
      "ds_bpermute_b32 v41, v5, v34 offset:8\n"
      "ds_bpermute_b32 v42, v5, v34 offset:16\n"
      "ds_bpermute_b32 v43, v5, v34 offset:24\n"
      "ds_bpermute_b32 v44, v5, v34 offset:32\n"
      "ds_bpermute_b32 v45, v5, v34 offset:40\n"
      "ds_bpermute_b32 v46, v5, v34 offset:48\n"
      "ds_bpermute_b32 v47, v5, v34 offset:56\n"
      "ds_bpermute_b32 v48, v5, v34 offset:64\n"
      "ds_bpermute_b32 v49, v5, v34 offset:72\n"
      "ds_bpermute_b32 v50, v5, v34 offset:80\n"
      "ds_bpermute_b32 v51, v5, v34 offset:88\n"
      "ds_bpermute_b32 v52, v5, v34 offset:96\n"
      "ds_bpermute_b32 v53, v5, v34 offset:104\n"
      "v_mov_b32 v35, v34\n"
      "v_and_b32 v77, 0xffff0000, v22\n"
      "v_lshlrev_b32 v78, 16, v23\n"
      "v_add_f32_dpp v35, v35, v35 quad_perm:[1,0,3,2] row_mask:0xf bank_mask:0xf\n"
      "v_and_b32 v79, 0xffff0000, v23\n"
      "v_lshlrev_b32 v80, 16, v24\n"
      "v_add_f32_dpp v35, v35, v35 quad_perm:[2,3,0,1] row_mask:0xf bank_mask:0xf\n"
      "v_and_b32 v81, 0xffff0000, v24\n"
      "v_lshlrev_b32 v82, 16, v25\n"
      "v_add_f32_dpp v35, v35, v35 row_half_mirror row_mask:0xf bank_mask:0xf\n"
      "v_and_b32 v83, 0xffff0000, v25\n"
      "v_lshlrev_b32 v84, 16, v26\n"
      "v_add_f32_dpp v35, v35, v35 row_ror:8 row_mask:0xf bank_mask:0xf\n"
      "v_mov_b32 v12, v35\n"
      "v_and_b32 v85, 0xffff0000, v26\n"
      "v_lshlrev_b32 v86, 16, v27\n"
      "v_permlane16_swap_b32 v12, v35\n"
      "v_add_f32 v35, v12, v35\n"
      "v_mov_b32 v12, v35\n"
      "v_and_b32 v87, 0xffff0000, v27\n"
      "v_lshlrev_b32 v88, 16, v28\n"
      "v_permlane32_swap_b32 v12, v35\n"
      "v_add_f32 v35, v12, v35\n"
      "v_and_b32 v89, 0xffff0000, v28\n"
      "v_lshlrev_b32 v90, 16, v29\n"
      "v_and_b32 v91, 0xffff0000, v29\n"
      "v_rcp_f32 v35, v35\n"
      "s_waitcnt lgkmcnt(10)\n"
      "v_mul_f32 v36, v40, v64\n"
      "v_mul_f32 v37, v40, v65\n"
      "v_mul_f32 v38, v41, v66\n"
      "v_mul_f32 v39, v41, v67\n"
      "v_fmac_f32 v36, v42, v68\n"
      "v_fmac_f32 v37, v42, v69\n"
      "v_fmac_f32 v38, v43, v70\n"
      "v_fmac_f32 v39, v43, v71\n"
      "s_waitcnt lgkmcnt(6)\n"
      "v_fmac_f32 v36, v44, v72\n"
      "v_fmac_f32 v37, v44, v73\n"
      "v_fmac_f32 v38, v45, v74\n"
      "v_fmac_f32 v39, v45, v75\n"
      "v_fmac_f32 v36, v46, v76\n"
      "v_fmac_f32 v37, v46, v77\n"
      "v_fmac_f32 v38, v47, v78\n"
      "v_fmac_f32 v39, v47, v79\n"
      "s_waitcnt lgkmcnt(2)\n"
      "v_fmac_f32 v36, v48, v80\n"
      "v_fmac_f32 v37, v48, v81\n"
      "v_fmac_f32 v38, v49, v82\n"
      "v_fmac_f32 v39, v49, v83\n"
      "v_fmac_f32 v36, v50, v84\n"
      "v_fmac_f32 v37, v50, v85\n"
      "v_fmac_f32 v38, v51, v86\n"
      "v_fmac_f32 v39, v51, v87\n"
      "s_waitcnt lgkmcnt(0)\n"
      "v_fmac_f32 v36, v52, v88\n"
      "v_fmac_f32 v37, v52, v89\n"
      "v_fmac_f32 v38, v53, v90\n"
      "v_fmac_f32 v39, v53, v91\n"
      "v_pk_add_f32 v[36:37], v[36:37], v[38:39]\n"
      "v_mov_b32 v38, v36\n"
      "v_mov_b32 v39, v37\n"
      "s_nop 0\n"
      "v_permlane32_swap_b32 v38, v36\n"
      "v_permlane32_swap_b32 v39, v37\n"
      "v_add_f32 v36, v38, v36\n"
      "v_add_f32 v37, v39, v37\n"
      "v_mul_f32 v36, v35, v36\n"
      "v_mul_f32 v37, v35, v37\n"
      "v_cvt_pk_bf16_f32 v36, v36, v37\n"
      "s_lshl_b32 s65, s53, 7\n"
      "s_add_u32 s65, s65, s79\n"
      "v_add_u32 v12, s65, v9\n"
      "v_add_u32 v13, s79, v6\n"
      "s_mov_b32 exec_lo, 0xffffffff\n"
      "s_mov_b32 exec_hi, 0x0\n"
      "ds_write_b32 v12, v36\n"
      "s_mov_b64 exec, -1\n"
      "v_mov_b32 v57, 0\n"
      "s_cmp_lt_u32 s55, 2\n"
      "s_cbranch_scc0 C_nores23_%=\n"
      "s_and_b32 s65, s62, 7\n"
      "s_lshl_b32 s65, s65, 11\n"
      "s_lshl_b32 s66, s55, 10\n"
      "s_add_u32 s65, s65, s66\n"
      "s_lshl_b32 s66, s53, 8\n"
      "s_add_u32 s65, s65, s66\n"
      "s_add_u32 s65, s65, 32768\n"
      "v_add_u32 v58, s65, v9\n"
      "ds_read_b32 v57, v58\n"
      "C_nores23_%=:\n"
      "s_waitcnt lgkmcnt(0)\n"
      "s_barrier\n"
      "ds_read_b128 v[64:67], v13 offset:0\n"
      "ds_read_b128 v[68:71], v13 offset:64\n"
      "ds_read_b128 v[72:75], v13 offset:128\n"
      "ds_read_b128 v[76:79], v13 offset:192\n"
      "ds_read_b128 v[80:83], v13 offset:256\n"
      "ds_read_b128 v[84:87], v13 offset:320\n"
      "ds_read_b128 v[88:91], v13 offset:384\n"
      "ds_read_b128 v[92:95], v13 offset:448\n"
      "s_mov_b32 s68, s61\n"
      "s_swappc_b64 s[34:35], s[36:37]\n"
      "s_lshl_b32 s65, s62, 14\n"
      "s_lshl_b32 s66, s55, 9\n"
      "s_add_u32 s65, s65, s66\n"
      "s_lshl_b32 s66, s53, 7\n"
      "s_add_u32 s65, s65, s66\n"
      "s_add_u32 s65, s65, 5967872\n"
      "s_add_u32 s50, s40, s65\n"
      "s_addc_u32 s51, s41, 0\n"
      "s_waitcnt lgkmcnt(7)\n"
      "v_mfma_f32_16x16x32_bf16 v[96:99], v[64:67], a[0:3], 0\n"
      "v_mfma_f32_16x16x32_bf16 v[100:103], v[64:67], a[32:35], 0\n"
      "v_mfma_f32_16x16x32_bf16 v[104:107], v[64:67], a[64:67], 0\n"
      "v_mfma_f32_16x16x32_bf16 v[108:111], v[64:67], a[96:99], 0\n"
      "s_waitcnt lgkmcnt(6)\n"
      "v_mfma_f32_16x16x32_bf16 v[96:99], v[68:71], a[4:7], v[96:99]\n"
      "v_mfma_f32_16x16x32_bf16 v[100:103], v[68:71], a[36:39], v[100:103]\n"
      "v_mfma_f32_16x16x32_bf16 v[104:107], v[68:71], a[68:71], v[104:107]\n"
      "v_mfma_f32_16x16x32_bf16 v[108:111], v[68:71], a[100:103], v[108:111]\n"
      "s_waitcnt lgkmcnt(5)\n"
      "v_mfma_f32_16x16x32_bf16 v[96:99], v[72:75], a[8:11], v[96:99]\n"
      "v_mfma_f32_16x16x32_bf16 v[100:103], v[72:75], a[40:43], v[100:103]\n"
      "v_mfma_f32_16x16x32_bf16 v[104:107], v[72:75], a[72:75], v[104:107]\n"
      "v_mfma_f32_16x16x32_bf16 v[108:111], v[72:75], a[104:107], v[108:111]\n"
      "s_waitcnt lgkmcnt(4)\n"
      "v_mfma_f32_16x16x32_bf16 v[96:99], v[76:79], a[12:15], v[96:99]\n"
      "v_mfma_f32_16x16x32_bf16 v[100:103], v[76:79], a[44:47], v[100:103]\n"
      "v_mfma_f32_16x16x32_bf16 v[104:107], v[76:79], a[76:79], v[104:107]\n"
      "v_mfma_f32_16x16x32_bf16 v[108:111], v[76:79], a[108:111], v[108:111]\n"
      "s_waitcnt lgkmcnt(3)\n"
      "v_mfma_f32_16x16x32_bf16 v[96:99], v[80:83], a[16:19], v[96:99]\n"
      "v_mfma_f32_16x16x32_bf16 v[100:103], v[80:83], a[48:51], v[100:103]\n"
      "v_mfma_f32_16x16x32_bf16 v[104:107], v[80:83], a[80:83], v[104:107]\n"
      "v_mfma_f32_16x16x32_bf16 v[108:111], v[80:83], a[112:115], v[108:111]\n"
      "s_waitcnt lgkmcnt(2)\n"
      "v_mfma_f32_16x16x32_bf16 v[96:99], v[84:87], a[20:23], v[96:99]\n"
      "v_mfma_f32_16x16x32_bf16 v[100:103], v[84:87], a[52:55], v[100:103]\n"
      "v_mfma_f32_16x16x32_bf16 v[104:107], v[84:87], a[84:87], v[104:107]\n"
      "v_mfma_f32_16x16x32_bf16 v[108:111], v[84:87], a[116:119], v[108:111]\n"
      "s_waitcnt lgkmcnt(1)\n"
      "v_mfma_f32_16x16x32_bf16 v[96:99], v[88:91], a[24:27], v[96:99]\n"
      "v_mfma_f32_16x16x32_bf16 v[100:103], v[88:91], a[56:59], v[100:103]\n"
      "v_mfma_f32_16x16x32_bf16 v[104:107], v[88:91], a[88:91], v[104:107]\n"
      "v_mfma_f32_16x16x32_bf16 v[108:111], v[88:91], a[120:123], v[108:111]\n"
      "s_waitcnt lgkmcnt(0)\n"
      "v_mfma_f32_16x16x32_bf16 v[96:99], v[92:95], a[28:31], v[96:99]\n"
      "v_mfma_f32_16x16x32_bf16 v[100:103], v[92:95], a[60:63], v[100:103]\n"
      "v_mfma_f32_16x16x32_bf16 v[104:107], v[92:95], a[92:95], v[104:107]\n"
      "v_mfma_f32_16x16x32_bf16 v[108:111], v[92:95], a[124:127], v[108:111]\n"
      "s_nop 5\n"
      "v_cndmask_b32 v32, v96, v100, s[94:95]\n"
      "s_nop 0\n"
      "v_cndmask_b32 v12, v104, v108, s[96:97]\n"
      "v_cndmask_b32 v32, v32, v12, s[98:99]\n"
      "v_add_f32 v32, v32, v57\n"
      "s_nop 1\n"
      "v_mov_b32_dpp v13, v32 quad_perm:[1,0,3,2] row_mask:0xf bank_mask:0xf\n"
      "v_cvt_pk_bf16_f32 v32, v32, v13\n"
      "v_min_u32 v32, -2, v32\n"
      "s_mov_b32 exec_lo, 0x55555555\n"
      "s_mov_b32 exec_hi, 0x55555555\n"
      "global_store_dword v10, v32, s[50:51] sc1\n"
      "s_mov_b64 exec, -1\n"
      "s_branch C_next1_%=\n"
      "C_s1b_%=:\n"
      "v_cndmask_b32 v32, v11, v30, s[92:93]\n"
      "v_mov_b32 v33, v32\n"
      "v_lshlrev_b32 v64, 16, v16\n"
      "v_and_b32 v65, 0xffff0000, v16\n"
      "v_max_f32_dpp v33, v33, v33 quad_perm:[1,0,3,2] row_mask:0xf bank_mask:0xf\n"
      "v_lshlrev_b32 v66, 16, v17\n"
      "v_and_b32 v67, 0xffff0000, v17\n"
      "v_max_f32_dpp v33, v33, v33 quad_perm:[2,3,0,1] row_mask:0xf bank_mask:0xf\n"
      "v_lshlrev_b32 v68, 16, v18\n"
      "v_and_b32 v69, 0xffff0000, v18\n"
      "v_max_f32_dpp v33, v33, v33 row_half_mirror row_mask:0xf bank_mask:0xf\n"
      "v_lshlrev_b32 v70, 16, v19\n"
      "v_and_b32 v71, 0xffff0000, v19\n"
      "v_max_f32_dpp v33, v33, v33 row_ror:8 row_mask:0xf bank_mask:0xf\n"
      "v_mov_b32 v12, v33\n"
      "v_lshlrev_b32 v72, 16, v20\n"
      "v_and_b32 v73, 0xffff0000, v20\n"
      "v_permlane16_swap_b32 v12, v33\n"
      "v_max_f32 v33, v12, v33\n"
      "v_mov_b32 v12, v33\n"
      "v_lshlrev_b32 v74, 16, v21\n"
      "v_and_b32 v75, 0xffff0000, v21\n"
      "v_permlane32_swap_b32 v12, v33\n"
      "v_max_f32 v33, v12, v33\n"
      "v_sub_f32 v34, v32, v33\n"
      "v_exp_f32 v34, v34\n"
      "v_lshlrev_b32 v76, 16, v22\n"
      "ds_bpermute_b32 v40, v5, v34 offset:0\n"
      "ds_bpermute_b32 v41, v5, v34 offset:8\n"
      "ds_bpermute_b32 v42, v5, v34 offset:16\n"
      "ds_bpermute_b32 v43, v5, v34 offset:24\n"
      "ds_bpermute_b32 v44, v5, v34 offset:32\n"
      "ds_bpermute_b32 v45, v5, v34 offset:40\n"
      "ds_bpermute_b32 v46, v5, v34 offset:48\n"
      "ds_bpermute_b32 v47, v5, v34 offset:56\n"
      "ds_bpermute_b32 v48, v5, v34 offset:64\n"
      "ds_bpermute_b32 v49, v5, v34 offset:72\n"
      "ds_bpermute_b32 v50, v5, v34 offset:80\n"
      "ds_bpermute_b32 v51, v5, v34 offset:88\n"
      "ds_bpermute_b32 v52, v5, v34 offset:96\n"
      "ds_bpermute_b32 v53, v5, v34 offset:104\n"
      "v_mov_b32 v35, v34\n"
      "v_and_b32 v77, 0xffff0000, v22\n"
      "v_lshlrev_b32 v78, 16, v23\n"
      "v_add_f32_dpp v35, v35, v35 quad_perm:[1,0,3,2] row_mask:0xf bank_mask:0xf\n"
      "v_and_b32 v79, 0xffff0000, v23\n"
      "v_lshlrev_b32 v80, 16, v24\n"
      "v_add_f32_dpp v35, v35, v35 quad_perm:[2,3,0,1] row_mask:0xf bank_mask:0xf\n"
      "v_and_b32 v81, 0xffff0000, v24\n"
      "v_lshlrev_b32 v82, 16, v25\n"
      "v_add_f32_dpp v35, v35, v35 row_half_mirror row_mask:0xf bank_mask:0xf\n"
      "v_and_b32 v83, 0xffff0000, v25\n"
      "v_lshlrev_b32 v84, 16, v26\n"
      "v_add_f32_dpp v35, v35, v35 row_ror:8 row_mask:0xf bank_mask:0xf\n"
      "v_mov_b32 v12, v35\n"
      "v_and_b32 v85, 0xffff0000, v26\n"
      "v_lshlrev_b32 v86, 16, v27\n"
      "v_permlane16_swap_b32 v12, v35\n"
      "v_add_f32 v35, v12, v35\n"
      "v_mov_b32 v12, v35\n"
      "v_and_b32 v87, 0xffff0000, v27\n"
      "v_lshlrev_b32 v88, 16, v28\n"
      "v_permlane32_swap_b32 v12, v35\n"
      "v_add_f32 v35, v12, v35\n"
      "v_and_b32 v89, 0xffff0000, v28\n"
      "v_lshlrev_b32 v90, 16, v29\n"
      "v_and_b32 v91, 0xffff0000, v29\n"
      "v_rcp_f32 v35, v35\n"
      "s_waitcnt lgkmcnt(10)\n"
      "v_mul_f32 v36, v40, v64\n"
      "v_mul_f32 v37, v40, v65\n"
      "v_mul_f32 v38, v41, v66\n"
      "v_mul_f32 v39, v41, v67\n"
      "v_fmac_f32 v36, v42, v68\n"
      "v_fmac_f32 v37, v42, v69\n"
      "v_fmac_f32 v38, v43, v70\n"
      "v_fmac_f32 v39, v43, v71\n"
      "s_waitcnt lgkmcnt(6)\n"
      "v_fmac_f32 v36, v44, v72\n"
      "v_fmac_f32 v37, v44, v73\n"
      "v_fmac_f32 v38, v45, v74\n"
      "v_fmac_f32 v39, v45, v75\n"
      "v_fmac_f32 v36, v46, v76\n"
      "v_fmac_f32 v37, v46, v77\n"
      "v_fmac_f32 v38, v47, v78\n"
      "v_fmac_f32 v39, v47, v79\n"
      "s_waitcnt lgkmcnt(2)\n"
      "v_fmac_f32 v36, v48, v80\n"
      "v_fmac_f32 v37, v48, v81\n"
      "v_fmac_f32 v38, v49, v82\n"
      "v_fmac_f32 v39, v49, v83\n"
      "v_fmac_f32 v36, v50, v84\n"
      "v_fmac_f32 v37, v50, v85\n"
      "v_fmac_f32 v38, v51, v86\n"
      "v_fmac_f32 v39, v51, v87\n"
      "s_waitcnt lgkmcnt(0)\n"
      "v_fmac_f32 v36, v52, v88\n"
      "v_fmac_f32 v37, v52, v89\n"
      "v_fmac_f32 v38, v53, v90\n"
      "v_fmac_f32 v39, v53, v91\n"
      "v_pk_add_f32 v[36:37], v[36:37], v[38:39]\n"
      "v_mov_b32 v38, v36\n"
      "v_mov_b32 v39, v37\n"
      "s_nop 0\n"
      "v_permlane32_swap_b32 v38, v36\n"
      "v_permlane32_swap_b32 v39, v37\n"
      "v_add_f32 v36, v38, v36\n"
      "v_add_f32 v37, v39, v37\n"
      "v_mul_f32 v36, v35, v36\n"
      "v_mul_f32 v37, v35, v37\n"
      "v_cvt_pk_bf16_f32 v36, v36, v37\n"
      "s_lshl_b32 s65, s53, 7\n"
      "s_add_u32 s65, s65, s79\n"
      "v_add_u32 v12, s65, v9\n"
      "v_add_u32 v13, s79, v6\n"
      "s_mov_b32 exec_lo, 0xffffffff\n"
      "s_mov_b32 exec_hi, 0x0\n"
      "ds_write_b32 v12, v36\n"
      "s_mov_b64 exec, -1\n"
      "s_waitcnt lgkmcnt(0)\n"
      "s_barrier\n"
      "ds_read_b128 v[64:67], v13 offset:0\n"
      "ds_read_b128 v[68:71], v13 offset:64\n"
      "ds_read_b128 v[72:75], v13 offset:128\n"
      "ds_read_b128 v[76:79], v13 offset:192\n"
      "ds_read_b128 v[80:83], v13 offset:256\n"
      "ds_read_b128 v[84:87], v13 offset:320\n"
      "ds_read_b128 v[88:91], v13 offset:384\n"
      "ds_read_b128 v[92:95], v13 offset:448\n"
      "s_mov_b32 s68, s61\n"
      "s_swappc_b64 s[34:35], s[36:37]\n"
      "s_lshl_b32 s65, s62, 14\n"
      "s_lshl_b32 s66, s55, 9\n"
      "s_add_u32 s65, s65, s66\n"
      "s_lshl_b32 s66, s53, 7\n"
      "s_add_u32 s65, s65, s66\n"
      "s_add_u32 s65, s65, 5967872\n"
      "s_add_u32 s50, s40, s65\n"
      "s_addc_u32 s51, s41, 0\n"
      "s_waitcnt lgkmcnt(7)\n"
      "v_mfma_f32_16x16x32_bf16 v[96:99], v[64:67], a[0:3], 0\n"
      "v_mfma_f32_16x16x32_bf16 v[100:103], v[64:67], a[32:35], 0\n"
      "v_mfma_f32_16x16x32_bf16 v[104:107], v[64:67], a[64:67], 0\n"
      "v_mfma_f32_16x16x32_bf16 v[108:111], v[64:67], a[96:99], 0\n"
      "v_mfma_f32_16x16x32_bf16 v[40:43], v[64:67], a[128:131], 0\n"
      "v_mfma_f32_16x16x32_bf16 v[44:47], v[64:67], a[160:163], 0\n"
      "v_mfma_f32_16x16x32_bf16 v[48:51], v[64:67], a[192:195], 0\n"
      "v_mfma_f32_16x16x32_bf16 v[52:55], v[64:67], a[224:227], 0\n"
      "s_waitcnt lgkmcnt(6)\n"
      "v_mfma_f32_16x16x32_bf16 v[96:99], v[68:71], a[4:7], v[96:99]\n"
      "v_mfma_f32_16x16x32_bf16 v[100:103], v[68:71], a[36:39], v[100:103]\n"
      "v_mfma_f32_16x16x32_bf16 v[104:107], v[68:71], a[68:71], v[104:107]\n"
      "v_mfma_f32_16x16x32_bf16 v[108:111], v[68:71], a[100:103], v[108:111]\n"
      "v_mfma_f32_16x16x32_bf16 v[40:43], v[68:71], a[132:135], v[40:43]\n"
      "v_mfma_f32_16x16x32_bf16 v[44:47], v[68:71], a[164:167], v[44:47]\n"
      "v_mfma_f32_16x16x32_bf16 v[48:51], v[68:71], a[196:199], v[48:51]\n"
      "v_mfma_f32_16x16x32_bf16 v[52:55], v[68:71], a[228:231], v[52:55]\n"
      "s_waitcnt lgkmcnt(5)\n"
      "v_mfma_f32_16x16x32_bf16 v[96:99], v[72:75], a[8:11], v[96:99]\n"
      "v_mfma_f32_16x16x32_bf16 v[100:103], v[72:75], a[40:43], v[100:103]\n"
      "v_mfma_f32_16x16x32_bf16 v[104:107], v[72:75], a[72:75], v[104:107]\n"
      "v_mfma_f32_16x16x32_bf16 v[108:111], v[72:75], a[104:107], v[108:111]\n"
      "v_mfma_f32_16x16x32_bf16 v[40:43], v[72:75], a[136:139], v[40:43]\n"
      "v_mfma_f32_16x16x32_bf16 v[44:47], v[72:75], a[168:171], v[44:47]\n"
      "v_mfma_f32_16x16x32_bf16 v[48:51], v[72:75], a[200:203], v[48:51]\n"
      "v_mfma_f32_16x16x32_bf16 v[52:55], v[72:75], a[232:235], v[52:55]\n"
      "s_waitcnt lgkmcnt(4)\n"
      "v_mfma_f32_16x16x32_bf16 v[96:99], v[76:79], a[12:15], v[96:99]\n"
      "v_mfma_f32_16x16x32_bf16 v[100:103], v[76:79], a[44:47], v[100:103]\n"
      "v_mfma_f32_16x16x32_bf16 v[104:107], v[76:79], a[76:79], v[104:107]\n"
      "v_mfma_f32_16x16x32_bf16 v[108:111], v[76:79], a[108:111], v[108:111]\n"
      "v_mfma_f32_16x16x32_bf16 v[40:43], v[76:79], a[140:143], v[40:43]\n"
      "v_mfma_f32_16x16x32_bf16 v[44:47], v[76:79], a[172:175], v[44:47]\n"
      "v_mfma_f32_16x16x32_bf16 v[48:51], v[76:79], a[204:207], v[48:51]\n"
      "v_mfma_f32_16x16x32_bf16 v[52:55], v[76:79], a[236:239], v[52:55]\n"
      "s_waitcnt lgkmcnt(3)\n"
      "v_mfma_f32_16x16x32_bf16 v[96:99], v[80:83], a[16:19], v[96:99]\n"
      "v_mfma_f32_16x16x32_bf16 v[100:103], v[80:83], a[48:51], v[100:103]\n"
      "v_mfma_f32_16x16x32_bf16 v[104:107], v[80:83], a[80:83], v[104:107]\n"
      "v_mfma_f32_16x16x32_bf16 v[108:111], v[80:83], a[112:115], v[108:111]\n"
      "v_mfma_f32_16x16x32_bf16 v[40:43], v[80:83], a[144:147], v[40:43]\n"
      "v_mfma_f32_16x16x32_bf16 v[44:47], v[80:83], a[176:179], v[44:47]\n"
      "v_mfma_f32_16x16x32_bf16 v[48:51], v[80:83], a[208:211], v[48:51]\n"
      "v_mfma_f32_16x16x32_bf16 v[52:55], v[80:83], a[240:243], v[52:55]\n"
      "s_waitcnt lgkmcnt(2)\n"
      "v_mfma_f32_16x16x32_bf16 v[96:99], v[84:87], a[20:23], v[96:99]\n"
      "v_mfma_f32_16x16x32_bf16 v[100:103], v[84:87], a[52:55], v[100:103]\n"
      "v_mfma_f32_16x16x32_bf16 v[104:107], v[84:87], a[84:87], v[104:107]\n"
      "v_mfma_f32_16x16x32_bf16 v[108:111], v[84:87], a[116:119], v[108:111]\n"
      "v_mfma_f32_16x16x32_bf16 v[40:43], v[84:87], a[148:151], v[40:43]\n"
      "v_mfma_f32_16x16x32_bf16 v[44:47], v[84:87], a[180:183], v[44:47]\n"
      "v_mfma_f32_16x16x32_bf16 v[48:51], v[84:87], a[212:215], v[48:51]\n"
      "v_mfma_f32_16x16x32_bf16 v[52:55], v[84:87], a[244:247], v[52:55]\n"
      "s_waitcnt lgkmcnt(1)\n"
      "v_mfma_f32_16x16x32_bf16 v[96:99], v[88:91], a[24:27], v[96:99]\n"
      "v_mfma_f32_16x16x32_bf16 v[100:103], v[88:91], a[56:59], v[100:103]\n"
      "v_mfma_f32_16x16x32_bf16 v[104:107], v[88:91], a[88:91], v[104:107]\n"
      "v_mfma_f32_16x16x32_bf16 v[108:111], v[88:91], a[120:123], v[108:111]\n"
      "v_mfma_f32_16x16x32_bf16 v[40:43], v[88:91], a[152:155], v[40:43]\n"
      "v_mfma_f32_16x16x32_bf16 v[44:47], v[88:91], a[184:187], v[44:47]\n"
      "v_mfma_f32_16x16x32_bf16 v[48:51], v[88:91], a[216:219], v[48:51]\n"
      "v_mfma_f32_16x16x32_bf16 v[52:55], v[88:91], a[248:251], v[52:55]\n"
      "s_waitcnt lgkmcnt(0)\n"
      "v_mfma_f32_16x16x32_bf16 v[96:99], v[92:95], a[28:31], v[96:99]\n"
      "v_mfma_f32_16x16x32_bf16 v[100:103], v[92:95], a[60:63], v[100:103]\n"
      "v_mfma_f32_16x16x32_bf16 v[104:107], v[92:95], a[92:95], v[104:107]\n"
      "v_mfma_f32_16x16x32_bf16 v[108:111], v[92:95], a[124:127], v[108:111]\n"
      "v_mfma_f32_16x16x32_bf16 v[40:43], v[92:95], a[156:159], v[40:43]\n"
      "v_mfma_f32_16x16x32_bf16 v[44:47], v[92:95], a[188:191], v[44:47]\n"
      "v_mfma_f32_16x16x32_bf16 v[48:51], v[92:95], a[220:223], v[48:51]\n"
      "v_mfma_f32_16x16x32_bf16 v[52:55], v[92:95], a[252:255], v[52:55]\n"
      "s_nop 1\n"
      "v_cndmask_b32 v32, v96, v100, s[94:95]\n"
      "s_nop 0\n"
      "v_cndmask_b32 v12, v104, v108, s[96:97]\n"
      "v_cndmask_b32 v32, v32, v12, s[98:99]\n"
      "v_cndmask_b32 v33, v40, v44, s[94:95]\n"
      "s_nop 0\n"
      "v_cndmask_b32 v12, v48, v52, s[96:97]\n"
      "v_cndmask_b32 v33, v33, v12, s[98:99]\n"
      "v_mov_b32_dpp v13, v32 quad_perm:[1,0,3,2] row_mask:0xf bank_mask:0xf\n"
      "v_cvt_pk_bf16_f32 v32, v32, v13\n"
      "v_min_u32 v32, -2, v32\n"
      "v_mov_b32_dpp v13, v33 quad_perm:[1,0,3,2] row_mask:0xf bank_mask:0xf\n"
      "v_cvt_pk_bf16_f32 v33, v33, v13\n"
      "v_min_u32 v33, -2, v33\n"
      "s_mov_b32 exec_lo, 0x55555555\n"
      "s_mov_b32 exec_hi, 0x55555555\n"
      "global_store_dword v10, v32, s[50:51] sc1\n"
      "global_store_dword v10, v33, s[50:51] offset:512 sc1\n"
      "s_mov_b64 exec, -1\n"
      "s_branch C_next2_%=\n"
      "C_s4_%=:\n"
      "v_dot2_f32_bf16 v32, v16, s24, 0\n"
      "v_dot2_f32_bf16 v33, v16, s25, 0\n"
      "v_dot2_f32_bf16 v34, v17, s24, 0\n"
      "v_dot2_f32_bf16 v35, v17, s25, 0\n"
      "v_dot2_f32_bf16 v36, v18, s24, 0\n"
      "v_dot2_f32_bf16 v37, v18, s25, 0\n"
      "v_dot2_f32_bf16 v38, v19, s24, 0\n"
      "v_dot2_f32_bf16 v39, v19, s25, 0\n"
      "v_dot2_f32_bf16 v32, v20, s24, v32\n"
      "v_dot2_f32_bf16 v33, v20, s25, v33\n"
      "v_dot2_f32_bf16 v34, v21, s24, v34\n"
      "v_dot2_f32_bf16 v35, v21, s25, v35\n"
      "v_dot2_f32_bf16 v36, v22, s24, v36\n"
      "v_dot2_f32_bf16 v37, v22, s25, v37\n"
      "v_dot2_f32_bf16 v38, v23, s24, v38\n"
      "v_dot2_f32_bf16 v39, v23, s25, v39\n"
      "s_cmp_eq_u32 s53, 3\n"
      "s_cbranch_scc1 C_g324_%=\n"
      "v_dot2_f32_bf16 v32, v24, s24, v32\n"
      "v_dot2_f32_bf16 v33, v24, s25, v33\n"
      "v_dot2_f32_bf16 v34, v25, s24, v34\n"
      "v_dot2_f32_bf16 v35, v25, s25, v35\n"
      "v_dot2_f32_bf16 v36, v26, s24, v36\n"
      "v_dot2_f32_bf16 v37, v26, s25, v37\n"
      "v_dot2_f32_bf16 v38, v27, s24, v38\n"
      "v_dot2_f32_bf16 v39, v27, s25, v39\n"
      "v_dot2_f32_bf16 v32, v28, s24, v32\n"
      "v_dot2_f32_bf16 v33, v28, s25, v33\n"
      "v_dot2_f32_bf16 v34, v29, s24, v34\n"
      "v_dot2_f32_bf16 v35, v29, s25, v35\n"
      "v_dot2_f32_bf16 v36, v30, s24, v36\n"
      "v_dot2_f32_bf16 v37, v30, s25, v37\n"
      "v_dot2_f32_bf16 v38, v31, s24, v38\n"
      "v_dot2_f32_bf16 v39, v31, s25, v39\n"
      "C_g324_%=:\n"
      "s_nop 2\n"
      "s_lshl_b32 s65, s53, 11\n"
      "s_add_u32 s65, s65, s39\n"
      "v_add_u32 v12, s65, v1\n"
      "v_add_u32 v13, s39, v1\n"
      "ds_write_b128 v12, v[32:35]\n"
      "ds_write_b128 v12, v[36:39] offset:1024\n"
      "s_waitcnt lgkmcnt(0)\n"
      "s_barrier\n"
      "ds_read_b128 v[64:67], v13 offset:0\n"
      "ds_read_b128 v[68:71], v13 offset:1024\n"
      "ds_read_b128 v[72:75], v13 offset:2048\n"
      "ds_read_b128 v[76:79], v13 offset:3072\n"
      "ds_read_b128 v[80:83], v13 offset:4096\n"
      "ds_read_b128 v[84:87], v13 offset:5120\n"
      "ds_read_b128 v[88:91], v13 offset:6144\n"
      "ds_read_b128 v[92:95], v13 offset:7168\n"
      "s_mov_b32 s68, s61\n"
      "s_swappc_b64 s[34:35], s[36:37]\n"
      "s_waitcnt lgkmcnt(4)\n"
      "v_pk_add_f32 v[32:33], v[64:65], v[72:73]\n"
      "v_pk_add_f32 v[34:35], v[66:67], v[74:75]\n"
      "v_pk_add_f32 v[36:37], v[68:69], v[76:77]\n"
      "v_pk_add_f32 v[38:39], v[70:71], v[78:79]\n"
      "s_waitcnt lgkmcnt(0)\n"
      "v_pk_add_f32 v[40:41], v[80:81], v[88:89]\n"
      "v_pk_add_f32 v[42:43], v[82:83], v[90:91]\n"
      "v_pk_add_f32 v[44:45], v[84:85], v[92:93]\n"
      "v_pk_add_f32 v[46:47], v[86:87], v[94:95]\n"
      "v_pk_add_f32 v[32:33], v[32:33], v[40:41]\n"
      "v_pk_add_f32 v[34:35], v[34:35], v[42:43]\n"
      "v_pk_add_f32 v[36:37], v[36:37], v[44:45]\n"
      "v_pk_add_f32 v[38:39], v[38:39], v[46:47]\n"
      "s_cmp_lt_u32 s55, 2\n"
      "s_cbranch_scc0 C_nokeep25_%=\n"
      "s_cmp_eq_u32 s53, 0\n"
      "s_cbranch_scc0 C_nokeep25_%=\n"
      "s_and_b32 s65, s62, 7\n"
      "s_lshl_b32 s65, s65, 11\n"
      "s_add_u32 s65, s65, 32768\n"
      "v_lshlrev_b32 v40, 1, v1\n"
      "v_add_u32 v40, s65, v40\n"
      "ds_write_b128 v40, v[32:35]\n"
      "ds_write_b128 v40, v[36:39] offset:16\n"
      "C_nokeep25_%=:\n"
      "v_readfirstlane_b32 s66, v32\n"
      "s_nop 0\n"
      "s_mov_b32 s67, s66\n"
      "v_pk_add_f32 v[40:41], v[32:33], s[66:67] neg_lo:[0,1] neg_hi:[0,1]\n"
      "v_pk_add_f32 v[42:43], v[34:35], s[66:67] neg_lo:[0,1] neg_hi:[0,1]\n"
      "v_pk_add_f32 v[44:45], v[36:37], s[66:67] neg_lo:[0,1] neg_hi:[0,1]\n"
      "v_pk_add_f32 v[46:47], v[38:39], s[66:67] neg_lo:[0,1] neg_hi:[0,1]\n"
      "v_pk_mul_f32 v[56:57], v[40:41], v[40:41]\n"
      "v_pk_add_f32 v[48:49], v[40:41], v[42:43]\n"
      "v_pk_fma_f32 v[56:57], v[42:43], v[42:43], v[56:57]\n"
      "v_pk_add_f32 v[50:51], v[44:45], v[46:47]\n"
      "v_pk_fma_f32 v[56:57], v[44:45], v[44:45], v[56:57]\n"
      "v_pk_add_f32 v[48:49], v[48:49], v[50:51]\n"
      "v_pk_fma_f32 v[56:57], v[46:47], v[46:47], v[56:57]\n"
      "v_add_f32 v48, v48, v49\n"
      "v_add_f32 v49, v56, v57\n"
      "s_nop 0\n"
      "v_add_f32_dpp v48, v48, v48 quad_perm:[1,0,3,2] row_mask:0xf bank_mask:0xf\n"
      "v_add_f32_dpp v49, v49, v49 quad_perm:[1,0,3,2] row_mask:0xf bank_mask:0xf\n"
      "s_nop 0\n"
      "v_add_f32_dpp v48, v48, v48 quad_perm:[2,3,0,1] row_mask:0xf bank_mask:0xf\n"
      "v_add_f32_dpp v49, v49, v49 quad_perm:[2,3,0,1] row_mask:0xf bank_mask:0xf\n"
      "s_nop 0\n"
      "v_add_f32_dpp v48, v48, v48 row_half_mirror row_mask:0xf bank_mask:0xf\n"
      "v_add_f32_dpp v49, v49, v49 row_half_mirror row_mask:0xf bank_mask:0xf\n"
      "s_nop 0\n"
      "v_add_f32_dpp v48, v48, v48 row_ror:8 row_mask:0xf bank_mask:0xf\n"
      "v_add_f32_dpp v49, v49, v49 row_ror:8 row_mask:0xf bank_mask:0xf\n"
      "v_mov_b32 v12, v48\n"
      "v_mov_b32 v13, v49\n"
      "s_nop 0\n"
      "v_permlane16_swap_b32 v12, v48\n"
      "v_permlane16_swap_b32 v13, v49\n"
      "v_add_f32 v48, v12, v48\n"
      "v_add_f32 v49, v13, v49\n"
      "v_mov_b32 v12, v48\n"
      "v_mov_b32 v13, v49\n"
      "s_nop 0\n"
      "v_permlane32_swap_b32 v12, v48\n"
      "v_permlane32_swap_b32 v13, v49\n"
      "v_add_f32 v48, v12, v48\n"
      "v_add_f32 v49, v13, v49\n"
      "v_mul_f32 v48, 0x3b000000, v48\n"
      "v_mul_f32 v49, 0x3b000000, v49\n"
      "v_fma_f32 v49, -v48, v48, v49\n"
      "v_add_f32 v49, 0x3727c5ac, v49\n"
      "v_rsq_f32 v49, v49\n"
      "s_nop 0\n"
      "v_mul_f32 v48, v48, v49\n"
      "v_pk_fma_f32 v[40:41], v[40:41], v[48:49], v[48:49] op_sel:[0,1,0] op_sel_hi:[1,1,0] neg_lo:[0,0,1] neg_hi:[0,0,1]\n"
      "v_pk_fma_f32 v[42:43], v[42:43], v[48:49], v[48:49] op_sel:[0,1,0] op_sel_hi:[1,1,0] neg_lo:[0,0,1] neg_hi:[0,0,1]\n"
      "v_pk_fma_f32 v[44:45], v[44:45], v[48:49], v[48:49] op_sel:[0,1,0] op_sel_hi:[1,1,0] neg_lo:[0,0,1] neg_hi:[0,0,1]\n"
      "v_pk_fma_f32 v[46:47], v[46:47], v[48:49], v[48:49] op_sel:[0,1,0] op_sel_hi:[1,1,0] neg_lo:[0,0,1] neg_hi:[0,0,1]\n"
      "v_cvt_pk_bf16_f32 v52, v40, v41\n"
      "v_cvt_pk_bf16_f32 v53, v42, v43\n"
      "v_cvt_pk_bf16_f32 v54, v44, v45\n"
      "v_cvt_pk_bf16_f32 v55, v46, v47\n"
      "s_lshl_b32 s65, s53, 10\n"
      "s_add_u32 s65, s65, 8192\n"
      "v_add_u32 v12, s65, v1\n"
      "v_add_u32 v13, s65, v6\n"
      "ds_write_b128 v12, v[52:55]\n"
      "ds_read_b128 v[64:67], v13 offset:0\n"
      "ds_read_b128 v[68:71], v13 offset:64\n"
      "ds_read_b128 v[72:75], v13 offset:128\n"
      "ds_read_b128 v[76:79], v13 offset:192\n"
      "ds_read_b128 v[80:83], v13 offset:256\n"
      "ds_read_b128 v[84:87], v13 offset:320\n"
      "ds_read_b128 v[88:91], v13 offset:384\n"
      "ds_read_b128 v[92:95], v13 offset:448\n"
      "ds_read_b128 v[96:99], v13 offset:512\n"
      "ds_read_b128 v[100:103], v13 offset:576\n"
      "ds_read_b128 v[104:107], v13 offset:640\n"
      "ds_read_b128 v[108:111], v13 offset:704\n"
      "s_waitcnt lgkmcnt(11)\n"
      "v_mfma_f32_16x16x32_bf16 v[56:59], v[64:67], a[128:131], 0\n"
      "v_mfma_f32_16x16x32_bf16 v[60:63], v[64:67], a[192:195], 0\n"
      "ds_read_b128 v[64:67], v13 offset:768\n"
      "s_waitcnt lgkmcnt(11)\n"
      "v_mfma_f32_16x16x32_bf16 v[56:59], v[68:71], a[132:135], v[56:59]\n"
      "v_mfma_f32_16x16x32_bf16 v[60:63], v[68:71], a[196:199], v[60:63]\n"
      "ds_read_b128 v[68:71], v13 offset:832\n"
      "s_waitcnt lgkmcnt(11)\n"
      "v_mfma_f32_16x16x32_bf16 v[56:59], v[72:75], a[136:139], v[56:59]\n"
      "v_mfma_f32_16x16x32_bf16 v[60:63], v[72:75], a[200:203], v[60:63]\n"
      "ds_read_b128 v[72:75], v13 offset:896\n"
      "s_waitcnt lgkmcnt(11)\n"
      "v_mfma_f32_16x16x32_bf16 v[56:59], v[76:79], a[140:143], v[56:59]\n"
      "v_mfma_f32_16x16x32_bf16 v[60:63], v[76:79], a[204:207], v[60:63]\n"
      "ds_read_b128 v[76:79], v13 offset:960\n"
      "s_waitcnt lgkmcnt(11)\n"
      "v_mfma_f32_16x16x32_bf16 v[56:59], v[80:83], a[144:147], v[56:59]\n"
      "v_mfma_f32_16x16x32_bf16 v[60:63], v[80:83], a[208:211], v[60:63]\n"
      "s_waitcnt lgkmcnt(10)\n"
      "v_mfma_f32_16x16x32_bf16 v[56:59], v[84:87], a[148:151], v[56:59]\n"
      "v_mfma_f32_16x16x32_bf16 v[60:63], v[84:87], a[212:215], v[60:63]\n"
      "s_waitcnt lgkmcnt(9)\n"
      "v_mfma_f32_16x16x32_bf16 v[56:59], v[88:91], a[152:155], v[56:59]\n"
      "v_mfma_f32_16x16x32_bf16 v[60:63], v[88:91], a[216:219], v[60:63]\n"
      "s_waitcnt lgkmcnt(8)\n"
      "v_mfma_f32_16x16x32_bf16 v[56:59], v[92:95], a[156:159], v[56:59]\n"
      "v_mfma_f32_16x16x32_bf16 v[60:63], v[92:95], a[220:223], v[60:63]\n"
      "s_waitcnt lgkmcnt(7)\n"
      "v_mfma_f32_16x16x32_bf16 v[56:59], v[96:99], a[160:163], v[56:59]\n"
      "v_mfma_f32_16x16x32_bf16 v[60:63], v[96:99], a[224:227], v[60:63]\n"
      "s_waitcnt lgkmcnt(6)\n"
      "v_mfma_f32_16x16x32_bf16 v[56:59], v[100:103], a[164:167], v[56:59]\n"
      "v_mfma_f32_16x16x32_bf16 v[60:63], v[100:103], a[228:231], v[60:63]\n"
      "s_waitcnt lgkmcnt(5)\n"
      "v_mfma_f32_16x16x32_bf16 v[56:59], v[104:107], a[168:171], v[56:59]\n"
      "v_mfma_f32_16x16x32_bf16 v[60:63], v[104:107], a[232:235], v[60:63]\n"
      "s_waitcnt lgkmcnt(4)\n"
      "v_mfma_f32_16x16x32_bf16 v[56:59], v[108:111], a[172:175], v[56:59]\n"
      "v_mfma_f32_16x16x32_bf16 v[60:63], v[108:111], a[236:239], v[60:63]\n"
      "s_waitcnt lgkmcnt(3)\n"
      "v_mfma_f32_16x16x32_bf16 v[56:59], v[64:67], a[176:179], v[56:59]\n"
      "v_mfma_f32_16x16x32_bf16 v[60:63], v[64:67], a[240:243], v[60:63]\n"
      "s_waitcnt lgkmcnt(2)\n"
      "v_mfma_f32_16x16x32_bf16 v[56:59], v[68:71], a[180:183], v[56:59]\n"
      "v_mfma_f32_16x16x32_bf16 v[60:63], v[68:71], a[244:247], v[60:63]\n"
      "s_waitcnt lgkmcnt(1)\n"
      "v_mfma_f32_16x16x32_bf16 v[56:59], v[72:75], a[184:187], v[56:59]\n"
      "v_mfma_f32_16x16x32_bf16 v[60:63], v[72:75], a[248:251], v[60:63]\n"
      "s_waitcnt lgkmcnt(0)\n"
      "v_mfma_f32_16x16x32_bf16 v[56:59], v[76:79], a[188:191], v[56:59]\n"
      "v_mfma_f32_16x16x32_bf16 v[60:63], v[76:79], a[252:255], v[60:63]\n"
      "s_and_b32 s65, s62, 7\n"
      "s_add_u32 s65, s65, 8\n"
      "s_lshl_b32 s65, s65, 13\n"
      "s_lshl_b32 s66, s56, 8\n"
      "s_add_u32 s65, s65, s66\n"
      "s_lshl_b32 s66, s53, 6\n"
      "s_add_u32 s65, s65, s66\n"
      "s_add_u32 s65, s65, 528384\n"
      "s_add_u32 s50, s40, s65\n"
      "s_addc_u32 s51, s41, 0\n"
      "v_cndmask_b32 v32, v56, v60, s[94:95]\n"
      "v_mul_f32 v32, 0x3e0293ee, v32\n"
      "s_nop 1\n"
      "v_mov_b32_dpp v13, v32 quad_perm:[1,0,3,2] row_mask:0xf bank_mask:0xf\n"
      "v_cvt_pk_bf16_f32 v32, v32, v13\n"
      "v_min_u32 v32, -2, v32\n"
      "s_mov_b32 exec_lo, 0x55555555\n"
      "s_mov_b32 exec_hi, 0x0\n"
      "global_store_dword v10, v32, s[50:51] sc1\n"
      "s_mov_b64 exec, -1\n"
      "s_branch C_next1_%=\n"
      "C_s23A_%=:\n"
      "v_dot2_f32_bf16 v32, v16, s24, 0\n"
      "v_dot2_f32_bf16 v33, v16, s25, 0\n"
      "v_dot2_f32_bf16 v34, v17, s24, 0\n"
      "v_dot2_f32_bf16 v35, v17, s25, 0\n"
      "v_dot2_f32_bf16 v36, v18, s24, 0\n"
      "v_dot2_f32_bf16 v37, v18, s25, 0\n"
      "v_dot2_f32_bf16 v38, v19, s24, 0\n"
      "v_dot2_f32_bf16 v39, v19, s25, 0\n"
      "v_dot2_f32_bf16 v32, v20, s24, v32\n"
      "v_dot2_f32_bf16 v33, v20, s25, v33\n"
      "v_dot2_f32_bf16 v34, v21, s24, v34\n"
      "v_dot2_f32_bf16 v35, v21, s25, v35\n"
      "v_dot2_f32_bf16 v36, v22, s24, v36\n"
      "v_dot2_f32_bf16 v37, v22, s25, v37\n"
      "v_dot2_f32_bf16 v38, v23, s24, v38\n"
      "v_dot2_f32_bf16 v39, v23, s25, v39\n"
      "v_dot2_f32_bf16 v32, v24, s24, v32\n"
      "v_dot2_f32_bf16 v33, v24, s25, v33\n"
      "v_dot2_f32_bf16 v34, v25, s24, v34\n"
      "v_dot2_f32_bf16 v35, v25, s25, v35\n"
      "v_dot2_f32_bf16 v36, v26, s24, v36\n"
      "v_dot2_f32_bf16 v37, v26, s25, v37\n"
      "v_dot2_f32_bf16 v38, v27, s24, v38\n"
      "v_dot2_f32_bf16 v39, v27, s25, v39\n"
      "v_dot2_f32_bf16 v32, v28, s24, v32\n"
      "v_dot2_f32_bf16 v33, v28, s25, v33\n"
      "v_dot2_f32_bf16 v34, v29, s24, v34\n"
      "v_dot2_f32_bf16 v35, v29, s25, v35\n"
      "v_dot2_f32_bf16 v36, v30, s24, v36\n"
      "v_dot2_f32_bf16 v37, v30, s25, v37\n"
      "v_dot2_f32_bf16 v38, v31, s24, v38\n"
      "v_dot2_f32_bf16 v39, v31, s25, v39\n"
      "s_lshl_b32 s65, s53, 11\n"
      "s_add_u32 s65, s65, s39\n"
      "v_add_u32 v12, s65, v1\n"
      "v_add_u32 v13, s39, v1\n"
      "ds_write_b128 v12, v[32:35]\n"
      "ds_write_b128 v12, v[36:39] offset:1024\n"
      "s_waitcnt lgkmcnt(0)\n"
      "s_barrier\n"
      "ds_read_b128 v[64:67], v13 offset:0\n"
      "ds_read_b128 v[68:71], v13 offset:1024\n"
      "ds_read_b128 v[72:75], v13 offset:2048\n"
      "ds_read_b128 v[76:79], v13 offset:3072\n"
      "ds_read_b128 v[80:83], v13 offset:4096\n"
      "ds_read_b128 v[84:87], v13 offset:5120\n"
      "ds_read_b128 v[88:91], v13 offset:6144\n"
      "ds_read_b128 v[92:95], v13 offset:7168\n"
      "s_mov_b32 s68, s61\n"
      "s_swappc_b64 s[34:35], s[36:37]\n"
      "s_waitcnt lgkmcnt(4)\n"
      "v_pk_add_f32 v[32:33], v[64:65], v[72:73]\n"
      "v_pk_add_f32 v[34:35], v[66:67], v[74:75]\n"
      "v_pk_add_f32 v[36:37], v[68:69], v[76:77]\n"
      "v_pk_add_f32 v[38:39], v[70:71], v[78:79]\n"
      "s_waitcnt lgkmcnt(0)\n"
      "v_pk_add_f32 v[40:41], v[80:81], v[88:89]\n"
      "v_pk_add_f32 v[42:43], v[82:83], v[90:91]\n"
      "v_pk_add_f32 v[44:45], v[84:85], v[92:93]\n"
      "v_pk_add_f32 v[46:47], v[86:87], v[94:95]\n"
      "v_pk_add_f32 v[32:33], v[32:33], v[40:41]\n"
      "v_pk_add_f32 v[34:35], v[34:35], v[42:43]\n"
      "v_pk_add_f32 v[36:37], v[36:37], v[44:45]\n"
      "v_pk_add_f32 v[38:39], v[38:39], v[46:47]\n"
      "s_lshl_b32 s65, s57, 2\n"
      "s_add_u32 s65, s65, s53\n"
      "s_cmp_eq_u32 s65, 0\n"
      "s_cbranch_scc0 C_nox127_%=\n"
      "v_lshlrev_b32 v40, 1, v1\n"
      "v_add_u32 v40, 49152, v40\n"
      "ds_write_b128 v40, v[32:35]\n"
      "ds_write_b128 v40, v[36:39] offset:16\n"
      "C_nox127_%=:\n"
      "v_readfirstlane_b32 s66, v32\n"
      "s_nop 0\n"
      "s_mov_b32 s67, s66\n"
      "v_pk_add_f32 v[40:41], v[32:33], s[66:67] neg_lo:[0,1] neg_hi:[0,1]\n"
      "v_pk_add_f32 v[42:43], v[34:35], s[66:67] neg_lo:[0,1] neg_hi:[0,1]\n"
      "v_pk_add_f32 v[44:45], v[36:37], s[66:67] neg_lo:[0,1] neg_hi:[0,1]\n"
      "v_pk_add_f32 v[46:47], v[38:39], s[66:67] neg_lo:[0,1] neg_hi:[0,1]\n"
      "v_pk_mul_f32 v[56:57], v[40:41], v[40:41]\n"
      "v_pk_add_f32 v[48:49], v[40:41], v[42:43]\n"
      "v_pk_fma_f32 v[56:57], v[42:43], v[42:43], v[56:57]\n"
      "v_pk_add_f32 v[50:51], v[44:45], v[46:47]\n"
      "v_pk_fma_f32 v[56:57], v[44:45], v[44:45], v[56:57]\n"
      "v_pk_add_f32 v[48:49], v[48:49], v[50:51]\n"
      "v_pk_fma_f32 v[56:57], v[46:47], v[46:47], v[56:57]\n"
      "v_add_f32 v48, v48, v49\n"
      "v_add_f32 v49, v56, v57\n"
      "s_nop 0\n"
      "v_add_f32_dpp v48, v48, v48 quad_perm:[1,0,3,2] row_mask:0xf bank_mask:0xf\n"
      "v_add_f32_dpp v49, v49, v49 quad_perm:[1,0,3,2] row_mask:0xf bank_mask:0xf\n"
      "s_nop 0\n"
      "v_add_f32_dpp v48, v48, v48 quad_perm:[2,3,0,1] row_mask:0xf bank_mask:0xf\n"
      "v_add_f32_dpp v49, v49, v49 quad_perm:[2,3,0,1] row_mask:0xf bank_mask:0xf\n"
      "s_nop 0\n"
      "v_add_f32_dpp v48, v48, v48 row_half_mirror row_mask:0xf bank_mask:0xf\n"
      "v_add_f32_dpp v49, v49, v49 row_half_mirror row_mask:0xf bank_mask:0xf\n"
      "s_nop 0\n"
      "v_add_f32_dpp v48, v48, v48 row_ror:8 row_mask:0xf bank_mask:0xf\n"
      "v_add_f32_dpp v49, v49, v49 row_ror:8 row_mask:0xf bank_mask:0xf\n"
      "v_mov_b32 v12, v48\n"
      "v_mov_b32 v13, v49\n"
      "s_nop 0\n"
      "v_permlane16_swap_b32 v12, v48\n"
      "v_permlane16_swap_b32 v13, v49\n"
      "v_add_f32 v48, v12, v48\n"
      "v_add_f32 v49, v13, v49\n"
      "v_mov_b32 v12, v48\n"
      "v_mov_b32 v13, v49\n"
      "s_nop 0\n"
      "v_permlane32_swap_b32 v12, v48\n"
      "v_permlane32_swap_b32 v13, v49\n"
      "v_add_f32 v48, v12, v48\n"
      "v_add_f32 v49, v13, v49\n"
      "v_mul_f32 v48, 0x3b000000, v48\n"
      "v_mul_f32 v49, 0x3b000000, v49\n"
      "v_fma_f32 v49, -v48, v48, v49\n"
      "v_add_f32 v49, 0x3727c5ac, v49\n"
      "v_rsq_f32 v49, v49\n"
      "s_nop 0\n"
      "v_mul_f32 v48, v48, v49\n"
      "v_pk_fma_f32 v[40:41], v[40:41], v[48:49], v[48:49] op_sel:[0,1,0] op_sel_hi:[1,1,0] neg_lo:[0,0,1] neg_hi:[0,0,1]\n"
      "v_pk_fma_f32 v[42:43], v[42:43], v[48:49], v[48:49] op_sel:[0,1,0] op_sel_hi:[1,1,0] neg_lo:[0,0,1] neg_hi:[0,0,1]\n"
      "v_pk_fma_f32 v[44:45], v[44:45], v[48:49], v[48:49] op_sel:[0,1,0] op_sel_hi:[1,1,0] neg_lo:[0,0,1] neg_hi:[0,0,1]\n"
      "v_pk_fma_f32 v[46:47], v[46:47], v[48:49], v[48:49] op_sel:[0,1,0] op_sel_hi:[1,1,0] neg_lo:[0,0,1] neg_hi:[0,0,1]\n"
      "v_cvt_pk_bf16_f32 v52, v40, v41\n"
      "v_cvt_pk_bf16_f32 v53, v42, v43\n"
      "v_cvt_pk_bf16_f32 v54, v44, v45\n"
      "v_cvt_pk_bf16_f32 v55, v46, v47\n"
      "s_lshl_b32 s65, s53, 10\n"
      "s_add_u32 s65, s65, 8192\n"
      "v_add_u32 v12, s65, v1\n"
      "v_add_u32 v13, s65, v6\n"
      "ds_write_b128 v12, v[52:55]\n"
      "ds_read_b128 v[64:67], v13 offset:0\n"
      "ds_read_b128 v[68:71], v13 offset:64\n"
      "ds_read_b128 v[72:75], v13 offset:128\n"
      "ds_read_b128 v[76:79], v13 offset:192\n"
      "ds_read_b128 v[80:83], v13 offset:256\n"
      "ds_read_b128 v[84:87], v13 offset:320\n"
      "ds_read_b128 v[88:91], v13 offset:384\n"
      "ds_read_b128 v[92:95], v13 offset:448\n"
      "ds_read_b128 v[96:99], v13 offset:512\n"
      "ds_read_b128 v[100:103], v13 offset:576\n"
      "ds_read_b128 v[104:107], v13 offset:640\n"
      "ds_read_b128 v[108:111], v13 offset:704\n"
      "s_waitcnt lgkmcnt(11)\n"
      "v_mfma_f32_16x16x32_bf16 v[56:59], v[64:67], a[0:3], 0\n"
      "v_mfma_f32_16x16x32_bf16 v[60:63], v[64:67], a[64:67], 0\n"
      "ds_read_b128 v[64:67], v13 offset:768\n"
      "s_waitcnt lgkmcnt(11)\n"
      "v_mfma_f32_16x16x32_bf16 v[56:59], v[68:71], a[4:7], v[56:59]\n"
      "v_mfma_f32_16x16x32_bf16 v[60:63], v[68:71], a[68:71], v[60:63]\n"
      "ds_read_b128 v[68:71], v13 offset:832\n"
      "s_waitcnt lgkmcnt(11)\n"
      "v_mfma_f32_16x16x32_bf16 v[56:59], v[72:75], a[8:11], v[56:59]\n"
      "v_mfma_f32_16x16x32_bf16 v[60:63], v[72:75], a[72:75], v[60:63]\n"
      "ds_read_b128 v[72:75], v13 offset:896\n"
      "s_waitcnt lgkmcnt(11)\n"
      "v_mfma_f32_16x16x32_bf16 v[56:59], v[76:79], a[12:15], v[56:59]\n"
      "v_mfma_f32_16x16x32_bf16 v[60:63], v[76:79], a[76:79], v[60:63]\n"
      "ds_read_b128 v[76:79], v13 offset:960\n"
      "s_waitcnt lgkmcnt(11)\n"
      "v_mfma_f32_16x16x32_bf16 v[56:59], v[80:83], a[16:19], v[56:59]\n"
      "v_mfma_f32_16x16x32_bf16 v[60:63], v[80:83], a[80:83], v[60:63]\n"
      "s_waitcnt lgkmcnt(10)\n"
      "v_mfma_f32_16x16x32_bf16 v[56:59], v[84:87], a[20:23], v[56:59]\n"
      "v_mfma_f32_16x16x32_bf16 v[60:63], v[84:87], a[84:87], v[60:63]\n"
      "s_waitcnt lgkmcnt(9)\n"
      "v_mfma_f32_16x16x32_bf16 v[56:59], v[88:91], a[24:27], v[56:59]\n"
      "v_mfma_f32_16x16x32_bf16 v[60:63], v[88:91], a[88:91], v[60:63]\n"
      "s_waitcnt lgkmcnt(8)\n"
      "v_mfma_f32_16x16x32_bf16 v[56:59], v[92:95], a[28:31], v[56:59]\n"
      "v_mfma_f32_16x16x32_bf16 v[60:63], v[92:95], a[92:95], v[60:63]\n"
      "s_waitcnt lgkmcnt(7)\n"
      "v_mfma_f32_16x16x32_bf16 v[56:59], v[96:99], a[32:35], v[56:59]\n"
      "v_mfma_f32_16x16x32_bf16 v[60:63], v[96:99], a[96:99], v[60:63]\n"
      "s_waitcnt lgkmcnt(6)\n"
      "v_mfma_f32_16x16x32_bf16 v[56:59], v[100:103], a[36:39], v[56:59]\n"
      "v_mfma_f32_16x16x32_bf16 v[60:63], v[100:103], a[100:103], v[60:63]\n"
      "s_waitcnt lgkmcnt(5)\n"
      "v_mfma_f32_16x16x32_bf16 v[56:59], v[104:107], a[40:43], v[56:59]\n"
      "v_mfma_f32_16x16x32_bf16 v[60:63], v[104:107], a[104:107], v[60:63]\n"
      "s_waitcnt lgkmcnt(4)\n"
      "v_mfma_f32_16x16x32_bf16 v[56:59], v[108:111], a[44:47], v[56:59]\n"
      "v_mfma_f32_16x16x32_bf16 v[60:63], v[108:111], a[108:111], v[60:63]\n"
      "s_waitcnt lgkmcnt(3)\n"
      "v_mfma_f32_16x16x32_bf16 v[56:59], v[64:67], a[48:51], v[56:59]\n"
      "v_mfma_f32_16x16x32_bf16 v[60:63], v[64:67], a[112:115], v[60:63]\n"
      "s_waitcnt lgkmcnt(2)\n"
      "v_mfma_f32_16x16x32_bf16 v[56:59], v[68:71], a[52:55], v[56:59]\n"
      "v_mfma_f32_16x16x32_bf16 v[60:63], v[68:71], a[116:119], v[60:63]\n"
      "s_waitcnt lgkmcnt(1)\n"
      "v_mfma_f32_16x16x32_bf16 v[56:59], v[72:75], a[56:59], v[56:59]\n"
      "v_mfma_f32_16x16x32_bf16 v[60:63], v[72:75], a[120:123], v[60:63]\n"
      "s_waitcnt lgkmcnt(0)\n"
      "v_mfma_f32_16x16x32_bf16 v[56:59], v[76:79], a[60:63], v[56:59]\n"
      "v_mfma_f32_16x16x32_bf16 v[60:63], v[76:79], a[124:127], v[60:63]\n"
      "s_lshl_b32 s65, s53, 6\n"
      "s_add_u32 s65, s65, s79\n"
      "v_add_u32 v40, s65, v10\n"
      "v_add_u32 v41, s79, v6\n"
      "s_nop 3\n"
      "v_cndmask_b32 v32, v56, v60, s[94:95]\n"
      "v_mul_f32 v33, 0xbfb8aa3b, v32\n"
      "v_exp_f32 v33, v33\n"
      "s_nop 0\n"
      "v_add_f32 v33, 1.0, v33\n"
      "v_rcp_f32 v33, v33\n"
      "s_nop 0\n"
      "v_mul_f32 v32, v32, v33\n"
      "s_nop 1\n"
      "v_mov_b32_dpp v33, v32 quad_perm:[1,0,3,2] row_mask:0xf bank_mask:0xf\n"
      "v_cvt_pk_bf16_f32 v32, v32, v33\n"
      "s_mov_b32 exec_lo, 0x55555555\n"
      "s_mov_b32 exec_hi, 0x0\n"
      "ds_write_b32 v40, v32\n"
      "s_mov_b64 exec, -1\n"
      "s_waitcnt lgkmcnt(0)\n"
      "s_barrier\n"
      "ds_read_b128 v[64:67], v41 offset:0\n"
      "ds_read_b128 v[68:71], v41 offset:64\n"
      "ds_read_b128 v[72:75], v41 offset:128\n"
      "ds_read_b128 v[76:79], v41 offset:192\n"
      "v_mov_b32 v34, 0\n"
      "v_mov_b32 v35, 0\n"
      "s_cmp_eq_u32 s57, 0\n"
      "s_cbranch_scc0 C_nor28_%=\n"
      "s_lshl_b32 s65, s53, 9\n"
      "s_add_u32 s65, s65, 49152\n"
      "v_add_u32 v42, s65, v9\n"
      "ds_read_b32 v34, v42\n"
      "ds_read_b32 v35, v42 offset:256\n"
      "s_waitcnt lgkmcnt(0)\n"
      "C_nor28_%=:\n"
      "s_mul_i32 s65, s62, 14336\n"
      "s_lshl_b32 s66, s57, 10\n"
      "s_add_u32 s65, s65, s66\n"
      "s_lshl_b32 s66, s53, 8\n"
      "s_add_u32 s65, s65, s66\n"
      "s_add_u32 s65, s65, 6230016\n"
      "s_add_u32 s50, s40, s65\n"
      "s_addc_u32 s51, s41, 0\n"
      "s_waitcnt lgkmcnt(3)\n"
      "v_mfma_f32_16x16x32_bf16 v[80:83], v[64:67], v[128:131], 0\n"
      "v_mfma_f32_16x16x32_bf16 v[84:87], v[64:67], v[144:147], 0\n"
      "v_mfma_f32_16x16x32_bf16 v[88:91], v[64:67], v[160:163], 0\n"
      "v_mfma_f32_16x16x32_bf16 v[92:95], v[64:67], v[176:179], 0\n"
      "v_mfma_f32_16x16x32_bf16 v[96:99], v[64:67], v[192:195], 0\n"
      "v_mfma_f32_16x16x32_bf16 v[100:103], v[64:67], v[208:211], 0\n"
      "v_mfma_f32_16x16x32_bf16 v[104:107], v[64:67], v[224:227], 0\n"
      "v_mfma_f32_16x16x32_bf16 v[108:111], v[64:67], v[240:243], 0\n"
      "s_waitcnt lgkmcnt(2)\n"
      "v_mfma_f32_16x16x32_bf16 v[80:83], v[68:71], v[132:135], v[80:83]\n"
      "v_mfma_f32_16x16x32_bf16 v[84:87], v[68:71], v[148:151], v[84:87]\n"
      "v_mfma_f32_16x16x32_bf16 v[88:91], v[68:71], v[164:167], v[88:91]\n"
      "v_mfma_f32_16x16x32_bf16 v[92:95], v[68:71], v[180:183], v[92:95]\n"
      "v_mfma_f32_16x16x32_bf16 v[96:99], v[68:71], v[196:199], v[96:99]\n"
      "v_mfma_f32_16x16x32_bf16 v[100:103], v[68:71], v[212:215], v[100:103]\n"
      "v_mfma_f32_16x16x32_bf16 v[104:107], v[68:71], v[228:231], v[104:107]\n"
      "v_mfma_f32_16x16x32_bf16 v[108:111], v[68:71], v[244:247], v[108:111]\n"
      "s_waitcnt lgkmcnt(1)\n"
      "v_mfma_f32_16x16x32_bf16 v[80:83], v[72:75], v[136:139], v[80:83]\n"
      "v_mfma_f32_16x16x32_bf16 v[84:87], v[72:75], v[152:155], v[84:87]\n"
      "v_mfma_f32_16x16x32_bf16 v[88:91], v[72:75], v[168:171], v[88:91]\n"
      "v_mfma_f32_16x16x32_bf16 v[92:95], v[72:75], v[184:187], v[92:95]\n"
      "v_mfma_f32_16x16x32_bf16 v[96:99], v[72:75], v[200:203], v[96:99]\n"
      "v_mfma_f32_16x16x32_bf16 v[100:103], v[72:75], v[216:219], v[100:103]\n"
      "v_mfma_f32_16x16x32_bf16 v[104:107], v[72:75], v[232:235], v[104:107]\n"
      "v_mfma_f32_16x16x32_bf16 v[108:111], v[72:75], v[248:251], v[108:111]\n"
      "s_waitcnt lgkmcnt(0)\n"
      "v_mfma_f32_16x16x32_bf16 v[80:83], v[76:79], v[140:143], v[80:83]\n"
      "v_mfma_f32_16x16x32_bf16 v[84:87], v[76:79], v[156:159], v[84:87]\n"
      "v_mfma_f32_16x16x32_bf16 v[88:91], v[76:79], v[172:175], v[88:91]\n"
      "v_mfma_f32_16x16x32_bf16 v[92:95], v[76:79], v[188:191], v[92:95]\n"
      "v_mfma_f32_16x16x32_bf16 v[96:99], v[76:79], v[204:207], v[96:99]\n"
      "v_mfma_f32_16x16x32_bf16 v[100:103], v[76:79], v[220:223], v[100:103]\n"
      "v_mfma_f32_16x16x32_bf16 v[104:107], v[76:79], v[236:239], v[104:107]\n"
      "v_mfma_f32_16x16x32_bf16 v[108:111], v[76:79], v[252:255], v[108:111]\n"
      "s_nop 1\n"
      "v_cndmask_b32 v32, v80, v84, s[94:95]\n"
      "s_nop 0\n"
      "v_cndmask_b32 v12, v88, v92, s[96:97]\n"
      "v_cndmask_b32 v32, v32, v12, s[98:99]\n"
      "v_cndmask_b32 v33, v96, v100, s[94:95]\n"
      "s_nop 0\n"
      "v_cndmask_b32 v12, v104, v108, s[96:97]\n"
      "v_cndmask_b32 v33, v33, v12, s[98:99]\n"
      "v_add_f32 v32, v32, v34\n"
      "v_add_f32 v33, v33, v35\n"
      "s_nop 0\n"
      "v_mov_b32_dpp v13, v32 quad_perm:[1,0,3,2] row_mask:0xf bank_mask:0xf\n"
      "v_cvt_pk_bf16_f32 v32, v32, v13\n"
      "v_min_u32 v32, -2, v32\n"
      "v_mov_b32_dpp v13, v33 quad_perm:[1,0,3,2] row_mask:0xf bank_mask:0xf\n"
      "v_cvt_pk_bf16_f32 v33, v33, v13\n"
      "v_min_u32 v33, -2, v33\n"
      "s_mov_b32 exec_lo, 0x55555555\n"
      "s_mov_b32 exec_hi, 0x55555555\n"
      "global_store_dword v10, v32, s[50:51] sc1\n"
      "global_store_dword v10, v33, s[50:51] offset:128 sc1\n"
      "s_mov_b64 exec, -1\n"
      "s_branch C_next2_%=\n"
      "C_fin_%=:\n"
      "v_cndmask_b32 v48, v20, 0, s[96:97]\n"
      "v_cndmask_b32 v49, v21, 0, s[96:97]\n"
      "v_cndmask_b32 v50, v22, 0, s[96:97]\n"
      "v_cndmask_b32 v51, v23, 0, s[96:97]\n"
      "v_lshlrev_b32 v32, 16, v16\n"
      "v_and_b32 v33, 0xffff0000, v16\n"
      "v_lshlrev_b32 v40, 16, v48\n"
      "v_and_b32 v41, 0xffff0000, v48\n"
      "v_lshlrev_b32 v34, 16, v17\n"
      "v_and_b32 v35, 0xffff0000, v17\n"
      "v_lshlrev_b32 v42, 16, v49\n"
      "v_and_b32 v43, 0xffff0000, v49\n"
      "v_lshlrev_b32 v36, 16, v18\n"
      "v_and_b32 v37, 0xffff0000, v18\n"
      "v_lshlrev_b32 v44, 16, v50\n"
      "v_and_b32 v45, 0xffff0000, v50\n"
      "v_lshlrev_b32 v38, 16, v19\n"
      "v_and_b32 v39, 0xffff0000, v19\n"
      "v_lshlrev_b32 v46, 16, v51\n"
      "v_and_b32 v47, 0xffff0000, v51\n"
      "v_pk_add_f32 v[32:33], v[32:33], v[40:41]\n"
      "v_pk_add_f32 v[34:35], v[34:35], v[42:43]\n"
      "v_pk_add_f32 v[36:37], v[36:37], v[44:45]\n"
      "v_pk_add_f32 v[38:39], v[38:39], v[46:47]\n"
      "s_mov_b32 s68, s61\n"
      "s_swappc_b64 s[34:35], s[36:37]\n"
      "v_add_f32_dpp v32, v32, v32 row_ror:8 row_mask:0xf bank_mask:0xf\n"
      "v_add_f32_dpp v33, v33, v33 row_ror:8 row_mask:0xf bank_mask:0xf\n"
      "v_add_f32_dpp v34, v34, v34 row_ror:8 row_mask:0xf bank_mask:0xf\n"
      "v_add_f32_dpp v35, v35, v35 row_ror:8 row_mask:0xf bank_mask:0xf\n"
      "v_add_f32_dpp v36, v36, v36 row_ror:8 row_mask:0xf bank_mask:0xf\n"
      "v_add_f32_dpp v37, v37, v37 row_ror:8 row_mask:0xf bank_mask:0xf\n"
      "v_add_f32_dpp v38, v38, v38 row_ror:8 row_mask:0xf bank_mask:0xf\n"
      "v_add_f32_dpp v39, v39, v39 row_ror:8 row_mask:0xf bank_mask:0xf\n"
      "v_mov_b32 v40, v32\n"
      "v_mov_b32 v41, v33\n"
      "v_mov_b32 v42, v34\n"
      "v_mov_b32 v43, v35\n"
      "v_mov_b32 v44, v36\n"
      "v_mov_b32 v45, v37\n"
      "v_mov_b32 v46, v38\n"
      "v_mov_b32 v47, v39\n"
      "v_permlane16_swap_b32 v40, v32\n"
      "v_permlane16_swap_b32 v41, v33\n"
      "v_permlane16_swap_b32 v42, v34\n"
      "v_permlane16_swap_b32 v43, v35\n"
      "v_permlane16_swap_b32 v44, v36\n"
      "v_permlane16_swap_b32 v45, v37\n"
      "v_permlane16_swap_b32 v46, v38\n"
      "v_permlane16_swap_b32 v47, v39\n"
      "v_add_f32 v32, v40, v32\n"
      "v_add_f32 v33, v41, v33\n"
      "v_add_f32 v34, v42, v34\n"
      "v_add_f32 v35, v43, v35\n"
      "v_add_f32 v36, v44, v36\n"
      "v_add_f32 v37, v45, v37\n"
      "v_add_f32 v38, v46, v38\n"
      "v_add_f32 v39, v47, v39\n"
      "v_mov_b32 v40, v32\n"
      "v_mov_b32 v41, v33\n"
      "v_mov_b32 v42, v34\n"
      "v_mov_b32 v43, v35\n"
      "v_mov_b32 v44, v36\n"
      "v_mov_b32 v45, v37\n"
      "v_mov_b32 v46, v38\n"
      "v_mov_b32 v47, v39\n"
      "v_permlane32_swap_b32 v40, v32\n"
      "v_permlane32_swap_b32 v41, v33\n"
      "v_permlane32_swap_b32 v42, v34\n"
      "v_permlane32_swap_b32 v43, v35\n"
      "v_permlane32_swap_b32 v44, v36\n"
      "v_permlane32_swap_b32 v45, v37\n"
      "v_permlane32_swap_b32 v46, v38\n"
      "v_permlane32_swap_b32 v47, v39\n"
      "v_add_f32 v32, v40, v32\n"
      "v_add_f32 v33, v41, v33\n"
      "v_add_f32 v34, v42, v34\n"
      "v_add_f32 v35, v43, v35\n"
      "v_add_f32 v36, v44, v36\n"
      "v_add_f32 v37, v45, v37\n"
      "v_add_f32 v38, v46, v38\n"
      "v_add_f32 v39, v47, v39\n"
      "v_cvt_pk_bf16_f32 v40, v32, v33\n"
      "v_cvt_pk_bf16_f32 v41, v34, v35\n"
      "v_cvt_pk_bf16_f32 v42, v36, v37\n"
      "v_cvt_pk_bf16_f32 v43, v38, v39\n"
      "s_and_b32 s65, s62, 7\n"
      "s_lshl_b32 s65, s65, 10\n"
      "s_sub_u32 s66, s52, 38\n"
      "s_lshl_b32 s66, s66, 2\n"
      "s_add_u32 s66, s66, s53\n"
      "s_lshl_b32 s66, s66, 7\n"
      "s_add_u32 s65, s65, s66\n"
      "s_add_u32 s50, s44, s65\n"
      "s_addc_u32 s51, s45, 0\n"
      "s_mov_b32 exec_lo, 0xff\n"
      "s_mov_b32 exec_hi, 0x0\n"
      "global_store_dwordx4 v1, v[40:43], s[50:51]\n"
      "s_mov_b64 exec, -1\n"
      "s_branch C_next1_%=\n"
      "C_issue_%=:\n"
      "s_cmp_eq_u32 s68, 255\n"
      "s_cbranch_scc1 C_ir_%=\n"
      "s_and_b32 s67, s68, 15\n"
      "s_lshr_b32 s68, s68, 4\n"
      "s_cmp_eq_u32 s68, 1\n"
      "s_cbranch_scc1 C_i23_%=\n"
      "s_cmp_eq_u32 s68, 2\n"
      "s_cbranch_scc1 C_i4_%=\n"
      "s_cmp_eq_u32 s68, 3\n"
      "s_cbranch_scc1 C_if_%=\n"
      "s_lshr_b32 s66, s55, 1\n"
      "s_lshl_b32 s66, s66, 1\n"
      "s_lshr_b32 s68, s53, 1\n"
      "s_add_u32 s66, s66, s68\n"
      "s_mul_i32 s68, s67, 331776\n"
      "s_mul_i32 s65, s66, 10368\n"
      "s_add_u32 s68, s68, s65\n"
      "s_add_u32 s68, s68, 659456\n"
      "s_add_u32 s46, s40, s68\n"
      "s_addc_u32 s47, s41, 0\n"
      "s_add_u32 s48, s46, 3840\n"
      "s_addc_u32 s49, s47, 0\n"
      "s_add_u32 s50, s46, 7680\n"
      "s_addc_u32 s51, s47, 0\n"
      "s_cmp_eq_u32 s23, 1\n"
      "s_cbranch_scc0 C_i1f_%=\n"
      "s_mov_b32 exec_lo, 0x4040201\n"
      "s_mov_b32 exec_hi, 0\n"
      "global_load_dword v30, v4, s[46:47] sc1\n"
      "s_mov_b64 exec, -1\n"
      "s_setpc_b64 s[34:35]\n"
      "C_i1f_%=:\n"
      "global_load_dword v30, v4, s[46:47] sc1\n"
      "global_load_dword v16, v2, s[46:47] offset:0 sc1\n"
      "global_load_dword v17, v2, s[46:47] offset:768 sc1\n"
      "global_load_dword v18, v2, s[46:47] offset:1536 sc1\n"
      "global_load_dword v19, v2, s[46:47] offset:2304 sc1\n"
      "global_load_dword v20, v2, s[46:47] offset:3072 sc1\n"
      "global_load_dword v21, v2, s[48:49] offset:0 sc1\n"
      "global_load_dword v22, v2, s[48:49] offset:768 sc1\n"
      "global_load_dword v23, v2, s[48:49] offset:1536 sc1\n"
      "global_load_dword v24, v2, s[48:49] offset:2304 sc1\n"
      "global_load_dword v25, v2, s[48:49] offset:3072 sc1\n"
      "global_load_dword v26, v2, s[50:51] offset:0 sc1\n"
      "global_load_dword v27, v2, s[50:51] offset:768 sc1\n"
      "global_load_dword v28, v2, s[50:51] offset:1536 sc1\n"
      "global_load_dword v29, v3, s[50:51] offset:2304 sc1\n"
      "v_mov_b32 v31, 0\n"
      "s_setpc_b64 s[34:35]\n"
      "C_i23_%=:\n"
      "s_lshl_b32 s68, s67, 14\n"
      "s_lshl_b32 s65, s53, 12\n"
      "s_add_u32 s68, s68, s65\n"
      "s_add_u32 s68, s68, 5967872\n"
      "s_add_u32 s46, s40, s68\n"
      "s_addc_u32 s47, s41, 0\n"
      "s_cmp_eq_u32 s23, 1\n"
      "s_cbranch_scc0 C_i23f_%=\n"
      "s_mov_b64 exec, 1\n"
      "global_load_dword v16, v1, s[46:47] sc1\n"
      "global_load_dword v20, v1, s[46:47] offset:1024 sc1\n"
      "s_mov_b64 exec, -1\n"
      "s_setpc_b64 s[34:35]\n"
      "C_i23f_%=:\n"
      "global_load_dwordx4 v[16:19], v1, s[46:47] offset:0 sc1\n"
      "global_load_dwordx4 v[20:23], v1, s[46:47] offset:1024 sc1\n"
      "global_load_dwordx4 v[24:27], v1, s[46:47] offset:2048 sc1\n"
      "global_load_dwordx4 v[28:31], v1, s[46:47] offset:3072 sc1\n"
      "s_setpc_b64 s[34:35]\n"
      "C_i4_%=:\n"
      "s_mul_i32 s68, s67, 14336\n"
      "s_lshl_b32 s65, s53, 12\n"
      "s_add_u32 s68, s68, s65\n"
      "s_add_u32 s68, s68, 6230016\n"
      "s_add_u32 s46, s40, s68\n"
      "s_addc_u32 s47, s41, 0\n"
      "s_cmp_eq_u32 s23, 1\n"
      "s_cbranch_scc0 C_i4f_%=\n"
      "s_mov_b64 exec, 1\n"
      "global_load_dword v16, v1, s[46:47] sc1\n"
      "global_load_dword v20, v1, s[46:47] offset:1024 sc1\n"
      "s_mov_b64 exec, -1\n"
      "s_setpc_b64 s[34:35]\n"
      "C_i4f_%=:\n"
      "s_cmp_eq_u32 s53, 3\n"
      "s_cselect_b32 s65, 0, 2048\n"
      "s_add_u32 s48, s46, s65\n"
      "s_addc_u32 s49, s47, 0\n"
      "global_load_dwordx4 v[16:19], v1, s[46:47] sc1\n"
      "global_load_dwordx4 v[20:23], v1, s[46:47] offset:1024 sc1\n"
      "global_load_dwordx4 v[24:27], v1, s[48:49] sc1\n"
      "global_load_dwordx4 v[28:31], v1, s[48:49] offset:1024 sc1\n"
      "s_setpc_b64 s[34:35]\n"
      "C_if_%=:\n"
      "s_mul_i32 s68, s67, 14336\n"
      "s_sub_u32 s65, s52, 38\n"
      "s_lshl_b32 s65, s65, 2\n"
      "s_add_u32 s65, s65, s53\n"
      "s_lshl_b32 s65, s65, 7\n"
      "s_add_u32 s68, s68, s65\n"
      "s_add_u32 s68, s68, 6230016\n"
      "s_add_u32 s46, s40, s68\n"
      "s_addc_u32 s47, s41, 0\n"
      "global_load_dwordx4 v[16:19], v7, s[46:47] sc1\n"
      "global_load_dwordx4 v[20:23], v8, s[46:47] sc1\n"
      "v_mov_b32 v24, 0\n"
      "v_mov_b32 v25, 0\n"
      "v_mov_b32 v26, 0\n"
      "v_mov_b32 v27, 0\n"
      "v_mov_b32 v28, 0\n"
      "v_mov_b32 v29, 0\n"
      "v_mov_b32 v30, 0\n"
      "v_mov_b32 v31, 0\n"
      "C_ir_%=:\n"
      "s_setpc_b64 s[34:35]\n"
      "C_issueB_%=:\n"
      "s_cmp_eq_u32 s68, 255\n"
      "s_cbranch_scc1 C_irB_%=\n"
      "s_and_b32 s67, s68, 15\n"
      "s_lshr_b32 s68, s68, 4\n"
      "s_cmp_eq_u32 s68, 1\n"
      "s_cbranch_scc1 C_i23B_%=\n"
      "s_cmp_eq_u32 s68, 2\n"
      "s_cbranch_scc1 C_i4B_%=\n"
      "s_cmp_eq_u32 s68, 3\n"
      "s_cbranch_scc1 C_ifB_%=\n"
      "s_lshr_b32 s66, s55, 1\n"
      "s_lshl_b32 s66, s66, 1\n"
      "s_lshr_b32 s68, s53, 1\n"
      "s_add_u32 s66, s66, s68\n"
      "s_mul_i32 s68, s67, 331776\n"
      "s_mul_i32 s65, s66, 10368\n"
      "s_add_u32 s68, s68, s65\n"
      "s_add_u32 s68, s68, 659456\n"
      "s_add_u32 s46, s40, s68\n"
      "s_addc_u32 s47, s41, 0\n"
      "s_add_u32 s48, s46, 3840\n"
      "s_addc_u32 s49, s47, 0\n"
      "s_add_u32 s50, s46, 7680\n"
      "s_addc_u32 s51, s47, 0\n"
      "global_load_dword v126, v4, s[46:47] sc1\n"
      "global_load_dword v112, v2, s[46:47] offset:0 sc1\n"
      "global_load_dword v113, v2, s[46:47] offset:768 sc1\n"
      "global_load_dword v114, v2, s[46:47] offset:1536 sc1\n"
      "global_load_dword v115, v2, s[46:47] offset:2304 sc1\n"
      "global_load_dword v116, v2, s[46:47] offset:3072 sc1\n"
      "global_load_dword v117, v2, s[48:49] offset:0 sc1\n"
      "global_load_dword v118, v2, s[48:49] offset:768 sc1\n"
      "global_load_dword v119, v2, s[48:49] offset:1536 sc1\n"
      "global_load_dword v120, v2, s[48:49] offset:2304 sc1\n"
      "global_load_dword v121, v2, s[48:49] offset:3072 sc1\n"
      "global_load_dword v122, v2, s[50:51] offset:0 sc1\n"
      "global_load_dword v123, v2, s[50:51] offset:768 sc1\n"
      "global_load_dword v124, v2, s[50:51] offset:1536 sc1\n"
      "global_load_dword v125, v3, s[50:51] offset:2304 sc1\n"
      "v_mov_b32 v127, 0\n"
      "s_setpc_b64 s[34:35]\n"
      "C_i23B_%=:\n"
      "s_lshl_b32 s68, s67, 14\n"
      "s_lshl_b32 s65, s53, 12\n"
      "s_add_u32 s68, s68, s65\n"
      "s_add_u32 s68, s68, 5967872\n"
      "s_add_u32 s46, s40, s68\n"
      "s_addc_u32 s47, s41, 0\n"
      "global_load_dwordx4 v[112:115], v1, s[46:47] offset:0 sc1\n"
      "global_load_dwordx4 v[116:119], v1, s[46:47] offset:1024 sc1\n"
      "global_load_dwordx4 v[120:123], v1, s[46:47] offset:2048 sc1\n"
      "global_load_dwordx4 v[124:127], v1, s[46:47] offset:3072 sc1\n"
      "s_setpc_b64 s[34:35]\n"
      "C_i4B_%=:\n"
      "s_mul_i32 s68, s67, 14336\n"
      "s_lshl_b32 s65, s53, 12\n"
      "s_add_u32 s68, s68, s65\n"
      "s_add_u32 s68, s68, 6230016\n"
      "s_add_u32 s46, s40, s68\n"
      "s_addc_u32 s47, s41, 0\n"
      "s_cmp_eq_u32 s53, 3\n"
      "s_cselect_b32 s65, 0, 2048\n"
      "s_add_u32 s48, s46, s65\n"
      "s_addc_u32 s49, s47, 0\n"
      "global_load_dwordx4 v[112:115], v1, s[46:47] sc1\n"
      "global_load_dwordx4 v[116:119], v1, s[46:47] offset:1024 sc1\n"
      "global_load_dwordx4 v[120:123], v1, s[48:49] sc1\n"
      "global_load_dwordx4 v[124:127], v1, s[48:49] offset:1024 sc1\n"
      "s_setpc_b64 s[34:35]\n"
      "C_ifB_%=:\n"
      "s_mul_i32 s68, s67, 14336\n"
      "s_sub_u32 s65, s52, 38\n"
      "s_lshl_b32 s65, s65, 2\n"
      "s_add_u32 s65, s65, s53\n"
      "s_lshl_b32 s65, s65, 7\n"
      "s_add_u32 s68, s68, s65\n"
      "s_add_u32 s68, s68, 6230016\n"
      "s_add_u32 s46, s40, s68\n"
      "s_addc_u32 s47, s41, 0\n"
      "global_load_dwordx4 v[112:115], v7, s[46:47] sc1\n"
      "global_load_dwordx4 v[116:119], v8, s[46:47] sc1\n"
      "v_mov_b32 v120, 0\n"
      "v_mov_b32 v121, 0\n"
      "v_mov_b32 v122, 0\n"
      "v_mov_b32 v123, 0\n"
      "v_mov_b32 v124, 0\n"
      "v_mov_b32 v125, 0\n"
      "v_mov_b32 v126, 0\n"
      "v_mov_b32 v127, 0\n"
      "C_irB_%=:\n"
      "s_setpc_b64 s[34:35]\n"
      "fdk_chain_end:\n"
      "C_exit_%=:\n"
      :
      : [x] "s"(x), [wq] "s"(wq), [wo] "s"(wo), [w1] "s"(w1), [w2] "s"(w2), [out] "s"(out), [ws] "s"(ws), [bid] "s"(bid), [wave] "s"(wave)
      : "v0", "v1", "v2", "v3", "v4", "v5", "v6", "v7", "v8", "v9", "v10", "v11", "v12", "v13", "v14", "v15", "v16", "v17", "v18", "v19", "v20", "v21", "v22", "v23", "v24", "v25", "v26", "v27", "v28", "v29", "v30", "v31", "v32", "v33", "v34", "v35", "v36", "v37", "v38", "v39", "v40", "v41", "v42", "v43", "v44", "v45", "v46", "v47", "v48", "v49", "v50", "v51", "v52", "v53", "v54", "v55", "v56", "v57", "v58", "v59", "v60", "v61", "v62", "v63", "v64", "v65", "v66", "v67", "v68", "v69", "v70", "v71", "v72", "v73", "v74", "v75", "v76", "v77", "v78", "v79", "v80", "v81", "v82", "v83", "v84", "v85", "v86", "v87", "v88", "v89", "v90", "v91", "v92", "v93", "v94", "v95", "v96", "v97", "v98", "v99", "v100", "v101", "v102", "v103", "v104", "v105", "v106", "v107", "v108", "v109", "v110", "v111", "v112", "v113", "v114", "v115", "v116", "v117", "v118", "v119", "v120", "v121", "v122", "v123", "v124", "v125", "v126", "v127", "v128", "v129", "v130", "v131", "v132", "v133", "v134", "v135", "v136", "v137", "v138", "v139", "v140", "v141", "v142", "v143", "v144", "v145", "v146", "v147", "v148", "v149", "v150", "v151", "v152", "v153", "v154", "v155", "v156", "v157", "v158", "v159", "v160", "v161", "v162", "v163", "v164", "v165", "v166", "v167", "v168", "v169", "v170", "v171", "v172", "v173", "v174", "v175", "v176", "v177", "v178", "v179", "v180", "v181", "v182", "v183", "v184", "v185", "v186", "v187", "v188", "v189", "v190", "v191", "v192", "v193", "v194", "v195", "v196", "v197", "v198", "v199", "v200", "v201", "v202", "v203", "v204", "v205", "v206", "v207", "v208", "v209", "v210", "v211", "v212", "v213", "v214", "v215", "v216", "v217", "v218", "v219", "v220", "v221", "v222", "v223", "v224", "v225", "v226", "v227", "v228", "v229", "v230", "v231", "v232", "v233", "v234", "v235", "v236", "v237", "v238", "v239", "v240", "v241", "v242", "v243", "v244", "v245", "v246", "v247", "v248", "v249", "v250", "v251", "v252", "v253", "v254", "v255", "a0", "a1", "a2", "a3", "a4", "a5", "a6", "a7", "a8", "a9", "a10", "a11", "a12", "a13", "a14", "a15", "a16", "a17", "a18", "a19", "a20", "a21", "a22", "a23", "a24", "a25", "a26", "a27", "a28", "a29", "a30", "a31", "a32", "a33", "a34", "a35", "a36", "a37", "a38", "a39", "a40", "a41", "a42", "a43", "a44", "a45", "a46", "a47", "a48", "a49", "a50", "a51", "a52", "a53", "a54", "a55", "a56", "a57", "a58", "a59", "a60", "a61", "a62", "a63", "a64", "a65", "a66", "a67", "a68", "a69", "a70", "a71", "a72", "a73", "a74", "a75", "a76", "a77", "a78", "a79", "a80", "a81", "a82", "a83", "a84", "a85", "a86", "a87", "a88", "a89", "a90", "a91", "a92", "a93", "a94", "a95", "a96", "a97", "a98", "a99", "a100", "a101", "a102", "a103", "a104", "a105", "a106", "a107", "a108", "a109", "a110", "a111", "a112", "a113", "a114", "a115", "a116", "a117", "a118", "a119", "a120", "a121", "a122", "a123", "a124", "a125", "a126", "a127", "a128", "a129", "a130", "a131", "a132", "a133", "a134", "a135", "a136", "a137", "a138", "a139", "a140", "a141", "a142", "a143", "a144", "a145", "a146", "a147", "a148", "a149", "a150", "a151", "a152", "a153", "a154", "a155", "a156", "a157", "a158", "a159", "a160", "a161", "a162", "a163", "a164", "a165", "a166", "a167", "a168", "a169", "a170", "a171", "a172", "a173", "a174", "a175", "a176", "a177", "a178", "a179", "a180", "a181", "a182", "a183", "a184", "a185", "a186", "a187", "a188", "a189", "a190", "a191", "a192", "a193", "a194", "a195", "a196", "a197", "a198", "a199", "a200", "a201", "a202", "a203", "a204", "a205", "a206", "a207", "a208", "a209", "a210", "a211", "a212", "a213", "a214", "a215", "a216", "a217", "a218", "a219", "a220", "a221", "a222", "a223", "a224", "a225", "a226", "a227", "a228", "a229", "a230", "a231", "a232", "a233", "a234", "a235", "a236", "a237", "a238", "a239", "a240", "a241", "a242", "a243", "a244", "a245", "a246", "a247", "a248", "a249", "a250", "a251", "a252", "a253", "a254", "a255", "s22", "s23", "s24", "s25", "s26", "s27", "s28", "s29", "s30", "s31", "s34", "s35", "s36", "s37", "s38", "s39", "s40", "s41", "s42", "s43", "s44", "s45", "s46", "s47", "s48", "s49", "s50", "s51", "s52", "s53", "s54", "s55", "s56", "s57", "s58", "s59", "s60", "s61", "s62", "s63", "s64", "s65", "s66", "s67", "s68", "s69", "s70", "s71", "s72", "s73", "s74", "s75", "s76", "s77", "s78", "s79", "s80", "s81", "s82", "s83", "s84", "s85", "s86", "s87", "s88", "s89", "s90", "s91", "s92", "s93", "s94", "s95", "s96", "s97", "s98", "s99", "vcc", "scc", "memory");
  }
}

extern "C" void kernel_launch(void* const* d_in, const int* in_sizes, int n_in, void* d_out, int out_size, void* d_ws, size_t ws_size,
                              hipStream_t stream) {
  (void)in_sizes; (void)n_in; (void)out_size; (void)ws_size;
  (void)hipFuncSetAttribute((const void*)fused_decode_kernel, hipFuncAttributeMaxDynamicSharedMemorySize, LDS_BYTES);
  hipLaunchKernelGGL(fused_decode_kernel, dim3(256), dim3(256), LDS_BYTES, stream,
                     (const u16*)d_in[0], (const u16*)d_in[1], (const u16*)d_in[2], (const u16*)d_in[3], (const u16*)d_in[4],
                     (const u16*)d_in[6], (const u16*)d_in[7], (u16*)d_out, (unsigned char*)d_ws);
}
```
